```python
import torch

def _softmax(x, axis=-1):
    return torch.softmax(x, dim=axis)
def _gelu(x):
    return torch.nn.functional.gelu(x, approximate="tanh")
def _silu(x):
    return torch.nn.functional.silu(x)
def _sigmoid(x):
    return torch.sigmoid(x)
def _layernorm(x, axis=-1, eps=1e-5):
    m = x.mean(dim=axis, keepdim=True)
    v = x.var(dim=axis, keepdim=True, unbiased=False)
    return (x - m) / torch.sqrt(v + eps)
def _scatter_add(tgt, idx, src, axis=0):
    return tgt.clone().index_add_(axis, idx, src)
def _segment_sum(src, seg, n):
    out = torch.zeros((n,) + src.shape[1:], dtype=src.dtype)
    return out.index_add_(0, seg, src)
def _pad(x, pw):
    flat = tuple(v for pair in reversed(pw) for v in pair)
    return torch.nn.functional.pad(x, flat)

kernel_name = 'standalone__cross_network_dcn__recsys'

def setup_inputs():

    _w = (lambda a: (lambda t: t.to(torch.bfloat16) if t.is_floating_point() else t)(torch.as_tensor(a)))
    B, F, D, L = 16384, 100, 16, 3
    Din = min(F * D, 1024)
    return {
        "x0": _w((lambda *s: torch.randn(*s, dtype=torch.bfloat16) * 0.05)(B, Din)),
        "ws": _w((lambda *s: torch.randn(*s, dtype=torch.bfloat16) * 0.8)(L, Din, Din) * (Din ** -0.5)),
        "bs": _w((lambda *s: torch.randn(*s, dtype=torch.bfloat16) * 3.2)(L, Din) * 0.01),
    }

def reference(x0, ws, bs):
    x = x0
    L = ws.shape[0]
    for l in range(L):
        proj = torch.einsum('bd,de->be', x, ws[l]) + bs[l]
        x = x0 * proj + x
    return x
```

```cpp
#include <hip/hip_runtime.h>
#include <hip/hip_bf16.h>

typedef unsigned short u16;
typedef short bf16x8 __attribute__((ext_vector_type(8)));
typedef float f32x4 __attribute__((ext_vector_type(4)));
typedef unsigned u32x4 __attribute__((ext_vector_type(4)));
typedef unsigned u32x2 __attribute__((ext_vector_type(2)));
#define LAS __attribute__((address_space(3)))
#define GAS __attribute__((address_space(1)))

#define ABL_NOLOAD 0
#define ABL_NOMFMA 0

constexpr int D = 1024, NL = 3, ROWS = 64, NWAVE = 8;
constexpr int KSTEPS = D / 32;
constexpr int WAVE_STREAM = NL * KSTEPS * 8 * 1024;
constexpr int WT_BYTES = NWAVE * WAVE_STREAM;

__global__ __launch_bounds__(256) void prep_w(const u16* __restrict__ W, u16* __restrict__ Wt) {
    const int wid = blockIdx.x * 4 + (threadIdx.x >> 6);
    const int lane = threadIdx.x & 63;
    const int w = wid & 7, t = (wid >> 3) & 31, l = wid >> 8;
    const int i = lane & 15, g = lane >> 4;
    const u16* src = W + (size_t)l * D * D + (size_t)(32 * t + 8 * g) * D + 128 * w + 8 * i;
    u32x4 in[8];
#pragma unroll
    for (int e = 0; e < 8; ++e) in[e] = *(const u32x4*)(src + (size_t)e * D);
    char* dst = (char*)Wt + (size_t)w * WAVE_STREAM + (size_t)(l * KSTEPS + t) * 8192;
#pragma unroll
    for (int m = 0; m < 8; ++m) {
        u32x4 o;
#pragma unroll
        for (int ep = 0; ep < 4; ++ep) {
            const unsigned a = in[2 * ep][m >> 1], b = in[2 * ep + 1][m >> 1];
            o[ep] = (m & 1) ? ((a >> 16) | (b & 0xffff0000u)) : ((a & 0xffffu) | (b << 16));
        }
        const int j = 2 * (i >> 2) + (m >> 2);
        const int lp = 16 * g + 4 * (i & 3) + (m & 3);
        *(u32x4*)(dst + j * 1024 + lp * 16) = o;
    }
}

__device__ __forceinline__ f32x4 mfma16(u32x4 a, u32x4 b, f32x4 c) {
    return __builtin_amdgcn_mfma_f32_16x16x32_bf16(__builtin_bit_cast(bf16x8, a), __builtin_bit_cast(bf16x8, b), c, 0, 0, 0);
}
__device__ __forceinline__ float bflo(unsigned v) { return __builtin_bit_cast(float, v << 16); }
__device__ __forceinline__ float bfhi(unsigned v) { return __builtin_bit_cast(float, v & 0xffff0000u); }
__device__ __forceinline__ unsigned cvt_pk_bf16(float lo, float hi) {
    unsigned r; asm("v_cvt_pk_bf16_f32 %0, %1, %2" : "=v"(r) : "v"(lo), "v"(hi)); return r;
}
#define SB() __builtin_amdgcn_sched_barrier(0)
#define STAMP_AT(k)

template <int L>
__device__ __forceinline__ void layer(LAS char* smem, const __amdgpu_buffer_rsrc_t wrs, const __amdgpu_buffer_rsrc_t xrs,
                                      const __amdgpu_buffer_rsrc_t ors, const __amdgpu_buffer_rsrc_t brs,
                                      u32x4 (&ring)[16], int& soff, int voff, int xvoff, int w, int lane, unsigned long long* stamps) {
    const int g = lane >> 4;
    const LAS char* xl = smem + lane * 16;
    f32x4 acc[4][8];
    {
#pragma unroll
        for (int u = 0; u < 4; ++u) {
            const u32x4 bv = __builtin_amdgcn_raw_buffer_load_b128(brs, 16 * g + 64 * u, (L * D + 128 * w) * 2, 0);
            const f32x4 b0 = {bflo(bv[0]), bfhi(bv[0]), bflo(bv[1]), bfhi(bv[1])};
            const f32x4 b1 = {bflo(bv[2]), bfhi(bv[2]), bflo(bv[3]), bfhi(bv[3])};
#pragma unroll
            for (int rb = 0; rb < 4; ++rb) { acc[rb][2 * u] = b0; acc[rb][2 * u + 1] = b1; }
        }
    }
    u32x4 xa[4], xb[4];
#pragma unroll
    for (int rb = 0; rb < 4; ++rb) xa[rb] = *(const LAS u32x4*)(xl + rb * 1024);
    SB();
    STAMP_AT(2 + 2 * L);
#pragma unroll 1
    for (int t = 0; t < KSTEPS; t += 2) {
        const LAS char* xp = xl + t * 4096;
#pragma unroll
        for (int j = 0; j < 8; ++j) {
#pragma unroll
            for (int rb = 0; rb < 4; ++rb) acc[rb][j] = mfma16(ring[j], xa[rb], acc[rb][j]);
            SB();
            if (!ABL_NOLOAD) ring[j] = __builtin_amdgcn_raw_buffer_load_b128(wrs, voff, soff + j * 1024, 0);
            if (j < 4) xb[j] = *(const LAS u32x4*)(xp + 4096 + j * 1024);
            SB();
        }
        const LAS char* xq = xl + ((t + 2) & 31) * 4096;
#pragma unroll
        for (int j = 0; j < 8; ++j) {
#pragma unroll
            for (int rb = 0; rb < 4; ++rb) acc[rb][j] = mfma16(ring[8 + j], xb[rb], acc[rb][j]);
            SB();
            if (!ABL_NOLOAD) ring[8 + j] = __builtin_amdgcn_raw_buffer_load_b128(wrs, voff, soff + 8192 + j * 1024, 0);
            if (j < 4) xa[j] = *(const LAS u32x4*)(xq + j * 1024);
            SB();
        }
        soff += 16384;
    }
    STAMP_AT(3 + 2 * L);
    u32x4 res[4][4];
#pragma unroll
    for (int u = 0; u < 4; ++u) {
#pragma unroll
        for (int rb = 0; rb < 4; ++rb) {
            const int tt = 4 * w + u;
            const u32x4 xv = *(const LAS u32x4*)(xl + (tt * 4 + rb) * 1024);
            u32x4 zv;
            if (L == 0) zv = xv;
            else zv = __builtin_amdgcn_raw_buffer_load_b128(xrs, xvoff + u * 64, rb * 16 * D * 2 + w * 256, 0);
            const f32x4 p0 = acc[rb][2 * u], p1 = acc[rb][2 * u + 1];
            u32x4 r;
            r[0] = cvt_pk_bf16(__builtin_fmaf(bflo(zv[0]), p0[0], bflo(xv[0])), __builtin_fmaf(bfhi(zv[0]), p0[1], bfhi(xv[0])));
            r[1] = cvt_pk_bf16(__builtin_fmaf(bflo(zv[1]), p0[2], bflo(xv[1])), __builtin_fmaf(bfhi(zv[1]), p0[3], bfhi(xv[1])));
            r[2] = cvt_pk_bf16(__builtin_fmaf(bflo(zv[2]), p1[0], bflo(xv[2])), __builtin_fmaf(bfhi(zv[2]), p1[1], bfhi(xv[2])));
            r[3] = cvt_pk_bf16(__builtin_fmaf(bflo(zv[3]), p1[2], bflo(xv[3])), __builtin_fmaf(bfhi(zv[3]), p1[3], bfhi(xv[3])));
            res[u][rb] = r;
        }
    }
    if (L + 1 < NL) {
        __builtin_amdgcn_s_waitcnt(0xc07f);
        __builtin_amdgcn_s_barrier();
#pragma unroll
        for (int u = 0; u < 4; ++u)
#pragma unroll
            for (int rb = 0; rb < 4; ++rb) *(LAS u32x4*)(smem + lane * 16 + ((4 * w + u) * 4 + rb) * 1024) = res[u][rb];
        __builtin_amdgcn_s_waitcnt(0xc07f);
        __builtin_amdgcn_s_barrier();
    } else {
#pragma unroll
        for (int u = 0; u < 4; ++u)
#pragma unroll
            for (int rb = 0; rb < 4; ++rb)
                __builtin_amdgcn_raw_buffer_store_b128(res[u][rb], ors, xvoff + u * 64, rb * 16 * D * 2 + w * 256, 0);
    }
}

__global__ __launch_bounds__(512, 2) void dcn_main(const u16* __restrict__ x0, const u16* __restrict__ Wt,
                                                   const u16* __restrict__ bs, u16* __restrict__ out) {
    extern __shared__ __attribute__((aligned(16))) char smem_raw[];
    LAS char* smem = (LAS char*)smem_raw;
    const int tid = threadIdx.x, lane = tid & 63;
    const int w = __builtin_amdgcn_readfirstlane(tid >> 6);
    const int r16 = lane & 15, g = lane >> 4;
    const int row0 = blockIdx.x * ROWS;
    unsigned long long stamps[11];
    STAMP_AT(0);

    const __amdgpu_buffer_rsrc_t wrs = __builtin_amdgcn_make_buffer_rsrc((void*)Wt, 0, WT_BYTES, 0x00020000);
    const __amdgpu_buffer_rsrc_t xrs = __builtin_amdgcn_make_buffer_rsrc((void*)(x0 + (size_t)row0 * D), 0, ROWS * D * 2, 0x00020000);
    const __amdgpu_buffer_rsrc_t ors = __builtin_amdgcn_make_buffer_rsrc((void*)(out + (size_t)row0 * D), 0, ROWS * D * 2, 0x00020000);
    const __amdgpu_buffer_rsrc_t brs = __builtin_amdgcn_make_buffer_rsrc((void*)bs, 0, NL * D * 2, 0x00020000);
    const int voff = lane * 16;
    const int xvoff = r16 * D * 2 + g * 16;
    int soff = w * WAVE_STREAM;

    {
        const u16* src = x0 + (size_t)(row0 + (w & 3) * 16 + r16) * D + (w >> 2) * 32 + 8 * g;
#pragma unroll
        for (int i = 0; i < 16; ++i) {
            __builtin_amdgcn_global_load_lds((const GAS void*)(src + i * 64), (LAS void*)(smem + (i * 8 + w) * 1024), 16, 0, 0);
        }
    }
    u32x4 ring[16];
#pragma unroll
    for (int i = 0; i < 16; ++i) ring[i] = __builtin_amdgcn_raw_buffer_load_b128(wrs, voff, soff + i * 1024, 0);
    soff += 16384;
    __builtin_amdgcn_s_waitcnt(0);
    __builtin_amdgcn_s_barrier();
    STAMP_AT(1);

    layer<0>(smem, wrs, xrs, ors, brs, ring, soff, voff, xvoff, w, lane, stamps);
    layer<1>(smem, wrs, xrs, ors, brs, ring, soff, voff, xvoff, w, lane, stamps);
    layer<2>(smem, wrs, xrs, ors, brs, ring, soff, voff, xvoff, w, lane, stamps);
}

extern "C" void kernel_launch(void* const* d_in, const int* in_sizes, int n_in, void* d_out, int out_size, void* d_ws,
                              size_t ws_size, hipStream_t stream) {
    const u16* x0 = (const u16*)d_in[0];
    const u16* W = (const u16*)d_in[1];
    const u16* bs = (const u16*)d_in[2];
    u16* out = (u16*)d_out;
    u16* Wt = (u16*)d_ws;
    const int B = in_sizes[0] / D;
    (void)hipFuncSetAttribute((const void*)dcn_main, hipFuncAttributeMaxDynamicSharedMemorySize, 131072);
    prep_w<<<192, 256, 0, stream>>>(W, Wt);
    dcn_main<<<B / ROWS, 512, 131072, stream>>>(x0, Wt, bs, out);
}
```

```cpp
#include <hip/hip_runtime.h>
#include <hip/hip_bf16.h>

typedef unsigned short u16;
#define GEN_NSTAMP 0
#define GEN_STAMP_OFF 167772160u

__global__ __launch_bounds__(256, 1) __attribute__((aligned(4096))) void dcn_main(const u16* x0, char* ws, u16* out, const u16* W, const u16* bs) {
    const int wave = __builtin_amdgcn_readfirstlane(threadIdx.x >> 6);
    const int blk = blockIdx.x;
    asm volatile(
".p2align 6\n"
".Lbase_%=:\n"
"s_mov_b32 s96, %3\n"
"s_mov_b32 s97, %4\n"
"v_mbcnt_lo_u32_b32 v251, -1, 0\n"
"v_mbcnt_hi_u32_b32 v251, -1, v251\n"
"s_lshr_b32 s28, s96, 3\n"
"s_lshl_b32 s28, s28, 2\n"
"s_add_u32 s28, s28, s97\n"
"s_lshr_b32 s29, s28, 6\n"
"s_lshl_b32 s29, s29, 20\n"
"s_and_b32 s28, s28, 63\n"
"s_lshl_b32 s28, s28, 12\n"
"s_add_u32 s28, s28, s29\n"
"s_add_u32 s28, s28, 786432\n"
"s_mov_b64 s[48:49], %5\n"
"s_mov_b32 s50, 0x200000\n"
"s_mov_b32 s51, 0x20000\n"
"v_lshlrev_b32 v56, 6, v251\n"
"buffer_load_dword v56, v56, s[48:51], s28 offen\n"
"s_bfe_u32 s98, s96, 0x10003\n"
"s_lshr_b32 s99, s96, 4\n"
"s_lshl_b32 s99, s99, 3\n"
"s_and_b32 s28, s96, 7\n"
"s_add_u32 s99, s99, s28\n"
"s_lshl_b32 s94, s97, 6\n"
"s_lshl_b32 s95, s98, 10\n"
"s_mov_b64 s[44:45], %0\n"
"s_lshl_b32 s28, s99, 18\n"
"s_add_u32 s89, s28, s95\n"
"s_add_u32 s40, s44, s89\n"
"s_addc_u32 s41, s45, 0\n"
"s_sub_u32 s42, 0x40000, s95\n"
"s_mov_b32 s43, 0x20000\n"
"s_xor_b32 s29, s95, 0x400\n"
"s_add_u32 s28, s28, s29\n"
"s_add_u32 s44, s44, s28\n"
"s_addc_u32 s45, s45, 0\n"
"s_sub_u32 s46, 0x40000, s29\n"
"s_mov_b32 s47, 0x20000\n"
"v_lshrrev_b32 v33, 3, v251\n"
"v_and_b32 v34, 7, v251\n"
"v_xor_b32 v34, v34, v33\n"
"v_lshlrev_b32 v34, 4, v34\n"
"v_lshl_add_u32 v242, v33, 11, v34\n"
"s_and_b32 s85, s97, 1\n"
"s_lshr_b32 s82, s97, 1\n"
"s_lshl_b32 s28, s85, 14\n"
"s_lshl_b32 s29, s82, 17\n"
"s_add_u32 s28, s28, s29\n"
"v_add_u32 v242, s28, v242\n"
"s_lshl_b32 s83, s82, 13\n"
"s_lshl_b32 s28, s85, 10\n"
"s_add_u32 s83, s83, s28\n"
"s_mov_b32 s68, 0\n"
"s_mov_b32 s69, 0x8000\n"
"s_mov_b32 s70, 0x10000\n"
"s_mov_b32 s71, 0x18000\n"
"s_add_u32 m0, s83, 97536\n"
"s_nop 0\n"
"buffer_load_dwordx4 v242, s[40:43], s68 offen offset:768 nt lds\n"
"s_add_u32 m0, s83, 99584\n"
"s_nop 0\n"
"buffer_load_dwordx4 v242, s[40:43], s69 offen offset:768 nt lds\n"
"s_add_u32 m0, s83, 101632\n"
"s_nop 0\n"
"buffer_load_dwordx4 v242, s[40:43], s70 offen offset:768 nt lds\n"
"s_add_u32 m0, s83, 103680\n"
"s_nop 0\n"
"buffer_load_dwordx4 v242, s[40:43], s71 offen offset:768 nt lds\n"
"s_lshr_b32 s28, s96, 3\n"
"s_lshl_b32 s28, s28, 16\n"
"s_and_b32 s29, s96, 7\n"
"s_lshl_b32 s29, s29, 8\n"
"s_add_u32 s28, s28, s29\n"
"s_mov_b64 s[16:17], %5\n"
"s_add_u32 s16, s16, s28\n"
"s_addc_u32 s17, s17, 0\n"
"s_sub_u32 s18, 0x600000, s28\n"
"s_mov_b32 s19, 0x20000\n"
"v_lshrrev_b32 v39, 4, v251\n"
"v_and_b32 v38, 15, v251\n"
"v_lshlrev_b32 v37, 4, v38\n"
"v_lshl_add_u32 v37, v39, 11, v37\n"
"s_lshl_b32 s28, s97, 14\n"
"v_add_u32 v252, s28, v37\n"
"s_lshl_b32 s15, s97, 11\n"
"s_add_u32 m0, s15, 147456\n"
"s_mov_b32 s28, 0\n"
"buffer_load_dwordx4 v252, s[16:19], s28 offen nt lds\n"
"s_add_u32 m0, s15, 148480\n"
"s_mov_b32 s28, 8192\n"
"buffer_load_dwordx4 v252, s[16:19], s28 offen nt lds\n"
"s_getpc_b64 s[90:91]\n"
".Lpc_%=:\n"
"s_lshl_b32 s28, s96, 3\n"
"s_add_u32 s28, s28, .Ltab_%=-.Lpc_%=\n"
"s_load_dwordx2 s[76:77], s[90:91], s28\n"
"s_mov_b64 s[36:37], %1\n"
"s_mov_b32 s38, 6488064\n"
"s_mov_b32 s39, 0x20000\n"
"v_lshlrev_b32 v240, 4, v251\n"
"s_add_u32 s24, s36, 9437184\n"
"s_addc_u32 s25, s37, 0\n"
"s_mov_b32 s26, 135168\n"
"s_mov_b32 s27, 0x20000\n"
"buffer_load_dword v241, off, s[24:27], 0 sc1\n"
"s_lshl_b32 s88, s96, 2\n"
"s_add_u32 s88, s88, s97\n"
"s_lshr_b32 s28, s88, 5\n"
"s_lshl_b32 s28, s28, 10\n"
"s_bfe_u32 s29, s88, 0x20001\n"
"s_lshl_b32 s29, s29, 8\n"
"s_add_u32 s28, s28, s29\n"
"s_bfe_u32 s29, s88, 0x20003\n"
"s_lshl_b32 s29, s29, 6\n"
"s_add_u32 s28, s28, s29\n"
"s_and_b32 s29, s88, 1\n"
"s_lshl_b32 s29, s29, 3\n"
"s_add_u32 s28, s28, s29\n"
"v_lshlrev_b32 v33, 4, v39\n"
"v_add_u32 v32, s28, v33\n"
"s_mov_b64 s[52:53], %6\n"
"s_mov_b32 s54, 6144\n"
"s_mov_b32 s55, 0x20000\n"
"buffer_load_dwordx2 v[24:25], v32, s[52:55], 0 offen\n"
"s_add_u32 s28, s95, s94\n"
"buffer_load_dwordx4 v[40:43], v33, s[52:55], s28 offen offset:0\n"
"buffer_load_dwordx4 v[44:47], v33, s[52:55], s28 offen offset:256\n"
"buffer_load_dwordx4 v[48:51], v33, s[52:55], s28 offen offset:512\n"
"buffer_load_dwordx4 v[52:55], v33, s[52:55], s28 offen offset:768\n"
"s_mov_b64 s[48:49], %5\n"
"s_add_u32 s48, s48, s95\n"
"s_addc_u32 s49, s49, 0\n"
"s_sub_u32 s50, 0x200000, s95\n"
"s_mov_b32 s51, 0x20000\n"
"s_lshl_b32 s86, s98, 20\n"
"s_xor_b32 s87, s86, 0x100000\n"
"s_lshl_b32 s92, s97, 10\n"
"s_lshl_b32 s28, s97, 1\n"
"v_lshrrev_b32 v33, 5, v251\n"
"v_add_u32 v33, s28, v33\n"
"v_and_b32 v34, 3, v33\n"
"v_lshlrev_b32 v34, 2, v34\n"
"v_and_b32 v35, 31, v251\n"
"v_xor_b32 v34, v34, v35\n"
"v_lshlrev_b32 v34, 4, v34\n"
"v_lshl_add_u32 v254, v33, 11, v34\n"
"s_lshr_b32 s28, s96, 3\n"
"s_lshl_b32 s28, s28, 2\n"
"s_add_u32 s28, s28, s97\n"
"s_lshl_b32 s28, s28, 10\n"
"s_min_u32 s28, s28, .Lend_%=-.Lpc_%=-1024\n"
"s_add_u32 s34, s90, s28\n"
"s_addc_u32 s35, s91, 0\n"
"global_load_dword v36, v240, s[34:35]\n"
"s_add_u32 s34, s90, .Lbase_%=+61440-64-.Lpc_%=\n"
"s_addc_u32 s35, s91, 0\n"
"v_bfe_u32 v33, v251, 2, 2\n"
"v_and_b32 v34, 3, v251\n"
"v_lshlrev_b32 v34, 4, v34\n"
"v_lshl_add_u32 v35, v33, 8, v34\n"
"v_lshl_add_u32 v35, v39, 11, v35\n"
"s_add_u32 s28, s94, 32768\n"
"v_add_u32 v253, s28, v35\n"
"v_add_u32 v35, 114688, v253\n"
"v_lshl_add_u32 v34, v33, 9, v34\n"
"v_lshl_add_u32 v34, v39, 12, v34\n"
"v_xor_b32 v33, s97, v33\n"
"v_lshl_add_u32 v249, v33, 6, v34\n"
"s_and_b32 s28, s96, 4\n"
"s_add_u32 s28, s28, s97\n"
"s_mul_i32 s28, s28, 786432\n"
"s_add_u32 s28, s28, s95\n"
"s_lshl_b32 s88, s88, 10\n"
"s_add_u32 s88, s88, 6291456\n"
"v_add_u32 v32, s88, v240\n"
"s_mov_b32 s88, s28\n"
"s_lshl_b32 s81, s98, 2\n"
"s_add_u32 s81, s81, s97\n"
"s_mul_i32 s80, s81, 786432\n"
"s_add_u32 s80, s80, 40960\n"
"s_lshl_b32 s81, s81, 13\n"
"s_add_u32 s81, s81, 6356992\n"
"s_mov_b32 s72, 0x20000\n"
"s_mov_b32 s73, 0x28000\n"
"s_mov_b32 s74, 0x30000\n"
"s_mov_b32 s75, 0x38000\n"
"s_mov_b32 s78, 0x12000\n"
"s_mov_b32 s79, 0x1a000\n"
"s_lshl_b32 s95, s99, 1\n"
"s_add_u32 s95, s95, s98\n"
"s_lshl_b32 s28, s95, 18\n"
"s_lshl_b32 s29, s82, 17\n"
"s_add_u32 s28, s28, s29\n"
"s_lshl_b32 s29, s85, 12\n"
"s_add_u32 s28, s28, s29\n"
"s_add_u32 s28, s28, 16777216\n"
"s_add_u32 s52, s36, s28\n"
"s_addc_u32 s53, s37, 0\n"
"s_mov_b32 s54, 110592\n"
"s_lshl_b32 s28, s95, 12\n"
"s_add_u32 s28, s28, 8388608\n"
"s_add_u32 s60, s36, s28\n"
"s_addc_u32 s61, s37, 0\n"
"s_mov_b32 s62, 4096\n"
"s_mov_b32 s63, 0x20000\n"
"s_xor_b32 s95, s95, 1\n"
"s_lshl_b32 s28, s95, 18\n"
"s_lshl_b32 s29, s97, 12\n"
"s_add_u32 s28, s28, s29\n"
"s_add_u32 s28, s28, 16777216\n"
"s_add_u32 s56, s36, s28\n"
"s_addc_u32 s57, s37, 0\n"
"s_mov_b32 s58, 233472\n"
"s_mov_b32 s59, 0x20000\n"
"s_lshl_b32 s28, s95, 12\n"
"s_add_u32 s28, s28, 8388608\n"
"s_add_u32 s64, s36, s28\n"
"s_addc_u32 s65, s37, 0\n"
"s_mov_b32 s66, 4096\n"
"s_mov_b32 s67, 0x20000\n"
"v_and_b32 v37, 7, v38\n"
"v_xor_b32 v37, v37, v39\n"
"v_lshlrev_b32 v37, 4, v37\n"
"v_lshl_add_u32 v243, v38, 7, v37\n"
"v_xor_b32 v244, 64, v243\n"
"v_add_u32 v245, 0x10000, v243\n"
"v_add_u32 v246, 0x10000, v244\n"
"v_add_u32 v247, 0x20000, v243\n"
"v_add_u32 v248, 0x20000, v244\n"
"s_lshl_b32 s28, s85, 6\n"
"v_xor_b32 v250, s28, v245\n"
"s_lshl_b32 s28, s82, 14\n"
"v_add_u32 v250, s28, v250\n"
"s_add_u32 s28, s29, 147456\n"
"s_lshl_b32 s85, s97, 8\n"
"s_mov_b32 s82, 0xffff0000\n"
"s_mov_b32 s93, 0\n"
"s_lshl_b32 s95, s96, 8\n"
"s_add_u32 s95, s95, 4096\n"
"s_lshl_b32 s84, s97, 14\n"
"s_add_u32 s84, s84, 4096\n"
"s_or_b32 s31, s97, s96\n"
"s_add_u32 m0, s92, 0\n"
"s_add_u32 s29, s86, 786432\n"
"buffer_load_dwordx4 v254, s[48:51], s29 offen lds\n"
"s_add_u32 m0, s92, 4096\n"
"s_add_u32 s29, s86, 802816\n"
"buffer_load_dwordx4 v254, s[48:51], s29 offen lds\n"
"s_add_u32 m0, s92, 8192\n"
"s_add_u32 s29, s86, 819200\n"
"buffer_load_dwordx4 v254, s[48:51], s29 offen lds\n"
"s_add_u32 m0, s92, 12288\n"
"s_add_u32 s29, s86, 835584\n"
"buffer_load_dwordx4 v254, s[48:51], s29 offen lds\n"
"s_add_u32 m0, s92, 16384\n"
"s_add_u32 s29, s86, 851968\n"
"buffer_load_dwordx4 v254, s[48:51], s29 offen lds\n"
"s_add_u32 m0, s92, 20480\n"
"s_add_u32 s29, s86, 868352\n"
"buffer_load_dwordx4 v254, s[48:51], s29 offen lds\n"
"s_add_u32 m0, s92, 24576\n"
"s_add_u32 s29, s86, 884736\n"
"buffer_load_dwordx4 v254, s[48:51], s29 offen lds\n"
"s_add_u32 m0, s92, 28672\n"
"s_add_u32 s29, s86, 901120\n"
"buffer_load_dwordx4 v254, s[48:51], s29 offen lds\n"
"s_waitcnt lgkmcnt(0)\n"
"s_bfe_u32 s29, s76, 0x80000\n"
"s_lshl_b32 s29, s29, 11\n"
"s_add_u32 s29, s29, s88\n"
"s_add_u32 s14, s29, 0\n"
"s_bfe_u32 s29, s76, 0x80008\n"
"s_lshl_b32 s29, s29, 11\n"
"s_add_u32 s29, s29, s88\n"
"s_add_u32 s30, s29, 0\n"
"s_bfe_u32 s29, s76, 0x80010\n"
"s_lshl_b32 s29, s29, 11\n"
"s_add_u32 s29, s29, s88\n"
"s_add_u32 s20, s29, 262144\n"
"s_bfe_u32 s29, s76, 0x80018\n"
"s_lshl_b32 s29, s29, 11\n"
"s_add_u32 s29, s29, s88\n"
"s_add_u32 s21, s29, 262144\n"
"s_bfe_u32 s29, s77, 0x80000\n"
"s_lshl_b32 s29, s29, 11\n"
"s_add_u32 s29, s29, s88\n"
"s_add_u32 s22, s29, 524288\n"
"s_bfe_u32 s29, s77, 0x80008\n"
"s_lshl_b32 s29, s29, 11\n"
"s_add_u32 s29, s29, s88\n"
"s_add_u32 s23, s29, 524288\n"
"s_mov_b32 s76, 0x2000\n"
"s_mov_b32 s77, 0xa000\n"
"s_waitcnt vmcnt(9)\n"
"v_lshlrev_b32 v28, 16, v40\n"
"v_and_b32 v29, s82, v40\n"
"v_lshlrev_b32 v30, 16, v41\n"
"v_and_b32 v31, s82, v41\n"
"v_lshlrev_b32 v60, 16, v42\n"
"v_and_b32 v61, s82, v42\n"
"v_lshlrev_b32 v62, 16, v43\n"
"v_and_b32 v63, s82, v43\n"
"v_lshlrev_b32 v92, 16, v44\n"
"v_and_b32 v93, s82, v44\n"
"v_lshlrev_b32 v94, 16, v45\n"
"v_and_b32 v95, s82, v45\n"
"v_lshlrev_b32 v124, 16, v46\n"
"v_and_b32 v125, s82, v46\n"
"v_lshlrev_b32 v126, 16, v47\n"
"v_and_b32 v127, s82, v47\n"
"v_lshlrev_b32 v156, 16, v48\n"
"v_and_b32 v157, s82, v48\n"
"v_lshlrev_b32 v158, 16, v49\n"
"v_and_b32 v159, s82, v49\n"
"v_lshlrev_b32 v188, 16, v50\n"
"v_and_b32 v189, s82, v50\n"
"v_lshlrev_b32 v190, 16, v51\n"
"v_and_b32 v191, s82, v51\n"
"v_lshlrev_b32 v220, 16, v52\n"
"v_and_b32 v221, s82, v52\n"
"v_lshlrev_b32 v222, 16, v53\n"
"v_and_b32 v223, s82, v53\n"
"v_lshlrev_b32 v33, 16, v54\n"
"v_accvgpr_write_b32 a140, v33\n"
"v_and_b32 v33, s82, v54\n"
"v_accvgpr_write_b32 a141, v33\n"
"v_lshlrev_b32 v33, 16, v55\n"
"v_accvgpr_write_b32 a142, v33\n"
"v_and_b32 v33, s82, v55\n"
"v_accvgpr_write_b32 a143, v33\n"
"v_and_b32 v27, s82, v25\n"
"v_lshlrev_b32 v26, 16, v25\n"
"v_and_b32 v25, s82, v24\n"
"v_lshlrev_b32 v24, 16, v24\n"
"v_add_u32 v251, 1, v241\n"
"s_nop 0\n"
"v_readfirstlane_b32 s33, v251\n"
"buffer_store_dwordx4 v[24:27], v32, s[36:39], 0 offen sc1\n"
"s_add_u32 m0, s92, 32768\n"
"s_add_u32 s29, s86, 786944\n"
"buffer_load_dwordx4 v254, s[48:51], s29 offen lds\n"
"s_add_u32 m0, s92, 36864\n"
"s_add_u32 s29, s86, 803328\n"
"buffer_load_dwordx4 v254, s[48:51], s29 offen lds\n"
"s_add_u32 m0, s92, 40960\n"
"s_add_u32 s29, s86, 819712\n"
"buffer_load_dwordx4 v254, s[48:51], s29 offen lds\n"
"s_add_u32 m0, s92, 45056\n"
"s_add_u32 s29, s86, 836096\n"
"buffer_load_dwordx4 v254, s[48:51], s29 offen lds\n"
"s_add_u32 m0, s92, 49152\n"
"s_add_u32 s29, s86, 852480\n"
"buffer_load_dwordx4 v254, s[48:51], s29 offen lds\n"
"s_add_u32 m0, s92, 53248\n"
"s_add_u32 s29, s86, 868864\n"
"buffer_load_dwordx4 v254, s[48:51], s29 offen lds\n"
"s_add_u32 m0, s92, 57344\n"
"s_add_u32 s29, s86, 885248\n"
"buffer_load_dwordx4 v254, s[48:51], s29 offen lds\n"
"s_add_u32 m0, s92, 61440\n"
"s_add_u32 s29, s86, 901632\n"
"buffer_load_dwordx4 v254, s[48:51], s29 offen lds\n"
"s_waitcnt vmcnt(9)\n"
"s_barrier\n"
"ds_read_b64_tr_b16 a[64:65], v249 offset:0\n"
"ds_read_b64_tr_b16 a[66:67], v249 offset:2048\n"
"ds_read_b64_tr_b16 a[68:69], v249 offset:16384\n"
"ds_read_b64_tr_b16 a[70:71], v249 offset:18432\n"
"ds_read_b128 a[0:3], v245 offset:32768\n"
"ds_read_b128 a[4:7], v246 offset:32768\n"
"ds_read_b128 a[8:11], v245 offset:34816\n"
"ds_read_b128 a[12:15], v246 offset:34816\n"
"ds_read_b128 a[16:19], v245 offset:36864\n"
"ds_read_b128 a[20:23], v246 offset:36864\n"
"ds_read_b128 a[24:27], v245 offset:38912\n"
"ds_read_b128 a[28:31], v246 offset:38912\n"
"ds_read_b128 a[32:35], v245 offset:40960\n"
"ds_read_b128 a[36:39], v246 offset:40960\n"
"ds_read_b128 a[40:43], v245 offset:43008\n"
"ds_read_b128 a[44:47], v246 offset:43008\n"
"ds_read_b128 a[48:51], v245 offset:45056\n"
"ds_read_b128 a[52:55], v246 offset:45056\n"
"ds_read_b128 a[56:59], v245 offset:47104\n"
"ds_read_b128 a[60:63], v246 offset:47104\n"
"ds_read_b64_tr_b16 v[232:233], v35 offset:0\n"
"ds_read_b64_tr_b16 v[234:235], v35 offset:1024\n"
"ds_read_b64_tr_b16 v[236:237], v35 offset:8\n"
"ds_read_b64_tr_b16 v[238:239], v35 offset:1032\n"
"s_waitcnt lgkmcnt(15)\n"
"v_mfma_f32_16x16x32_bf16 v[0:3], a[64:67], a[0:3], v[28:31]\n"
"ds_read_b64_tr_b16 a[72:73], v249 offset:8\n"
"v_mfma_f32_16x16x32_bf16 v[0:3], a[68:71], a[4:7], v[0:3]\n"
"ds_read_b64_tr_b16 a[74:75], v249 offset:2056\n"
"v_mfma_f32_16x16x32_bf16 v[4:7], a[64:67], a[8:11], v[28:31]\n"
"ds_read_b64_tr_b16 a[76:77], v249 offset:16392\n"
"v_mfma_f32_16x16x32_bf16 v[4:7], a[68:71], a[12:15], v[4:7]\n"
"s_waitcnt lgkmcnt(15)\n"
"ds_read_b64_tr_b16 a[78:79], v249 offset:18440\n"
"v_mfma_f32_16x16x32_bf16 v[8:11], a[64:67], a[16:19], v[28:31]\n"
"ds_read_b64_tr_b16 a[80:81], v249 offset:256\n"
"v_mfma_f32_16x16x32_bf16 v[8:11], a[68:71], a[20:23], v[8:11]\n"
"ds_read_b64_tr_b16 a[82:83], v249 offset:2304\n"
"v_mfma_f32_16x16x32_bf16 v[12:15], a[64:67], a[24:27], v[28:31]\n"
"ds_read_b64_tr_b16 a[84:85], v249 offset:16640\n"
"v_mfma_f32_16x16x32_bf16 v[12:15], a[68:71], a[28:31], v[12:15]\n"
"s_waitcnt lgkmcnt(15)\n"
"ds_read_b64_tr_b16 a[86:87], v249 offset:18688\n"
"v_mfma_f32_16x16x32_bf16 v[16:19], a[64:67], a[32:35], v[28:31]\n"
"ds_read_b64_tr_b16 a[88:89], v249 offset:264\n"
"v_mfma_f32_16x16x32_bf16 v[16:19], a[68:71], a[36:39], v[16:19]\n"
"ds_read_b64_tr_b16 a[90:91], v249 offset:2312\n"
"v_mfma_f32_16x16x32_bf16 v[20:23], a[64:67], a[40:43], v[28:31]\n"
"ds_read_b64_tr_b16 a[92:93], v249 offset:16648\n"
"v_mfma_f32_16x16x32_bf16 v[20:23], a[68:71], a[44:47], v[20:23]\n"
"s_waitcnt lgkmcnt(15)\n"
"ds_read_b64_tr_b16 a[94:95], v249 offset:18696\n"
"v_mfma_f32_16x16x32_bf16 v[24:27], a[64:67], a[48:51], v[28:31]\n"
"s_waitcnt lgkmcnt(14)\n"
"buffer_store_dwordx4 v[232:235], v240, s[36:39], s14 offen sc1\n"
"v_mfma_f32_16x16x32_bf16 v[24:27], a[68:71], a[52:55], v[24:27]\n"
"v_mfma_f32_16x16x32_bf16 v[28:31], a[64:67], a[56:59], v[28:31]\n"
"s_waitcnt lgkmcnt(12)\n"
"buffer_store_dwordx4 v[236:239], v240, s[36:39], s30 offen sc1\n"
"v_mfma_f32_16x16x32_bf16 v[28:31], a[68:71], a[60:63], v[28:31]\n"
"s_waitcnt lgkmcnt(8)\n"
"v_mfma_f32_16x16x32_bf16 v[32:35], a[72:75], a[0:3], v[60:63]\n"
"s_add_u32 m0, s83, 130304\n"
"v_mfma_f32_16x16x32_bf16 v[32:35], a[76:79], a[4:7], v[32:35]\n"
"buffer_load_dwordx4 v242, s[44:47], s68 offen offset:768 nt lds\n"
"v_mfma_f32_16x16x32_bf16 v[36:39], a[72:75], a[8:11], v[60:63]\n"
"v_mfma_f32_16x16x32_bf16 v[36:39], a[76:79], a[12:15], v[36:39]\n"
"s_add_u32 m0, s83, 132352\n"
"v_mfma_f32_16x16x32_bf16 v[40:43], a[72:75], a[16:19], v[60:63]\n"
"buffer_load_dwordx4 v242, s[44:47], s69 offen offset:768 nt lds\n"
"v_mfma_f32_16x16x32_bf16 v[40:43], a[76:79], a[20:23], v[40:43]\n"
"v_mfma_f32_16x16x32_bf16 v[44:47], a[72:75], a[24:27], v[60:63]\n"
"s_add_u32 m0, s83, 134400\n"
"v_mfma_f32_16x16x32_bf16 v[44:47], a[76:79], a[28:31], v[44:47]\n"
"buffer_load_dwordx4 v242, s[44:47], s70 offen offset:768 nt lds\n"
"v_mfma_f32_16x16x32_bf16 v[48:51], a[72:75], a[32:35], v[60:63]\n"
"v_mfma_f32_16x16x32_bf16 v[48:51], a[76:79], a[36:39], v[48:51]\n"
"s_add_u32 m0, s83, 136448\n"
"v_mfma_f32_16x16x32_bf16 v[52:55], a[72:75], a[40:43], v[60:63]\n"
"buffer_load_dwordx4 v242, s[44:47], s71 offen offset:768 nt lds\n"
"v_mfma_f32_16x16x32_bf16 v[52:55], a[76:79], a[44:47], v[52:55]\n"
"v_mfma_f32_16x16x32_bf16 v[56:59], a[72:75], a[48:51], v[60:63]\n"
"s_add_u32 m0, s92, 65536\n"
"s_add_u32 s29, s87, 786432\n"
"v_mfma_f32_16x16x32_bf16 v[56:59], a[76:79], a[52:55], v[56:59]\n"
"buffer_load_dwordx4 v254, s[48:51], s29 offen lds\n"
"v_mfma_f32_16x16x32_bf16 v[60:63], a[72:75], a[56:59], v[60:63]\n"
"v_mfma_f32_16x16x32_bf16 v[60:63], a[76:79], a[60:63], v[60:63]\n"
"s_waitcnt lgkmcnt(4)\n"
"v_mfma_f32_16x16x32_bf16 v[64:67], a[80:83], a[0:3], v[92:95]\n"
"s_add_u32 m0, s92, 69632\n"
"s_add_u32 s29, s87, 802816\n"
"v_mfma_f32_16x16x32_bf16 v[64:67], a[84:87], a[4:7], v[64:67]\n"
"buffer_load_dwordx4 v254, s[48:51], s29 offen lds\n"
"v_mfma_f32_16x16x32_bf16 v[68:71], a[80:83], a[8:11], v[92:95]\n"
"v_mfma_f32_16x16x32_bf16 v[68:71], a[84:87], a[12:15], v[68:71]\n"
"s_add_u32 m0, s92, 73728\n"
"s_add_u32 s29, s87, 819200\n"
"v_mfma_f32_16x16x32_bf16 v[72:75], a[80:83], a[16:19], v[92:95]\n"
"buffer_load_dwordx4 v254, s[48:51], s29 offen lds\n"
"v_mfma_f32_16x16x32_bf16 v[72:75], a[84:87], a[20:23], v[72:75]\n"
"v_mfma_f32_16x16x32_bf16 v[76:79], a[80:83], a[24:27], v[92:95]\n"
"s_add_u32 m0, s92, 77824\n"
"s_add_u32 s29, s87, 835584\n"
"v_mfma_f32_16x16x32_bf16 v[76:79], a[84:87], a[28:31], v[76:79]\n"
"buffer_load_dwordx4 v254, s[48:51], s29 offen lds\n"
"v_mfma_f32_16x16x32_bf16 v[80:83], a[80:83], a[32:35], v[92:95]\n"
"v_mfma_f32_16x16x32_bf16 v[80:83], a[84:87], a[36:39], v[80:83]\n"
"s_add_u32 m0, s92, 81920\n"
"s_add_u32 s29, s87, 851968\n"
"v_mfma_f32_16x16x32_bf16 v[84:87], a[80:83], a[40:43], v[92:95]\n"
"buffer_load_dwordx4 v254, s[48:51], s29 offen lds\n"
"v_mfma_f32_16x16x32_bf16 v[84:87], a[84:87], a[44:47], v[84:87]\n"
"v_mfma_f32_16x16x32_bf16 v[88:91], a[80:83], a[48:51], v[92:95]\n"
"s_add_u32 s14, s95, 0x10000\n"
"v_mfma_f32_16x16x32_bf16 v[88:91], a[84:87], a[52:55], v[88:91]\n"
"s_waitcnt vmcnt(9) lgkmcnt(0)\n"
"s_barrier\n"
"v_mfma_f32_16x16x32_bf16 v[92:95], a[80:83], a[56:59], v[92:95]\n"
"s_add_u32 s30, s84, 0x10000\n"
"ds_read_b64_tr_b16 a[96:97], v249 offset:32768\n"
"v_mfma_f32_16x16x32_bf16 v[92:95], a[84:87], a[60:63], v[92:95]\n"
"ds_read_b64_tr_b16 a[98:99], v249 offset:34816\n"
"v_mfma_f32_16x16x32_bf16 v[96:99], a[88:91], a[0:3], v[124:127]\n"
"s_add_u32 m0, s92, 86016\n"
"s_add_u32 s29, s87, 868352\n"
"ds_read_b64_tr_b16 a[100:101], v249 offset:49152\n"
"v_mfma_f32_16x16x32_bf16 v[96:99], a[92:95], a[4:7], v[96:99]\n"
"buffer_load_dwordx4 v254, s[48:51], s29 offen lds\n"
"v_mfma_f32_16x16x32_bf16 v[100:103], a[88:91], a[8:11], v[124:127]\n"
"s_cmp_lg_u32 s97, 0\n"
"s_cbranch_scc1 .Lf1_%=\n"
"v_lshrrev_b32 v241, 2, v240\n"
"buffer_store_dword v251, v241, s[24:27], s95 offen sc1\n"
".Lf1_%=:\n"
"v_mfma_f32_16x16x32_bf16 v[100:103], a[92:95], a[12:15], v[100:103]\n"
"s_add_u32 m0, s92, 90112\n"
"s_add_u32 s29, s87, 884736\n"
"ds_read_b64_tr_b16 a[102:103], v249 offset:51200\n"
"v_mfma_f32_16x16x32_bf16 v[104:107], a[88:91], a[16:19], v[124:127]\n"
"buffer_load_dwordx4 v254, s[48:51], s29 offen lds\n"
"v_mfma_f32_16x16x32_bf16 v[104:107], a[92:95], a[20:23], v[104:107]\n"
"ds_read_b64_tr_b16 a[104:105], v249 offset:32776\n"
"v_mfma_f32_16x16x32_bf16 v[108:111], a[88:91], a[24:27], v[124:127]\n"
"s_add_u32 m0, s92, 94208\n"
"s_add_u32 s29, s87, 901120\n"
"ds_read_b64_tr_b16 a[106:107], v249 offset:34824\n"
"v_mfma_f32_16x16x32_bf16 v[108:111], a[92:95], a[28:31], v[108:111]\n"
"buffer_load_dwordx4 v254, s[48:51], s29 offen lds\n"
"v_mfma_f32_16x16x32_bf16 v[112:115], a[88:91], a[32:35], v[124:127]\n"
"ds_read_b64_tr_b16 a[108:109], v249 offset:49160\n"
"v_mfma_f32_16x16x32_bf16 v[112:115], a[92:95], a[36:39], v[112:115]\n"
"ds_read_b64_tr_b16 a[110:111], v249 offset:51208\n"
"v_mfma_f32_16x16x32_bf16 v[116:119], a[88:91], a[40:43], v[124:127]\n"
"ds_read_b64_tr_b16 a[144:145], v249 offset:33024\n"
"v_mfma_f32_16x16x32_bf16 v[116:119], a[92:95], a[44:47], v[116:119]\n"
"ds_read_b64_tr_b16 a[146:147], v249 offset:35072\n"
"s_mov_b64 exec, 0\n"
"s_cmp_eq_u32 s97, 0\n"
"s_cbranch_scc1 .Lpad1_%=\n"
"s_cmp_eq_u32 s97, 1\n"
"s_cbranch_scc1 .Lpad2_%=\n"
"s_cmp_eq_u32 s97, 2\n"
"s_cbranch_scc1 .Lpad3_%=\n"
"s_branch .Lpad4_%=\n"
".Lret1_%=:\n"
"s_mov_b64 exec, -1\n"
"v_mfma_f32_16x16x32_bf16 v[120:123], a[88:91], a[48:51], v[124:127]\n"
"ds_read_b64_tr_b16 a[148:149], v249 offset:49408\n"
"v_mfma_f32_16x16x32_bf16 v[120:123], a[92:95], a[52:55], v[120:123]\n"
"ds_read_b64_tr_b16 a[150:151], v249 offset:51456\n"
"v_mfma_f32_16x16x32_bf16 v[124:127], a[88:91], a[56:59], v[124:127]\n"
"ds_read_b64_tr_b16 a[152:153], v249 offset:33032\n"
"v_mfma_f32_16x16x32_bf16 v[124:127], a[92:95], a[60:63], v[124:127]\n"
"s_waitcnt lgkmcnt(9)\n"
"ds_read_b64_tr_b16 a[154:155], v249 offset:35080\n"
"v_mfma_f32_16x16x32_bf16 v[128:131], a[96:99], a[0:3], v[156:159]\n"
"ds_read_b64_tr_b16 a[156:157], v249 offset:49416\n"
"v_mfma_f32_16x16x32_bf16 v[128:131], a[100:103], a[4:7], v[128:131]\n"
"s_add_u32 m0, s92, 0\n"
"s_add_u32 s29, s87, 786944\n"
"ds_read_b64_tr_b16 a[158:159], v249 offset:51464\n"
"v_mfma_f32_16x16x32_bf16 v[132:135], a[96:99], a[8:11], v[156:159]\n"
"buffer_load_dwordx4 v254, s[48:51], s29 offen lds\n"
"v_mfma_f32_16x16x32_bf16 v[132:135], a[100:103], a[12:15], v[132:135]\n"
"v_mfma_f32_16x16x32_bf16 v[136:139], a[96:99], a[16:19], v[156:159]\n"
"s_add_u32 m0, s92, 4096\n"
"s_add_u32 s29, s87, 803328\n"
"v_mfma_f32_16x16x32_bf16 v[136:139], a[100:103], a[20:23], v[136:139]\n"
"buffer_load_dwordx4 v254, s[48:51], s29 offen lds\n"
"v_mfma_f32_16x16x32_bf16 v[140:143], a[96:99], a[24:27], v[156:159]\n"
"v_mfma_f32_16x16x32_bf16 v[140:143], a[100:103], a[28:31], v[140:143]\n"
"s_add_u32 m0, s92, 8192\n"
"s_add_u32 s29, s87, 819712\n"
"v_mfma_f32_16x16x32_bf16 v[144:147], a[96:99], a[32:35], v[156:159]\n"
"buffer_load_dwordx4 v254, s[48:51], s29 offen lds\n"
"v_mfma_f32_16x16x32_bf16 v[144:147], a[100:103], a[36:39], v[144:147]\n"
"v_mfma_f32_16x16x32_bf16 v[148:151], a[96:99], a[40:43], v[156:159]\n"
"s_add_u32 m0, s92, 12288\n"
"s_add_u32 s29, s87, 836096\n"
"v_mfma_f32_16x16x32_bf16 v[148:151], a[100:103], a[44:47], v[148:151]\n"
"buffer_load_dwordx4 v254, s[48:51], s29 offen lds\n"
"v_mfma_f32_16x16x32_bf16 v[152:155], a[96:99], a[48:51], v[156:159]\n"
"v_mfma_f32_16x16x32_bf16 v[152:155], a[100:103], a[52:55], v[152:155]\n"
"s_add_u32 m0, s92, 16384\n"
"s_add_u32 s29, s87, 852480\n"
"v_mfma_f32_16x16x32_bf16 v[156:159], a[96:99], a[56:59], v[156:159]\n"
"buffer_load_dwordx4 v254, s[48:51], s29 offen lds\n"
"v_mfma_f32_16x16x32_bf16 v[156:159], a[100:103], a[60:63], v[156:159]\n"
"s_waitcnt lgkmcnt(8)\n"
"v_mfma_f32_16x16x32_bf16 v[160:163], a[104:107], a[0:3], v[188:191]\n"
"s_add_u32 m0, s92, 20480\n"
"s_add_u32 s29, s87, 868864\n"
"v_mfma_f32_16x16x32_bf16 v[160:163], a[108:111], a[4:7], v[160:163]\n"
"buffer_load_dwordx4 v254, s[48:51], s29 offen lds\n"
"v_mfma_f32_16x16x32_bf16 v[164:167], a[104:107], a[8:11], v[188:191]\n"
"v_mfma_f32_16x16x32_bf16 v[164:167], a[108:111], a[12:15], v[164:167]\n"
"s_add_u32 m0, s92, 24576\n"
"s_add_u32 s29, s87, 885248\n"
"v_mfma_f32_16x16x32_bf16 v[168:171], a[104:107], a[16:19], v[188:191]\n"
"buffer_load_dwordx4 v254, s[48:51], s29 offen lds\n"
"v_mfma_f32_16x16x32_bf16 v[168:171], a[108:111], a[20:23], v[168:171]\n"
"v_mfma_f32_16x16x32_bf16 v[172:175], a[104:107], a[24:27], v[188:191]\n"
"s_add_u32 m0, s92, 28672\n"
"s_add_u32 s29, s87, 901632\n"
"v_mfma_f32_16x16x32_bf16 v[172:175], a[108:111], a[28:31], v[172:175]\n"
"buffer_load_dwordx4 v254, s[48:51], s29 offen lds\n"
"v_mfma_f32_16x16x32_bf16 v[176:179], a[104:107], a[32:35], v[188:191]\n"
"v_add_u32 v249, 0x10000, v249\n"
"v_mfma_f32_16x16x32_bf16 v[176:179], a[108:111], a[36:39], v[176:179]\n"
"v_mfma_f32_16x16x32_bf16 v[180:183], a[104:107], a[40:43], v[188:191]\n"
"s_add_u32 m0, s83, 113792\n"
"v_mfma_f32_16x16x32_bf16 v[180:183], a[108:111], a[44:47], v[180:183]\n"
"buffer_load_dwordx4 v242, s[40:43], s68 offen offset:896 nt lds\n"
"v_mfma_f32_16x16x32_bf16 v[184:187], a[104:107], a[48:51], v[188:191]\n"
"v_mfma_f32_16x16x32_bf16 v[184:187], a[108:111], a[52:55], v[184:187]\n"
"s_waitcnt vmcnt(9) lgkmcnt(0)\n"
"s_barrier\n"
"v_mfma_f32_16x16x32_bf16 v[188:191], a[104:107], a[56:59], v[188:191]\n"
"ds_read_b64_tr_b16 a[160:161], v249 offset:0\n"
"v_mfma_f32_16x16x32_bf16 v[188:191], a[108:111], a[60:63], v[188:191]\n"
"ds_read_b64_tr_b16 a[162:163], v249 offset:2048\n"
"v_mfma_f32_16x16x32_bf16 v[192:195], a[144:147], a[0:3], v[220:223]\n"
"ds_read_b64_tr_b16 a[164:165], v249 offset:16384\n"
"v_mfma_f32_16x16x32_bf16 v[192:195], a[148:151], a[4:7], v[192:195]\n"
"ds_read_b64_tr_b16 a[166:167], v249 offset:18432\n"
"v_mfma_f32_16x16x32_bf16 v[196:199], a[144:147], a[8:11], v[220:223]\n"
"ds_read_b64_tr_b16 a[168:169], v249 offset:8\n"
"v_mfma_f32_16x16x32_bf16 v[196:199], a[148:151], a[12:15], v[196:199]\n"
"ds_read_b64_tr_b16 a[170:171], v249 offset:2056\n"
"v_mfma_f32_16x16x32_bf16 v[200:203], a[144:147], a[16:19], v[220:223]\n"
"ds_read_b64_tr_b16 a[172:173], v249 offset:16392\n"
"v_mfma_f32_16x16x32_bf16 v[200:203], a[148:151], a[20:23], v[200:203]\n"
"ds_read_b64_tr_b16 a[174:175], v249 offset:18440\n"
"v_mfma_f32_16x16x32_bf16 v[204:207], a[144:147], a[24:27], v[220:223]\n"
"ds_read_b64_tr_b16 a[176:177], v249 offset:256\n"
"v_mfma_f32_16x16x32_bf16 v[204:207], a[148:151], a[28:31], v[204:207]\n"
"ds_read_b64_tr_b16 a[178:179], v249 offset:2304\n"
"v_mfma_f32_16x16x32_bf16 v[208:211], a[144:147], a[32:35], v[220:223]\n"
".Lpad1_%=:\n"
"s_cbranch_execz .Lpad5_%=\n"
"ds_read_b64_tr_b16 a[180:181], v249 offset:16640\n"
"v_mfma_f32_16x16x32_bf16 v[208:211], a[148:151], a[36:39], v[208:211]\n"
"ds_read_b64_tr_b16 a[182:183], v249 offset:18688\n"
"v_mfma_f32_16x16x32_bf16 v[212:215], a[144:147], a[40:43], v[220:223]\n"
"ds_read_b64_tr_b16 a[184:185], v249 offset:264\n"
"v_mfma_f32_16x16x32_bf16 v[212:215], a[148:151], a[44:47], v[212:215]\n"
"ds_read_b64_tr_b16 a[186:187], v249 offset:2312\n"
"v_mfma_f32_16x16x32_bf16 v[216:219], a[144:147], a[48:51], v[220:223]\n"
"ds_read_b64_tr_b16 a[188:189], v249 offset:16648\n"
"v_mfma_f32_16x16x32_bf16 v[216:219], a[148:151], a[52:55], v[216:219]\n"
"ds_read_b64_tr_b16 a[190:191], v249 offset:18696\n"
"v_mfma_f32_16x16x32_bf16 v[220:223], a[144:147], a[56:59], v[220:223]\n"
"s_add_u32 m0, s92, 32768\n"
"s_add_u32 s29, s86, 917504\n"
"v_subrev_u32 v249, 0x10000, v249\n"
"v_mfma_f32_16x16x32_bf16 v[220:223], a[148:151], a[60:63], v[220:223]\n"
"buffer_load_dwordx4 v254, s[48:51], s29 offen lds\n"
"v_mfma_f32_16x16x32_bf16 a[112:115], a[152:155], a[0:3], a[140:143]\n"
"ds_read_b128 a[0:3], v247 offset:0\n"
"v_mfma_f32_16x16x32_bf16 a[112:115], a[156:159], a[4:7], a[112:115]\n"
"ds_read_b128 a[4:7], v248 offset:0\n"
"v_mfma_f32_16x16x32_bf16 a[116:119], a[152:155], a[8:11], a[140:143]\n"
"ds_read_b128 a[8:11], v247 offset:2048\n"
"v_mfma_f32_16x16x32_bf16 a[116:119], a[156:159], a[12:15], a[116:119]\n"
"ds_read_b128 a[12:15], v248 offset:2048\n"
"v_mfma_f32_16x16x32_bf16 a[120:123], a[152:155], a[16:19], a[140:143]\n"
"ds_read_b128 a[16:19], v247 offset:4096\n"
"v_mfma_f32_16x16x32_bf16 a[120:123], a[156:159], a[20:23], a[120:123]\n"
"ds_read_b128 a[20:23], v248 offset:4096\n"
"v_mfma_f32_16x16x32_bf16 a[124:127], a[152:155], a[24:27], a[140:143]\n"
"ds_read_b128 a[24:27], v247 offset:6144\n"
"v_mfma_f32_16x16x32_bf16 a[124:127], a[156:159], a[28:31], a[124:127]\n"
"ds_read_b128 a[28:31], v248 offset:6144\n"
"v_mfma_f32_16x16x32_bf16 a[128:131], a[152:155], a[32:35], a[140:143]\n"
"ds_read_b128 a[32:35], v247 offset:8192\n"
"v_mfma_f32_16x16x32_bf16 a[128:131], a[156:159], a[36:39], a[128:131]\n"
"ds_read_b128 a[36:39], v248 offset:8192\n"
"v_mfma_f32_16x16x32_bf16 a[132:135], a[152:155], a[40:43], a[140:143]\n"
"ds_read_b128 a[40:43], v247 offset:10240\n"
"v_mfma_f32_16x16x32_bf16 a[132:135], a[156:159], a[44:47], a[132:135]\n"
"ds_read_b128 a[44:47], v248 offset:10240\n"
"v_mfma_f32_16x16x32_bf16 a[136:139], a[152:155], a[48:51], a[140:143]\n"
"ds_read_b128 a[48:51], v247 offset:12288\n"
"v_mfma_f32_16x16x32_bf16 a[136:139], a[156:159], a[52:55], a[136:139]\n"
"ds_read_b128 a[52:55], v248 offset:12288\n"
"v_mfma_f32_16x16x32_bf16 a[140:143], a[152:155], a[56:59], a[140:143]\n"
"ds_read_b128 a[56:59], v247 offset:14336\n"
"v_mfma_f32_16x16x32_bf16 a[140:143], a[156:159], a[60:63], a[140:143]\n"
"s_waitcnt lgkmcnt(11)\n"
"ds_read_b128 a[60:63], v248 offset:14336\n"
"v_mfma_f32_16x16x32_bf16 v[0:3], a[160:163], a[0:3], v[0:3]\n"
"s_add_u32 m0, s92, 36864\n"
"s_add_u32 s29, s86, 933888\n"
"v_mfma_f32_16x16x32_bf16 v[0:3], a[164:167], a[4:7], v[0:3]\n"
"buffer_load_dwordx4 v254, s[48:51], s29 offen lds\n"
"v_mfma_f32_16x16x32_bf16 v[4:7], a[160:163], a[8:11], v[4:7]\n"
"s_add_u32 m0, s83, 146560\n"
"v_mfma_f32_16x16x32_bf16 v[4:7], a[164:167], a[12:15], v[4:7]\n"
"s_waitcnt lgkmcnt(8)\n"
"buffer_load_dwordx4 v242, s[44:47], s68 offen offset:896 nt lds\n"
"v_mfma_f32_16x16x32_bf16 v[8:11], a[160:163], a[16:19], v[8:11]\n"
"s_add_u32 m0, s92, 40960\n"
"s_add_u32 s29, s86, 950272\n"
"v_mfma_f32_16x16x32_bf16 v[8:11], a[164:167], a[20:23], v[8:11]\n"
"buffer_load_dwordx4 v254, s[48:51], s29 offen lds\n"
"v_mfma_f32_16x16x32_bf16 v[12:15], a[160:163], a[24:27], v[12:15]\n"
"s_add_u32 m0, s83, 115840\n"
"v_mfma_f32_16x16x32_bf16 v[12:15], a[164:167], a[28:31], v[12:15]\n"
"s_waitcnt lgkmcnt(4)\n"
"buffer_load_dwordx4 v242, s[40:43], s69 offen offset:896 nt lds\n"
"v_mfma_f32_16x16x32_bf16 v[16:19], a[160:163], a[32:35], v[16:19]\n"
"s_add_u32 m0, s92, 45056\n"
"s_add_u32 s29, s86, 966656\n"
"v_mfma_f32_16x16x32_bf16 v[16:19], a[164:167], a[36:39], v[16:19]\n"
"buffer_load_dwordx4 v254, s[48:51], s29 offen lds\n"
"v_mfma_f32_16x16x32_bf16 v[20:23], a[160:163], a[40:43], v[20:23]\n"
"s_add_u32 m0, s83, 148608\n"
"v_mfma_f32_16x16x32_bf16 v[20:23], a[164:167], a[44:47], v[20:23]\n"
"s_waitcnt lgkmcnt(0)\n"
"buffer_load_dwordx4 v242, s[44:47], s69 offen offset:896 nt lds\n"
"v_mfma_f32_16x16x32_bf16 v[24:27], a[160:163], a[48:51], v[24:27]\n"
"s_add_u32 m0, s92, 49152\n"
"s_add_u32 s29, s86, 983040\n"
"v_mfma_f32_16x16x32_bf16 v[24:27], a[164:167], a[52:55], v[24:27]\n"
"buffer_load_dwordx4 v254, s[48:51], s29 offen lds\n"
"v_mfma_f32_16x16x32_bf16 v[28:31], a[160:163], a[56:59], v[28:31]\n"
"s_add_u32 m0, s83, 117888\n"
"v_mfma_f32_16x16x32_bf16 v[28:31], a[164:167], a[60:63], v[28:31]\n"
"buffer_load_dwordx4 v242, s[40:43], s70 offen offset:896 nt lds\n"
"v_mfma_f32_16x16x32_bf16 v[32:35], a[168:171], a[0:3], v[32:35]\n"
"s_add_u32 m0, s92, 53248\n"
"s_add_u32 s29, s86, 999424\n"
"v_mfma_f32_16x16x32_bf16 v[32:35], a[172:175], a[4:7], v[32:35]\n"
"buffer_load_dwordx4 v254, s[48:51], s29 offen lds\n"
"v_mfma_f32_16x16x32_bf16 v[36:39], a[168:171], a[8:11], v[36:39]\n"
"v_mfma_f32_16x16x32_bf16 v[36:39], a[172:175], a[12:15], v[36:39]\n"
"s_add_u32 m0, s92, 57344\n"
"s_add_u32 s29, s86, 1015808\n"
"v_mfma_f32_16x16x32_bf16 v[40:43], a[168:171], a[16:19], v[40:43]\n"
"buffer_load_dwordx4 v254, s[48:51], s29 offen lds\n"
"v_mfma_f32_16x16x32_bf16 v[40:43], a[172:175], a[20:23], v[40:43]\n"
"s_waitcnt vmcnt(12)\n"
"s_barrier\n"
"v_mfma_f32_16x16x32_bf16 v[44:47], a[168:171], a[24:27], v[44:47]\n"
"s_add_u32 m0, s92, 61440\n"
"s_add_u32 s29, s86, 1032192\n"
"ds_read_b64_tr_b16 a[192:193], v249 offset:0\n"
"v_mfma_f32_16x16x32_bf16 v[44:47], a[172:175], a[28:31], v[44:47]\n"
"buffer_load_dwordx4 v254, s[48:51], s29 offen lds\n"
"v_mfma_f32_16x16x32_bf16 v[48:51], a[168:171], a[32:35], v[48:51]\n"
"ds_read_b64_tr_b16 a[194:195], v249 offset:2048\n"
"v_mfma_f32_16x16x32_bf16 v[48:51], a[172:175], a[36:39], v[48:51]\n"
"ds_read_b64_tr_b16 a[196:197], v249 offset:16384\n"
"v_mfma_f32_16x16x32_bf16 v[52:55], a[168:171], a[40:43], v[52:55]\n"
"ds_read_b64_tr_b16 a[198:199], v249 offset:18432\n"
"v_mfma_f32_16x16x32_bf16 v[52:55], a[172:175], a[44:47], v[52:55]\n"
"ds_read_b64_tr_b16 a[200:201], v249 offset:8\n"
"v_mfma_f32_16x16x32_bf16 v[56:59], a[168:171], a[48:51], v[56:59]\n"
"ds_read_b64_tr_b16 a[202:203], v249 offset:2056\n"
"v_mfma_f32_16x16x32_bf16 v[56:59], a[172:175], a[52:55], v[56:59]\n"
"ds_read_b64_tr_b16 a[204:205], v249 offset:16392\n"
"v_mfma_f32_16x16x32_bf16 v[60:63], a[168:171], a[56:59], v[60:63]\n"
"ds_read_b64_tr_b16 a[206:207], v249 offset:18440\n"
"v_mfma_f32_16x16x32_bf16 v[60:63], a[172:175], a[60:63], v[60:63]\n"
"ds_read_b64_tr_b16 a[208:209], v249 offset:256\n"
"v_mfma_f32_16x16x32_bf16 v[64:67], a[176:179], a[0:3], v[64:67]\n"
"ds_read_b64_tr_b16 a[210:211], v249 offset:2304\n"
"v_mfma_f32_16x16x32_bf16 v[64:67], a[180:183], a[4:7], v[64:67]\n"
"ds_read_b64_tr_b16 a[212:213], v249 offset:16640\n"
"v_mfma_f32_16x16x32_bf16 v[68:71], a[176:179], a[8:11], v[68:71]\n"
"ds_read_b64_tr_b16 a[214:215], v249 offset:18688\n"
"v_mfma_f32_16x16x32_bf16 v[68:71], a[180:183], a[12:15], v[68:71]\n"
"ds_read_b64_tr_b16 a[216:217], v249 offset:264\n"
"v_mfma_f32_16x16x32_bf16 v[72:75], a[176:179], a[16:19], v[72:75]\n"
"ds_read_b64_tr_b16 a[218:219], v249 offset:2312\n"
"v_mfma_f32_16x16x32_bf16 v[72:75], a[180:183], a[20:23], v[72:75]\n"
"ds_read_b64_tr_b16 a[220:221], v249 offset:16648\n"
"v_mfma_f32_16x16x32_bf16 v[76:79], a[176:179], a[24:27], v[76:79]\n"
"s_add_u32 m0, s83, 150656\n"
"ds_read_b64_tr_b16 a[222:223], v249 offset:18696\n"
"v_mfma_f32_16x16x32_bf16 v[76:79], a[180:183], a[28:31], v[76:79]\n"
"buffer_load_dwordx4 v242, s[44:47], s70 offen offset:896 nt lds\n"
"v_mfma_f32_16x16x32_bf16 v[80:83], a[176:179], a[32:35], v[80:83]\n"
"s_add_u32 m0, s83, 119936\n"
"v_mfma_f32_16x16x32_bf16 v[80:83], a[180:183], a[36:39], v[80:83]\n"
"buffer_load_dwordx4 v242, s[40:43], s71 offen offset:896 nt lds\n"
"v_mfma_f32_16x16x32_bf16 v[84:87], a[176:179], a[40:43], v[84:87]\n"
"v_mfma_f32_16x16x32_bf16 v[84:87], a[180:183], a[44:47], v[84:87]\n"
"v_mfma_f32_16x16x32_bf16 v[88:91], a[176:179], a[48:51], v[88:91]\n"
"v_mfma_f32_16x16x32_bf16 v[88:91], a[180:183], a[52:55], v[88:91]\n"
"v_mfma_f32_16x16x32_bf16 v[92:95], a[176:179], a[56:59], v[92:95]\n"
"s_add_u32 m0, s83, 152704\n"
"v_mfma_f32_16x16x32_bf16 v[92:95], a[180:183], a[60:63], v[92:95]\n"
"buffer_load_dwordx4 v242, s[44:47], s71 offen offset:896 nt lds\n"
"v_mfma_f32_16x16x32_bf16 v[96:99], a[184:187], a[0:3], v[96:99]\n"
"v_mfma_f32_16x16x32_bf16 v[96:99], a[188:191], a[4:7], v[96:99]\n"
"v_mfma_f32_16x16x32_bf16 v[100:103], a[184:187], a[8:11], v[100:103]\n"
"v_mfma_f32_16x16x32_bf16 v[100:103], a[188:191], a[12:15], v[100:103]\n"
"v_mfma_f32_16x16x32_bf16 v[104:107], a[184:187], a[16:19], v[104:107]\n"
"v_mfma_f32_16x16x32_bf16 v[104:107], a[188:191], a[20:23], v[104:107]\n"
"v_mfma_f32_16x16x32_bf16 v[108:111], a[184:187], a[24:27], v[108:111]\n"
"v_mfma_f32_16x16x32_bf16 v[108:111], a[188:191], a[28:31], v[108:111]\n"
"v_mfma_f32_16x16x32_bf16 v[112:115], a[184:187], a[32:35], v[112:115]\n"
"v_mfma_f32_16x16x32_bf16 v[112:115], a[188:191], a[36:39], v[112:115]\n"
"v_mfma_f32_16x16x32_bf16 v[116:119], a[184:187], a[40:43], v[116:119]\n"
"v_mfma_f32_16x16x32_bf16 v[116:119], a[188:191], a[44:47], v[116:119]\n"
"v_mfma_f32_16x16x32_bf16 v[120:123], a[184:187], a[48:51], v[120:123]\n"
"v_mfma_f32_16x16x32_bf16 v[120:123], a[188:191], a[52:55], v[120:123]\n"
"v_mfma_f32_16x16x32_bf16 v[124:127], a[184:187], a[56:59], v[124:127]\n"
"v_mfma_f32_16x16x32_bf16 v[124:127], a[188:191], a[60:63], v[124:127]\n"
"s_waitcnt lgkmcnt(12)\n"
"v_mfma_f32_16x16x32_bf16 v[128:131], a[192:195], a[0:3], v[128:131]\n"
"v_mfma_f32_16x16x32_bf16 v[128:131], a[196:199], a[4:7], v[128:131]\n"
"v_mfma_f32_16x16x32_bf16 v[132:135], a[192:195], a[8:11], v[132:135]\n"
"v_mfma_f32_16x16x32_bf16 v[132:135], a[196:199], a[12:15], v[132:135]\n"
"v_mfma_f32_16x16x32_bf16 v[136:139], a[192:195], a[16:19], v[136:139]\n"
"v_mfma_f32_16x16x32_bf16 v[136:139], a[196:199], a[20:23], v[136:139]\n"
"v_mfma_f32_16x16x32_bf16 v[140:143], a[192:195], a[24:27], v[140:143]\n"
"v_mfma_f32_16x16x32_bf16 v[140:143], a[196:199], a[28:31], v[140:143]\n"
"v_mfma_f32_16x16x32_bf16 v[144:147], a[192:195], a[32:35], v[144:147]\n"
"v_mfma_f32_16x16x32_bf16 v[144:147], a[196:199], a[36:39], v[144:147]\n"
"v_mfma_f32_16x16x32_bf16 v[148:151], a[192:195], a[40:43], v[148:151]\n"
"v_mfma_f32_16x16x32_bf16 v[148:151], a[196:199], a[44:47], v[148:151]\n"
"v_mfma_f32_16x16x32_bf16 v[152:155], a[192:195], a[48:51], v[152:155]\n"
"v_mfma_f32_16x16x32_bf16 v[152:155], a[196:199], a[52:55], v[152:155]\n"
"s_waitcnt vmcnt(3)\n"
"s_barrier\n"
"v_mfma_f32_16x16x32_bf16 v[156:159], a[192:195], a[56:59], v[156:159]\n"
"ds_read_b64_tr_b16 a[224:225], v249 offset:32768\n"
"v_mfma_f32_16x16x32_bf16 v[156:159], a[196:199], a[60:63], v[156:159]\n"
"s_waitcnt lgkmcnt(9)\n"
"ds_read_b64_tr_b16 a[226:227], v249 offset:34816\n"
"v_mfma_f32_16x16x32_bf16 v[160:163], a[200:203], a[0:3], v[160:163]\n"
"ds_read_b64_tr_b16 a[228:229], v249 offset:49152\n"
"v_mfma_f32_16x16x32_bf16 v[160:163], a[204:207], a[4:7], v[160:163]\n"
"ds_read_b64_tr_b16 a[230:231], v249 offset:51200\n"
"v_mfma_f32_16x16x32_bf16 v[164:167], a[200:203], a[8:11], v[164:167]\n"
"ds_read_b64_tr_b16 a[232:233], v249 offset:32776\n"
"v_mfma_f32_16x16x32_bf16 v[164:167], a[204:207], a[12:15], v[164:167]\n"
"ds_read_b64_tr_b16 a[234:235], v249 offset:34824\n"
"v_mfma_f32_16x16x32_bf16 v[168:171], a[200:203], a[16:19], v[168:171]\n"
"ds_read_b64_tr_b16 a[236:237], v249 offset:49160\n"
"v_mfma_f32_16x16x32_bf16 v[168:171], a[204:207], a[20:23], v[168:171]\n"
"ds_read_b64_tr_b16 a[238:239], v249 offset:51208\n"
"v_mfma_f32_16x16x32_bf16 v[172:175], a[200:203], a[24:27], v[172:175]\n"
"ds_read_b64_tr_b16 a[240:241], v249 offset:33024\n"
"v_mfma_f32_16x16x32_bf16 v[172:175], a[204:207], a[28:31], v[172:175]\n"
"ds_read_b64_tr_b16 a[242:243], v249 offset:35072\n"
"v_mfma_f32_16x16x32_bf16 v[176:179], a[200:203], a[32:35], v[176:179]\n"
"ds_read_b64_tr_b16 a[244:245], v249 offset:49408\n"
"v_mfma_f32_16x16x32_bf16 v[176:179], a[204:207], a[36:39], v[176:179]\n"
"ds_read_b64_tr_b16 a[246:247], v249 offset:51456\n"
"v_mfma_f32_16x16x32_bf16 v[180:183], a[200:203], a[40:43], v[180:183]\n"
"ds_read_b64_tr_b16 a[248:249], v249 offset:33032\n"
"v_mfma_f32_16x16x32_bf16 v[180:183], a[204:207], a[44:47], v[180:183]\n"
"ds_read_b64_tr_b16 a[250:251], v249 offset:35080\n"
"v_mfma_f32_16x16x32_bf16 v[184:187], a[200:203], a[48:51], v[184:187]\n"
"ds_read_b64_tr_b16 a[252:253], v249 offset:49416\n"
"v_mfma_f32_16x16x32_bf16 v[184:187], a[204:207], a[52:55], v[184:187]\n"
"ds_read_b64_tr_b16 a[254:255], v249 offset:51464\n"
"v_mfma_f32_16x16x32_bf16 v[188:191], a[200:203], a[56:59], v[188:191]\n"
"v_subrev_u32 v249, 0x10000, v250\n"
"v_add_u32 v254, s28, v240\n"
"v_mfma_f32_16x16x32_bf16 v[188:191], a[204:207], a[60:63], v[188:191]\n"
"s_waitcnt lgkmcnt(15)\n"
"v_lshlrev_b32 v241, 4, v240\n"
"v_mfma_f32_16x16x32_bf16 v[192:195], a[208:211], a[0:3], v[192:195]\n"
"buffer_load_dword v241, v241, s[24:27], s84 offen sc1\n"
"v_mfma_f32_16x16x32_bf16 v[192:195], a[212:215], a[4:7], v[192:195]\n"
"v_mfma_f32_16x16x32_bf16 v[196:199], a[208:211], a[8:11], v[196:199]\n"
"v_mfma_f32_16x16x32_bf16 v[196:199], a[212:215], a[12:15], v[196:199]\n"
"v_mfma_f32_16x16x32_bf16 v[200:203], a[208:211], a[16:19], v[200:203]\n"
"v_mfma_f32_16x16x32_bf16 v[200:203], a[212:215], a[20:23], v[200:203]\n"
"v_mfma_f32_16x16x32_bf16 v[204:207], a[208:211], a[24:27], v[204:207]\n"
"v_mfma_f32_16x16x32_bf16 v[204:207], a[212:215], a[28:31], v[204:207]\n"
"v_mfma_f32_16x16x32_bf16 v[208:211], a[208:211], a[32:35], v[208:211]\n"
"v_mfma_f32_16x16x32_bf16 v[208:211], a[212:215], a[36:39], v[208:211]\n"
"v_mfma_f32_16x16x32_bf16 v[212:215], a[208:211], a[40:43], v[212:215]\n"
"v_mfma_f32_16x16x32_bf16 v[212:215], a[212:215], a[44:47], v[212:215]\n"
"v_mfma_f32_16x16x32_bf16 v[216:219], a[208:211], a[48:51], v[216:219]\n"
"v_mfma_f32_16x16x32_bf16 v[216:219], a[212:215], a[52:55], v[216:219]\n"
"s_waitcnt vmcnt(1) lgkmcnt(0)\n"
"s_barrier\n"
"v_mfma_f32_16x16x32_bf16 v[220:223], a[208:211], a[56:59], v[220:223]\n"
"v_mfma_f32_16x16x32_bf16 v[220:223], a[212:215], a[60:63], v[220:223]\n"
"v_mfma_f32_16x16x32_bf16 a[112:115], a[216:219], a[0:3], a[112:115]\n"
"ds_read_b128 a[0:3], v245 offset:49152\n"
"v_mfma_f32_16x16x32_bf16 a[112:115], a[220:223], a[4:7], a[112:115]\n"
"ds_read_b128 a[4:7], v246 offset:49152\n"
"v_mfma_f32_16x16x32_bf16 a[116:119], a[216:219], a[8:11], a[116:119]\n"
"ds_read_b128 a[8:11], v245 offset:51200\n"
"v_mfma_f32_16x16x32_bf16 a[116:119], a[220:223], a[12:15], a[116:119]\n"
"ds_read_b128 a[12:15], v246 offset:51200\n"
"v_mfma_f32_16x16x32_bf16 a[120:123], a[216:219], a[16:19], a[120:123]\n"
"ds_read_b128 a[16:19], v245 offset:53248\n"
"v_mfma_f32_16x16x32_bf16 a[120:123], a[220:223], a[20:23], a[120:123]\n"
"ds_read_b128 a[20:23], v246 offset:53248\n"
"v_mfma_f32_16x16x32_bf16 a[124:127], a[216:219], a[24:27], a[124:127]\n"
"ds_read_b128 a[24:27], v245 offset:55296\n"
"v_mfma_f32_16x16x32_bf16 a[124:127], a[220:223], a[28:31], a[124:127]\n"
"ds_read_b128 a[28:31], v246 offset:55296\n"
"v_mfma_f32_16x16x32_bf16 a[128:131], a[216:219], a[32:35], a[128:131]\n"
"ds_read_b128 a[32:35], v245 offset:57344\n"
"v_mfma_f32_16x16x32_bf16 a[128:131], a[220:223], a[36:39], a[128:131]\n"
"ds_read_b128 a[36:39], v246 offset:57344\n"
"v_mfma_f32_16x16x32_bf16 a[132:135], a[216:219], a[40:43], a[132:135]\n"
"ds_read_b128 a[40:43], v245 offset:59392\n"
"v_mfma_f32_16x16x32_bf16 a[132:135], a[220:223], a[44:47], a[132:135]\n"
"ds_read_b128 a[44:47], v246 offset:59392\n"
"v_mfma_f32_16x16x32_bf16 a[136:139], a[216:219], a[48:51], a[136:139]\n"
"ds_read_b128 a[48:51], v245 offset:61440\n"
"v_mfma_f32_16x16x32_bf16 a[136:139], a[220:223], a[52:55], a[136:139]\n"
"ds_read_b128 a[52:55], v246 offset:61440\n"
"v_mfma_f32_16x16x32_bf16 a[140:143], a[216:219], a[56:59], a[140:143]\n"
"ds_read_b128 a[56:59], v245 offset:63488\n"
"v_mfma_f32_16x16x32_bf16 a[140:143], a[220:223], a[60:63], a[140:143]\n"
"s_waitcnt lgkmcnt(11)\n"
"ds_read_b128 a[60:63], v246 offset:63488\n"
"v_mfma_f32_16x16x32_bf16 v[0:3], a[224:227], a[0:3], v[0:3]\n"
"v_mfma_f32_16x16x32_bf16 v[0:3], a[228:231], a[4:7], v[0:3]\n"
"v_mfma_f32_16x16x32_bf16 v[4:7], a[224:227], a[8:11], v[4:7]\n"
"v_mfma_f32_16x16x32_bf16 v[4:7], a[228:231], a[12:15], v[4:7]\n"
"s_waitcnt lgkmcnt(8)\n"
"v_mfma_f32_16x16x32_bf16 v[8:11], a[224:227], a[16:19], v[8:11]\n"
"v_mfma_f32_16x16x32_bf16 v[8:11], a[228:231], a[20:23], v[8:11]\n"
"s_waitcnt vmcnt(0)\n"
"v_cmp_ne_u32 vcc, s33, v241\n"
"v_mfma_f32_16x16x32_bf16 v[12:15], a[224:227], a[24:27], v[12:15]\n"
"s_mov_b64 s[48:49], %2\n"
"v_mfma_f32_16x16x32_bf16 v[12:15], a[228:231], a[28:31], v[12:15]\n"
"s_waitcnt lgkmcnt(4)\n"
"v_mfma_f32_16x16x32_bf16 v[16:19], a[224:227], a[32:35], v[16:19]\n"
"s_add_u32 s48, s48, s89\n"
"s_addc_u32 s49, s49, 0\n"
"v_mfma_f32_16x16x32_bf16 v[16:19], a[228:231], a[36:39], v[16:19]\n"
"s_mov_b32 s50, s42\n"
"v_mfma_f32_16x16x32_bf16 v[20:23], a[224:227], a[40:43], v[20:23]\n"
"s_cmp_eq_u64 vcc, 0\n"
"s_cbranch_scc0 .Lslow6_%=\n"
".Lback6_%=:\n"
"v_mfma_f32_16x16x32_bf16 v[20:23], a[228:231], a[44:47], v[20:23]\n"
"s_waitcnt lgkmcnt(0)\n"
"v_mfma_f32_16x16x32_bf16 v[24:27], a[224:227], a[48:51], v[24:27]\n"
"v_mfma_f32_16x16x32_bf16 v[24:27], a[228:231], a[52:55], v[24:27]\n"
"s_barrier\n"
"v_mfma_f32_16x16x32_bf16 v[28:31], a[224:227], a[56:59], v[28:31]\n"
"buffer_load_dwordx4 v[224:227], v240, s[36:39], s80 offen offset:0 sc1\n"
"v_mfma_f32_16x16x32_bf16 v[28:31], a[228:231], a[60:63], v[28:31]\n"
"v_mfma_f32_16x16x32_bf16 v[32:35], a[232:235], a[0:3], v[32:35]\n"
"v_mfma_f32_16x16x32_bf16 v[32:35], a[236:239], a[4:7], v[32:35]\n"
"buffer_load_dwordx4 v[228:231], v240, s[36:39], s80 offen offset:1024 sc1\n"
"v_mfma_f32_16x16x32_bf16 v[36:39], a[232:235], a[8:11], v[36:39]\n"
"v_mfma_f32_16x16x32_bf16 v[36:39], a[236:239], a[12:15], v[36:39]\n"
"v_mfma_f32_16x16x32_bf16 v[40:43], a[232:235], a[16:19], v[40:43]\n"
"buffer_load_dwordx4 a[64:67], v240, s[36:39], s80 offen offset:2048 sc1\n"
"v_mfma_f32_16x16x32_bf16 v[40:43], a[236:239], a[20:23], v[40:43]\n"
"v_mfma_f32_16x16x32_bf16 v[44:47], a[232:235], a[24:27], v[44:47]\n"
"v_mfma_f32_16x16x32_bf16 v[44:47], a[236:239], a[28:31], v[44:47]\n"
"buffer_load_dwordx4 a[68:71], v240, s[36:39], s80 offen offset:3072 sc1\n"
"v_mfma_f32_16x16x32_bf16 v[48:51], a[232:235], a[32:35], v[48:51]\n"
"s_add_u32 s80, s80, 0x1000\n"
"v_mfma_f32_16x16x32_bf16 v[48:51], a[236:239], a[36:39], v[48:51]\n"
"v_mfma_f32_16x16x32_bf16 v[52:55], a[232:235], a[40:43], v[52:55]\n"
"buffer_load_dwordx4 a[72:75], v240, s[36:39], s80 offen offset:0 sc1\n"
"v_mfma_f32_16x16x32_bf16 v[52:55], a[236:239], a[44:47], v[52:55]\n"
"v_mfma_f32_16x16x32_bf16 v[56:59], a[232:235], a[48:51], v[56:59]\n"
"v_mfma_f32_16x16x32_bf16 v[56:59], a[236:239], a[52:55], v[56:59]\n"
"buffer_load_dwordx4 a[76:79], v240, s[36:39], s80 offen offset:1024 sc1\n"
"v_mfma_f32_16x16x32_bf16 v[60:63], a[232:235], a[56:59], v[60:63]\n"
"v_mfma_f32_16x16x32_bf16 v[60:63], a[236:239], a[60:63], v[60:63]\n"
"v_mfma_f32_16x16x32_bf16 v[64:67], a[240:243], a[0:3], v[64:67]\n"
"buffer_load_dwordx4 a[80:83], v240, s[36:39], s80 offen offset:2048 sc1\n"
"v_mfma_f32_16x16x32_bf16 v[64:67], a[244:247], a[4:7], v[64:67]\n"
"v_mfma_f32_16x16x32_bf16 v[68:71], a[240:243], a[8:11], v[68:71]\n"
"v_mfma_f32_16x16x32_bf16 v[68:71], a[244:247], a[12:15], v[68:71]\n"
"buffer_load_dwordx4 a[84:87], v240, s[36:39], s80 offen offset:3072 sc1\n"
"v_mfma_f32_16x16x32_bf16 v[72:75], a[240:243], a[16:19], v[72:75]\n"
"s_add_u32 s80, s80, 0x1000\n"
"v_mfma_f32_16x16x32_bf16 v[72:75], a[244:247], a[20:23], v[72:75]\n"
"v_mfma_f32_16x16x32_bf16 v[76:79], a[240:243], a[24:27], v[76:79]\n"
"buffer_load_dwordx4 a[88:91], v240, s[36:39], s80 offen offset:0 sc1\n"
"v_mfma_f32_16x16x32_bf16 v[76:79], a[244:247], a[28:31], v[76:79]\n"
"v_mfma_f32_16x16x32_bf16 v[80:83], a[240:243], a[32:35], v[80:83]\n"
"v_mfma_f32_16x16x32_bf16 v[80:83], a[244:247], a[36:39], v[80:83]\n"
"buffer_load_dwordx4 a[92:95], v240, s[36:39], s80 offen offset:1024 sc1\n"
"v_mfma_f32_16x16x32_bf16 v[84:87], a[240:243], a[40:43], v[84:87]\n"
"v_mfma_f32_16x16x32_bf16 v[84:87], a[244:247], a[44:47], v[84:87]\n"
"s_cmp_lg_u32 s31, 0\n"
"s_cbranch_scc1 .Lf2_%=\n"
"v_mov_b32 v241, s33\n"
"buffer_store_dword v241, off, s[24:27], 0 sc1\n"
".Lf2_%=:\n"
"v_mfma_f32_16x16x32_bf16 v[88:91], a[240:243], a[48:51], v[88:91]\n"
"buffer_load_dwordx4 a[96:99], v240, s[36:39], s80 offen offset:2048 sc1\n"
"v_mfma_f32_16x16x32_bf16 v[88:91], a[244:247], a[52:55], v[88:91]\n"
"v_mfma_f32_16x16x32_bf16 v[92:95], a[240:243], a[56:59], v[92:95]\n"
"v_mfma_f32_16x16x32_bf16 v[92:95], a[244:247], a[60:63], v[92:95]\n"
"buffer_load_dwordx4 a[100:103], v240, s[36:39], s80 offen offset:3072 sc1\n"
"v_mfma_f32_16x16x32_bf16 v[96:99], a[248:251], a[0:3], v[96:99]\n"
"s_add_u32 s80, s80, 0x1000\n"
"v_mfma_f32_16x16x32_bf16 v[96:99], a[252:255], a[4:7], v[96:99]\n"
"v_mfma_f32_16x16x32_bf16 v[100:103], a[248:251], a[8:11], v[100:103]\n"
"buffer_load_dwordx4 a[104:107], v240, s[36:39], s80 offen offset:0 sc1\n"
"v_mfma_f32_16x16x32_bf16 v[100:103], a[252:255], a[12:15], v[100:103]\n"
"v_mfma_f32_16x16x32_bf16 v[104:107], a[248:251], a[16:19], v[104:107]\n"
"v_mfma_f32_16x16x32_bf16 v[104:107], a[252:255], a[20:23], v[104:107]\n"
"buffer_load_dwordx4 a[108:111], v240, s[36:39], s80 offen offset:1024 sc1\n"
"v_mfma_f32_16x16x32_bf16 v[108:111], a[248:251], a[24:27], v[108:111]\n"
"v_mfma_f32_16x16x32_bf16 v[108:111], a[252:255], a[28:31], v[108:111]\n"
"v_mfma_f32_16x16x32_bf16 v[112:115], a[248:251], a[32:35], v[112:115]\n"
"v_mfma_f32_16x16x32_bf16 v[112:115], a[252:255], a[36:39], v[112:115]\n"
"v_mfma_f32_16x16x32_bf16 v[116:119], a[248:251], a[40:43], v[116:119]\n"
"v_mfma_f32_16x16x32_bf16 v[116:119], a[252:255], a[44:47], v[116:119]\n"
"v_mfma_f32_16x16x32_bf16 v[120:123], a[248:251], a[48:51], v[120:123]\n"
"v_mfma_f32_16x16x32_bf16 v[120:123], a[252:255], a[52:55], v[120:123]\n"
"v_mfma_f32_16x16x32_bf16 v[124:127], a[248:251], a[56:59], v[124:127]\n"
"v_mfma_f32_16x16x32_bf16 v[124:127], a[252:255], a[60:63], v[124:127]\n"
"s_waitcnt vmcnt(12)\n"
"v_mfma_f32_16x16x32_bf16 v[128:131], v[224:227], a[0:3], v[128:131]\n"
"v_mfma_f32_16x16x32_bf16 v[128:131], v[228:231], a[4:7], v[128:131]\n"
"v_mfma_f32_16x16x32_bf16 v[132:135], v[224:227], a[8:11], v[132:135]\n"
"v_mfma_f32_16x16x32_bf16 v[132:135], v[228:231], a[12:15], v[132:135]\n"
"v_mfma_f32_16x16x32_bf16 v[136:139], v[224:227], a[16:19], v[136:139]\n"
"v_mfma_f32_16x16x32_bf16 v[136:139], v[228:231], a[20:23], v[136:139]\n"
"v_mfma_f32_16x16x32_bf16 v[140:143], v[224:227], a[24:27], v[140:143]\n"
"v_mfma_f32_16x16x32_bf16 v[140:143], v[228:231], a[28:31], v[140:143]\n"
"v_mfma_f32_16x16x32_bf16 v[144:147], v[224:227], a[32:35], v[144:147]\n"
"v_mfma_f32_16x16x32_bf16 v[144:147], v[228:231], a[36:39], v[144:147]\n"
"s_add_u32 m0, s83, 65024\n"
"v_mfma_f32_16x16x32_bf16 v[148:151], v[224:227], a[40:43], v[148:151]\n"
"buffer_load_dwordx4 v242, s[40:43], s68 offen offset:512 nt lds\n"
"v_mfma_f32_16x16x32_bf16 v[148:151], v[228:231], a[44:47], v[148:151]\n"
"v_mfma_f32_16x16x32_bf16 v[152:155], v[224:227], a[48:51], v[152:155]\n"
"v_mfma_f32_16x16x32_bf16 v[152:155], v[228:231], a[52:55], v[152:155]\n"
"v_mfma_f32_16x16x32_bf16 v[156:159], v[224:227], a[56:59], v[156:159]\n"
"buffer_load_dwordx4 v[224:227], v240, s[36:39], s80 offen offset:2048 sc1\n"
"v_mfma_f32_16x16x32_bf16 v[156:159], v[228:231], a[60:63], v[156:159]\n"
"s_waitcnt vmcnt(12)\n"
"v_mfma_f32_16x16x32_bf16 v[160:163], a[64:67], a[0:3], v[160:163]\n"
"s_add_u32 m0, s15, 40960\n"
"s_mov_b32 s29, 2097152\n"
"v_mfma_f32_16x16x32_bf16 v[160:163], a[68:71], a[4:7], v[160:163]\n"
"buffer_load_dwordx4 v252, s[16:19], s29 offen nt lds\n"
"v_mfma_f32_16x16x32_bf16 v[164:167], a[64:67], a[8:11], v[164:167]\n"
"v_mfma_f32_16x16x32_bf16 v[164:167], a[68:71], a[12:15], v[164:167]\n"
"v_mfma_f32_16x16x32_bf16 v[168:171], a[64:67], a[16:19], v[168:171]\n"
"v_mfma_f32_16x16x32_bf16 v[168:171], a[68:71], a[20:23], v[168:171]\n"
"v_mfma_f32_16x16x32_bf16 v[172:175], a[64:67], a[24:27], v[172:175]\n"
"s_add_u32 m0, s83, 130560\n"
"buffer_load_dwordx4 v[228:231], v240, s[36:39], s80 offen offset:3072 sc1\n"
"v_mfma_f32_16x16x32_bf16 v[172:175], a[68:71], a[28:31], v[172:175]\n"
"s_add_u32 s80, s80, 0x1000\n"
"buffer_load_dwordx4 v242, s[44:47], s68 offen offset:512 nt lds\n"
"v_mfma_f32_16x16x32_bf16 v[176:179], a[64:67], a[32:35], v[176:179]\n"
"ds_read_b128 a[228:231], v250 offset:32768\n"
"v_mfma_f32_16x16x32_bf16 v[176:179], a[68:71], a[36:39], v[176:179]\n"
"v_mfma_f32_16x16x32_bf16 v[180:183], a[64:67], a[40:43], v[180:183]\n"
"v_mfma_f32_16x16x32_bf16 v[180:183], a[68:71], a[44:47], v[180:183]\n"
"ds_read_b128 a[232:235], v250 offset:34816\n"
"v_mfma_f32_16x16x32_bf16 v[184:187], a[64:67], a[48:51], v[184:187]\n"
"v_mfma_f32_16x16x32_bf16 v[184:187], a[68:71], a[52:55], v[184:187]\n"
"v_mfma_f32_16x16x32_bf16 v[188:191], a[64:67], a[56:59], v[188:191]\n"
"ds_read_b128 a[236:239], v250 offset:36864\n"
"buffer_load_dwordx4 a[64:67], v240, s[36:39], s80 offen offset:0 sc1\n"
"v_mfma_f32_16x16x32_bf16 v[188:191], a[68:71], a[60:63], v[188:191]\n"
"s_waitcnt vmcnt(14)\n"
"v_mfma_f32_16x16x32_bf16 v[192:195], a[72:75], a[0:3], v[192:195]\n"
"v_mfma_f32_16x16x32_bf16 v[192:195], a[76:79], a[4:7], v[192:195]\n"
"ds_read_b128 a[240:243], v250 offset:38912\n"
"v_mfma_f32_16x16x32_bf16 v[196:199], a[72:75], a[8:11], v[196:199]\n"
"v_mfma_f32_16x16x32_bf16 v[196:199], a[76:79], a[12:15], v[196:199]\n"
"s_add_u32 m0, s83, 67072\n"
"v_mfma_f32_16x16x32_bf16 v[200:203], a[72:75], a[16:19], v[200:203]\n"
"buffer_load_dwordx4 v242, s[40:43], s69 offen offset:512 nt lds\n"
"v_mfma_f32_16x16x32_bf16 v[200:203], a[76:79], a[20:23], v[200:203]\n"
"ds_read_b128 a[244:247], v250 offset:40960\n"
"v_mfma_f32_16x16x32_bf16 v[204:207], a[72:75], a[24:27], v[204:207]\n"
"s_add_u32 m0, s15, 41984\n"
"s_mov_b32 s29, 2105344\n"
"buffer_load_dwordx4 a[68:71], v240, s[36:39], s80 offen offset:1024 sc1\n"
"v_mfma_f32_16x16x32_bf16 v[204:207], a[76:79], a[28:31], v[204:207]\n"
"buffer_load_dwordx4 v252, s[16:19], s29 offen nt lds\n"
"v_mfma_f32_16x16x32_bf16 v[208:211], a[72:75], a[32:35], v[208:211]\n"
"ds_read_b128 a[248:251], v250 offset:43008\n"
"v_mfma_f32_16x16x32_bf16 v[208:211], a[76:79], a[36:39], v[208:211]\n"
"v_mfma_f32_16x16x32_bf16 v[212:215], a[72:75], a[40:43], v[212:215]\n"
"v_mfma_f32_16x16x32_bf16 v[212:215], a[76:79], a[44:47], v[212:215]\n"
"ds_read_b128 a[252:255], v250 offset:45056\n"
"v_mfma_f32_16x16x32_bf16 v[216:219], a[72:75], a[48:51], v[216:219]\n"
"v_mfma_f32_16x16x32_bf16 v[216:219], a[76:79], a[52:55], v[216:219]\n"
"v_mfma_f32_16x16x32_bf16 v[220:223], a[72:75], a[56:59], v[220:223]\n"
"ds_read_b128 a[224:227], v250 offset:47104\n"
"buffer_load_dwordx4 a[72:75], v240, s[36:39], s80 offen offset:2048 sc1\n"
"v_mfma_f32_16x16x32_bf16 v[220:223], a[76:79], a[60:63], v[220:223]\n"
"s_waitcnt vmcnt(16)\n"
"v_mfma_f32_16x16x32_bf16 a[112:115], a[80:83], a[0:3], a[112:115]\n"
"ds_read_b128 a[0:3], v247 offset:16384\n"
"v_mfma_f32_16x16x32_bf16 a[112:115], a[84:87], a[4:7], a[112:115]\n"
"ds_read_b128 a[4:7], v248 offset:16384\n"
"v_mfma_f32_16x16x32_bf16 a[116:119], a[80:83], a[8:11], a[116:119]\n"
"ds_read_b128 a[8:11], v247 offset:18432\n"
"v_mfma_f32_16x16x32_bf16 a[116:119], a[84:87], a[12:15], a[116:119]\n"
"ds_read_b128 a[12:15], v248 offset:18432\n"
"v_mfma_f32_16x16x32_bf16 a[120:123], a[80:83], a[16:19], a[120:123]\n"
"ds_read_b128 a[16:19], v247 offset:20480\n"
"v_mfma_f32_16x16x32_bf16 a[120:123], a[84:87], a[20:23], a[120:123]\n"
"ds_read_b128 a[20:23], v248 offset:20480\n"
"v_mfma_f32_16x16x32_bf16 a[124:127], a[80:83], a[24:27], a[124:127]\n"
"ds_read_b128 a[24:27], v247 offset:22528\n"
"buffer_load_dwordx4 a[76:79], v240, s[36:39], s80 offen offset:3072 sc1\n"
"v_mfma_f32_16x16x32_bf16 a[124:127], a[84:87], a[28:31], a[124:127]\n"
"s_add_u32 s80, s80, 0x1000\n"
"ds_read_b128 a[28:31], v248 offset:22528\n"
"v_mfma_f32_16x16x32_bf16 a[128:131], a[80:83], a[32:35], a[128:131]\n"
"ds_read_b128 a[32:35], v247 offset:24576\n"
"v_mfma_f32_16x16x32_bf16 a[128:131], a[84:87], a[36:39], a[128:131]\n"
"ds_read_b128 a[36:39], v248 offset:24576\n"
"v_mfma_f32_16x16x32_bf16 a[132:135], a[80:83], a[40:43], a[132:135]\n"
"ds_read_b128 a[40:43], v247 offset:26624\n"
"v_mfma_f32_16x16x32_bf16 a[132:135], a[84:87], a[44:47], a[132:135]\n"
"ds_read_b128 a[44:47], v248 offset:26624\n"
"v_mfma_f32_16x16x32_bf16 a[136:139], a[80:83], a[48:51], a[136:139]\n"
"ds_read_b128 a[48:51], v247 offset:28672\n"
"v_mfma_f32_16x16x32_bf16 a[136:139], a[84:87], a[52:55], a[136:139]\n"
"ds_read_b128 a[52:55], v248 offset:28672\n"
"v_mfma_f32_16x16x32_bf16 a[140:143], a[80:83], a[56:59], a[140:143]\n"
"ds_read_b128 a[56:59], v247 offset:30720\n"
"buffer_load_dwordx4 a[80:83], v240, s[36:39], s80 offen offset:0 sc1\n"
"v_mfma_f32_16x16x32_bf16 a[140:143], a[84:87], a[60:63], a[140:143]\n"
"s_waitcnt vmcnt(16) lgkmcnt(11)\n"
"ds_read_b128 a[60:63], v248 offset:30720\n"
"v_mfma_f32_16x16x32_bf16 v[0:3], a[88:91], a[0:3], v[0:3]\n"
"s_add_u32 m0, s83, 132608\n"
"v_mfma_f32_16x16x32_bf16 v[0:3], a[92:95], a[4:7], v[0:3]\n"
"buffer_load_dwordx4 v242, s[44:47], s69 offen offset:512 nt lds\n"
"v_mfma_f32_16x16x32_bf16 v[4:7], a[88:91], a[8:11], v[4:7]\n"
"s_add_u32 m0, s83, 69120\n"
"v_mfma_f32_16x16x32_bf16 v[4:7], a[92:95], a[12:15], v[4:7]\n"
"s_waitcnt lgkmcnt(8)\n"
"buffer_load_dwordx4 v242, s[40:43], s70 offen offset:512 nt lds\n"
"v_mfma_f32_16x16x32_bf16 v[8:11], a[88:91], a[16:19], v[8:11]\n"
"s_add_u32 m0, s15, 49152\n"
"s_mov_b32 s29, 4194304\n"
"v_mfma_f32_16x16x32_bf16 v[8:11], a[92:95], a[20:23], v[8:11]\n"
"buffer_load_dwordx4 v252, s[16:19], s29 offen nt lds\n"
"v_mfma_f32_16x16x32_bf16 v[12:15], a[88:91], a[24:27], v[12:15]\n"
"buffer_load_dwordx4 a[84:87], v240, s[36:39], s80 offen offset:1024 sc1\n"
"v_mfma_f32_16x16x32_bf16 v[12:15], a[92:95], a[28:31], v[12:15]\n"
"s_waitcnt lgkmcnt(4)\n"
"v_mfma_f32_16x16x32_bf16 v[16:19], a[88:91], a[32:35], v[16:19]\n"
"v_mfma_f32_16x16x32_bf16 v[16:19], a[92:95], a[36:39], v[16:19]\n"
"v_mfma_f32_16x16x32_bf16 v[20:23], a[88:91], a[40:43], v[20:23]\n"
"s_add_u32 m0, s83, 134656\n"
"v_mfma_f32_16x16x32_bf16 v[20:23], a[92:95], a[44:47], v[20:23]\n"
"s_waitcnt lgkmcnt(0)\n"
"buffer_load_dwordx4 v242, s[44:47], s70 offen offset:512 nt lds\n"
"v_mfma_f32_16x16x32_bf16 v[24:27], a[88:91], a[48:51], v[24:27]\n"
"v_mfma_f32_16x16x32_bf16 v[24:27], a[92:95], a[52:55], v[24:27]\n"
"v_mfma_f32_16x16x32_bf16 v[28:31], a[88:91], a[56:59], v[28:31]\n"
"buffer_load_dwordx4 a[88:91], v240, s[36:39], s80 offen offset:2048 sc1\n"
"v_mfma_f32_16x16x32_bf16 v[28:31], a[92:95], a[60:63], v[28:31]\n"
"s_waitcnt vmcnt(20)\n"
"v_mfma_f32_16x16x32_bf16 v[32:35], a[96:99], a[0:3], v[32:35]\n"
"v_mfma_f32_16x16x32_bf16 v[32:35], a[100:103], a[4:7], v[32:35]\n"
"v_mfma_f32_16x16x32_bf16 v[36:39], a[96:99], a[8:11], v[36:39]\n"
"v_mfma_f32_16x16x32_bf16 v[36:39], a[100:103], a[12:15], v[36:39]\n"
"v_mfma_f32_16x16x32_bf16 v[40:43], a[96:99], a[16:19], v[40:43]\n"
"v_mfma_f32_16x16x32_bf16 v[40:43], a[100:103], a[20:23], v[40:43]\n"
"v_mfma_f32_16x16x32_bf16 v[44:47], a[96:99], a[24:27], v[44:47]\n"
"buffer_load_dwordx4 a[92:95], v240, s[36:39], s80 offen offset:3072 sc1\n"
"v_mfma_f32_16x16x32_bf16 v[44:47], a[100:103], a[28:31], v[44:47]\n"
"s_add_u32 s80, s80, 0x1000\n"
"v_mfma_f32_16x16x32_bf16 v[48:51], a[96:99], a[32:35], v[48:51]\n"
".Lpad2_%=:\n"
"s_cbranch_execz .Lpad6_%=\n"
"s_add_u32 m0, s83, 71168\n"
"v_mfma_f32_16x16x32_bf16 v[48:51], a[100:103], a[36:39], v[48:51]\n"
"buffer_load_dwordx4 v242, s[40:43], s71 offen offset:512 nt lds\n"
"v_mfma_f32_16x16x32_bf16 v[52:55], a[96:99], a[40:43], v[52:55]\n"
"s_add_u32 m0, s15, 50176\n"
"s_mov_b32 s29, 4202496\n"
"v_mfma_f32_16x16x32_bf16 v[52:55], a[100:103], a[44:47], v[52:55]\n"
"buffer_load_dwordx4 v252, s[16:19], s29 offen nt lds\n"
"v_mfma_f32_16x16x32_bf16 v[56:59], a[96:99], a[48:51], v[56:59]\n"
"v_mfma_f32_16x16x32_bf16 v[56:59], a[100:103], a[52:55], v[56:59]\n"
"v_mfma_f32_16x16x32_bf16 v[60:63], a[96:99], a[56:59], v[60:63]\n"
"buffer_load_dwordx4 a[96:99], v240, s[36:39], s80 offen offset:0 sc1\n"
"v_mfma_f32_16x16x32_bf16 v[60:63], a[100:103], a[60:63], v[60:63]\n"
"s_waitcnt vmcnt(22)\n"
"v_mfma_f32_16x16x32_bf16 v[64:67], a[104:107], a[0:3], v[64:67]\n"
"v_mfma_f32_16x16x32_bf16 v[64:67], a[108:111], a[4:7], v[64:67]\n"
"v_mfma_f32_16x16x32_bf16 v[68:71], a[104:107], a[8:11], v[68:71]\n"
"v_mfma_f32_16x16x32_bf16 v[68:71], a[108:111], a[12:15], v[68:71]\n"
"v_mfma_f32_16x16x32_bf16 v[72:75], a[104:107], a[16:19], v[72:75]\n"
"s_add_u32 m0, s83, 136704\n"
"v_mfma_f32_16x16x32_bf16 v[72:75], a[108:111], a[20:23], v[72:75]\n"
"buffer_load_dwordx4 v242, s[44:47], s71 offen offset:512 nt lds\n"
"v_mfma_f32_16x16x32_bf16 v[76:79], a[104:107], a[24:27], v[76:79]\n"
"buffer_load_dwordx4 a[100:103], v240, s[36:39], s80 offen offset:1024 sc1\n"
"v_mfma_f32_16x16x32_bf16 v[76:79], a[108:111], a[28:31], v[76:79]\n"
"v_mfma_f32_16x16x32_bf16 v[80:83], a[104:107], a[32:35], v[80:83]\n"
"v_mfma_f32_16x16x32_bf16 v[80:83], a[108:111], a[36:39], v[80:83]\n"
"v_mfma_f32_16x16x32_bf16 v[84:87], a[104:107], a[40:43], v[84:87]\n"
"v_mfma_f32_16x16x32_bf16 v[84:87], a[108:111], a[44:47], v[84:87]\n"
"v_mfma_f32_16x16x32_bf16 v[88:91], a[104:107], a[48:51], v[88:91]\n"
"v_mfma_f32_16x16x32_bf16 v[88:91], a[108:111], a[52:55], v[88:91]\n"
"v_mfma_f32_16x16x32_bf16 v[92:95], a[104:107], a[56:59], v[92:95]\n"
"buffer_load_dwordx4 a[104:107], v240, s[36:39], s80 offen offset:2048 sc1\n"
"v_mfma_f32_16x16x32_bf16 v[92:95], a[108:111], a[60:63], v[92:95]\n"
"s_waitcnt vmcnt(21)\n"
"v_mfma_f32_16x16x32_bf16 v[96:99], v[224:227], a[0:3], v[96:99]\n"
"v_mfma_f32_16x16x32_bf16 v[96:99], v[228:231], a[4:7], v[96:99]\n"
"v_mov_b32 v252, s33\n"
"v_mfma_f32_16x16x32_bf16 v[100:103], v[224:227], a[8:11], v[100:103]\n"
"v_mfma_f32_16x16x32_bf16 v[100:103], v[228:231], a[12:15], v[100:103]\n"
"v_mfma_f32_16x16x32_bf16 v[104:107], v[224:227], a[16:19], v[104:107]\n"
"v_mfma_f32_16x16x32_bf16 v[104:107], v[228:231], a[20:23], v[104:107]\n"
"v_mfma_f32_16x16x32_bf16 v[108:111], v[224:227], a[24:27], v[108:111]\n"
"buffer_load_dwordx4 a[108:111], v240, s[36:39], s80 offen offset:3072 sc1\n"
"v_mfma_f32_16x16x32_bf16 v[108:111], v[228:231], a[28:31], v[108:111]\n"
"s_add_u32 s80, s80, 0x1000\n"
"v_mfma_f32_16x16x32_bf16 v[112:115], v[224:227], a[32:35], v[112:115]\n"
"v_mfma_f32_16x16x32_bf16 v[112:115], v[228:231], a[36:39], v[112:115]\n"
"v_mfma_f32_16x16x32_bf16 v[116:119], v[224:227], a[40:43], v[116:119]\n"
"v_mfma_f32_16x16x32_bf16 v[116:119], v[228:231], a[44:47], v[116:119]\n"
"v_mfma_f32_16x16x32_bf16 v[120:123], v[224:227], a[48:51], v[120:123]\n"
"v_mfma_f32_16x16x32_bf16 v[120:123], v[228:231], a[52:55], v[120:123]\n"
"v_mfma_f32_16x16x32_bf16 v[124:127], v[224:227], a[56:59], v[124:127]\n"
"buffer_load_dwordx4 v[224:227], v240, s[36:39], s80 offen offset:0 sc1\n"
"v_mfma_f32_16x16x32_bf16 v[124:127], v[228:231], a[60:63], v[124:127]\n"
"s_waitcnt vmcnt(19)\n"
"v_mfma_f32_16x16x32_bf16 v[128:131], a[64:67], a[0:3], v[128:131]\n"
"v_mfma_f32_16x16x32_bf16 v[128:131], a[68:71], a[4:7], v[128:131]\n"
"v_mfma_f32_16x16x32_bf16 v[132:135], a[64:67], a[8:11], v[132:135]\n"
"v_mfma_f32_16x16x32_bf16 v[132:135], a[68:71], a[12:15], v[132:135]\n"
"v_mfma_f32_16x16x32_bf16 v[136:139], a[64:67], a[16:19], v[136:139]\n"
"v_mfma_f32_16x16x32_bf16 v[136:139], a[68:71], a[20:23], v[136:139]\n"
"v_mfma_f32_16x16x32_bf16 v[140:143], a[64:67], a[24:27], v[140:143]\n"
"buffer_load_dwordx4 v[228:231], v240, s[36:39], s80 offen offset:1024 sc1\n"
"v_mfma_f32_16x16x32_bf16 v[140:143], a[68:71], a[28:31], v[140:143]\n"
"v_mfma_f32_16x16x32_bf16 v[144:147], a[64:67], a[32:35], v[144:147]\n"
"v_mfma_f32_16x16x32_bf16 v[144:147], a[68:71], a[36:39], v[144:147]\n"
"v_mfma_f32_16x16x32_bf16 v[148:151], a[64:67], a[40:43], v[148:151]\n"
"v_mfma_f32_16x16x32_bf16 v[148:151], a[68:71], a[44:47], v[148:151]\n"
"v_mfma_f32_16x16x32_bf16 v[152:155], a[64:67], a[48:51], v[152:155]\n"
"v_mfma_f32_16x16x32_bf16 v[152:155], a[68:71], a[52:55], v[152:155]\n"
"v_mfma_f32_16x16x32_bf16 v[156:159], a[64:67], a[56:59], v[156:159]\n"
"buffer_load_dwordx4 a[64:67], v240, s[36:39], s80 offen offset:2048 sc1\n"
"v_mfma_f32_16x16x32_bf16 v[156:159], a[68:71], a[60:63], v[156:159]\n"
"s_waitcnt vmcnt(18)\n"
"v_mfma_f32_16x16x32_bf16 v[160:163], a[72:75], a[0:3], v[160:163]\n"
"v_mfma_f32_16x16x32_bf16 v[160:163], a[76:79], a[4:7], v[160:163]\n"
"v_mfma_f32_16x16x32_bf16 v[164:167], a[72:75], a[8:11], v[164:167]\n"
"v_mfma_f32_16x16x32_bf16 v[164:167], a[76:79], a[12:15], v[164:167]\n"
"v_mfma_f32_16x16x32_bf16 v[168:171], a[72:75], a[16:19], v[168:171]\n"
"v_mfma_f32_16x16x32_bf16 v[168:171], a[76:79], a[20:23], v[168:171]\n"
"v_mfma_f32_16x16x32_bf16 v[172:175], a[72:75], a[24:27], v[172:175]\n"
"buffer_load_dwordx4 a[68:71], v240, s[36:39], s80 offen offset:3072 sc1\n"
"v_mfma_f32_16x16x32_bf16 v[172:175], a[76:79], a[28:31], v[172:175]\n"
"s_add_u32 s80, s80, 0x1000\n"
"v_mfma_f32_16x16x32_bf16 v[176:179], a[72:75], a[32:35], v[176:179]\n"
"v_mfma_f32_16x16x32_bf16 v[176:179], a[76:79], a[36:39], v[176:179]\n"
"v_mfma_f32_16x16x32_bf16 v[180:183], a[72:75], a[40:43], v[180:183]\n"
"v_mfma_f32_16x16x32_bf16 v[180:183], a[76:79], a[44:47], v[180:183]\n"
"v_mfma_f32_16x16x32_bf16 v[184:187], a[72:75], a[48:51], v[184:187]\n"
"v_mfma_f32_16x16x32_bf16 v[184:187], a[76:79], a[52:55], v[184:187]\n"
"v_mfma_f32_16x16x32_bf16 v[188:191], a[72:75], a[56:59], v[188:191]\n"
"buffer_load_dwordx4 a[72:75], v240, s[36:39], s80 offen offset:0 sc1\n"
"v_mfma_f32_16x16x32_bf16 v[188:191], a[76:79], a[60:63], v[188:191]\n"
"s_waitcnt vmcnt(15)\n"
"v_mfma_f32_16x16x32_bf16 v[192:195], a[80:83], a[0:3], v[192:195]\n"
"v_mfma_f32_16x16x32_bf16 v[192:195], a[84:87], a[4:7], v[192:195]\n"
"v_mfma_f32_16x16x32_bf16 v[196:199], a[80:83], a[8:11], v[196:199]\n"
"v_mfma_f32_16x16x32_bf16 v[196:199], a[84:87], a[12:15], v[196:199]\n"
"v_mfma_f32_16x16x32_bf16 v[200:203], a[80:83], a[16:19], v[200:203]\n"
"v_mfma_f32_16x16x32_bf16 v[200:203], a[84:87], a[20:23], v[200:203]\n"
"v_mfma_f32_16x16x32_bf16 v[204:207], a[80:83], a[24:27], v[204:207]\n"
"buffer_load_dwordx4 a[76:79], v240, s[36:39], s80 offen offset:1024 sc1\n"
"v_mfma_f32_16x16x32_bf16 v[204:207], a[84:87], a[28:31], v[204:207]\n"
"v_mfma_f32_16x16x32_bf16 v[208:211], a[80:83], a[32:35], v[208:211]\n"
"v_mfma_f32_16x16x32_bf16 v[208:211], a[84:87], a[36:39], v[208:211]\n"
"v_mfma_f32_16x16x32_bf16 v[212:215], a[80:83], a[40:43], v[212:215]\n"
"v_mfma_f32_16x16x32_bf16 v[212:215], a[84:87], a[44:47], v[212:215]\n"
"v_mfma_f32_16x16x32_bf16 v[216:219], a[80:83], a[48:51], v[216:219]\n"
"v_mfma_f32_16x16x32_bf16 v[216:219], a[84:87], a[52:55], v[216:219]\n"
"s_waitcnt vmcnt(9)\n"
"s_barrier\n"
"v_mfma_f32_16x16x32_bf16 v[220:223], a[80:83], a[56:59], v[220:223]\n"
"buffer_load_dwordx4 a[80:83], v240, s[36:39], s80 offen offset:2048 sc1\n"
"v_mfma_f32_16x16x32_bf16 v[220:223], a[84:87], a[60:63], v[220:223]\n"
"v_mfma_f32_16x16x32_bf16 a[112:115], a[88:91], a[0:3], a[112:115]\n"
"ds_read_b128 a[0:3], v245 offset:0\n"
"v_mfma_f32_16x16x32_bf16 a[112:115], a[92:95], a[4:7], a[112:115]\n"
"ds_read_b128 a[4:7], v246 offset:0\n"
"v_mfma_f32_16x16x32_bf16 a[116:119], a[88:91], a[8:11], a[116:119]\n"
"ds_read_b128 a[8:11], v245 offset:2048\n"
"v_mfma_f32_16x16x32_bf16 a[116:119], a[92:95], a[12:15], a[116:119]\n"
"ds_read_b128 a[12:15], v246 offset:2048\n"
"v_mfma_f32_16x16x32_bf16 a[120:123], a[88:91], a[16:19], a[120:123]\n"
"ds_read_b128 a[16:19], v245 offset:4096\n"
"v_mfma_f32_16x16x32_bf16 a[120:123], a[92:95], a[20:23], a[120:123]\n"
"ds_read_b128 a[20:23], v246 offset:4096\n"
"v_mfma_f32_16x16x32_bf16 a[124:127], a[88:91], a[24:27], a[124:127]\n"
"ds_read_b128 a[24:27], v245 offset:6144\n"
"buffer_load_dwordx4 a[84:87], v240, s[36:39], s80 offen offset:3072 sc1\n"
"v_mfma_f32_16x16x32_bf16 a[124:127], a[92:95], a[28:31], a[124:127]\n"
"s_add_u32 s80, s80, 0x1000\n"
"ds_read_b128 a[28:31], v246 offset:6144\n"
"v_mfma_f32_16x16x32_bf16 a[128:131], a[88:91], a[32:35], a[128:131]\n"
"ds_read_b128 a[32:35], v245 offset:8192\n"
"v_mfma_f32_16x16x32_bf16 a[128:131], a[92:95], a[36:39], a[128:131]\n"
"ds_read_b128 a[36:39], v246 offset:8192\n"
"v_mfma_f32_16x16x32_bf16 a[132:135], a[88:91], a[40:43], a[132:135]\n"
"ds_read_b128 a[40:43], v245 offset:10240\n"
"v_mfma_f32_16x16x32_bf16 a[132:135], a[92:95], a[44:47], a[132:135]\n"
"ds_read_b128 a[44:47], v246 offset:10240\n"
"v_mfma_f32_16x16x32_bf16 a[136:139], a[88:91], a[48:51], a[136:139]\n"
"ds_read_b128 a[48:51], v245 offset:12288\n"
"v_mfma_f32_16x16x32_bf16 a[136:139], a[92:95], a[52:55], a[136:139]\n"
"ds_read_b128 a[52:55], v246 offset:12288\n"
"v_mfma_f32_16x16x32_bf16 a[140:143], a[88:91], a[56:59], a[140:143]\n"
"ds_read_b128 a[56:59], v245 offset:14336\n"
"buffer_load_dwordx4 a[88:91], v240, s[36:39], s80 offen offset:0 sc1\n"
"v_mfma_f32_16x16x32_bf16 a[140:143], a[92:95], a[60:63], a[140:143]\n"
"s_waitcnt vmcnt(11) lgkmcnt(11)\n"
"ds_read_b128 a[60:63], v246 offset:14336\n"
"v_mfma_f32_16x16x32_bf16 v[0:3], a[96:99], a[0:3], v[0:3]\n"
"v_mfma_f32_16x16x32_bf16 v[0:3], a[100:103], a[4:7], v[0:3]\n"
"v_mfma_f32_16x16x32_bf16 v[4:7], a[96:99], a[8:11], v[4:7]\n"
"v_mfma_f32_16x16x32_bf16 v[4:7], a[100:103], a[12:15], v[4:7]\n"
"s_waitcnt lgkmcnt(8)\n"
"v_mfma_f32_16x16x32_bf16 v[8:11], a[96:99], a[16:19], v[8:11]\n"
"s_add_u32 m0, s83, 81280\n"
"v_mfma_f32_16x16x32_bf16 v[8:11], a[100:103], a[20:23], v[8:11]\n"
"buffer_load_dwordx4 v242, s[40:43], s68 offen offset:640 nt lds\n"
"v_mfma_f32_16x16x32_bf16 v[12:15], a[96:99], a[24:27], v[12:15]\n"
"buffer_load_dwordx4 a[92:95], v240, s[36:39], s80 offen offset:1024 sc1\n"
"v_mfma_f32_16x16x32_bf16 v[12:15], a[100:103], a[28:31], v[12:15]\n"
"s_waitcnt lgkmcnt(4)\n"
"v_mfma_f32_16x16x32_bf16 v[16:19], a[96:99], a[32:35], v[16:19]\n"
"v_mfma_f32_16x16x32_bf16 v[16:19], a[100:103], a[36:39], v[16:19]\n"
"v_mfma_f32_16x16x32_bf16 v[20:23], a[96:99], a[40:43], v[20:23]\n"
"v_mfma_f32_16x16x32_bf16 v[20:23], a[100:103], a[44:47], v[20:23]\n"
"s_waitcnt lgkmcnt(0)\n"
"v_mfma_f32_16x16x32_bf16 v[24:27], a[96:99], a[48:51], v[24:27]\n"
"v_mfma_f32_16x16x32_bf16 v[24:27], a[100:103], a[52:55], v[24:27]\n"
"v_mfma_f32_16x16x32_bf16 v[28:31], a[96:99], a[56:59], v[28:31]\n"
"buffer_load_dwordx4 a[96:99], v240, s[36:39], s80 offen offset:2048 sc1\n"
"v_mfma_f32_16x16x32_bf16 v[28:31], a[100:103], a[60:63], v[28:31]\n"
"s_waitcnt vmcnt(12)\n"
"v_mfma_f32_16x16x32_bf16 v[32:35], a[104:107], a[0:3], v[32:35]\n"
"v_mfma_f32_16x16x32_bf16 v[32:35], a[108:111], a[4:7], v[32:35]\n"
"s_add_u32 m0, s83, 146816\n"
"v_mfma_f32_16x16x32_bf16 v[36:39], a[104:107], a[8:11], v[36:39]\n"
"buffer_load_dwordx4 v242, s[44:47], s68 offen offset:640 nt lds\n"
"v_mfma_f32_16x16x32_bf16 v[36:39], a[108:111], a[12:15], v[36:39]\n"
"v_mfma_f32_16x16x32_bf16 v[40:43], a[104:107], a[16:19], v[40:43]\n"
"v_mfma_f32_16x16x32_bf16 v[40:43], a[108:111], a[20:23], v[40:43]\n"
"v_mfma_f32_16x16x32_bf16 v[44:47], a[104:107], a[24:27], v[44:47]\n"
"buffer_load_dwordx4 a[100:103], v240, s[36:39], s80 offen offset:3072 sc1\n"
"v_mfma_f32_16x16x32_bf16 v[44:47], a[108:111], a[28:31], v[44:47]\n"
"s_add_u32 s80, s80, 0x1000\n"
"v_mfma_f32_16x16x32_bf16 v[48:51], a[104:107], a[32:35], v[48:51]\n"
"v_mfma_f32_16x16x32_bf16 v[48:51], a[108:111], a[36:39], v[48:51]\n"
"v_mfma_f32_16x16x32_bf16 v[52:55], a[104:107], a[40:43], v[52:55]\n"
"v_mfma_f32_16x16x32_bf16 v[52:55], a[108:111], a[44:47], v[52:55]\n"
"v_mfma_f32_16x16x32_bf16 v[56:59], a[104:107], a[48:51], v[56:59]\n"
"v_mfma_f32_16x16x32_bf16 v[56:59], a[108:111], a[52:55], v[56:59]\n"
"v_mfma_f32_16x16x32_bf16 v[60:63], a[104:107], a[56:59], v[60:63]\n"
"buffer_load_dwordx4 a[104:107], v240, s[36:39], s80 offen offset:0 sc1\n"
"v_mfma_f32_16x16x32_bf16 v[60:63], a[108:111], a[60:63], v[60:63]\n"
"s_waitcnt vmcnt(13)\n"
"v_mfma_f32_16x16x32_bf16 v[64:67], v[224:227], a[0:3], v[64:67]\n"
"s_add_u32 m0, s83, 83328\n"
"v_mfma_f32_16x16x32_bf16 v[64:67], v[228:231], a[4:7], v[64:67]\n"
"buffer_load_dwordx4 v242, s[40:43], s69 offen offset:640 nt lds\n"
"v_mfma_f32_16x16x32_bf16 v[68:71], v[224:227], a[8:11], v[68:71]\n"
"v_mfma_f32_16x16x32_bf16 v[68:71], v[228:231], a[12:15], v[68:71]\n"
"v_mfma_f32_16x16x32_bf16 v[72:75], v[224:227], a[16:19], v[72:75]\n"
"v_mfma_f32_16x16x32_bf16 v[72:75], v[228:231], a[20:23], v[72:75]\n"
"v_mfma_f32_16x16x32_bf16 v[76:79], v[224:227], a[24:27], v[76:79]\n"
"buffer_load_dwordx4 a[108:111], v240, s[36:39], s80 offen offset:1024 sc1\n"
"v_mfma_f32_16x16x32_bf16 v[76:79], v[228:231], a[28:31], v[76:79]\n"
"v_mfma_f32_16x16x32_bf16 v[80:83], v[224:227], a[32:35], v[80:83]\n"
"v_mfma_f32_16x16x32_bf16 v[80:83], v[228:231], a[36:39], v[80:83]\n"
"v_mfma_f32_16x16x32_bf16 v[84:87], v[224:227], a[40:43], v[84:87]\n"
"v_mfma_f32_16x16x32_bf16 v[84:87], v[228:231], a[44:47], v[84:87]\n"
"v_mfma_f32_16x16x32_bf16 v[88:91], v[224:227], a[48:51], v[88:91]\n"
"v_mfma_f32_16x16x32_bf16 v[88:91], v[228:231], a[52:55], v[88:91]\n"
"v_mfma_f32_16x16x32_bf16 v[92:95], v[224:227], a[56:59], v[92:95]\n"
"s_add_u32 m0, s83, 148864\n"
"buffer_load_dwordx4 v[224:227], v240, s[36:39], s80 offen offset:2048 sc1\n"
"v_mfma_f32_16x16x32_bf16 v[92:95], v[228:231], a[60:63], v[92:95]\n"
"s_waitcnt vmcnt(14)\n"
"buffer_load_dwordx4 v242, s[44:47], s69 offen offset:640 nt lds\n"
"v_mfma_f32_16x16x32_bf16 v[96:99], a[64:67], a[0:3], v[96:99]\n"
"ds_read_b128 a[0:3], v247 offset:0\n"
"v_mfma_f32_16x16x32_bf16 v[96:99], a[68:71], a[4:7], v[96:99]\n"
"ds_read_b128 a[4:7], v248 offset:0\n"
"v_mfma_f32_16x16x32_bf16 v[100:103], a[64:67], a[8:11], v[100:103]\n"
"ds_read_b128 a[8:11], v247 offset:2048\n"
"v_mfma_f32_16x16x32_bf16 v[100:103], a[68:71], a[12:15], v[100:103]\n"
"ds_read_b128 a[12:15], v248 offset:2048\n"
"v_mfma_f32_16x16x32_bf16 v[104:107], a[64:67], a[16:19], v[104:107]\n"
"ds_read_b128 a[16:19], v247 offset:4096\n"
"v_mfma_f32_16x16x32_bf16 v[104:107], a[68:71], a[20:23], v[104:107]\n"
"ds_read_b128 a[20:23], v248 offset:4096\n"
"v_mfma_f32_16x16x32_bf16 v[108:111], a[64:67], a[24:27], v[108:111]\n"
"ds_read_b128 a[24:27], v247 offset:6144\n"
"buffer_load_dwordx4 v[228:231], v240, s[36:39], s80 offen offset:3072 sc1\n"
"v_mfma_f32_16x16x32_bf16 v[108:111], a[68:71], a[28:31], v[108:111]\n"
"s_add_u32 s80, s80, 0x1000\n"
"ds_read_b128 a[28:31], v248 offset:6144\n"
"v_mfma_f32_16x16x32_bf16 v[112:115], a[64:67], a[32:35], v[112:115]\n"
"ds_read_b128 a[32:35], v247 offset:8192\n"
"v_mfma_f32_16x16x32_bf16 v[112:115], a[68:71], a[36:39], v[112:115]\n"
"ds_read_b128 a[36:39], v248 offset:8192\n"
"v_mfma_f32_16x16x32_bf16 v[116:119], a[64:67], a[40:43], v[116:119]\n"
"ds_read_b128 a[40:43], v247 offset:10240\n"
"v_mfma_f32_16x16x32_bf16 v[116:119], a[68:71], a[44:47], v[116:119]\n"
"ds_read_b128 a[44:47], v248 offset:10240\n"
"v_mfma_f32_16x16x32_bf16 v[120:123], a[64:67], a[48:51], v[120:123]\n"
"ds_read_b128 a[48:51], v247 offset:12288\n"
"v_mfma_f32_16x16x32_bf16 v[120:123], a[68:71], a[52:55], v[120:123]\n"
"ds_read_b128 a[52:55], v248 offset:12288\n"
"v_mfma_f32_16x16x32_bf16 v[124:127], a[64:67], a[56:59], v[124:127]\n"
"ds_read_b128 a[56:59], v247 offset:14336\n"
"buffer_load_dwordx4 a[64:67], v240, s[36:39], s80 offen offset:0 sc1\n"
"v_mfma_f32_16x16x32_bf16 v[124:127], a[68:71], a[60:63], v[124:127]\n"
"s_waitcnt vmcnt(15) lgkmcnt(11)\n"
"ds_read_b128 a[60:63], v248 offset:14336\n"
"v_mfma_f32_16x16x32_bf16 v[0:3], a[72:75], a[0:3], v[0:3]\n"
"s_add_u32 m0, s83, 85376\n"
"v_mfma_f32_16x16x32_bf16 v[0:3], a[76:79], a[4:7], v[0:3]\n"
"buffer_load_dwordx4 v242, s[40:43], s70 offen offset:640 nt lds\n"
"v_mfma_f32_16x16x32_bf16 v[4:7], a[72:75], a[8:11], v[4:7]\n"
"v_mfma_f32_16x16x32_bf16 v[4:7], a[76:79], a[12:15], v[4:7]\n"
"s_waitcnt lgkmcnt(8)\n"
"v_mfma_f32_16x16x32_bf16 v[8:11], a[72:75], a[16:19], v[8:11]\n"
"v_mfma_f32_16x16x32_bf16 v[8:11], a[76:79], a[20:23], v[8:11]\n"
"v_mfma_f32_16x16x32_bf16 v[12:15], a[72:75], a[24:27], v[12:15]\n"
"buffer_load_dwordx4 a[68:71], v240, s[36:39], s80 offen offset:1024 sc1\n"
"v_mfma_f32_16x16x32_bf16 v[12:15], a[76:79], a[28:31], v[12:15]\n"
"s_waitcnt lgkmcnt(4)\n"
"v_mfma_f32_16x16x32_bf16 v[16:19], a[72:75], a[32:35], v[16:19]\n"
"v_mfma_f32_16x16x32_bf16 v[16:19], a[76:79], a[36:39], v[16:19]\n"
"s_add_u32 m0, s83, 150912\n"
"v_mfma_f32_16x16x32_bf16 v[20:23], a[72:75], a[40:43], v[20:23]\n"
"buffer_load_dwordx4 v242, s[44:47], s70 offen offset:640 nt lds\n"
"v_mfma_f32_16x16x32_bf16 v[20:23], a[76:79], a[44:47], v[20:23]\n"
"s_waitcnt lgkmcnt(0)\n"
"v_mfma_f32_16x16x32_bf16 v[24:27], a[72:75], a[48:51], v[24:27]\n"
"v_mfma_f32_16x16x32_bf16 v[24:27], a[76:79], a[52:55], v[24:27]\n"
"v_mfma_f32_16x16x32_bf16 v[28:31], a[72:75], a[56:59], v[28:31]\n"
"buffer_load_dwordx4 a[72:75], v240, s[36:39], s80 offen offset:2048 sc1\n"
"v_mfma_f32_16x16x32_bf16 v[28:31], a[76:79], a[60:63], v[28:31]\n"
"s_waitcnt vmcnt(17)\n"
"v_mfma_f32_16x16x32_bf16 v[32:35], a[80:83], a[0:3], v[32:35]\n"
"v_mfma_f32_16x16x32_bf16 v[32:35], a[84:87], a[4:7], v[32:35]\n"
"v_mfma_f32_16x16x32_bf16 v[36:39], a[80:83], a[8:11], v[36:39]\n"
"v_mfma_f32_16x16x32_bf16 v[36:39], a[84:87], a[12:15], v[36:39]\n"
"v_mfma_f32_16x16x32_bf16 v[40:43], a[80:83], a[16:19], v[40:43]\n"
"v_mfma_f32_16x16x32_bf16 v[40:43], a[84:87], a[20:23], v[40:43]\n"
"v_mfma_f32_16x16x32_bf16 v[44:47], a[80:83], a[24:27], v[44:47]\n"
"buffer_load_dwordx4 a[76:79], v240, s[36:39], s80 offen offset:3072 sc1\n"
"v_mfma_f32_16x16x32_bf16 v[44:47], a[84:87], a[28:31], v[44:47]\n"
"s_add_u32 s80, s80, 0x1000\n"
"v_mfma_f32_16x16x32_bf16 v[48:51], a[80:83], a[32:35], v[48:51]\n"
"s_add_u32 m0, s83, 87424\n"
"v_mfma_f32_16x16x32_bf16 v[48:51], a[84:87], a[36:39], v[48:51]\n"
"buffer_load_dwordx4 v242, s[40:43], s71 offen offset:640 nt lds\n"
"v_mfma_f32_16x16x32_bf16 v[52:55], a[80:83], a[40:43], v[52:55]\n"
"v_mfma_f32_16x16x32_bf16 v[52:55], a[84:87], a[44:47], v[52:55]\n"
"v_mfma_f32_16x16x32_bf16 v[56:59], a[80:83], a[48:51], v[56:59]\n"
"v_mfma_f32_16x16x32_bf16 v[56:59], a[84:87], a[52:55], v[56:59]\n"
"v_mfma_f32_16x16x32_bf16 v[60:63], a[80:83], a[56:59], v[60:63]\n"
"buffer_load_dwordx4 a[80:83], v240, s[36:39], s80 offen offset:0 sc1\n"
"v_mfma_f32_16x16x32_bf16 v[60:63], a[84:87], a[60:63], v[60:63]\n"
"s_waitcnt vmcnt(17)\n"
"v_mfma_f32_16x16x32_bf16 v[64:67], a[88:91], a[0:3], v[64:67]\n"
"v_mfma_f32_16x16x32_bf16 v[64:67], a[92:95], a[4:7], v[64:67]\n"
"v_mfma_f32_16x16x32_bf16 v[68:71], a[88:91], a[8:11], v[68:71]\n"
"v_mfma_f32_16x16x32_bf16 v[68:71], a[92:95], a[12:15], v[68:71]\n"
"v_mfma_f32_16x16x32_bf16 v[72:75], a[88:91], a[16:19], v[72:75]\n"
"v_mfma_f32_16x16x32_bf16 v[72:75], a[92:95], a[20:23], v[72:75]\n"
"v_mfma_f32_16x16x32_bf16 v[76:79], a[88:91], a[24:27], v[76:79]\n"
"s_add_u32 m0, s83, 152960\n"
"buffer_load_dwordx4 a[84:87], v240, s[36:39], s80 offen offset:1024 sc1\n"
"v_mfma_f32_16x16x32_bf16 v[76:79], a[92:95], a[28:31], v[76:79]\n"
"buffer_load_dwordx4 v242, s[44:47], s71 offen offset:640 nt lds\n"
"v_mfma_f32_16x16x32_bf16 v[80:83], a[88:91], a[32:35], v[80:83]\n"
"v_mfma_f32_16x16x32_bf16 v[80:83], a[92:95], a[36:39], v[80:83]\n"
"v_mfma_f32_16x16x32_bf16 v[84:87], a[88:91], a[40:43], v[84:87]\n"
"v_mfma_f32_16x16x32_bf16 v[84:87], a[92:95], a[44:47], v[84:87]\n"
"v_mfma_f32_16x16x32_bf16 v[88:91], a[88:91], a[48:51], v[88:91]\n"
"v_mfma_f32_16x16x32_bf16 v[88:91], a[92:95], a[52:55], v[88:91]\n"
"v_mfma_f32_16x16x32_bf16 v[92:95], a[88:91], a[56:59], v[92:95]\n"
"buffer_load_dwordx4 a[88:91], v240, s[36:39], s80 offen offset:2048 sc1\n"
"v_mfma_f32_16x16x32_bf16 v[92:95], a[92:95], a[60:63], v[92:95]\n"
"s_waitcnt vmcnt(17)\n"
"v_mfma_f32_16x16x32_bf16 v[96:99], a[96:99], a[0:3], v[96:99]\n"
"v_mfma_f32_16x16x32_bf16 v[96:99], a[100:103], a[4:7], v[96:99]\n"
"v_mfma_f32_16x16x32_bf16 v[100:103], a[96:99], a[8:11], v[100:103]\n"
"v_mfma_f32_16x16x32_bf16 v[100:103], a[100:103], a[12:15], v[100:103]\n"
"v_mfma_f32_16x16x32_bf16 v[104:107], a[96:99], a[16:19], v[104:107]\n"
"v_mfma_f32_16x16x32_bf16 v[104:107], a[100:103], a[20:23], v[104:107]\n"
"v_mfma_f32_16x16x32_bf16 v[108:111], a[96:99], a[24:27], v[108:111]\n"
"buffer_load_dwordx4 a[92:95], v240, s[36:39], s80 offen offset:3072 sc1\n"
"v_mfma_f32_16x16x32_bf16 v[108:111], a[100:103], a[28:31], v[108:111]\n"
"s_add_u32 s80, s80, 0x1000\n"
"v_mfma_f32_16x16x32_bf16 v[112:115], a[96:99], a[32:35], v[112:115]\n"
"v_mfma_f32_16x16x32_bf16 v[112:115], a[100:103], a[36:39], v[112:115]\n"
"v_mfma_f32_16x16x32_bf16 v[116:119], a[96:99], a[40:43], v[116:119]\n"
"v_mfma_f32_16x16x32_bf16 v[116:119], a[100:103], a[44:47], v[116:119]\n"
"v_mfma_f32_16x16x32_bf16 v[120:123], a[96:99], a[48:51], v[120:123]\n"
"v_mfma_f32_16x16x32_bf16 v[120:123], a[100:103], a[52:55], v[120:123]\n"
"v_mfma_f32_16x16x32_bf16 v[124:127], a[96:99], a[56:59], v[124:127]\n"
"buffer_load_dwordx4 a[96:99], v240, s[36:39], s80 offen offset:0 sc1\n"
"v_mfma_f32_16x16x32_bf16 v[124:127], a[100:103], a[60:63], v[124:127]\n"
"s_waitcnt vmcnt(16)\n"
"v_mfma_f32_16x16x32_bf16 v[128:131], a[104:107], a[0:3], v[128:131]\n"
"v_mfma_f32_16x16x32_bf16 v[128:131], a[108:111], a[4:7], v[128:131]\n"
"v_mfma_f32_16x16x32_bf16 v[132:135], a[104:107], a[8:11], v[132:135]\n"
"v_mfma_f32_16x16x32_bf16 v[132:135], a[108:111], a[12:15], v[132:135]\n"
"v_mfma_f32_16x16x32_bf16 v[136:139], a[104:107], a[16:19], v[136:139]\n"
"v_mfma_f32_16x16x32_bf16 v[136:139], a[108:111], a[20:23], v[136:139]\n"
"ds_read_b64_tr_b16 v[232:233], v253 offset:8192\n"
"v_mfma_f32_16x16x32_bf16 v[140:143], a[104:107], a[24:27], v[140:143]\n"
"ds_read_b64_tr_b16 v[234:235], v253 offset:9216\n"
"buffer_load_dwordx4 a[100:103], v240, s[36:39], s80 offen offset:1024 sc1\n"
"v_mfma_f32_16x16x32_bf16 v[140:143], a[108:111], a[28:31], v[140:143]\n"
"ds_read_b64_tr_b16 v[236:237], v253 offset:8200\n"
"v_mfma_f32_16x16x32_bf16 v[144:147], a[104:107], a[32:35], v[144:147]\n"
"ds_read_b64_tr_b16 v[238:239], v253 offset:9224\n"
"v_mfma_f32_16x16x32_bf16 v[144:147], a[108:111], a[36:39], v[144:147]\n"
"v_mfma_f32_16x16x32_bf16 v[148:151], a[104:107], a[40:43], v[148:151]\n"
"v_mfma_f32_16x16x32_bf16 v[148:151], a[108:111], a[44:47], v[148:151]\n"
"v_mfma_f32_16x16x32_bf16 v[152:155], a[104:107], a[48:51], v[152:155]\n"
"v_mfma_f32_16x16x32_bf16 v[152:155], a[108:111], a[52:55], v[152:155]\n"
"v_mfma_f32_16x16x32_bf16 v[156:159], a[104:107], a[56:59], v[156:159]\n"
"buffer_load_dwordx4 a[104:107], v240, s[36:39], s80 offen offset:2048 sc1\n"
"v_mfma_f32_16x16x32_bf16 v[156:159], a[108:111], a[60:63], v[156:159]\n"
"s_waitcnt vmcnt(15)\n"
"v_mfma_f32_16x16x32_bf16 v[160:163], v[224:227], a[0:3], v[160:163]\n"
"v_mfma_f32_16x16x32_bf16 v[160:163], v[228:231], a[4:7], v[160:163]\n"
"v_mfma_f32_16x16x32_bf16 v[164:167], v[224:227], a[8:11], v[164:167]\n"
"v_mfma_f32_16x16x32_bf16 v[164:167], v[228:231], a[12:15], v[164:167]\n"
"v_mfma_f32_16x16x32_bf16 v[168:171], v[224:227], a[16:19], v[168:171]\n"
"v_mfma_f32_16x16x32_bf16 v[168:171], v[228:231], a[20:23], v[168:171]\n"
"v_mfma_f32_16x16x32_bf16 v[172:175], v[224:227], a[24:27], v[172:175]\n"
"buffer_load_dwordx4 a[108:111], v240, s[36:39], s80 offen offset:3072 sc1\n"
"v_mfma_f32_16x16x32_bf16 v[172:175], v[228:231], a[28:31], v[172:175]\n"
"s_add_u32 s80, s80, 0x1000\n"
"v_mfma_f32_16x16x32_bf16 v[176:179], v[224:227], a[32:35], v[176:179]\n"
"v_mfma_f32_16x16x32_bf16 v[176:179], v[228:231], a[36:39], v[176:179]\n"
"v_mfma_f32_16x16x32_bf16 v[180:183], v[224:227], a[40:43], v[180:183]\n"
"v_mfma_f32_16x16x32_bf16 v[180:183], v[228:231], a[44:47], v[180:183]\n"
"v_mfma_f32_16x16x32_bf16 v[184:187], v[224:227], a[48:51], v[184:187]\n"
"v_mfma_f32_16x16x32_bf16 v[184:187], v[228:231], a[52:55], v[184:187]\n"
"v_mfma_f32_16x16x32_bf16 v[188:191], v[224:227], a[56:59], v[188:191]\n"
"buffer_load_dwordx4 v[224:227], v240, s[36:39], s80 offen offset:0 sc1\n"
"v_mfma_f32_16x16x32_bf16 v[188:191], v[228:231], a[60:63], v[188:191]\n"
"s_waitcnt vmcnt(14)\n"
"v_mfma_f32_16x16x32_bf16 v[192:195], a[64:67], a[0:3], v[192:195]\n"
"v_mfma_f32_16x16x32_bf16 v[192:195], a[68:71], a[4:7], v[192:195]\n"
"v_mfma_f32_16x16x32_bf16 v[196:199], a[64:67], a[8:11], v[196:199]\n"
"s_waitcnt lgkmcnt(2)\n"
"buffer_store_dwordx4 v[232:235], v240, s[36:39], s20 offen sc1\n"
"v_mfma_f32_16x16x32_bf16 v[196:199], a[68:71], a[12:15], v[196:199]\n"
"v_mfma_f32_16x16x32_bf16 v[200:203], a[64:67], a[16:19], v[200:203]\n"
"v_mfma_f32_16x16x32_bf16 v[200:203], a[68:71], a[20:23], v[200:203]\n"
"v_mfma_f32_16x16x32_bf16 v[204:207], a[64:67], a[24:27], v[204:207]\n"
"buffer_load_dwordx4 v[228:231], v240, s[36:39], s80 offen offset:1024 sc1\n"
"v_mfma_f32_16x16x32_bf16 v[204:207], a[68:71], a[28:31], v[204:207]\n"
"v_mfma_f32_16x16x32_bf16 v[208:211], a[64:67], a[32:35], v[208:211]\n"
"v_mfma_f32_16x16x32_bf16 v[208:211], a[68:71], a[36:39], v[208:211]\n"
"v_mfma_f32_16x16x32_bf16 v[212:215], a[64:67], a[40:43], v[212:215]\n"
"v_mfma_f32_16x16x32_bf16 v[212:215], a[68:71], a[44:47], v[212:215]\n"
"v_mfma_f32_16x16x32_bf16 v[216:219], a[64:67], a[48:51], v[216:219]\n"
"v_mfma_f32_16x16x32_bf16 v[216:219], a[68:71], a[52:55], v[216:219]\n"
"s_waitcnt vmcnt(9)\n"
"s_barrier\n"
"v_mfma_f32_16x16x32_bf16 v[220:223], a[64:67], a[56:59], v[220:223]\n"
"buffer_load_dwordx4 a[64:67], v240, s[36:39], s80 offen offset:2048 sc1\n"
"v_mfma_f32_16x16x32_bf16 v[220:223], a[68:71], a[60:63], v[220:223]\n"
"s_waitcnt lgkmcnt(0)\n"
"buffer_store_dwordx4 v[236:239], v240, s[36:39], s21 offen sc1\n"
"v_mfma_f32_16x16x32_bf16 a[112:115], a[72:75], a[0:3], a[112:115]\n"
"ds_read_b128 a[0:3], v245 offset:16384\n"
"v_mfma_f32_16x16x32_bf16 a[112:115], a[76:79], a[4:7], a[112:115]\n"
"ds_read_b128 a[4:7], v246 offset:16384\n"
"v_mfma_f32_16x16x32_bf16 a[116:119], a[72:75], a[8:11], a[116:119]\n"
"ds_read_b128 a[8:11], v245 offset:18432\n"
"v_mfma_f32_16x16x32_bf16 a[116:119], a[76:79], a[12:15], a[116:119]\n"
"ds_read_b128 a[12:15], v246 offset:18432\n"
"v_mfma_f32_16x16x32_bf16 a[120:123], a[72:75], a[16:19], a[120:123]\n"
"ds_read_b128 a[16:19], v245 offset:20480\n"
"v_mfma_f32_16x16x32_bf16 a[120:123], a[76:79], a[20:23], a[120:123]\n"
"ds_read_b128 a[20:23], v246 offset:20480\n"
"v_mfma_f32_16x16x32_bf16 a[124:127], a[72:75], a[24:27], a[124:127]\n"
"ds_read_b128 a[24:27], v245 offset:22528\n"
"buffer_load_dwordx4 a[68:71], v240, s[36:39], s80 offen offset:3072 sc1\n"
"v_mfma_f32_16x16x32_bf16 a[124:127], a[76:79], a[28:31], a[124:127]\n"
"s_add_u32 s80, s80, 0x1000\n"
"ds_read_b128 a[28:31], v246 offset:22528\n"
"v_mfma_f32_16x16x32_bf16 a[128:131], a[72:75], a[32:35], a[128:131]\n"
"ds_read_b128 a[32:35], v245 offset:24576\n"
"v_mfma_f32_16x16x32_bf16 a[128:131], a[76:79], a[36:39], a[128:131]\n"
"ds_read_b128 a[36:39], v246 offset:24576\n"
"v_mfma_f32_16x16x32_bf16 a[132:135], a[72:75], a[40:43], a[132:135]\n"
"ds_read_b128 a[40:43], v245 offset:26624\n"
"v_mfma_f32_16x16x32_bf16 a[132:135], a[76:79], a[44:47], a[132:135]\n"
"ds_read_b128 a[44:47], v246 offset:26624\n"
"v_mfma_f32_16x16x32_bf16 a[136:139], a[72:75], a[48:51], a[136:139]\n"
"ds_read_b128 a[48:51], v245 offset:28672\n"
"v_mfma_f32_16x16x32_bf16 a[136:139], a[76:79], a[52:55], a[136:139]\n"
"ds_read_b128 a[52:55], v246 offset:28672\n"
"v_mfma_f32_16x16x32_bf16 a[140:143], a[72:75], a[56:59], a[140:143]\n"
"ds_read_b128 a[56:59], v245 offset:30720\n"
"buffer_load_dwordx4 a[72:75], v240, s[36:39], s80 offen offset:0 sc1\n"
"v_mfma_f32_16x16x32_bf16 a[140:143], a[76:79], a[60:63], a[140:143]\n"
"s_waitcnt lgkmcnt(11)\n"
"ds_read_b128 a[60:63], v246 offset:30720\n"
"v_mfma_f32_16x16x32_bf16 v[0:3], a[80:83], a[0:3], v[0:3]\n"
"v_mfma_f32_16x16x32_bf16 v[0:3], a[84:87], a[4:7], v[0:3]\n"
"v_mfma_f32_16x16x32_bf16 v[4:7], a[80:83], a[8:11], v[4:7]\n"
"v_mfma_f32_16x16x32_bf16 v[4:7], a[84:87], a[12:15], v[4:7]\n"
"s_waitcnt lgkmcnt(8)\n"
"v_mfma_f32_16x16x32_bf16 v[8:11], a[80:83], a[16:19], v[8:11]\n"
"s_add_u32 m0, s83, 0\n"
"v_mfma_f32_16x16x32_bf16 v[8:11], a[84:87], a[20:23], v[8:11]\n"
"buffer_load_dwordx4 v242, s[40:43], s68 offen offset:0 nt lds\n"
"v_mfma_f32_16x16x32_bf16 v[12:15], a[80:83], a[24:27], v[12:15]\n"
"ds_read_b128 a[212:215], v250 offset:0\n"
"buffer_load_dwordx4 a[76:79], v240, s[36:39], s80 offen offset:1024 sc1\n"
"v_mfma_f32_16x16x32_bf16 v[12:15], a[84:87], a[28:31], v[12:15]\n"
"s_waitcnt lgkmcnt(5)\n"
"v_mfma_f32_16x16x32_bf16 v[16:19], a[80:83], a[32:35], v[16:19]\n"
"v_mfma_f32_16x16x32_bf16 v[16:19], a[84:87], a[36:39], v[16:19]\n"
"ds_read_b128 a[216:219], v250 offset:2048\n"
"v_mfma_f32_16x16x32_bf16 v[20:23], a[80:83], a[40:43], v[20:23]\n"
"v_mfma_f32_16x16x32_bf16 v[20:23], a[84:87], a[44:47], v[20:23]\n"
"s_waitcnt lgkmcnt(2)\n"
"v_mfma_f32_16x16x32_bf16 v[24:27], a[80:83], a[48:51], v[24:27]\n"
"ds_read_b128 a[220:223], v250 offset:12288\n"
"v_mfma_f32_16x16x32_bf16 v[24:27], a[84:87], a[52:55], v[24:27]\n"
"v_mfma_f32_16x16x32_bf16 v[28:31], a[80:83], a[56:59], v[28:31]\n"
"buffer_load_dwordx4 a[80:83], v240, s[36:39], s80 offen offset:2048 sc1\n"
"v_mfma_f32_16x16x32_bf16 v[28:31], a[84:87], a[60:63], v[28:31]\n"
"s_waitcnt vmcnt(14)\n"
"ds_read_b128 a[208:211], v250 offset:14336\n"
"v_mfma_f32_16x16x32_bf16 v[32:35], a[88:91], a[0:3], v[32:35]\n"
"v_mfma_f32_16x16x32_bf16 v[32:35], a[92:95], a[4:7], v[32:35]\n"
"s_add_u32 m0, s83, 131072\n"
"v_mfma_f32_16x16x32_bf16 v[36:39], a[88:91], a[8:11], v[36:39]\n"
"buffer_load_dwordx4 v242, s[44:47], s68 offen offset:0 nt lds\n"
"v_mfma_f32_16x16x32_bf16 v[36:39], a[92:95], a[12:15], v[36:39]\n"
"v_mfma_f32_16x16x32_bf16 v[40:43], a[88:91], a[16:19], v[40:43]\n"
"v_mfma_f32_16x16x32_bf16 v[40:43], a[92:95], a[20:23], v[40:43]\n"
"v_mfma_f32_16x16x32_bf16 v[44:47], a[88:91], a[24:27], v[44:47]\n"
"buffer_load_dwordx4 a[84:87], v240, s[36:39], s80 offen offset:3072 sc1\n"
"v_mfma_f32_16x16x32_bf16 v[44:47], a[92:95], a[28:31], v[44:47]\n"
"s_add_u32 s80, s80, 0x1000\n"
"v_mfma_f32_16x16x32_bf16 v[48:51], a[88:91], a[32:35], v[48:51]\n"
"v_mfma_f32_16x16x32_bf16 v[48:51], a[92:95], a[36:39], v[48:51]\n"
"v_mfma_f32_16x16x32_bf16 v[52:55], a[88:91], a[40:43], v[52:55]\n"
"v_mfma_f32_16x16x32_bf16 v[52:55], a[92:95], a[44:47], v[52:55]\n"
"v_mfma_f32_16x16x32_bf16 v[56:59], a[88:91], a[48:51], v[56:59]\n"
"v_mfma_f32_16x16x32_bf16 v[56:59], a[92:95], a[52:55], v[56:59]\n"
"v_mfma_f32_16x16x32_bf16 v[60:63], a[88:91], a[56:59], v[60:63]\n"
"buffer_load_dwordx4 a[88:91], v240, s[36:39], s80 offen offset:0 sc1\n"
"v_mfma_f32_16x16x32_bf16 v[60:63], a[92:95], a[60:63], v[60:63]\n"
"s_waitcnt vmcnt(15)\n"
"v_mfma_f32_16x16x32_bf16 v[64:67], a[96:99], a[0:3], v[64:67]\n"
"s_add_u32 m0, s83, 2048\n"
"v_mfma_f32_16x16x32_bf16 v[64:67], a[100:103], a[4:7], v[64:67]\n"
"buffer_load_dwordx4 v242, s[40:43], s69 offen offset:0 nt lds\n"
"v_mfma_f32_16x16x32_bf16 v[68:71], a[96:99], a[8:11], v[68:71]\n"
"v_mfma_f32_16x16x32_bf16 v[68:71], a[100:103], a[12:15], v[68:71]\n"
"v_mfma_f32_16x16x32_bf16 v[72:75], a[96:99], a[16:19], v[72:75]\n"
"v_mfma_f32_16x16x32_bf16 v[72:75], a[100:103], a[20:23], v[72:75]\n"
"v_mfma_f32_16x16x32_bf16 v[76:79], a[96:99], a[24:27], v[76:79]\n"
"buffer_load_dwordx4 a[92:95], v240, s[36:39], s80 offen offset:1024 sc1\n"
"v_mfma_f32_16x16x32_bf16 v[76:79], a[100:103], a[28:31], v[76:79]\n"
"v_mfma_f32_16x16x32_bf16 v[80:83], a[96:99], a[32:35], v[80:83]\n"
"v_mfma_f32_16x16x32_bf16 v[80:83], a[100:103], a[36:39], v[80:83]\n"
"v_mfma_f32_16x16x32_bf16 v[84:87], a[96:99], a[40:43], v[84:87]\n"
"v_mfma_f32_16x16x32_bf16 v[84:87], a[100:103], a[44:47], v[84:87]\n"
"v_mfma_f32_16x16x32_bf16 v[88:91], a[96:99], a[48:51], v[88:91]\n"
"v_mfma_f32_16x16x32_bf16 v[88:91], a[100:103], a[52:55], v[88:91]\n"
"v_mfma_f32_16x16x32_bf16 v[92:95], a[96:99], a[56:59], v[92:95]\n"
"s_add_u32 m0, s83, 133120\n"
"buffer_load_dwordx4 a[96:99], v240, s[36:39], s80 offen offset:2048 sc1\n"
"v_mfma_f32_16x16x32_bf16 v[92:95], a[100:103], a[60:63], v[92:95]\n"
"s_waitcnt vmcnt(16)\n"
"buffer_load_dwordx4 v242, s[44:47], s69 offen offset:0 nt lds\n"
"v_mfma_f32_16x16x32_bf16 v[96:99], a[104:107], a[0:3], v[96:99]\n"
"ds_read_b128 a[0:3], v247 offset:16384\n"
"v_mfma_f32_16x16x32_bf16 v[96:99], a[108:111], a[4:7], v[96:99]\n"
"ds_read_b128 a[4:7], v248 offset:16384\n"
"v_mfma_f32_16x16x32_bf16 v[100:103], a[104:107], a[8:11], v[100:103]\n"
"ds_read_b128 a[8:11], v247 offset:18432\n"
"v_mfma_f32_16x16x32_bf16 v[100:103], a[108:111], a[12:15], v[100:103]\n"
"ds_read_b128 a[12:15], v248 offset:18432\n"
"v_mfma_f32_16x16x32_bf16 v[104:107], a[104:107], a[16:19], v[104:107]\n"
".Lpad3_%=:\n"
"s_cbranch_execz .Lpad7_%=\n"
"ds_read_b128 a[16:19], v247 offset:20480\n"
"v_mfma_f32_16x16x32_bf16 v[104:107], a[108:111], a[20:23], v[104:107]\n"
"ds_read_b128 a[20:23], v248 offset:20480\n"
"v_mfma_f32_16x16x32_bf16 v[108:111], a[104:107], a[24:27], v[108:111]\n"
"ds_read_b128 a[24:27], v247 offset:22528\n"
"buffer_load_dwordx4 a[100:103], v240, s[36:39], s80 offen offset:3072 sc1\n"
"v_mfma_f32_16x16x32_bf16 v[108:111], a[108:111], a[28:31], v[108:111]\n"
"s_add_u32 s80, s80, 0x1000\n"
"ds_read_b128 a[28:31], v248 offset:22528\n"
"v_mfma_f32_16x16x32_bf16 v[112:115], a[104:107], a[32:35], v[112:115]\n"
"ds_read_b128 a[32:35], v247 offset:24576\n"
"v_mfma_f32_16x16x32_bf16 v[112:115], a[108:111], a[36:39], v[112:115]\n"
"ds_read_b128 a[36:39], v248 offset:24576\n"
"v_mfma_f32_16x16x32_bf16 v[116:119], a[104:107], a[40:43], v[116:119]\n"
"ds_read_b128 a[40:43], v247 offset:26624\n"
"v_mfma_f32_16x16x32_bf16 v[116:119], a[108:111], a[44:47], v[116:119]\n"
"ds_read_b128 a[44:47], v248 offset:26624\n"
"v_mfma_f32_16x16x32_bf16 v[120:123], a[104:107], a[48:51], v[120:123]\n"
"ds_read_b128 a[48:51], v247 offset:28672\n"
"v_mfma_f32_16x16x32_bf16 v[120:123], a[108:111], a[52:55], v[120:123]\n"
"ds_read_b128 a[52:55], v248 offset:28672\n"
"v_mfma_f32_16x16x32_bf16 v[124:127], a[104:107], a[56:59], v[124:127]\n"
"ds_read_b128 a[56:59], v247 offset:30720\n"
"buffer_load_dwordx4 a[104:107], v240, s[36:39], s80 offen offset:0 sc1\n"
"v_mfma_f32_16x16x32_bf16 v[124:127], a[108:111], a[60:63], v[124:127]\n"
"s_waitcnt vmcnt(16) lgkmcnt(11)\n"
"ds_read_b128 a[60:63], v248 offset:30720\n"
"v_mfma_f32_16x16x32_bf16 v[0:3], v[224:227], a[0:3], v[0:3]\n"
"s_add_u32 m0, s83, 4096\n"
"v_mfma_f32_16x16x32_bf16 v[0:3], v[228:231], a[4:7], v[0:3]\n"
"buffer_load_dwordx4 v242, s[40:43], s70 offen offset:0 nt lds\n"
"v_mfma_f32_16x16x32_bf16 v[4:7], v[224:227], a[8:11], v[4:7]\n"
"v_mfma_f32_16x16x32_bf16 v[4:7], v[228:231], a[12:15], v[4:7]\n"
"s_waitcnt lgkmcnt(8)\n"
"v_mfma_f32_16x16x32_bf16 v[8:11], v[224:227], a[16:19], v[8:11]\n"
"v_mfma_f32_16x16x32_bf16 v[8:11], v[228:231], a[20:23], v[8:11]\n"
"v_mfma_f32_16x16x32_bf16 v[12:15], v[224:227], a[24:27], v[12:15]\n"
"buffer_load_dwordx4 a[108:111], v240, s[36:39], s80 offen offset:1024 sc1\n"
"v_mfma_f32_16x16x32_bf16 v[12:15], v[228:231], a[28:31], v[12:15]\n"
"s_waitcnt lgkmcnt(4)\n"
"v_mfma_f32_16x16x32_bf16 v[16:19], v[224:227], a[32:35], v[16:19]\n"
"v_mfma_f32_16x16x32_bf16 v[16:19], v[228:231], a[36:39], v[16:19]\n"
"s_add_u32 m0, s83, 135168\n"
"v_mfma_f32_16x16x32_bf16 v[20:23], v[224:227], a[40:43], v[20:23]\n"
"buffer_load_dwordx4 v242, s[44:47], s70 offen offset:0 nt lds\n"
"v_mfma_f32_16x16x32_bf16 v[20:23], v[228:231], a[44:47], v[20:23]\n"
"s_waitcnt lgkmcnt(0)\n"
"v_mfma_f32_16x16x32_bf16 v[24:27], v[224:227], a[48:51], v[24:27]\n"
"v_mfma_f32_16x16x32_bf16 v[24:27], v[228:231], a[52:55], v[24:27]\n"
"v_mfma_f32_16x16x32_bf16 v[28:31], v[224:227], a[56:59], v[28:31]\n"
"buffer_load_dwordx4 v[224:227], v240, s[36:39], s80 offen offset:2048 sc1\n"
"v_mfma_f32_16x16x32_bf16 v[28:31], v[228:231], a[60:63], v[28:31]\n"
"s_waitcnt vmcnt(17)\n"
"v_mfma_f32_16x16x32_bf16 v[32:35], a[64:67], a[0:3], v[32:35]\n"
"v_mfma_f32_16x16x32_bf16 v[32:35], a[68:71], a[4:7], v[32:35]\n"
"v_mfma_f32_16x16x32_bf16 v[36:39], a[64:67], a[8:11], v[36:39]\n"
"v_mfma_f32_16x16x32_bf16 v[36:39], a[68:71], a[12:15], v[36:39]\n"
"v_mfma_f32_16x16x32_bf16 v[40:43], a[64:67], a[16:19], v[40:43]\n"
"v_mfma_f32_16x16x32_bf16 v[40:43], a[68:71], a[20:23], v[40:43]\n"
"v_mfma_f32_16x16x32_bf16 v[44:47], a[64:67], a[24:27], v[44:47]\n"
"buffer_load_dwordx4 v[228:231], v240, s[36:39], s80 offen offset:3072 sc1\n"
"v_mfma_f32_16x16x32_bf16 v[44:47], a[68:71], a[28:31], v[44:47]\n"
"s_add_u32 s80, s80, 0x1000\n"
"v_mfma_f32_16x16x32_bf16 v[48:51], a[64:67], a[32:35], v[48:51]\n"
"s_add_u32 m0, s83, 6144\n"
"v_mfma_f32_16x16x32_bf16 v[48:51], a[68:71], a[36:39], v[48:51]\n"
"buffer_load_dwordx4 v242, s[40:43], s71 offen offset:0 nt lds\n"
"v_mfma_f32_16x16x32_bf16 v[52:55], a[64:67], a[40:43], v[52:55]\n"
"v_mfma_f32_16x16x32_bf16 v[52:55], a[68:71], a[44:47], v[52:55]\n"
"v_mfma_f32_16x16x32_bf16 v[56:59], a[64:67], a[48:51], v[56:59]\n"
"v_mfma_f32_16x16x32_bf16 v[56:59], a[68:71], a[52:55], v[56:59]\n"
"v_mfma_f32_16x16x32_bf16 v[60:63], a[64:67], a[56:59], v[60:63]\n"
"buffer_load_dwordx4 a[64:67], v240, s[36:39], s80 offen offset:0 sc1\n"
"v_mfma_f32_16x16x32_bf16 v[60:63], a[68:71], a[60:63], v[60:63]\n"
"s_waitcnt vmcnt(17)\n"
"v_mfma_f32_16x16x32_bf16 v[64:67], a[72:75], a[0:3], v[64:67]\n"
"v_mfma_f32_16x16x32_bf16 v[64:67], a[76:79], a[4:7], v[64:67]\n"
"v_mfma_f32_16x16x32_bf16 v[68:71], a[72:75], a[8:11], v[68:71]\n"
"v_mfma_f32_16x16x32_bf16 v[68:71], a[76:79], a[12:15], v[68:71]\n"
"v_mfma_f32_16x16x32_bf16 v[72:75], a[72:75], a[16:19], v[72:75]\n"
"v_mfma_f32_16x16x32_bf16 v[72:75], a[76:79], a[20:23], v[72:75]\n"
"v_mfma_f32_16x16x32_bf16 v[76:79], a[72:75], a[24:27], v[76:79]\n"
"s_add_u32 m0, s83, 137216\n"
"buffer_load_dwordx4 a[68:71], v240, s[36:39], s80 offen offset:1024 sc1\n"
"v_mfma_f32_16x16x32_bf16 v[76:79], a[76:79], a[28:31], v[76:79]\n"
"buffer_load_dwordx4 v242, s[44:47], s71 offen offset:0 nt lds\n"
"v_mfma_f32_16x16x32_bf16 v[80:83], a[72:75], a[32:35], v[80:83]\n"
"v_mfma_f32_16x16x32_bf16 v[80:83], a[76:79], a[36:39], v[80:83]\n"
"v_mfma_f32_16x16x32_bf16 v[84:87], a[72:75], a[40:43], v[84:87]\n"
"v_mfma_f32_16x16x32_bf16 v[84:87], a[76:79], a[44:47], v[84:87]\n"
"v_mfma_f32_16x16x32_bf16 v[88:91], a[72:75], a[48:51], v[88:91]\n"
"v_mfma_f32_16x16x32_bf16 v[88:91], a[76:79], a[52:55], v[88:91]\n"
"v_mfma_f32_16x16x32_bf16 v[92:95], a[72:75], a[56:59], v[92:95]\n"
"buffer_load_dwordx4 a[72:75], v240, s[36:39], s80 offen offset:2048 sc1\n"
"v_mfma_f32_16x16x32_bf16 v[92:95], a[76:79], a[60:63], v[92:95]\n"
"s_waitcnt vmcnt(17)\n"
"v_mfma_f32_16x16x32_bf16 v[96:99], a[80:83], a[0:3], v[96:99]\n"
"v_mfma_f32_16x16x32_bf16 v[96:99], a[84:87], a[4:7], v[96:99]\n"
"v_mfma_f32_16x16x32_bf16 v[100:103], a[80:83], a[8:11], v[100:103]\n"
"v_mfma_f32_16x16x32_bf16 v[100:103], a[84:87], a[12:15], v[100:103]\n"
"v_mfma_f32_16x16x32_bf16 v[104:107], a[80:83], a[16:19], v[104:107]\n"
"v_mfma_f32_16x16x32_bf16 v[104:107], a[84:87], a[20:23], v[104:107]\n"
"v_mfma_f32_16x16x32_bf16 v[108:111], a[80:83], a[24:27], v[108:111]\n"
"buffer_load_dwordx4 a[76:79], v240, s[36:39], s80 offen offset:3072 sc1\n"
"v_mfma_f32_16x16x32_bf16 v[108:111], a[84:87], a[28:31], v[108:111]\n"
"s_add_u32 s80, s80, 0x1000\n"
"v_mfma_f32_16x16x32_bf16 v[112:115], a[80:83], a[32:35], v[112:115]\n"
"v_mfma_f32_16x16x32_bf16 v[112:115], a[84:87], a[36:39], v[112:115]\n"
"v_mfma_f32_16x16x32_bf16 v[116:119], a[80:83], a[40:43], v[116:119]\n"
"v_mfma_f32_16x16x32_bf16 v[116:119], a[84:87], a[44:47], v[116:119]\n"
"v_mfma_f32_16x16x32_bf16 v[120:123], a[80:83], a[48:51], v[120:123]\n"
"v_mfma_f32_16x16x32_bf16 v[120:123], a[84:87], a[52:55], v[120:123]\n"
"v_mfma_f32_16x16x32_bf16 v[124:127], a[80:83], a[56:59], v[124:127]\n"
"buffer_load_dwordx4 a[80:83], v240, s[36:39], s80 offen offset:0 sc1\n"
"v_mfma_f32_16x16x32_bf16 v[124:127], a[84:87], a[60:63], v[124:127]\n"
"s_waitcnt vmcnt(16)\n"
"v_mfma_f32_16x16x32_bf16 v[128:131], a[88:91], a[0:3], v[128:131]\n"
"v_mfma_f32_16x16x32_bf16 v[128:131], a[92:95], a[4:7], v[128:131]\n"
"v_mfma_f32_16x16x32_bf16 v[132:135], a[88:91], a[8:11], v[132:135]\n"
"v_mfma_f32_16x16x32_bf16 v[132:135], a[92:95], a[12:15], v[132:135]\n"
"v_mfma_f32_16x16x32_bf16 v[136:139], a[88:91], a[16:19], v[136:139]\n"
"v_mfma_f32_16x16x32_bf16 v[136:139], a[92:95], a[20:23], v[136:139]\n"
"ds_read_b64_tr_b16 v[232:233], v253 offset:16384\n"
"v_mfma_f32_16x16x32_bf16 v[140:143], a[88:91], a[24:27], v[140:143]\n"
"ds_read_b64_tr_b16 v[234:235], v253 offset:17408\n"
"buffer_load_dwordx4 a[84:87], v240, s[36:39], s80 offen offset:1024 sc1\n"
"v_mfma_f32_16x16x32_bf16 v[140:143], a[92:95], a[28:31], v[140:143]\n"
"ds_read_b64_tr_b16 v[236:237], v253 offset:16392\n"
"v_mfma_f32_16x16x32_bf16 v[144:147], a[88:91], a[32:35], v[144:147]\n"
"ds_read_b64_tr_b16 v[238:239], v253 offset:17416\n"
"v_mfma_f32_16x16x32_bf16 v[144:147], a[92:95], a[36:39], v[144:147]\n"
"v_mfma_f32_16x16x32_bf16 v[148:151], a[88:91], a[40:43], v[148:151]\n"
"v_mfma_f32_16x16x32_bf16 v[148:151], a[92:95], a[44:47], v[148:151]\n"
"v_mfma_f32_16x16x32_bf16 v[152:155], a[88:91], a[48:51], v[152:155]\n"
"v_mfma_f32_16x16x32_bf16 v[152:155], a[92:95], a[52:55], v[152:155]\n"
"v_mfma_f32_16x16x32_bf16 v[156:159], a[88:91], a[56:59], v[156:159]\n"
"buffer_load_dwordx4 a[88:91], v240, s[36:39], s80 offen offset:2048 sc1\n"
"v_mfma_f32_16x16x32_bf16 v[156:159], a[92:95], a[60:63], v[156:159]\n"
"s_waitcnt vmcnt(15)\n"
"v_mfma_f32_16x16x32_bf16 v[160:163], a[96:99], a[0:3], v[160:163]\n"
"v_mfma_f32_16x16x32_bf16 v[160:163], a[100:103], a[4:7], v[160:163]\n"
"v_mfma_f32_16x16x32_bf16 v[164:167], a[96:99], a[8:11], v[164:167]\n"
"v_mfma_f32_16x16x32_bf16 v[164:167], a[100:103], a[12:15], v[164:167]\n"
"v_mfma_f32_16x16x32_bf16 v[168:171], a[96:99], a[16:19], v[168:171]\n"
"v_mfma_f32_16x16x32_bf16 v[168:171], a[100:103], a[20:23], v[168:171]\n"
"v_mfma_f32_16x16x32_bf16 v[172:175], a[96:99], a[24:27], v[172:175]\n"
"buffer_load_dwordx4 a[92:95], v240, s[36:39], s80 offen offset:3072 sc1\n"
"v_mfma_f32_16x16x32_bf16 v[172:175], a[100:103], a[28:31], v[172:175]\n"
"s_add_u32 s80, s80, 0x1000\n"
"v_mfma_f32_16x16x32_bf16 v[176:179], a[96:99], a[32:35], v[176:179]\n"
"v_mfma_f32_16x16x32_bf16 v[176:179], a[100:103], a[36:39], v[176:179]\n"
"v_mfma_f32_16x16x32_bf16 v[180:183], a[96:99], a[40:43], v[180:183]\n"
"v_mfma_f32_16x16x32_bf16 v[180:183], a[100:103], a[44:47], v[180:183]\n"
"v_mfma_f32_16x16x32_bf16 v[184:187], a[96:99], a[48:51], v[184:187]\n"
"v_mfma_f32_16x16x32_bf16 v[184:187], a[100:103], a[52:55], v[184:187]\n"
"v_mfma_f32_16x16x32_bf16 v[188:191], a[96:99], a[56:59], v[188:191]\n"
"buffer_load_dwordx4 a[96:99], v240, s[36:39], s80 offen offset:0 sc1\n"
"v_mfma_f32_16x16x32_bf16 v[188:191], a[100:103], a[60:63], v[188:191]\n"
"s_waitcnt vmcnt(14)\n"
"v_mfma_f32_16x16x32_bf16 v[192:195], a[104:107], a[0:3], v[192:195]\n"
"v_mfma_f32_16x16x32_bf16 v[192:195], a[108:111], a[4:7], v[192:195]\n"
"v_mfma_f32_16x16x32_bf16 v[196:199], a[104:107], a[8:11], v[196:199]\n"
"s_waitcnt lgkmcnt(2)\n"
"buffer_store_dwordx4 v[232:235], v240, s[36:39], s22 offen sc1\n"
"v_mfma_f32_16x16x32_bf16 v[196:199], a[108:111], a[12:15], v[196:199]\n"
"v_mfma_f32_16x16x32_bf16 v[200:203], a[104:107], a[16:19], v[200:203]\n"
"v_mfma_f32_16x16x32_bf16 v[200:203], a[108:111], a[20:23], v[200:203]\n"
"v_mfma_f32_16x16x32_bf16 v[204:207], a[104:107], a[24:27], v[204:207]\n"
"buffer_load_dwordx4 a[100:103], v240, s[36:39], s80 offen offset:1024 sc1\n"
"v_mfma_f32_16x16x32_bf16 v[204:207], a[108:111], a[28:31], v[204:207]\n"
"v_mfma_f32_16x16x32_bf16 v[208:211], a[104:107], a[32:35], v[208:211]\n"
"v_mfma_f32_16x16x32_bf16 v[208:211], a[108:111], a[36:39], v[208:211]\n"
"v_mfma_f32_16x16x32_bf16 v[212:215], a[104:107], a[40:43], v[212:215]\n"
"v_mfma_f32_16x16x32_bf16 v[212:215], a[108:111], a[44:47], v[212:215]\n"
"v_mfma_f32_16x16x32_bf16 v[216:219], a[104:107], a[48:51], v[216:219]\n"
"v_mfma_f32_16x16x32_bf16 v[216:219], a[108:111], a[52:55], v[216:219]\n"
"s_waitcnt vmcnt(9)\n"
"s_barrier\n"
"v_mfma_f32_16x16x32_bf16 v[220:223], a[104:107], a[56:59], v[220:223]\n"
"buffer_load_dwordx4 a[104:107], v240, s[36:39], s80 offen offset:2048 sc1\n"
"v_mfma_f32_16x16x32_bf16 v[220:223], a[108:111], a[60:63], v[220:223]\n"
"s_waitcnt lgkmcnt(0)\n"
"buffer_store_dwordx4 v[236:239], v240, s[36:39], s23 offen sc1\n"
"v_mfma_f32_16x16x32_bf16 a[112:115], v[224:227], a[0:3], a[112:115]\n"
"ds_read_b128 a[0:3], v243 offset:0\n"
"v_mfma_f32_16x16x32_bf16 a[112:115], v[228:231], a[4:7], a[112:115]\n"
"ds_read_b128 a[4:7], v244 offset:0\n"
"v_mfma_f32_16x16x32_bf16 a[116:119], v[224:227], a[8:11], a[116:119]\n"
"ds_read_b128 a[8:11], v243 offset:2048\n"
"v_mfma_f32_16x16x32_bf16 a[116:119], v[228:231], a[12:15], a[116:119]\n"
"ds_read_b128 a[12:15], v244 offset:2048\n"
"v_mfma_f32_16x16x32_bf16 a[120:123], v[224:227], a[16:19], a[120:123]\n"
"ds_read_b128 a[16:19], v243 offset:4096\n"
"v_mfma_f32_16x16x32_bf16 a[120:123], v[228:231], a[20:23], a[120:123]\n"
"ds_read_b128 a[20:23], v244 offset:4096\n"
"v_mfma_f32_16x16x32_bf16 a[124:127], v[224:227], a[24:27], a[124:127]\n"
"ds_read_b128 a[24:27], v243 offset:6144\n"
"buffer_load_dwordx4 a[108:111], v240, s[36:39], s80 offen offset:3072 sc1\n"
"v_mfma_f32_16x16x32_bf16 a[124:127], v[228:231], a[28:31], a[124:127]\n"
"s_add_u32 s80, s80, 0x1000\n"
"ds_read_b128 a[28:31], v244 offset:6144\n"
"v_mfma_f32_16x16x32_bf16 a[128:131], v[224:227], a[32:35], a[128:131]\n"
"ds_read_b128 a[32:35], v243 offset:8192\n"
"v_mfma_f32_16x16x32_bf16 a[128:131], v[228:231], a[36:39], a[128:131]\n"
"ds_read_b128 a[36:39], v244 offset:8192\n"
"v_mfma_f32_16x16x32_bf16 a[132:135], v[224:227], a[40:43], a[132:135]\n"
"ds_read_b128 a[40:43], v243 offset:10240\n"
"v_mfma_f32_16x16x32_bf16 a[132:135], v[228:231], a[44:47], a[132:135]\n"
"ds_read_b128 a[44:47], v244 offset:10240\n"
"v_mfma_f32_16x16x32_bf16 a[136:139], v[224:227], a[48:51], a[136:139]\n"
"ds_read_b128 a[48:51], v243 offset:12288\n"
"v_mfma_f32_16x16x32_bf16 a[136:139], v[228:231], a[52:55], a[136:139]\n"
"ds_read_b128 a[52:55], v244 offset:12288\n"
"v_mfma_f32_16x16x32_bf16 a[140:143], v[224:227], a[56:59], a[140:143]\n"
"ds_read_b128 a[56:59], v243 offset:14336\n"
"buffer_load_dwordx4 v[224:227], v240, s[36:39], s80 offen offset:0 sc1\n"
"v_mfma_f32_16x16x32_bf16 a[140:143], v[228:231], a[60:63], a[140:143]\n"
"s_waitcnt lgkmcnt(11)\n"
"ds_read_b128 a[60:63], v244 offset:14336\n"
"v_mfma_f32_16x16x32_bf16 v[0:3], a[64:67], a[0:3], v[0:3]\n"
"v_mfma_f32_16x16x32_bf16 v[0:3], a[68:71], a[4:7], v[0:3]\n"
"v_mfma_f32_16x16x32_bf16 v[4:7], a[64:67], a[8:11], v[4:7]\n"
"v_mfma_f32_16x16x32_bf16 v[4:7], a[68:71], a[12:15], v[4:7]\n"
"s_waitcnt lgkmcnt(8)\n"
"v_mfma_f32_16x16x32_bf16 v[8:11], a[64:67], a[16:19], v[8:11]\n"
"s_add_u32 m0, s83, 16256\n"
"v_mfma_f32_16x16x32_bf16 v[8:11], a[68:71], a[20:23], v[8:11]\n"
"buffer_load_dwordx4 v242, s[40:43], s68 offen offset:128 nt lds\n"
"v_mfma_f32_16x16x32_bf16 v[12:15], a[64:67], a[24:27], v[12:15]\n"
"buffer_load_dwordx4 v[228:231], v240, s[36:39], s80 offen offset:1024 sc1\n"
"v_mfma_f32_16x16x32_bf16 v[12:15], a[68:71], a[28:31], v[12:15]\n"
"s_waitcnt lgkmcnt(4)\n"
"v_mfma_f32_16x16x32_bf16 v[16:19], a[64:67], a[32:35], v[16:19]\n"
"v_mfma_f32_16x16x32_bf16 v[16:19], a[68:71], a[36:39], v[16:19]\n"
"v_mfma_f32_16x16x32_bf16 v[20:23], a[64:67], a[40:43], v[20:23]\n"
"v_mfma_f32_16x16x32_bf16 v[20:23], a[68:71], a[44:47], v[20:23]\n"
"s_waitcnt lgkmcnt(0)\n"
"v_mfma_f32_16x16x32_bf16 v[24:27], a[64:67], a[48:51], v[24:27]\n"
"v_mfma_f32_16x16x32_bf16 v[24:27], a[68:71], a[52:55], v[24:27]\n"
"v_mfma_f32_16x16x32_bf16 v[28:31], a[64:67], a[56:59], v[28:31]\n"
"buffer_load_dwordx4 a[64:67], v240, s[36:39], s80 offen offset:2048 sc1\n"
"v_mfma_f32_16x16x32_bf16 v[28:31], a[68:71], a[60:63], v[28:31]\n"
"s_waitcnt vmcnt(14)\n"
"v_mfma_f32_16x16x32_bf16 v[32:35], a[72:75], a[0:3], v[32:35]\n"
"v_mfma_f32_16x16x32_bf16 v[32:35], a[76:79], a[4:7], v[32:35]\n"
"s_add_u32 m0, s83, 147328\n"
"v_mfma_f32_16x16x32_bf16 v[36:39], a[72:75], a[8:11], v[36:39]\n"
"buffer_load_dwordx4 v242, s[44:47], s68 offen offset:128 nt lds\n"
"v_mfma_f32_16x16x32_bf16 v[36:39], a[76:79], a[12:15], v[36:39]\n"
"v_mfma_f32_16x16x32_bf16 v[40:43], a[72:75], a[16:19], v[40:43]\n"
"v_mfma_f32_16x16x32_bf16 v[40:43], a[76:79], a[20:23], v[40:43]\n"
"v_mfma_f32_16x16x32_bf16 v[44:47], a[72:75], a[24:27], v[44:47]\n"
"buffer_load_dwordx4 a[68:71], v240, s[36:39], s80 offen offset:3072 sc1\n"
"v_mfma_f32_16x16x32_bf16 v[44:47], a[76:79], a[28:31], v[44:47]\n"
"s_add_u32 s80, s80, 0x1000\n"
"v_mfma_f32_16x16x32_bf16 v[48:51], a[72:75], a[32:35], v[48:51]\n"
"v_mfma_f32_16x16x32_bf16 v[48:51], a[76:79], a[36:39], v[48:51]\n"
"v_mfma_f32_16x16x32_bf16 v[52:55], a[72:75], a[40:43], v[52:55]\n"
"v_mfma_f32_16x16x32_bf16 v[52:55], a[76:79], a[44:47], v[52:55]\n"
"v_mfma_f32_16x16x32_bf16 v[56:59], a[72:75], a[48:51], v[56:59]\n"
"v_mfma_f32_16x16x32_bf16 v[56:59], a[76:79], a[52:55], v[56:59]\n"
"v_mfma_f32_16x16x32_bf16 v[60:63], a[72:75], a[56:59], v[60:63]\n"
"buffer_load_dwordx4 a[72:75], v240, s[36:39], s80 offen offset:0 sc1\n"
"v_mfma_f32_16x16x32_bf16 v[60:63], a[76:79], a[60:63], v[60:63]\n"
"s_waitcnt vmcnt(15)\n"
"v_mfma_f32_16x16x32_bf16 v[64:67], a[80:83], a[0:3], v[64:67]\n"
"s_add_u32 m0, s83, 18304\n"
"v_mfma_f32_16x16x32_bf16 v[64:67], a[84:87], a[4:7], v[64:67]\n"
"buffer_load_dwordx4 v242, s[40:43], s69 offen offset:128 nt lds\n"
"v_mfma_f32_16x16x32_bf16 v[68:71], a[80:83], a[8:11], v[68:71]\n"
"v_mfma_f32_16x16x32_bf16 v[68:71], a[84:87], a[12:15], v[68:71]\n"
"v_mfma_f32_16x16x32_bf16 v[72:75], a[80:83], a[16:19], v[72:75]\n"
"v_mfma_f32_16x16x32_bf16 v[72:75], a[84:87], a[20:23], v[72:75]\n"
"v_mfma_f32_16x16x32_bf16 v[76:79], a[80:83], a[24:27], v[76:79]\n"
"buffer_load_dwordx4 a[76:79], v240, s[36:39], s80 offen offset:1024 sc1\n"
"v_mfma_f32_16x16x32_bf16 v[76:79], a[84:87], a[28:31], v[76:79]\n"
"v_mfma_f32_16x16x32_bf16 v[80:83], a[80:83], a[32:35], v[80:83]\n"
"v_mfma_f32_16x16x32_bf16 v[80:83], a[84:87], a[36:39], v[80:83]\n"
"v_mfma_f32_16x16x32_bf16 v[84:87], a[80:83], a[40:43], v[84:87]\n"
"v_mfma_f32_16x16x32_bf16 v[84:87], a[84:87], a[44:47], v[84:87]\n"
"v_mfma_f32_16x16x32_bf16 v[88:91], a[80:83], a[48:51], v[88:91]\n"
"v_mfma_f32_16x16x32_bf16 v[88:91], a[84:87], a[52:55], v[88:91]\n"
"v_mfma_f32_16x16x32_bf16 v[92:95], a[80:83], a[56:59], v[92:95]\n"
"s_add_u32 m0, s83, 149376\n"
"buffer_load_dwordx4 a[80:83], v240, s[36:39], s80 offen offset:2048 sc1\n"
"v_mfma_f32_16x16x32_bf16 v[92:95], a[84:87], a[60:63], v[92:95]\n"
"s_waitcnt vmcnt(16)\n"
"buffer_load_dwordx4 v242, s[44:47], s69 offen offset:128 nt lds\n"
"v_mfma_f32_16x16x32_bf16 v[96:99], a[88:91], a[0:3], v[96:99]\n"
"ds_read_b128 a[0:3], v247 offset:0\n"
"v_mfma_f32_16x16x32_bf16 v[96:99], a[92:95], a[4:7], v[96:99]\n"
"ds_read_b128 a[4:7], v248 offset:0\n"
"v_mfma_f32_16x16x32_bf16 v[100:103], a[88:91], a[8:11], v[100:103]\n"
"ds_read_b128 a[8:11], v247 offset:2048\n"
"v_mfma_f32_16x16x32_bf16 v[100:103], a[92:95], a[12:15], v[100:103]\n"
"ds_read_b128 a[12:15], v248 offset:2048\n"
"v_mfma_f32_16x16x32_bf16 v[104:107], a[88:91], a[16:19], v[104:107]\n"
"ds_read_b128 a[16:19], v247 offset:4096\n"
"v_mfma_f32_16x16x32_bf16 v[104:107], a[92:95], a[20:23], v[104:107]\n"
"ds_read_b128 a[20:23], v248 offset:4096\n"
"v_mfma_f32_16x16x32_bf16 v[108:111], a[88:91], a[24:27], v[108:111]\n"
"ds_read_b128 a[24:27], v247 offset:6144\n"
"buffer_load_dwordx4 a[84:87], v240, s[36:39], s80 offen offset:3072 sc1\n"
"v_mfma_f32_16x16x32_bf16 v[108:111], a[92:95], a[28:31], v[108:111]\n"
"s_add_u32 s80, s80, 0x1000\n"
"ds_read_b128 a[28:31], v248 offset:6144\n"
"v_mfma_f32_16x16x32_bf16 v[112:115], a[88:91], a[32:35], v[112:115]\n"
"ds_read_b128 a[32:35], v247 offset:8192\n"
"v_mfma_f32_16x16x32_bf16 v[112:115], a[92:95], a[36:39], v[112:115]\n"
"ds_read_b128 a[36:39], v248 offset:8192\n"
"v_mfma_f32_16x16x32_bf16 v[116:119], a[88:91], a[40:43], v[116:119]\n"
"ds_read_b128 a[40:43], v247 offset:10240\n"
"v_mfma_f32_16x16x32_bf16 v[116:119], a[92:95], a[44:47], v[116:119]\n"
"ds_read_b128 a[44:47], v248 offset:10240\n"
"v_mfma_f32_16x16x32_bf16 v[120:123], a[88:91], a[48:51], v[120:123]\n"
"ds_read_b128 a[48:51], v247 offset:12288\n"
"v_mfma_f32_16x16x32_bf16 v[120:123], a[92:95], a[52:55], v[120:123]\n"
"ds_read_b128 a[52:55], v248 offset:12288\n"
"v_mfma_f32_16x16x32_bf16 v[124:127], a[88:91], a[56:59], v[124:127]\n"
"ds_read_b128 a[56:59], v247 offset:14336\n"
"buffer_load_dwordx4 a[88:91], v240, s[36:39], s80 offen offset:0 sc1\n"
"v_mfma_f32_16x16x32_bf16 v[124:127], a[92:95], a[60:63], v[124:127]\n"
"s_waitcnt vmcnt(16) lgkmcnt(11)\n"
"ds_read_b128 a[60:63], v248 offset:14336\n"
"v_mfma_f32_16x16x32_bf16 v[0:3], a[96:99], a[0:3], v[0:3]\n"
"s_add_u32 m0, s83, 20352\n"
"v_mfma_f32_16x16x32_bf16 v[0:3], a[100:103], a[4:7], v[0:3]\n"
"buffer_load_dwordx4 v242, s[40:43], s70 offen offset:128 nt lds\n"
"v_mfma_f32_16x16x32_bf16 v[4:7], a[96:99], a[8:11], v[4:7]\n"
"v_mfma_f32_16x16x32_bf16 v[4:7], a[100:103], a[12:15], v[4:7]\n"
"s_waitcnt lgkmcnt(8)\n"
"v_mfma_f32_16x16x32_bf16 v[8:11], a[96:99], a[16:19], v[8:11]\n"
"v_mfma_f32_16x16x32_bf16 v[8:11], a[100:103], a[20:23], v[8:11]\n"
"v_mfma_f32_16x16x32_bf16 v[12:15], a[96:99], a[24:27], v[12:15]\n"
"buffer_load_dwordx4 a[92:95], v240, s[36:39], s80 offen offset:1024 sc1\n"
"v_mfma_f32_16x16x32_bf16 v[12:15], a[100:103], a[28:31], v[12:15]\n"
"s_waitcnt lgkmcnt(4)\n"
"v_mfma_f32_16x16x32_bf16 v[16:19], a[96:99], a[32:35], v[16:19]\n"
"v_mfma_f32_16x16x32_bf16 v[16:19], a[100:103], a[36:39], v[16:19]\n"
"s_add_u32 m0, s83, 151424\n"
"v_mfma_f32_16x16x32_bf16 v[20:23], a[96:99], a[40:43], v[20:23]\n"
"buffer_load_dwordx4 v242, s[44:47], s70 offen offset:128 nt lds\n"
"v_mfma_f32_16x16x32_bf16 v[20:23], a[100:103], a[44:47], v[20:23]\n"
"s_waitcnt lgkmcnt(0)\n"
"v_mfma_f32_16x16x32_bf16 v[24:27], a[96:99], a[48:51], v[24:27]\n"
"v_mfma_f32_16x16x32_bf16 v[24:27], a[100:103], a[52:55], v[24:27]\n"
"v_mfma_f32_16x16x32_bf16 v[28:31], a[96:99], a[56:59], v[28:31]\n"
"buffer_load_dwordx4 a[96:99], v240, s[36:39], s80 offen offset:2048 sc1\n"
"v_mfma_f32_16x16x32_bf16 v[28:31], a[100:103], a[60:63], v[28:31]\n"
"s_waitcnt vmcnt(17)\n"
"v_mfma_f32_16x16x32_bf16 v[32:35], a[104:107], a[0:3], v[32:35]\n"
"v_mfma_f32_16x16x32_bf16 v[32:35], a[108:111], a[4:7], v[32:35]\n"
"v_mfma_f32_16x16x32_bf16 v[36:39], a[104:107], a[8:11], v[36:39]\n"
"v_mfma_f32_16x16x32_bf16 v[36:39], a[108:111], a[12:15], v[36:39]\n"
"v_mfma_f32_16x16x32_bf16 v[40:43], a[104:107], a[16:19], v[40:43]\n"
"v_mfma_f32_16x16x32_bf16 v[40:43], a[108:111], a[20:23], v[40:43]\n"
"v_mfma_f32_16x16x32_bf16 v[44:47], a[104:107], a[24:27], v[44:47]\n"
"buffer_load_dwordx4 a[100:103], v240, s[36:39], s80 offen offset:3072 sc1\n"
"v_mfma_f32_16x16x32_bf16 v[44:47], a[108:111], a[28:31], v[44:47]\n"
"s_add_u32 s80, s80, 0x1000\n"
"v_mfma_f32_16x16x32_bf16 v[48:51], a[104:107], a[32:35], v[48:51]\n"
"s_add_u32 m0, s83, 22400\n"
"v_mfma_f32_16x16x32_bf16 v[48:51], a[108:111], a[36:39], v[48:51]\n"
"buffer_load_dwordx4 v242, s[40:43], s71 offen offset:128 nt lds\n"
"v_mfma_f32_16x16x32_bf16 v[52:55], a[104:107], a[40:43], v[52:55]\n"
"v_mfma_f32_16x16x32_bf16 v[52:55], a[108:111], a[44:47], v[52:55]\n"
"v_mfma_f32_16x16x32_bf16 v[56:59], a[104:107], a[48:51], v[56:59]\n"
"v_mfma_f32_16x16x32_bf16 v[56:59], a[108:111], a[52:55], v[56:59]\n"
"v_mfma_f32_16x16x32_bf16 v[60:63], a[104:107], a[56:59], v[60:63]\n"
"buffer_load_dwordx4 a[104:107], v240, s[36:39], s80 offen offset:0 sc1\n"
"v_mfma_f32_16x16x32_bf16 v[60:63], a[108:111], a[60:63], v[60:63]\n"
"s_waitcnt vmcnt(17)\n"
"v_mfma_f32_16x16x32_bf16 v[64:67], v[224:227], a[0:3], v[64:67]\n"
"v_mfma_f32_16x16x32_bf16 v[64:67], v[228:231], a[4:7], v[64:67]\n"
"v_mfma_f32_16x16x32_bf16 v[68:71], v[224:227], a[8:11], v[68:71]\n"
"v_mfma_f32_16x16x32_bf16 v[68:71], v[228:231], a[12:15], v[68:71]\n"
"v_mfma_f32_16x16x32_bf16 v[72:75], v[224:227], a[16:19], v[72:75]\n"
"v_mfma_f32_16x16x32_bf16 v[72:75], v[228:231], a[20:23], v[72:75]\n"
"v_mfma_f32_16x16x32_bf16 v[76:79], v[224:227], a[24:27], v[76:79]\n"
"s_add_u32 m0, s83, 153472\n"
"buffer_load_dwordx4 a[108:111], v240, s[36:39], s80 offen offset:1024 sc1\n"
"v_mfma_f32_16x16x32_bf16 v[76:79], v[228:231], a[28:31], v[76:79]\n"
"buffer_load_dwordx4 v242, s[44:47], s71 offen offset:128 nt lds\n"
"v_mfma_f32_16x16x32_bf16 v[80:83], v[224:227], a[32:35], v[80:83]\n"
"v_mfma_f32_16x16x32_bf16 v[80:83], v[228:231], a[36:39], v[80:83]\n"
"v_mfma_f32_16x16x32_bf16 v[84:87], v[224:227], a[40:43], v[84:87]\n"
"v_mfma_f32_16x16x32_bf16 v[84:87], v[228:231], a[44:47], v[84:87]\n"
"v_mfma_f32_16x16x32_bf16 v[88:91], v[224:227], a[48:51], v[88:91]\n"
"v_mfma_f32_16x16x32_bf16 v[88:91], v[228:231], a[52:55], v[88:91]\n"
"v_mfma_f32_16x16x32_bf16 v[92:95], v[224:227], a[56:59], v[92:95]\n"
"buffer_load_dwordx4 v[224:227], v240, s[36:39], s80 offen offset:2048 sc1\n"
"v_mfma_f32_16x16x32_bf16 v[92:95], v[228:231], a[60:63], v[92:95]\n"
"s_waitcnt vmcnt(17)\n"
"v_mfma_f32_16x16x32_bf16 v[96:99], a[64:67], a[0:3], v[96:99]\n"
"v_mfma_f32_16x16x32_bf16 v[96:99], a[68:71], a[4:7], v[96:99]\n"
"v_mfma_f32_16x16x32_bf16 v[100:103], a[64:67], a[8:11], v[100:103]\n"
"v_mfma_f32_16x16x32_bf16 v[100:103], a[68:71], a[12:15], v[100:103]\n"
"v_mfma_f32_16x16x32_bf16 v[104:107], a[64:67], a[16:19], v[104:107]\n"
"v_mfma_f32_16x16x32_bf16 v[104:107], a[68:71], a[20:23], v[104:107]\n"
"v_mfma_f32_16x16x32_bf16 v[108:111], a[64:67], a[24:27], v[108:111]\n"
"buffer_load_dwordx4 v[228:231], v240, s[36:39], s80 offen offset:3072 sc1\n"
"v_mfma_f32_16x16x32_bf16 v[108:111], a[68:71], a[28:31], v[108:111]\n"
"s_add_u32 s80, s80, 0x1000\n"
"v_mfma_f32_16x16x32_bf16 v[112:115], a[64:67], a[32:35], v[112:115]\n"
"v_mfma_f32_16x16x32_bf16 v[112:115], a[68:71], a[36:39], v[112:115]\n"
"v_mfma_f32_16x16x32_bf16 v[116:119], a[64:67], a[40:43], v[116:119]\n"
"v_mfma_f32_16x16x32_bf16 v[116:119], a[68:71], a[44:47], v[116:119]\n"
"v_mfma_f32_16x16x32_bf16 v[120:123], a[64:67], a[48:51], v[120:123]\n"
"v_mfma_f32_16x16x32_bf16 v[120:123], a[68:71], a[52:55], v[120:123]\n"
"v_mfma_f32_16x16x32_bf16 v[124:127], a[64:67], a[56:59], v[124:127]\n"
"buffer_load_dwordx4 a[64:67], v240, s[36:39], s80 offen offset:0 sc1\n"
"v_mfma_f32_16x16x32_bf16 v[124:127], a[68:71], a[60:63], v[124:127]\n"
"s_waitcnt vmcnt(16)\n"
"v_mfma_f32_16x16x32_bf16 v[128:131], a[72:75], a[0:3], v[128:131]\n"
"v_mfma_f32_16x16x32_bf16 v[128:131], a[76:79], a[4:7], v[128:131]\n"
"v_mfma_f32_16x16x32_bf16 v[132:135], a[72:75], a[8:11], v[132:135]\n"
"v_mfma_f32_16x16x32_bf16 v[132:135], a[76:79], a[12:15], v[132:135]\n"
"v_mfma_f32_16x16x32_bf16 v[136:139], a[72:75], a[16:19], v[136:139]\n"
"v_mfma_f32_16x16x32_bf16 v[136:139], a[76:79], a[20:23], v[136:139]\n"
"v_mfma_f32_16x16x32_bf16 v[140:143], a[72:75], a[24:27], v[140:143]\n"
"buffer_load_dwordx4 a[68:71], v240, s[36:39], s80 offen offset:1024 sc1\n"
"v_mfma_f32_16x16x32_bf16 v[140:143], a[76:79], a[28:31], v[140:143]\n"
"v_mfma_f32_16x16x32_bf16 v[144:147], a[72:75], a[32:35], v[144:147]\n"
"v_mfma_f32_16x16x32_bf16 v[144:147], a[76:79], a[36:39], v[144:147]\n"
"v_mfma_f32_16x16x32_bf16 v[148:151], a[72:75], a[40:43], v[148:151]\n"
"v_mfma_f32_16x16x32_bf16 v[148:151], a[76:79], a[44:47], v[148:151]\n"
"v_mfma_f32_16x16x32_bf16 v[152:155], a[72:75], a[48:51], v[152:155]\n"
"v_mfma_f32_16x16x32_bf16 v[152:155], a[76:79], a[52:55], v[152:155]\n"
"v_mfma_f32_16x16x32_bf16 v[156:159], a[72:75], a[56:59], v[156:159]\n"
"buffer_load_dwordx4 a[72:75], v240, s[36:39], s80 offen offset:2048 sc1\n"
"v_mfma_f32_16x16x32_bf16 v[156:159], a[76:79], a[60:63], v[156:159]\n"
"s_waitcnt vmcnt(15)\n"
"v_mfma_f32_16x16x32_bf16 v[160:163], a[80:83], a[0:3], v[160:163]\n"
"v_mfma_f32_16x16x32_bf16 v[160:163], a[84:87], a[4:7], v[160:163]\n"
"v_mfma_f32_16x16x32_bf16 v[164:167], a[80:83], a[8:11], v[164:167]\n"
"v_mfma_f32_16x16x32_bf16 v[164:167], a[84:87], a[12:15], v[164:167]\n"
"v_mfma_f32_16x16x32_bf16 v[168:171], a[80:83], a[16:19], v[168:171]\n"
"v_mfma_f32_16x16x32_bf16 v[168:171], a[84:87], a[20:23], v[168:171]\n"
"v_mfma_f32_16x16x32_bf16 v[172:175], a[80:83], a[24:27], v[172:175]\n"
"buffer_load_dwordx4 a[76:79], v240, s[36:39], s80 offen offset:3072 sc1\n"
"v_mfma_f32_16x16x32_bf16 v[172:175], a[84:87], a[28:31], v[172:175]\n"
"s_add_u32 s80, s80, 0x1000\n"
"v_mfma_f32_16x16x32_bf16 v[176:179], a[80:83], a[32:35], v[176:179]\n"
"v_mfma_f32_16x16x32_bf16 v[176:179], a[84:87], a[36:39], v[176:179]\n"
"v_mfma_f32_16x16x32_bf16 v[180:183], a[80:83], a[40:43], v[180:183]\n"
"v_mfma_f32_16x16x32_bf16 v[180:183], a[84:87], a[44:47], v[180:183]\n"
"v_mfma_f32_16x16x32_bf16 v[184:187], a[80:83], a[48:51], v[184:187]\n"
"v_mfma_f32_16x16x32_bf16 v[184:187], a[84:87], a[52:55], v[184:187]\n"
"v_mfma_f32_16x16x32_bf16 v[188:191], a[80:83], a[56:59], v[188:191]\n"
"buffer_load_dwordx4 a[80:83], v240, s[36:39], s80 offen offset:0 sc1\n"
"v_mfma_f32_16x16x32_bf16 v[188:191], a[84:87], a[60:63], v[188:191]\n"
"s_waitcnt vmcnt(14)\n"
"v_mfma_f32_16x16x32_bf16 v[192:195], a[88:91], a[0:3], v[192:195]\n"
"v_mfma_f32_16x16x32_bf16 v[192:195], a[92:95], a[4:7], v[192:195]\n"
"v_mfma_f32_16x16x32_bf16 v[196:199], a[88:91], a[8:11], v[196:199]\n"
"v_mfma_f32_16x16x32_bf16 v[196:199], a[92:95], a[12:15], v[196:199]\n"
"v_mfma_f32_16x16x32_bf16 v[200:203], a[88:91], a[16:19], v[200:203]\n"
"v_mfma_f32_16x16x32_bf16 v[200:203], a[92:95], a[20:23], v[200:203]\n"
"v_mfma_f32_16x16x32_bf16 v[204:207], a[88:91], a[24:27], v[204:207]\n"
"buffer_load_dwordx4 a[84:87], v240, s[36:39], s80 offen offset:1024 sc1\n"
"v_mfma_f32_16x16x32_bf16 v[204:207], a[92:95], a[28:31], v[204:207]\n"
"v_mfma_f32_16x16x32_bf16 v[208:211], a[88:91], a[32:35], v[208:211]\n"
"v_mfma_f32_16x16x32_bf16 v[208:211], a[92:95], a[36:39], v[208:211]\n"
"v_mfma_f32_16x16x32_bf16 v[212:215], a[88:91], a[40:43], v[212:215]\n"
"v_mfma_f32_16x16x32_bf16 v[212:215], a[92:95], a[44:47], v[212:215]\n"
"v_mfma_f32_16x16x32_bf16 v[216:219], a[88:91], a[48:51], v[216:219]\n"
"v_mfma_f32_16x16x32_bf16 v[216:219], a[92:95], a[52:55], v[216:219]\n"
"s_waitcnt vmcnt(8)\n"
"s_barrier\n"
"v_mfma_f32_16x16x32_bf16 v[220:223], a[88:91], a[56:59], v[220:223]\n"
"buffer_load_dwordx4 a[88:91], v240, s[36:39], s80 offen offset:2048 sc1\n"
"v_lshrrev_b32 v241, 2, v240\n"
"v_mfma_f32_16x16x32_bf16 v[220:223], a[92:95], a[60:63], v[220:223]\n"
"buffer_store_dword v252, v241, s[24:27], s14 offen sc1\n"
"v_mfma_f32_16x16x32_bf16 a[112:115], a[96:99], a[0:3], a[112:115]\n"
"ds_read_b128 a[0:3], v243 offset:16384\n"
"v_mfma_f32_16x16x32_bf16 a[112:115], a[100:103], a[4:7], a[112:115]\n"
"ds_read_b128 a[4:7], v244 offset:16384\n"
"v_mfma_f32_16x16x32_bf16 a[116:119], a[96:99], a[8:11], a[116:119]\n"
"ds_read_b128 a[8:11], v243 offset:18432\n"
"v_mfma_f32_16x16x32_bf16 a[116:119], a[100:103], a[12:15], a[116:119]\n"
"ds_read_b128 a[12:15], v244 offset:18432\n"
"v_mfma_f32_16x16x32_bf16 a[120:123], a[96:99], a[16:19], a[120:123]\n"
"ds_read_b128 a[16:19], v243 offset:20480\n"
"v_mfma_f32_16x16x32_bf16 a[120:123], a[100:103], a[20:23], a[120:123]\n"
"ds_read_b128 a[20:23], v244 offset:20480\n"
"v_mfma_f32_16x16x32_bf16 a[124:127], a[96:99], a[24:27], a[124:127]\n"
"ds_read_b128 a[24:27], v243 offset:22528\n"
"buffer_load_dwordx4 a[92:95], v240, s[36:39], s80 offen offset:3072 sc1\n"
"v_mfma_f32_16x16x32_bf16 a[124:127], a[100:103], a[28:31], a[124:127]\n"
"s_add_u32 s80, s80, 0x1000\n"
"ds_read_b128 a[28:31], v244 offset:22528\n"
"v_mfma_f32_16x16x32_bf16 a[128:131], a[96:99], a[32:35], a[128:131]\n"
"ds_read_b128 a[32:35], v243 offset:24576\n"
"v_mfma_f32_16x16x32_bf16 a[128:131], a[100:103], a[36:39], a[128:131]\n"
"ds_read_b128 a[36:39], v244 offset:24576\n"
"v_mfma_f32_16x16x32_bf16 a[132:135], a[96:99], a[40:43], a[132:135]\n"
"ds_read_b128 a[40:43], v243 offset:26624\n"
"v_mfma_f32_16x16x32_bf16 a[132:135], a[100:103], a[44:47], a[132:135]\n"
"ds_read_b128 a[44:47], v244 offset:26624\n"
"v_mfma_f32_16x16x32_bf16 a[136:139], a[96:99], a[48:51], a[136:139]\n"
"ds_read_b128 a[48:51], v243 offset:28672\n"
"v_mfma_f32_16x16x32_bf16 a[136:139], a[100:103], a[52:55], a[136:139]\n"
"ds_read_b128 a[52:55], v244 offset:28672\n"
"v_mfma_f32_16x16x32_bf16 a[140:143], a[96:99], a[56:59], a[140:143]\n"
"ds_read_b128 a[56:59], v243 offset:30720\n"
"buffer_load_dwordx4 a[96:99], v240, s[36:39], s80 offen offset:0 sc1\n"
"v_mfma_f32_16x16x32_bf16 a[140:143], a[100:103], a[60:63], a[140:143]\n"
"s_waitcnt lgkmcnt(11)\n"
"ds_read_b128 a[60:63], v244 offset:30720\n"
"v_mfma_f32_16x16x32_bf16 v[0:3], a[104:107], a[0:3], v[0:3]\n"
"v_mfma_f32_16x16x32_bf16 v[0:3], a[108:111], a[4:7], v[0:3]\n"
"v_mfma_f32_16x16x32_bf16 v[4:7], a[104:107], a[8:11], v[4:7]\n"
"v_mfma_f32_16x16x32_bf16 v[4:7], a[108:111], a[12:15], v[4:7]\n"
"s_waitcnt lgkmcnt(8)\n"
"v_mfma_f32_16x16x32_bf16 v[8:11], a[104:107], a[16:19], v[8:11]\n"
"s_add_u32 m0, s83, 32512\n"
"v_mfma_f32_16x16x32_bf16 v[8:11], a[108:111], a[20:23], v[8:11]\n"
"buffer_load_dwordx4 v242, s[40:43], s68 offen offset:256 nt lds\n"
"v_mfma_f32_16x16x32_bf16 v[12:15], a[104:107], a[24:27], v[12:15]\n"
"ds_read_b128 a[148:151], v249 offset:0\n"
"buffer_load_dwordx4 a[100:103], v240, s[36:39], s80 offen offset:1024 sc1\n"
"v_mfma_f32_16x16x32_bf16 v[12:15], a[108:111], a[28:31], v[12:15]\n"
"s_waitcnt lgkmcnt(5)\n"
"v_mfma_f32_16x16x32_bf16 v[16:19], a[104:107], a[32:35], v[16:19]\n"
"v_mfma_f32_16x16x32_bf16 v[16:19], a[108:111], a[36:39], v[16:19]\n"
"ds_read_b128 a[152:155], v249 offset:2048\n"
"v_mfma_f32_16x16x32_bf16 v[20:23], a[104:107], a[40:43], v[20:23]\n"
"v_mfma_f32_16x16x32_bf16 v[20:23], a[108:111], a[44:47], v[20:23]\n"
"s_waitcnt lgkmcnt(2)\n"
"v_mfma_f32_16x16x32_bf16 v[24:27], a[104:107], a[48:51], v[24:27]\n"
".Lpad4_%=:\n"
"s_cbranch_execz .Lpad8_%=\n"
"ds_read_b128 a[156:159], v249 offset:4096\n"
"v_mfma_f32_16x16x32_bf16 v[24:27], a[108:111], a[52:55], v[24:27]\n"
"v_mfma_f32_16x16x32_bf16 v[28:31], a[104:107], a[56:59], v[28:31]\n"
"buffer_load_dwordx4 a[104:107], v240, s[36:39], s80 offen offset:2048 sc1\n"
"v_mfma_f32_16x16x32_bf16 v[28:31], a[108:111], a[60:63], v[28:31]\n"
"s_waitcnt vmcnt(13)\n"
"ds_read_b128 a[160:163], v249 offset:6144\n"
"v_mfma_f32_16x16x32_bf16 v[32:35], v[224:227], a[0:3], v[32:35]\n"
"v_mfma_f32_16x16x32_bf16 v[32:35], v[228:231], a[4:7], v[32:35]\n"
"s_add_u32 m0, s83, 130816\n"
"v_mfma_f32_16x16x32_bf16 v[36:39], v[224:227], a[8:11], v[36:39]\n"
"buffer_load_dwordx4 v242, s[44:47], s68 offen offset:256 nt lds\n"
"v_mfma_f32_16x16x32_bf16 v[36:39], v[228:231], a[12:15], v[36:39]\n"
"ds_read_b128 a[164:167], v249 offset:8192\n"
"v_mfma_f32_16x16x32_bf16 v[40:43], v[224:227], a[16:19], v[40:43]\n"
"v_mfma_f32_16x16x32_bf16 v[40:43], v[228:231], a[20:23], v[40:43]\n"
"v_mfma_f32_16x16x32_bf16 v[44:47], v[224:227], a[24:27], v[44:47]\n"
"ds_read_b128 a[168:171], v249 offset:10240\n"
"buffer_load_dwordx4 a[108:111], v240, s[36:39], s80 offen offset:3072 sc1\n"
"v_mfma_f32_16x16x32_bf16 v[44:47], v[228:231], a[28:31], v[44:47]\n"
"s_add_u32 s80, s80, 0x1000\n"
"v_mfma_f32_16x16x32_bf16 v[48:51], v[224:227], a[32:35], v[48:51]\n"
"v_mfma_f32_16x16x32_bf16 v[48:51], v[228:231], a[36:39], v[48:51]\n"
"ds_read_b128 a[172:175], v249 offset:12288\n"
"v_mfma_f32_16x16x32_bf16 v[52:55], v[224:227], a[40:43], v[52:55]\n"
"v_mfma_f32_16x16x32_bf16 v[52:55], v[228:231], a[44:47], v[52:55]\n"
"v_mfma_f32_16x16x32_bf16 v[56:59], v[224:227], a[48:51], v[56:59]\n"
"ds_read_b128 a[144:147], v249 offset:14336\n"
"v_mfma_f32_16x16x32_bf16 v[56:59], v[228:231], a[52:55], v[56:59]\n"
"v_mfma_f32_16x16x32_bf16 v[60:63], v[224:227], a[56:59], v[60:63]\n"
"buffer_load_dwordx4 v[224:227], v240, s[36:39], s80 offen offset:0 sc1\n"
"v_mfma_f32_16x16x32_bf16 v[60:63], v[228:231], a[60:63], v[60:63]\n"
"s_waitcnt vmcnt(14)\n"
"v_mfma_f32_16x16x32_bf16 v[64:67], a[64:67], a[0:3], v[64:67]\n"
"s_add_u32 m0, s83, 34560\n"
"v_mfma_f32_16x16x32_bf16 v[64:67], a[68:71], a[4:7], v[64:67]\n"
"buffer_load_dwordx4 v242, s[40:43], s69 offen offset:256 nt lds\n"
"v_mfma_f32_16x16x32_bf16 v[68:71], a[64:67], a[8:11], v[68:71]\n"
"v_mfma_f32_16x16x32_bf16 v[68:71], a[68:71], a[12:15], v[68:71]\n"
"v_mfma_f32_16x16x32_bf16 v[72:75], a[64:67], a[16:19], v[72:75]\n"
"v_mfma_f32_16x16x32_bf16 v[72:75], a[68:71], a[20:23], v[72:75]\n"
"v_mfma_f32_16x16x32_bf16 v[76:79], a[64:67], a[24:27], v[76:79]\n"
"buffer_load_dwordx4 v[228:231], v240, s[36:39], s80 offen offset:1024 sc1\n"
"v_mfma_f32_16x16x32_bf16 v[76:79], a[68:71], a[28:31], v[76:79]\n"
"v_mfma_f32_16x16x32_bf16 v[80:83], a[64:67], a[32:35], v[80:83]\n"
"v_mfma_f32_16x16x32_bf16 v[80:83], a[68:71], a[36:39], v[80:83]\n"
"v_mfma_f32_16x16x32_bf16 v[84:87], a[64:67], a[40:43], v[84:87]\n"
"v_mfma_f32_16x16x32_bf16 v[84:87], a[68:71], a[44:47], v[84:87]\n"
"v_mfma_f32_16x16x32_bf16 v[88:91], a[64:67], a[48:51], v[88:91]\n"
"v_mfma_f32_16x16x32_bf16 v[88:91], a[68:71], a[52:55], v[88:91]\n"
"v_mfma_f32_16x16x32_bf16 v[92:95], a[64:67], a[56:59], v[92:95]\n"
"s_add_u32 m0, s83, 132864\n"
"buffer_load_dwordx4 a[64:67], v240, s[36:39], s80 offen offset:2048 sc1\n"
"v_mfma_f32_16x16x32_bf16 v[92:95], a[68:71], a[60:63], v[92:95]\n"
"s_waitcnt vmcnt(15)\n"
"buffer_load_dwordx4 v242, s[44:47], s69 offen offset:256 nt lds\n"
"v_mfma_f32_16x16x32_bf16 v[96:99], a[72:75], a[0:3], v[96:99]\n"
"ds_read_b128 a[0:3], v247 offset:16384\n"
"v_mfma_f32_16x16x32_bf16 v[96:99], a[76:79], a[4:7], v[96:99]\n"
"ds_read_b128 a[4:7], v248 offset:16384\n"
"v_mfma_f32_16x16x32_bf16 v[100:103], a[72:75], a[8:11], v[100:103]\n"
"ds_read_b128 a[8:11], v247 offset:18432\n"
"v_mfma_f32_16x16x32_bf16 v[100:103], a[76:79], a[12:15], v[100:103]\n"
"ds_read_b128 a[12:15], v248 offset:18432\n"
"v_mfma_f32_16x16x32_bf16 v[104:107], a[72:75], a[16:19], v[104:107]\n"
"ds_read_b128 a[16:19], v247 offset:20480\n"
"v_mfma_f32_16x16x32_bf16 v[104:107], a[76:79], a[20:23], v[104:107]\n"
"ds_read_b128 a[20:23], v248 offset:20480\n"
"v_mfma_f32_16x16x32_bf16 v[108:111], a[72:75], a[24:27], v[108:111]\n"
"ds_read_b128 a[24:27], v247 offset:22528\n"
"buffer_load_dwordx4 a[68:71], v240, s[36:39], s80 offen offset:3072 sc1\n"
"v_mfma_f32_16x16x32_bf16 v[108:111], a[76:79], a[28:31], v[108:111]\n"
"s_add_u32 s80, s80, 0x1000\n"
"ds_read_b128 a[28:31], v248 offset:22528\n"
"v_mfma_f32_16x16x32_bf16 v[112:115], a[72:75], a[32:35], v[112:115]\n"
"ds_read_b128 a[32:35], v247 offset:24576\n"
"v_mfma_f32_16x16x32_bf16 v[112:115], a[76:79], a[36:39], v[112:115]\n"
"ds_read_b128 a[36:39], v248 offset:24576\n"
"v_mfma_f32_16x16x32_bf16 v[116:119], a[72:75], a[40:43], v[116:119]\n"
"ds_read_b128 a[40:43], v247 offset:26624\n"
"v_mfma_f32_16x16x32_bf16 v[116:119], a[76:79], a[44:47], v[116:119]\n"
"ds_read_b128 a[44:47], v248 offset:26624\n"
"v_mfma_f32_16x16x32_bf16 v[120:123], a[72:75], a[48:51], v[120:123]\n"
"ds_read_b128 a[48:51], v247 offset:28672\n"
"v_mfma_f32_16x16x32_bf16 v[120:123], a[76:79], a[52:55], v[120:123]\n"
"ds_read_b128 a[52:55], v248 offset:28672\n"
"v_mfma_f32_16x16x32_bf16 v[124:127], a[72:75], a[56:59], v[124:127]\n"
"ds_read_b128 a[56:59], v247 offset:30720\n"
"buffer_load_dwordx4 a[72:75], v240, s[36:39], s80 offen offset:0 sc1\n"
"v_mfma_f32_16x16x32_bf16 v[124:127], a[76:79], a[60:63], v[124:127]\n"
"s_waitcnt vmcnt(16) lgkmcnt(11)\n"
"ds_read_b128 a[60:63], v248 offset:30720\n"
"v_mfma_f32_16x16x32_bf16 v[0:3], a[80:83], a[0:3], v[0:3]\n"
"s_add_u32 m0, s83, 36608\n"
"v_mfma_f32_16x16x32_bf16 v[0:3], a[84:87], a[4:7], v[0:3]\n"
"buffer_load_dwordx4 v242, s[40:43], s70 offen offset:256 nt lds\n"
"v_mfma_f32_16x16x32_bf16 v[4:7], a[80:83], a[8:11], v[4:7]\n"
"v_mfma_f32_16x16x32_bf16 v[4:7], a[84:87], a[12:15], v[4:7]\n"
"s_waitcnt lgkmcnt(8)\n"
"v_mfma_f32_16x16x32_bf16 v[8:11], a[80:83], a[16:19], v[8:11]\n"
"v_mfma_f32_16x16x32_bf16 v[8:11], a[84:87], a[20:23], v[8:11]\n"
"v_mfma_f32_16x16x32_bf16 v[12:15], a[80:83], a[24:27], v[12:15]\n"
"buffer_load_dwordx4 a[76:79], v240, s[36:39], s80 offen offset:1024 sc1\n"
"v_mfma_f32_16x16x32_bf16 v[12:15], a[84:87], a[28:31], v[12:15]\n"
"s_waitcnt lgkmcnt(4)\n"
"v_mfma_f32_16x16x32_bf16 v[16:19], a[80:83], a[32:35], v[16:19]\n"
"v_mfma_f32_16x16x32_bf16 v[16:19], a[84:87], a[36:39], v[16:19]\n"
"s_add_u32 m0, s83, 134912\n"
"v_mfma_f32_16x16x32_bf16 v[20:23], a[80:83], a[40:43], v[20:23]\n"
"buffer_load_dwordx4 v242, s[44:47], s70 offen offset:256 nt lds\n"
"v_mfma_f32_16x16x32_bf16 v[20:23], a[84:87], a[44:47], v[20:23]\n"
"s_waitcnt lgkmcnt(0)\n"
"v_mfma_f32_16x16x32_bf16 v[24:27], a[80:83], a[48:51], v[24:27]\n"
"v_mfma_f32_16x16x32_bf16 v[24:27], a[84:87], a[52:55], v[24:27]\n"
"v_mfma_f32_16x16x32_bf16 v[28:31], a[80:83], a[56:59], v[28:31]\n"
"buffer_load_dwordx4 a[80:83], v240, s[36:39], s80 offen offset:2048 sc1\n"
"v_mfma_f32_16x16x32_bf16 v[28:31], a[84:87], a[60:63], v[28:31]\n"
"s_waitcnt vmcnt(17)\n"
"v_mfma_f32_16x16x32_bf16 v[32:35], a[88:91], a[0:3], v[32:35]\n"
"v_mfma_f32_16x16x32_bf16 v[32:35], a[92:95], a[4:7], v[32:35]\n"
"v_mfma_f32_16x16x32_bf16 v[36:39], a[88:91], a[8:11], v[36:39]\n"
"v_mfma_f32_16x16x32_bf16 v[36:39], a[92:95], a[12:15], v[36:39]\n"
"v_mfma_f32_16x16x32_bf16 v[40:43], a[88:91], a[16:19], v[40:43]\n"
"v_mfma_f32_16x16x32_bf16 v[40:43], a[92:95], a[20:23], v[40:43]\n"
"v_mfma_f32_16x16x32_bf16 v[44:47], a[88:91], a[24:27], v[44:47]\n"
"buffer_load_dwordx4 a[84:87], v240, s[36:39], s80 offen offset:3072 sc1\n"
"v_mfma_f32_16x16x32_bf16 v[44:47], a[92:95], a[28:31], v[44:47]\n"
"s_add_u32 s80, s80, 0x1000\n"
"v_mfma_f32_16x16x32_bf16 v[48:51], a[88:91], a[32:35], v[48:51]\n"
"s_add_u32 m0, s83, 38656\n"
"v_mfma_f32_16x16x32_bf16 v[48:51], a[92:95], a[36:39], v[48:51]\n"
"buffer_load_dwordx4 v242, s[40:43], s71 offen offset:256 nt lds\n"
"v_mfma_f32_16x16x32_bf16 v[52:55], a[88:91], a[40:43], v[52:55]\n"
"v_mfma_f32_16x16x32_bf16 v[52:55], a[92:95], a[44:47], v[52:55]\n"
"v_mfma_f32_16x16x32_bf16 v[56:59], a[88:91], a[48:51], v[56:59]\n"
"v_mfma_f32_16x16x32_bf16 v[56:59], a[92:95], a[52:55], v[56:59]\n"
"v_mfma_f32_16x16x32_bf16 v[60:63], a[88:91], a[56:59], v[60:63]\n"
"buffer_load_dwordx4 a[88:91], v240, s[36:39], s80 offen offset:0 sc1\n"
"v_mfma_f32_16x16x32_bf16 v[60:63], a[92:95], a[60:63], v[60:63]\n"
"s_waitcnt vmcnt(17)\n"
"v_mfma_f32_16x16x32_bf16 v[64:67], a[96:99], a[0:3], v[64:67]\n"
"v_mfma_f32_16x16x32_bf16 v[64:67], a[100:103], a[4:7], v[64:67]\n"
"v_mfma_f32_16x16x32_bf16 v[68:71], a[96:99], a[8:11], v[68:71]\n"
"v_mfma_f32_16x16x32_bf16 v[68:71], a[100:103], a[12:15], v[68:71]\n"
"v_mfma_f32_16x16x32_bf16 v[72:75], a[96:99], a[16:19], v[72:75]\n"
"v_mfma_f32_16x16x32_bf16 v[72:75], a[100:103], a[20:23], v[72:75]\n"
"v_mfma_f32_16x16x32_bf16 v[76:79], a[96:99], a[24:27], v[76:79]\n"
"s_add_u32 m0, s83, 136960\n"
"buffer_load_dwordx4 a[92:95], v240, s[36:39], s80 offen offset:1024 sc1\n"
"v_mfma_f32_16x16x32_bf16 v[76:79], a[100:103], a[28:31], v[76:79]\n"
"buffer_load_dwordx4 v242, s[44:47], s71 offen offset:256 nt lds\n"
"v_mfma_f32_16x16x32_bf16 v[80:83], a[96:99], a[32:35], v[80:83]\n"
"v_mfma_f32_16x16x32_bf16 v[80:83], a[100:103], a[36:39], v[80:83]\n"
"v_mfma_f32_16x16x32_bf16 v[84:87], a[96:99], a[40:43], v[84:87]\n"
"v_mfma_f32_16x16x32_bf16 v[84:87], a[100:103], a[44:47], v[84:87]\n"
"v_mfma_f32_16x16x32_bf16 v[88:91], a[96:99], a[48:51], v[88:91]\n"
"v_mfma_f32_16x16x32_bf16 v[88:91], a[100:103], a[52:55], v[88:91]\n"
"v_mfma_f32_16x16x32_bf16 v[92:95], a[96:99], a[56:59], v[92:95]\n"
"buffer_load_dwordx4 a[96:99], v240, s[36:39], s80 offen offset:2048 sc1\n"
"v_mfma_f32_16x16x32_bf16 v[92:95], a[100:103], a[60:63], v[92:95]\n"
"s_waitcnt vmcnt(17)\n"
"v_mfma_f32_16x16x32_bf16 v[96:99], a[104:107], a[0:3], v[96:99]\n"
"v_mfma_f32_16x16x32_bf16 v[96:99], a[108:111], a[4:7], v[96:99]\n"
"v_mfma_f32_16x16x32_bf16 v[100:103], a[104:107], a[8:11], v[100:103]\n"
"v_mfma_f32_16x16x32_bf16 v[100:103], a[108:111], a[12:15], v[100:103]\n"
"v_mfma_f32_16x16x32_bf16 v[104:107], a[104:107], a[16:19], v[104:107]\n"
"v_mfma_f32_16x16x32_bf16 v[104:107], a[108:111], a[20:23], v[104:107]\n"
"v_mfma_f32_16x16x32_bf16 v[108:111], a[104:107], a[24:27], v[108:111]\n"
"buffer_load_dwordx4 a[100:103], v240, s[36:39], s80 offen offset:3072 sc1\n"
"v_mfma_f32_16x16x32_bf16 v[108:111], a[108:111], a[28:31], v[108:111]\n"
"s_add_u32 s80, s80, 0x1000\n"
"v_mfma_f32_16x16x32_bf16 v[112:115], a[104:107], a[32:35], v[112:115]\n"
"v_mfma_f32_16x16x32_bf16 v[112:115], a[108:111], a[36:39], v[112:115]\n"
"v_mfma_f32_16x16x32_bf16 v[116:119], a[104:107], a[40:43], v[116:119]\n"
"v_mfma_f32_16x16x32_bf16 v[116:119], a[108:111], a[44:47], v[116:119]\n"
"v_mfma_f32_16x16x32_bf16 v[120:123], a[104:107], a[48:51], v[120:123]\n"
"v_mfma_f32_16x16x32_bf16 v[120:123], a[108:111], a[52:55], v[120:123]\n"
"v_mfma_f32_16x16x32_bf16 v[124:127], a[104:107], a[56:59], v[124:127]\n"
"buffer_load_dwordx4 a[104:107], v240, s[36:39], s80 offen offset:0 sc1\n"
"v_mfma_f32_16x16x32_bf16 v[124:127], a[108:111], a[60:63], v[124:127]\n"
"s_waitcnt vmcnt(16)\n"
"v_mfma_f32_16x16x32_bf16 v[128:131], v[224:227], a[0:3], v[128:131]\n"
"v_mfma_f32_16x16x32_bf16 v[128:131], v[228:231], a[4:7], v[128:131]\n"
"v_mfma_f32_16x16x32_bf16 v[132:135], v[224:227], a[8:11], v[132:135]\n"
"v_mfma_f32_16x16x32_bf16 v[132:135], v[228:231], a[12:15], v[132:135]\n"
"v_mfma_f32_16x16x32_bf16 v[136:139], v[224:227], a[16:19], v[136:139]\n"
"v_mfma_f32_16x16x32_bf16 v[136:139], v[228:231], a[20:23], v[136:139]\n"
"v_mfma_f32_16x16x32_bf16 v[140:143], v[224:227], a[24:27], v[140:143]\n"
"buffer_load_dwordx4 a[108:111], v240, s[36:39], s80 offen offset:1024 sc1\n"
"v_mfma_f32_16x16x32_bf16 v[140:143], v[228:231], a[28:31], v[140:143]\n"
"v_mfma_f32_16x16x32_bf16 v[144:147], v[224:227], a[32:35], v[144:147]\n"
"v_mfma_f32_16x16x32_bf16 v[144:147], v[228:231], a[36:39], v[144:147]\n"
"v_mfma_f32_16x16x32_bf16 v[148:151], v[224:227], a[40:43], v[148:151]\n"
"v_mfma_f32_16x16x32_bf16 v[148:151], v[228:231], a[44:47], v[148:151]\n"
"v_mfma_f32_16x16x32_bf16 v[152:155], v[224:227], a[48:51], v[152:155]\n"
"v_mfma_f32_16x16x32_bf16 v[152:155], v[228:231], a[52:55], v[152:155]\n"
"v_mfma_f32_16x16x32_bf16 v[156:159], v[224:227], a[56:59], v[156:159]\n"
"buffer_load_dwordx4 v[224:227], v240, s[36:39], s80 offen offset:2048 sc1\n"
"v_mfma_f32_16x16x32_bf16 v[156:159], v[228:231], a[60:63], v[156:159]\n"
"s_waitcnt vmcnt(15)\n"
"v_mfma_f32_16x16x32_bf16 v[160:163], a[64:67], a[0:3], v[160:163]\n"
"v_mfma_f32_16x16x32_bf16 v[160:163], a[68:71], a[4:7], v[160:163]\n"
"v_mfma_f32_16x16x32_bf16 v[164:167], a[64:67], a[8:11], v[164:167]\n"
"v_mfma_f32_16x16x32_bf16 v[164:167], a[68:71], a[12:15], v[164:167]\n"
"v_mfma_f32_16x16x32_bf16 v[168:171], a[64:67], a[16:19], v[168:171]\n"
"v_mfma_f32_16x16x32_bf16 v[168:171], a[68:71], a[20:23], v[168:171]\n"
"v_mfma_f32_16x16x32_bf16 v[172:175], a[64:67], a[24:27], v[172:175]\n"
"buffer_load_dwordx4 v[228:231], v240, s[36:39], s80 offen offset:3072 sc1\n"
"v_mfma_f32_16x16x32_bf16 v[172:175], a[68:71], a[28:31], v[172:175]\n"
"s_add_u32 s80, s80, 0x1000\n"
"v_mfma_f32_16x16x32_bf16 v[176:179], a[64:67], a[32:35], v[176:179]\n"
"v_mfma_f32_16x16x32_bf16 v[176:179], a[68:71], a[36:39], v[176:179]\n"
"v_mfma_f32_16x16x32_bf16 v[180:183], a[64:67], a[40:43], v[180:183]\n"
"v_mfma_f32_16x16x32_bf16 v[180:183], a[68:71], a[44:47], v[180:183]\n"
"v_mfma_f32_16x16x32_bf16 v[184:187], a[64:67], a[48:51], v[184:187]\n"
"v_mfma_f32_16x16x32_bf16 v[184:187], a[68:71], a[52:55], v[184:187]\n"
"v_mfma_f32_16x16x32_bf16 v[188:191], a[64:67], a[56:59], v[188:191]\n"
"buffer_load_dwordx4 a[64:67], v240, s[36:39], s80 offen offset:0 sc1\n"
"v_mfma_f32_16x16x32_bf16 v[188:191], a[68:71], a[60:63], v[188:191]\n"
"s_waitcnt vmcnt(14)\n"
"v_mfma_f32_16x16x32_bf16 v[192:195], a[72:75], a[0:3], v[192:195]\n"
"v_mfma_f32_16x16x32_bf16 v[192:195], a[76:79], a[4:7], v[192:195]\n"
"v_mfma_f32_16x16x32_bf16 v[196:199], a[72:75], a[8:11], v[196:199]\n"
"v_mfma_f32_16x16x32_bf16 v[196:199], a[76:79], a[12:15], v[196:199]\n"
"v_mfma_f32_16x16x32_bf16 v[200:203], a[72:75], a[16:19], v[200:203]\n"
"v_mfma_f32_16x16x32_bf16 v[200:203], a[76:79], a[20:23], v[200:203]\n"
"v_mfma_f32_16x16x32_bf16 v[204:207], a[72:75], a[24:27], v[204:207]\n"
"buffer_load_dwordx4 a[68:71], v240, s[36:39], s80 offen offset:1024 sc1\n"
"v_mfma_f32_16x16x32_bf16 v[204:207], a[76:79], a[28:31], v[204:207]\n"
"v_mfma_f32_16x16x32_bf16 v[208:211], a[72:75], a[32:35], v[208:211]\n"
"v_mfma_f32_16x16x32_bf16 v[208:211], a[76:79], a[36:39], v[208:211]\n"
"v_mfma_f32_16x16x32_bf16 v[212:215], a[72:75], a[40:43], v[212:215]\n"
"v_mfma_f32_16x16x32_bf16 v[212:215], a[76:79], a[44:47], v[212:215]\n"
"v_mfma_f32_16x16x32_bf16 v[216:219], a[72:75], a[48:51], v[216:219]\n"
"v_mfma_f32_16x16x32_bf16 v[216:219], a[76:79], a[52:55], v[216:219]\n"
"s_waitcnt vmcnt(8)\n"
"s_barrier\n"
"v_mfma_f32_16x16x32_bf16 v[220:223], a[72:75], a[56:59], v[220:223]\n"
"buffer_load_dwordx4 a[72:75], v240, s[36:39], s80 offen offset:2048 sc1\n"
"v_mfma_f32_16x16x32_bf16 v[220:223], a[76:79], a[60:63], v[220:223]\n"
"v_mfma_f32_16x16x32_bf16 a[112:115], a[80:83], a[0:3], a[112:115]\n"
"ds_read_b128 a[0:3], v243 offset:32768\n"
"v_mfma_f32_16x16x32_bf16 a[112:115], a[84:87], a[4:7], a[112:115]\n"
"ds_read_b128 a[4:7], v244 offset:32768\n"
"v_mfma_f32_16x16x32_bf16 a[116:119], a[80:83], a[8:11], a[116:119]\n"
"ds_read_b128 a[8:11], v243 offset:34816\n"
"v_mfma_f32_16x16x32_bf16 a[116:119], a[84:87], a[12:15], a[116:119]\n"
"ds_read_b128 a[12:15], v244 offset:34816\n"
"v_mfma_f32_16x16x32_bf16 a[120:123], a[80:83], a[16:19], a[120:123]\n"
"ds_read_b128 a[16:19], v243 offset:36864\n"
"v_mfma_f32_16x16x32_bf16 a[120:123], a[84:87], a[20:23], a[120:123]\n"
"ds_read_b128 a[20:23], v244 offset:36864\n"
"v_mfma_f32_16x16x32_bf16 a[124:127], a[80:83], a[24:27], a[124:127]\n"
"ds_read_b128 a[24:27], v243 offset:38912\n"
"buffer_load_dwordx4 a[76:79], v240, s[36:39], s80 offen offset:3072 sc1\n"
"v_mfma_f32_16x16x32_bf16 a[124:127], a[84:87], a[28:31], a[124:127]\n"
"s_add_u32 s80, s80, 0x1000\n"
"ds_read_b128 a[28:31], v244 offset:38912\n"
"v_mfma_f32_16x16x32_bf16 a[128:131], a[80:83], a[32:35], a[128:131]\n"
"ds_read_b128 a[32:35], v243 offset:40960\n"
"v_mfma_f32_16x16x32_bf16 a[128:131], a[84:87], a[36:39], a[128:131]\n"
"ds_read_b128 a[36:39], v244 offset:40960\n"
"v_mfma_f32_16x16x32_bf16 a[132:135], a[80:83], a[40:43], a[132:135]\n"
"ds_read_b128 a[40:43], v243 offset:43008\n"
"v_mfma_f32_16x16x32_bf16 a[132:135], a[84:87], a[44:47], a[132:135]\n"
"ds_read_b128 a[44:47], v244 offset:43008\n"
"v_mfma_f32_16x16x32_bf16 a[136:139], a[80:83], a[48:51], a[136:139]\n"
"ds_read_b128 a[48:51], v243 offset:45056\n"
"v_mfma_f32_16x16x32_bf16 a[136:139], a[84:87], a[52:55], a[136:139]\n"
"ds_read_b128 a[52:55], v244 offset:45056\n"
"v_mfma_f32_16x16x32_bf16 a[140:143], a[80:83], a[56:59], a[140:143]\n"
"ds_read_b128 a[56:59], v243 offset:47104\n"
"buffer_load_dwordx4 a[80:83], v240, s[36:39], s80 offen offset:0 sc1\n"
"v_mfma_f32_16x16x32_bf16 a[140:143], a[84:87], a[60:63], a[140:143]\n"
"s_waitcnt lgkmcnt(11)\n"
"ds_read_b128 a[60:63], v244 offset:47104\n"
"v_mfma_f32_16x16x32_bf16 v[0:3], a[88:91], a[0:3], v[0:3]\n"
"v_mfma_f32_16x16x32_bf16 v[0:3], a[92:95], a[4:7], v[0:3]\n"
"v_mfma_f32_16x16x32_bf16 v[4:7], a[88:91], a[8:11], v[4:7]\n"
"v_mfma_f32_16x16x32_bf16 v[4:7], a[92:95], a[12:15], v[4:7]\n"
"s_waitcnt lgkmcnt(8)\n"
"v_mfma_f32_16x16x32_bf16 v[8:11], a[88:91], a[16:19], v[8:11]\n"
"s_add_u32 m0, s83, 48768\n"
"v_mfma_f32_16x16x32_bf16 v[8:11], a[92:95], a[20:23], v[8:11]\n"
"buffer_load_dwordx4 v242, s[40:43], s68 offen offset:384 nt lds\n"
"v_mfma_f32_16x16x32_bf16 v[12:15], a[88:91], a[24:27], v[12:15]\n"
"buffer_load_dwordx4 a[84:87], v240, s[36:39], s80 offen offset:1024 sc1\n"
"v_mfma_f32_16x16x32_bf16 v[12:15], a[92:95], a[28:31], v[12:15]\n"
"s_waitcnt lgkmcnt(4)\n"
"v_mfma_f32_16x16x32_bf16 v[16:19], a[88:91], a[32:35], v[16:19]\n"
"v_mfma_f32_16x16x32_bf16 v[16:19], a[92:95], a[36:39], v[16:19]\n"
"v_mfma_f32_16x16x32_bf16 v[20:23], a[88:91], a[40:43], v[20:23]\n"
"v_mfma_f32_16x16x32_bf16 v[20:23], a[92:95], a[44:47], v[20:23]\n"
"s_waitcnt lgkmcnt(0)\n"
"v_mfma_f32_16x16x32_bf16 v[24:27], a[88:91], a[48:51], v[24:27]\n"
"v_mfma_f32_16x16x32_bf16 v[24:27], a[92:95], a[52:55], v[24:27]\n"
"v_mfma_f32_16x16x32_bf16 v[28:31], a[88:91], a[56:59], v[28:31]\n"
"buffer_load_dwordx4 a[88:91], v240, s[36:39], s80 offen offset:2048 sc1\n"
"v_mfma_f32_16x16x32_bf16 v[28:31], a[92:95], a[60:63], v[28:31]\n"
"s_waitcnt vmcnt(12)\n"
"v_mfma_f32_16x16x32_bf16 v[32:35], a[96:99], a[0:3], v[32:35]\n"
"v_mfma_f32_16x16x32_bf16 v[32:35], a[100:103], a[4:7], v[32:35]\n"
"s_add_u32 m0, s83, 147072\n"
"v_mfma_f32_16x16x32_bf16 v[36:39], a[96:99], a[8:11], v[36:39]\n"
"buffer_load_dwordx4 v242, s[44:47], s68 offen offset:384 nt lds\n"
"v_mfma_f32_16x16x32_bf16 v[36:39], a[100:103], a[12:15], v[36:39]\n"
"v_mfma_f32_16x16x32_bf16 v[40:43], a[96:99], a[16:19], v[40:43]\n"
"v_mfma_f32_16x16x32_bf16 v[40:43], a[100:103], a[20:23], v[40:43]\n"
"v_mfma_f32_16x16x32_bf16 v[44:47], a[96:99], a[24:27], v[44:47]\n"
"buffer_load_dwordx4 a[92:95], v240, s[36:39], s80 offen offset:3072 sc1\n"
"v_mfma_f32_16x16x32_bf16 v[44:47], a[100:103], a[28:31], v[44:47]\n"
"s_add_u32 s80, s80, 0x1000\n"
"v_mfma_f32_16x16x32_bf16 v[48:51], a[96:99], a[32:35], v[48:51]\n"
"v_mfma_f32_16x16x32_bf16 v[48:51], a[100:103], a[36:39], v[48:51]\n"
"v_mfma_f32_16x16x32_bf16 v[52:55], a[96:99], a[40:43], v[52:55]\n"
"v_mfma_f32_16x16x32_bf16 v[52:55], a[100:103], a[44:47], v[52:55]\n"
"v_mfma_f32_16x16x32_bf16 v[56:59], a[96:99], a[48:51], v[56:59]\n"
"v_mfma_f32_16x16x32_bf16 v[56:59], a[100:103], a[52:55], v[56:59]\n"
"v_mfma_f32_16x16x32_bf16 v[60:63], a[96:99], a[56:59], v[60:63]\n"
"buffer_load_dwordx4 a[96:99], v240, s[36:39], s80 offen offset:0 sc1\n"
"v_mfma_f32_16x16x32_bf16 v[60:63], a[100:103], a[60:63], v[60:63]\n"
"s_waitcnt vmcnt(13)\n"
"v_mfma_f32_16x16x32_bf16 v[64:67], a[104:107], a[0:3], v[64:67]\n"
"s_add_u32 m0, s83, 50816\n"
"v_mfma_f32_16x16x32_bf16 v[64:67], a[108:111], a[4:7], v[64:67]\n"
"buffer_load_dwordx4 v242, s[40:43], s69 offen offset:384 nt lds\n"
"v_mfma_f32_16x16x32_bf16 v[68:71], a[104:107], a[8:11], v[68:71]\n"
"v_mfma_f32_16x16x32_bf16 v[68:71], a[108:111], a[12:15], v[68:71]\n"
"v_mfma_f32_16x16x32_bf16 v[72:75], a[104:107], a[16:19], v[72:75]\n"
"v_mfma_f32_16x16x32_bf16 v[72:75], a[108:111], a[20:23], v[72:75]\n"
"v_mfma_f32_16x16x32_bf16 v[76:79], a[104:107], a[24:27], v[76:79]\n"
"buffer_load_dwordx4 a[100:103], v240, s[36:39], s80 offen offset:1024 sc1\n"
"v_mfma_f32_16x16x32_bf16 v[76:79], a[108:111], a[28:31], v[76:79]\n"
"v_mfma_f32_16x16x32_bf16 v[80:83], a[104:107], a[32:35], v[80:83]\n"
"v_mfma_f32_16x16x32_bf16 v[80:83], a[108:111], a[36:39], v[80:83]\n"
"v_mfma_f32_16x16x32_bf16 v[84:87], a[104:107], a[40:43], v[84:87]\n"
"v_mfma_f32_16x16x32_bf16 v[84:87], a[108:111], a[44:47], v[84:87]\n"
"v_mfma_f32_16x16x32_bf16 v[88:91], a[104:107], a[48:51], v[88:91]\n"
"v_mfma_f32_16x16x32_bf16 v[88:91], a[108:111], a[52:55], v[88:91]\n"
"v_mfma_f32_16x16x32_bf16 v[92:95], a[104:107], a[56:59], v[92:95]\n"
"s_add_u32 m0, s83, 149120\n"
"buffer_load_dwordx4 a[104:107], v240, s[36:39], s80 offen offset:2048 sc1\n"
"v_mfma_f32_16x16x32_bf16 v[92:95], a[108:111], a[60:63], v[92:95]\n"
"s_waitcnt vmcnt(14)\n"
"buffer_load_dwordx4 v242, s[44:47], s69 offen offset:384 nt lds\n"
"v_mfma_f32_16x16x32_bf16 v[96:99], v[224:227], a[0:3], v[96:99]\n"
"ds_read_b128 a[0:3], v247 offset:0\n"
"v_mfma_f32_16x16x32_bf16 v[96:99], v[228:231], a[4:7], v[96:99]\n"
"ds_read_b128 a[4:7], v248 offset:0\n"
"v_mfma_f32_16x16x32_bf16 v[100:103], v[224:227], a[8:11], v[100:103]\n"
"ds_read_b128 a[8:11], v247 offset:2048\n"
"v_mfma_f32_16x16x32_bf16 v[100:103], v[228:231], a[12:15], v[100:103]\n"
"ds_read_b128 a[12:15], v248 offset:2048\n"
"v_mfma_f32_16x16x32_bf16 v[104:107], v[224:227], a[16:19], v[104:107]\n"
"ds_read_b128 a[16:19], v247 offset:4096\n"
"v_mfma_f32_16x16x32_bf16 v[104:107], v[228:231], a[20:23], v[104:107]\n"
"ds_read_b128 a[20:23], v248 offset:4096\n"
"v_mfma_f32_16x16x32_bf16 v[108:111], v[224:227], a[24:27], v[108:111]\n"
"ds_read_b128 a[24:27], v247 offset:6144\n"
"buffer_load_dwordx4 a[108:111], v240, s[36:39], s80 offen offset:3072 sc1\n"
"v_mfma_f32_16x16x32_bf16 v[108:111], v[228:231], a[28:31], v[108:111]\n"
"s_add_u32 s80, s80, 0x1000\n"
"ds_read_b128 a[28:31], v248 offset:6144\n"
"v_mfma_f32_16x16x32_bf16 v[112:115], v[224:227], a[32:35], v[112:115]\n"
"ds_read_b128 a[32:35], v247 offset:8192\n"
"v_mfma_f32_16x16x32_bf16 v[112:115], v[228:231], a[36:39], v[112:115]\n"
"ds_read_b128 a[36:39], v248 offset:8192\n"
"v_mfma_f32_16x16x32_bf16 v[116:119], v[224:227], a[40:43], v[116:119]\n"
"ds_read_b128 a[40:43], v247 offset:10240\n"
"v_mfma_f32_16x16x32_bf16 v[116:119], v[228:231], a[44:47], v[116:119]\n"
"ds_read_b128 a[44:47], v248 offset:10240\n"
"v_mfma_f32_16x16x32_bf16 v[120:123], v[224:227], a[48:51], v[120:123]\n"
"ds_read_b128 a[48:51], v247 offset:12288\n"
"v_mfma_f32_16x16x32_bf16 v[120:123], v[228:231], a[52:55], v[120:123]\n"
"ds_read_b128 a[52:55], v248 offset:12288\n"
"v_mfma_f32_16x16x32_bf16 v[124:127], v[224:227], a[56:59], v[124:127]\n"
"ds_read_b128 a[56:59], v247 offset:14336\n"
"buffer_load_dwordx4 v[224:227], v240, s[36:39], s80 offen offset:0 sc1\n"
"v_mfma_f32_16x16x32_bf16 v[124:127], v[228:231], a[60:63], v[124:127]\n"
"s_waitcnt vmcnt(15) lgkmcnt(11)\n"
"ds_read_b128 a[60:63], v248 offset:14336\n"
"v_mfma_f32_16x16x32_bf16 v[0:3], a[64:67], a[0:3], v[0:3]\n"
"s_add_u32 m0, s83, 52864\n"
"v_mfma_f32_16x16x32_bf16 v[0:3], a[68:71], a[4:7], v[0:3]\n"
"buffer_load_dwordx4 v242, s[40:43], s70 offen offset:384 nt lds\n"
"v_mfma_f32_16x16x32_bf16 v[4:7], a[64:67], a[8:11], v[4:7]\n"
"v_mfma_f32_16x16x32_bf16 v[4:7], a[68:71], a[12:15], v[4:7]\n"
"s_waitcnt lgkmcnt(8)\n"
"v_mfma_f32_16x16x32_bf16 v[8:11], a[64:67], a[16:19], v[8:11]\n"
"v_mfma_f32_16x16x32_bf16 v[8:11], a[68:71], a[20:23], v[8:11]\n"
"v_mfma_f32_16x16x32_bf16 v[12:15], a[64:67], a[24:27], v[12:15]\n"
"buffer_load_dwordx4 v[228:231], v240, s[36:39], s80 offen offset:1024 sc1\n"
"v_mfma_f32_16x16x32_bf16 v[12:15], a[68:71], a[28:31], v[12:15]\n"
"s_waitcnt lgkmcnt(4)\n"
"v_mfma_f32_16x16x32_bf16 v[16:19], a[64:67], a[32:35], v[16:19]\n"
"v_mfma_f32_16x16x32_bf16 v[16:19], a[68:71], a[36:39], v[16:19]\n"
"s_add_u32 m0, s83, 151168\n"
"v_mfma_f32_16x16x32_bf16 v[20:23], a[64:67], a[40:43], v[20:23]\n"
"buffer_load_dwordx4 v242, s[44:47], s70 offen offset:384 nt lds\n"
"v_mfma_f32_16x16x32_bf16 v[20:23], a[68:71], a[44:47], v[20:23]\n"
"s_waitcnt lgkmcnt(0)\n"
"v_mfma_f32_16x16x32_bf16 v[24:27], a[64:67], a[48:51], v[24:27]\n"
"v_mfma_f32_16x16x32_bf16 v[24:27], a[68:71], a[52:55], v[24:27]\n"
"v_mfma_f32_16x16x32_bf16 v[28:31], a[64:67], a[56:59], v[28:31]\n"
"buffer_load_dwordx4 a[64:67], v240, s[36:39], s80 offen offset:2048 sc1\n"
"v_mfma_f32_16x16x32_bf16 v[28:31], a[68:71], a[60:63], v[28:31]\n"
"s_waitcnt vmcnt(17)\n"
"v_mfma_f32_16x16x32_bf16 v[32:35], a[72:75], a[0:3], v[32:35]\n"
"v_mfma_f32_16x16x32_bf16 v[32:35], a[76:79], a[4:7], v[32:35]\n"
"v_mfma_f32_16x16x32_bf16 v[36:39], a[72:75], a[8:11], v[36:39]\n"
"v_mfma_f32_16x16x32_bf16 v[36:39], a[76:79], a[12:15], v[36:39]\n"
"v_mfma_f32_16x16x32_bf16 v[40:43], a[72:75], a[16:19], v[40:43]\n"
"v_mfma_f32_16x16x32_bf16 v[40:43], a[76:79], a[20:23], v[40:43]\n"
"v_mfma_f32_16x16x32_bf16 v[44:47], a[72:75], a[24:27], v[44:47]\n"
"buffer_load_dwordx4 a[68:71], v240, s[36:39], s80 offen offset:3072 sc1\n"
"v_mfma_f32_16x16x32_bf16 v[44:47], a[76:79], a[28:31], v[44:47]\n"
"s_add_u32 s80, s80, 0x1000\n"
"v_mfma_f32_16x16x32_bf16 v[48:51], a[72:75], a[32:35], v[48:51]\n"
"s_add_u32 m0, s83, 54912\n"
"v_mfma_f32_16x16x32_bf16 v[48:51], a[76:79], a[36:39], v[48:51]\n"
"buffer_load_dwordx4 v242, s[40:43], s71 offen offset:384 nt lds\n"
"v_mfma_f32_16x16x32_bf16 v[52:55], a[72:75], a[40:43], v[52:55]\n"
"v_mfma_f32_16x16x32_bf16 v[52:55], a[76:79], a[44:47], v[52:55]\n"
"v_mfma_f32_16x16x32_bf16 v[56:59], a[72:75], a[48:51], v[56:59]\n"
"v_mfma_f32_16x16x32_bf16 v[56:59], a[76:79], a[52:55], v[56:59]\n"
"v_mfma_f32_16x16x32_bf16 v[60:63], a[72:75], a[56:59], v[60:63]\n"
"buffer_load_dwordx4 a[72:75], v240, s[36:39], s80 offen offset:0 sc1\n"
"v_mfma_f32_16x16x32_bf16 v[60:63], a[76:79], a[60:63], v[60:63]\n"
"s_waitcnt vmcnt(17)\n"
"v_mfma_f32_16x16x32_bf16 v[64:67], a[80:83], a[0:3], v[64:67]\n"
"v_mfma_f32_16x16x32_bf16 v[64:67], a[84:87], a[4:7], v[64:67]\n"
"v_mfma_f32_16x16x32_bf16 v[68:71], a[80:83], a[8:11], v[68:71]\n"
"v_mfma_f32_16x16x32_bf16 v[68:71], a[84:87], a[12:15], v[68:71]\n"
"v_mfma_f32_16x16x32_bf16 v[72:75], a[80:83], a[16:19], v[72:75]\n"
"v_mfma_f32_16x16x32_bf16 v[72:75], a[84:87], a[20:23], v[72:75]\n"
"v_mfma_f32_16x16x32_bf16 v[76:79], a[80:83], a[24:27], v[76:79]\n"
"s_add_u32 m0, s83, 153216\n"
"buffer_load_dwordx4 a[76:79], v240, s[36:39], s80 offen offset:1024 sc1\n"
"v_mfma_f32_16x16x32_bf16 v[76:79], a[84:87], a[28:31], v[76:79]\n"
"buffer_load_dwordx4 v242, s[44:47], s71 offen offset:384 nt lds\n"
"v_mfma_f32_16x16x32_bf16 v[80:83], a[80:83], a[32:35], v[80:83]\n"
"v_mfma_f32_16x16x32_bf16 v[80:83], a[84:87], a[36:39], v[80:83]\n"
"v_mfma_f32_16x16x32_bf16 v[84:87], a[80:83], a[40:43], v[84:87]\n"
"v_mfma_f32_16x16x32_bf16 v[84:87], a[84:87], a[44:47], v[84:87]\n"
"v_mov_b32 v242, s33\n"
"v_mfma_f32_16x16x32_bf16 v[88:91], a[80:83], a[48:51], v[88:91]\n"
"v_mfma_f32_16x16x32_bf16 v[88:91], a[84:87], a[52:55], v[88:91]\n"
"v_mfma_f32_16x16x32_bf16 v[92:95], a[80:83], a[56:59], v[92:95]\n"
"buffer_load_dwordx4 a[80:83], v240, s[36:39], s80 offen offset:2048 sc1\n"
"v_mfma_f32_16x16x32_bf16 v[92:95], a[84:87], a[60:63], v[92:95]\n"
"s_waitcnt vmcnt(17)\n"
"v_mfma_f32_16x16x32_bf16 v[96:99], a[88:91], a[0:3], v[96:99]\n"
"v_mfma_f32_16x16x32_bf16 v[96:99], a[92:95], a[4:7], v[96:99]\n"
"v_mfma_f32_16x16x32_bf16 v[100:103], a[88:91], a[8:11], v[100:103]\n"
"v_mfma_f32_16x16x32_bf16 v[100:103], a[92:95], a[12:15], v[100:103]\n"
"v_mfma_f32_16x16x32_bf16 v[104:107], a[88:91], a[16:19], v[104:107]\n"
"v_mfma_f32_16x16x32_bf16 v[104:107], a[92:95], a[20:23], v[104:107]\n"
"v_mfma_f32_16x16x32_bf16 v[108:111], a[88:91], a[24:27], v[108:111]\n"
"buffer_load_dwordx4 a[84:87], v240, s[36:39], s80 offen offset:3072 sc1\n"
"v_mfma_f32_16x16x32_bf16 v[108:111], a[92:95], a[28:31], v[108:111]\n"
"s_add_u32 s80, s80, 0x1000\n"
"v_mfma_f32_16x16x32_bf16 v[112:115], a[88:91], a[32:35], v[112:115]\n"
"v_mfma_f32_16x16x32_bf16 v[112:115], a[92:95], a[36:39], v[112:115]\n"
"v_mfma_f32_16x16x32_bf16 v[116:119], a[88:91], a[40:43], v[116:119]\n"
"v_mfma_f32_16x16x32_bf16 v[116:119], a[92:95], a[44:47], v[116:119]\n"
"v_mfma_f32_16x16x32_bf16 v[120:123], a[88:91], a[48:51], v[120:123]\n"
"v_mfma_f32_16x16x32_bf16 v[120:123], a[92:95], a[52:55], v[120:123]\n"
"v_mfma_f32_16x16x32_bf16 v[124:127], a[88:91], a[56:59], v[124:127]\n"
"buffer_load_dwordx4 a[88:91], v240, s[36:39], s80 offen offset:0 sc1\n"
"v_mfma_f32_16x16x32_bf16 v[124:127], a[92:95], a[60:63], v[124:127]\n"
"s_waitcnt vmcnt(16)\n"
"v_mfma_f32_16x16x32_bf16 v[128:131], a[96:99], a[0:3], v[128:131]\n"
"v_mfma_f32_16x16x32_bf16 v[128:131], a[100:103], a[4:7], v[128:131]\n"
"v_mfma_f32_16x16x32_bf16 v[132:135], a[96:99], a[8:11], v[132:135]\n"
"v_mfma_f32_16x16x32_bf16 v[132:135], a[100:103], a[12:15], v[132:135]\n"
"v_mfma_f32_16x16x32_bf16 v[136:139], a[96:99], a[16:19], v[136:139]\n"
"v_mfma_f32_16x16x32_bf16 v[136:139], a[100:103], a[20:23], v[136:139]\n"
"v_mfma_f32_16x16x32_bf16 v[140:143], a[96:99], a[24:27], v[140:143]\n"
"buffer_load_dwordx4 a[92:95], v240, s[36:39], s80 offen offset:1024 sc1\n"
"v_mfma_f32_16x16x32_bf16 v[140:143], a[100:103], a[28:31], v[140:143]\n"
"v_mfma_f32_16x16x32_bf16 v[144:147], a[96:99], a[32:35], v[144:147]\n"
"v_mfma_f32_16x16x32_bf16 v[144:147], a[100:103], a[36:39], v[144:147]\n"
"v_mfma_f32_16x16x32_bf16 v[148:151], a[96:99], a[40:43], v[148:151]\n"
"v_mfma_f32_16x16x32_bf16 v[148:151], a[100:103], a[44:47], v[148:151]\n"
"v_mfma_f32_16x16x32_bf16 v[152:155], a[96:99], a[48:51], v[152:155]\n"
"v_mfma_f32_16x16x32_bf16 v[152:155], a[100:103], a[52:55], v[152:155]\n"
"v_mfma_f32_16x16x32_bf16 v[156:159], a[96:99], a[56:59], v[156:159]\n"
"buffer_load_dwordx4 a[96:99], v240, s[36:39], s80 offen offset:2048 sc1\n"
"v_mfma_f32_16x16x32_bf16 v[156:159], a[100:103], a[60:63], v[156:159]\n"
"s_waitcnt vmcnt(15)\n"
"v_mfma_f32_16x16x32_bf16 v[160:163], a[104:107], a[0:3], v[160:163]\n"
"v_mfma_f32_16x16x32_bf16 v[160:163], a[108:111], a[4:7], v[160:163]\n"
"v_mfma_f32_16x16x32_bf16 v[164:167], a[104:107], a[8:11], v[164:167]\n"
"v_mfma_f32_16x16x32_bf16 v[164:167], a[108:111], a[12:15], v[164:167]\n"
"v_mfma_f32_16x16x32_bf16 v[168:171], a[104:107], a[16:19], v[168:171]\n"
"v_mfma_f32_16x16x32_bf16 v[168:171], a[108:111], a[20:23], v[168:171]\n"
"v_mfma_f32_16x16x32_bf16 v[172:175], a[104:107], a[24:27], v[172:175]\n"
"buffer_load_dwordx4 a[100:103], v240, s[36:39], s80 offen offset:3072 sc1\n"
"v_mfma_f32_16x16x32_bf16 v[172:175], a[108:111], a[28:31], v[172:175]\n"
"s_add_u32 s80, s80, 0x1000\n"
"v_mfma_f32_16x16x32_bf16 v[176:179], a[104:107], a[32:35], v[176:179]\n"
"v_mfma_f32_16x16x32_bf16 v[176:179], a[108:111], a[36:39], v[176:179]\n"
"v_mfma_f32_16x16x32_bf16 v[180:183], a[104:107], a[40:43], v[180:183]\n"
"v_mfma_f32_16x16x32_bf16 v[180:183], a[108:111], a[44:47], v[180:183]\n"
"v_mfma_f32_16x16x32_bf16 v[184:187], a[104:107], a[48:51], v[184:187]\n"
"v_mfma_f32_16x16x32_bf16 v[184:187], a[108:111], a[52:55], v[184:187]\n"
"v_mfma_f32_16x16x32_bf16 v[188:191], a[104:107], a[56:59], v[188:191]\n"
"buffer_load_dwordx4 a[104:107], v240, s[36:39], s80 offen offset:0 sc1\n"
"v_mfma_f32_16x16x32_bf16 v[188:191], a[108:111], a[60:63], v[188:191]\n"
"s_waitcnt vmcnt(14)\n"
"v_mfma_f32_16x16x32_bf16 v[192:195], v[224:227], a[0:3], v[192:195]\n"
"v_mfma_f32_16x16x32_bf16 v[192:195], v[228:231], a[4:7], v[192:195]\n"
"v_mfma_f32_16x16x32_bf16 v[196:199], v[224:227], a[8:11], v[196:199]\n"
"v_mfma_f32_16x16x32_bf16 v[196:199], v[228:231], a[12:15], v[196:199]\n"
"v_mfma_f32_16x16x32_bf16 v[200:203], v[224:227], a[16:19], v[200:203]\n"
"v_mfma_f32_16x16x32_bf16 v[200:203], v[228:231], a[20:23], v[200:203]\n"
"v_mfma_f32_16x16x32_bf16 v[204:207], v[224:227], a[24:27], v[204:207]\n"
"buffer_load_dwordx4 a[108:111], v240, s[36:39], s80 offen offset:1024 sc1\n"
"v_mfma_f32_16x16x32_bf16 v[204:207], v[228:231], a[28:31], v[204:207]\n"
"v_mfma_f32_16x16x32_bf16 v[208:211], v[224:227], a[32:35], v[208:211]\n"
"v_mfma_f32_16x16x32_bf16 v[208:211], v[228:231], a[36:39], v[208:211]\n"
"v_mfma_f32_16x16x32_bf16 v[212:215], v[224:227], a[40:43], v[212:215]\n"
"v_mfma_f32_16x16x32_bf16 v[212:215], v[228:231], a[44:47], v[212:215]\n"
"v_mfma_f32_16x16x32_bf16 v[216:219], v[224:227], a[48:51], v[216:219]\n"
"v_mfma_f32_16x16x32_bf16 v[216:219], v[228:231], a[52:55], v[216:219]\n"
"s_waitcnt vmcnt(8)\n"
"s_barrier\n"
"v_mfma_f32_16x16x32_bf16 v[220:223], v[224:227], a[56:59], v[220:223]\n"
"buffer_load_dwordx4 v[224:227], v240, s[36:39], s80 offen offset:2048 sc1\n"
"v_mfma_f32_16x16x32_bf16 v[220:223], v[228:231], a[60:63], v[220:223]\n"
".Lpad5_%=:\n"
"s_cbranch_execz .Lpad9_%=\n"
"v_mfma_f32_16x16x32_bf16 a[112:115], a[64:67], a[0:3], a[112:115]\n"
"ds_read_b128 a[0:3], v243 offset:49152\n"
"v_mfma_f32_16x16x32_bf16 a[112:115], a[68:71], a[4:7], a[112:115]\n"
"ds_read_b128 a[4:7], v244 offset:49152\n"
"v_mfma_f32_16x16x32_bf16 a[116:119], a[64:67], a[8:11], a[116:119]\n"
"ds_read_b128 a[8:11], v243 offset:51200\n"
"v_mfma_f32_16x16x32_bf16 a[116:119], a[68:71], a[12:15], a[116:119]\n"
"ds_read_b128 a[12:15], v244 offset:51200\n"
"v_mfma_f32_16x16x32_bf16 a[120:123], a[64:67], a[16:19], a[120:123]\n"
"ds_read_b128 a[16:19], v243 offset:53248\n"
"v_mfma_f32_16x16x32_bf16 a[120:123], a[68:71], a[20:23], a[120:123]\n"
"ds_read_b128 a[20:23], v244 offset:53248\n"
"v_mfma_f32_16x16x32_bf16 a[124:127], a[64:67], a[24:27], a[124:127]\n"
"ds_read_b128 a[24:27], v243 offset:55296\n"
"buffer_load_dwordx4 v[228:231], v240, s[36:39], s80 offen offset:3072 sc1\n"
"v_mfma_f32_16x16x32_bf16 a[124:127], a[68:71], a[28:31], a[124:127]\n"
"s_add_u32 s80, s80, 0x1000\n"
"ds_read_b128 a[28:31], v244 offset:55296\n"
"v_mfma_f32_16x16x32_bf16 a[128:131], a[64:67], a[32:35], a[128:131]\n"
"ds_read_b128 a[32:35], v243 offset:57344\n"
"v_mfma_f32_16x16x32_bf16 a[128:131], a[68:71], a[36:39], a[128:131]\n"
"ds_read_b128 a[36:39], v244 offset:57344\n"
"v_mfma_f32_16x16x32_bf16 a[132:135], a[64:67], a[40:43], a[132:135]\n"
"ds_read_b128 a[40:43], v243 offset:59392\n"
"v_mfma_f32_16x16x32_bf16 a[132:135], a[68:71], a[44:47], a[132:135]\n"
"ds_read_b128 a[44:47], v244 offset:59392\n"
"v_mfma_f32_16x16x32_bf16 a[136:139], a[64:67], a[48:51], a[136:139]\n"
"ds_read_b128 a[48:51], v243 offset:61440\n"
"v_mfma_f32_16x16x32_bf16 a[136:139], a[68:71], a[52:55], a[136:139]\n"
"ds_read_b128 a[52:55], v244 offset:61440\n"
"v_mfma_f32_16x16x32_bf16 a[140:143], a[64:67], a[56:59], a[140:143]\n"
"ds_read_b128 a[56:59], v243 offset:63488\n"
"buffer_load_dwordx4 a[64:67], v240, s[36:39], s80 offen offset:0 sc1\n"
"v_mfma_f32_16x16x32_bf16 a[140:143], a[68:71], a[60:63], a[140:143]\n"
"s_waitcnt lgkmcnt(11)\n"
"ds_read_b128 a[60:63], v244 offset:63488\n"
"v_mfma_f32_16x16x32_bf16 v[0:3], a[72:75], a[0:3], v[0:3]\n"
"v_lshlrev_b32 v241, 4, v240\n"
"v_mfma_f32_16x16x32_bf16 v[0:3], a[76:79], a[4:7], v[0:3]\n"
"buffer_load_dword v241, v241, s[24:27], s30 offen sc1\n"
"v_mfma_f32_16x16x32_bf16 v[4:7], a[72:75], a[8:11], v[4:7]\n"
"v_mfma_f32_16x16x32_bf16 v[4:7], a[76:79], a[12:15], v[4:7]\n"
"s_waitcnt lgkmcnt(8)\n"
"v_mfma_f32_16x16x32_bf16 v[8:11], a[72:75], a[16:19], v[8:11]\n"
"v_mfma_f32_16x16x32_bf16 v[8:11], a[76:79], a[20:23], v[8:11]\n"
"ds_read_b128 a[180:183], v249 offset:32768\n"
"v_mfma_f32_16x16x32_bf16 v[12:15], a[72:75], a[24:27], v[12:15]\n"
"buffer_load_dwordx4 a[68:71], v240, s[36:39], s80 offen offset:1024 sc1\n"
"v_mfma_f32_16x16x32_bf16 v[12:15], a[76:79], a[28:31], v[12:15]\n"
"s_waitcnt lgkmcnt(5)\n"
"v_mfma_f32_16x16x32_bf16 v[16:19], a[72:75], a[32:35], v[16:19]\n"
"ds_read_b128 a[184:187], v249 offset:34816\n"
"v_mfma_f32_16x16x32_bf16 v[16:19], a[76:79], a[36:39], v[16:19]\n"
"v_mfma_f32_16x16x32_bf16 v[20:23], a[72:75], a[40:43], v[20:23]\n"
"v_mfma_f32_16x16x32_bf16 v[20:23], a[76:79], a[44:47], v[20:23]\n"
"s_waitcnt lgkmcnt(2)\n"
"ds_read_b128 a[188:191], v249 offset:36864\n"
"v_mfma_f32_16x16x32_bf16 v[24:27], a[72:75], a[48:51], v[24:27]\n"
"v_mfma_f32_16x16x32_bf16 v[24:27], a[76:79], a[52:55], v[24:27]\n"
"v_mfma_f32_16x16x32_bf16 v[28:31], a[72:75], a[56:59], v[28:31]\n"
"ds_read_b128 a[192:195], v249 offset:38912\n"
"buffer_load_dwordx4 a[72:75], v240, s[36:39], s80 offen offset:2048 sc1\n"
"v_mfma_f32_16x16x32_bf16 v[28:31], a[76:79], a[60:63], v[28:31]\n"
"s_waitcnt vmcnt(12)\n"
"v_mfma_f32_16x16x32_bf16 v[32:35], a[80:83], a[0:3], v[32:35]\n"
"v_mfma_f32_16x16x32_bf16 v[32:35], a[84:87], a[4:7], v[32:35]\n"
"ds_read_b128 a[196:199], v249 offset:40960\n"
"v_mfma_f32_16x16x32_bf16 v[36:39], a[80:83], a[8:11], v[36:39]\n"
"v_mfma_f32_16x16x32_bf16 v[36:39], a[84:87], a[12:15], v[36:39]\n"
"v_mfma_f32_16x16x32_bf16 v[40:43], a[80:83], a[16:19], v[40:43]\n"
"ds_read_b128 a[200:203], v249 offset:43008\n"
"v_mfma_f32_16x16x32_bf16 v[40:43], a[84:87], a[20:23], v[40:43]\n"
"v_mfma_f32_16x16x32_bf16 v[44:47], a[80:83], a[24:27], v[44:47]\n"
"buffer_load_dwordx4 a[76:79], v240, s[36:39], s80 offen offset:3072 sc1\n"
"v_mfma_f32_16x16x32_bf16 v[44:47], a[84:87], a[28:31], v[44:47]\n"
"s_add_u32 s80, s80, 0x1000\n"
"ds_read_b128 a[204:207], v249 offset:45056\n"
"v_mfma_f32_16x16x32_bf16 v[48:51], a[80:83], a[32:35], v[48:51]\n"
"v_mfma_f32_16x16x32_bf16 v[48:51], a[84:87], a[36:39], v[48:51]\n"
"v_mfma_f32_16x16x32_bf16 v[52:55], a[80:83], a[40:43], v[52:55]\n"
"ds_read_b128 a[176:179], v249 offset:47104\n"
"v_mfma_f32_16x16x32_bf16 v[52:55], a[84:87], a[44:47], v[52:55]\n"
"v_mfma_f32_16x16x32_bf16 v[56:59], a[80:83], a[48:51], v[56:59]\n"
"v_mfma_f32_16x16x32_bf16 v[56:59], a[84:87], a[52:55], v[56:59]\n"
"v_mfma_f32_16x16x32_bf16 v[60:63], a[80:83], a[56:59], v[60:63]\n"
"buffer_load_dwordx4 a[80:83], v240, s[36:39], s80 offen offset:0 sc1\n"
"v_mfma_f32_16x16x32_bf16 v[60:63], a[84:87], a[60:63], v[60:63]\n"
"s_waitcnt vmcnt(12)\n"
"v_mfma_f32_16x16x32_bf16 v[64:67], a[88:91], a[0:3], v[64:67]\n"
"v_mfma_f32_16x16x32_bf16 v[64:67], a[92:95], a[4:7], v[64:67]\n"
"v_mfma_f32_16x16x32_bf16 v[68:71], a[88:91], a[8:11], v[68:71]\n"
"v_mfma_f32_16x16x32_bf16 v[68:71], a[92:95], a[12:15], v[68:71]\n"
"v_mfma_f32_16x16x32_bf16 v[72:75], a[88:91], a[16:19], v[72:75]\n"
"v_mfma_f32_16x16x32_bf16 v[72:75], a[92:95], a[20:23], v[72:75]\n"
"v_mfma_f32_16x16x32_bf16 v[76:79], a[88:91], a[24:27], v[76:79]\n"
"buffer_load_dwordx4 a[84:87], v240, s[36:39], s80 offen offset:1024 sc1\n"
"v_mfma_f32_16x16x32_bf16 v[76:79], a[92:95], a[28:31], v[76:79]\n"
"v_mfma_f32_16x16x32_bf16 v[80:83], a[88:91], a[32:35], v[80:83]\n"
"v_mfma_f32_16x16x32_bf16 v[80:83], a[92:95], a[36:39], v[80:83]\n"
"v_mfma_f32_16x16x32_bf16 v[84:87], a[88:91], a[40:43], v[84:87]\n"
"v_mfma_f32_16x16x32_bf16 v[84:87], a[92:95], a[44:47], v[84:87]\n"
"v_mfma_f32_16x16x32_bf16 v[88:91], a[88:91], a[48:51], v[88:91]\n"
"v_mfma_f32_16x16x32_bf16 v[88:91], a[92:95], a[52:55], v[88:91]\n"
"v_mfma_f32_16x16x32_bf16 v[92:95], a[88:91], a[56:59], v[92:95]\n"
"buffer_load_dwordx4 a[88:91], v240, s[36:39], s80 offen offset:2048 sc1\n"
"v_mfma_f32_16x16x32_bf16 v[92:95], a[92:95], a[60:63], v[92:95]\n"
"s_waitcnt vmcnt(12)\n"
"v_mfma_f32_16x16x32_bf16 v[96:99], a[96:99], a[0:3], v[96:99]\n"
"ds_read_b128 a[0:3], v247 offset:16384\n"
"v_mfma_f32_16x16x32_bf16 v[96:99], a[100:103], a[4:7], v[96:99]\n"
"ds_read_b128 a[4:7], v248 offset:16384\n"
"v_mfma_f32_16x16x32_bf16 v[100:103], a[96:99], a[8:11], v[100:103]\n"
"ds_read_b128 a[8:11], v247 offset:18432\n"
"v_mfma_f32_16x16x32_bf16 v[100:103], a[100:103], a[12:15], v[100:103]\n"
"ds_read_b128 a[12:15], v248 offset:18432\n"
"v_mfma_f32_16x16x32_bf16 v[104:107], a[96:99], a[16:19], v[104:107]\n"
"ds_read_b128 a[16:19], v247 offset:20480\n"
"v_mfma_f32_16x16x32_bf16 v[104:107], a[100:103], a[20:23], v[104:107]\n"
"ds_read_b128 a[20:23], v248 offset:20480\n"
"v_mfma_f32_16x16x32_bf16 v[108:111], a[96:99], a[24:27], v[108:111]\n"
"ds_read_b128 a[24:27], v247 offset:22528\n"
"buffer_load_dwordx4 a[92:95], v240, s[36:39], s80 offen offset:3072 sc1\n"
"v_mfma_f32_16x16x32_bf16 v[108:111], a[100:103], a[28:31], v[108:111]\n"
"s_add_u32 s80, s80, 0x1000\n"
"ds_read_b128 a[28:31], v248 offset:22528\n"
"v_mfma_f32_16x16x32_bf16 v[112:115], a[96:99], a[32:35], v[112:115]\n"
"ds_read_b128 a[32:35], v247 offset:24576\n"
"v_mfma_f32_16x16x32_bf16 v[112:115], a[100:103], a[36:39], v[112:115]\n"
"ds_read_b128 a[36:39], v248 offset:24576\n"
"v_mfma_f32_16x16x32_bf16 v[116:119], a[96:99], a[40:43], v[116:119]\n"
"ds_read_b128 a[40:43], v247 offset:26624\n"
"v_mfma_f32_16x16x32_bf16 v[116:119], a[100:103], a[44:47], v[116:119]\n"
"ds_read_b128 a[44:47], v248 offset:26624\n"
"v_mfma_f32_16x16x32_bf16 v[120:123], a[96:99], a[48:51], v[120:123]\n"
"ds_read_b128 a[48:51], v247 offset:28672\n"
"v_mfma_f32_16x16x32_bf16 v[120:123], a[100:103], a[52:55], v[120:123]\n"
"ds_read_b128 a[52:55], v248 offset:28672\n"
"v_mfma_f32_16x16x32_bf16 v[124:127], a[96:99], a[56:59], v[124:127]\n"
"ds_read_b128 a[56:59], v247 offset:30720\n"
"buffer_load_dwordx4 a[96:99], v240, s[36:39], s80 offen offset:0 sc1\n"
"v_mfma_f32_16x16x32_bf16 v[124:127], a[100:103], a[60:63], v[124:127]\n"
"s_waitcnt vmcnt(12) lgkmcnt(11)\n"
"ds_read_b128 a[60:63], v248 offset:30720\n"
"v_mfma_f32_16x16x32_bf16 v[0:3], a[104:107], a[0:3], v[0:3]\n"
"v_mfma_f32_16x16x32_bf16 v[0:3], a[108:111], a[4:7], v[0:3]\n"
"v_add_u32 v247, 131072, v240\n"
"v_add_u32 v248, 132096, v240\n"
"v_mfma_f32_16x16x32_bf16 v[4:7], a[104:107], a[8:11], v[4:7]\n"
"v_mfma_f32_16x16x32_bf16 v[4:7], a[108:111], a[12:15], v[4:7]\n"
"s_waitcnt lgkmcnt(8)\n"
"v_mfma_f32_16x16x32_bf16 v[8:11], a[104:107], a[16:19], v[8:11]\n"
"v_mfma_f32_16x16x32_bf16 v[8:11], a[108:111], a[20:23], v[8:11]\n"
"v_mfma_f32_16x16x32_bf16 v[12:15], a[104:107], a[24:27], v[12:15]\n"
"buffer_load_dwordx4 a[100:103], v240, s[36:39], s80 offen offset:1024 sc1\n"
"v_mfma_f32_16x16x32_bf16 v[12:15], a[108:111], a[28:31], v[12:15]\n"
"s_waitcnt lgkmcnt(4)\n"
"v_mfma_f32_16x16x32_bf16 v[16:19], a[104:107], a[32:35], v[16:19]\n"
"v_mfma_f32_16x16x32_bf16 v[16:19], a[108:111], a[36:39], v[16:19]\n"
"v_mfma_f32_16x16x32_bf16 v[20:23], a[104:107], a[40:43], v[20:23]\n"
"v_mfma_f32_16x16x32_bf16 v[20:23], a[108:111], a[44:47], v[20:23]\n"
"s_waitcnt lgkmcnt(0)\n"
"v_mfma_f32_16x16x32_bf16 v[24:27], a[104:107], a[48:51], v[24:27]\n"
"v_mfma_f32_16x16x32_bf16 v[24:27], a[108:111], a[52:55], v[24:27]\n"
"v_mfma_f32_16x16x32_bf16 v[28:31], a[104:107], a[56:59], v[28:31]\n"
"buffer_load_dwordx4 a[104:107], v240, s[36:39], s80 offen offset:2048 sc1\n"
"v_mfma_f32_16x16x32_bf16 v[28:31], a[108:111], a[60:63], v[28:31]\n"
"s_waitcnt vmcnt(12)\n"
"v_mfma_f32_16x16x32_bf16 v[32:35], v[224:227], a[0:3], v[32:35]\n"
"v_mfma_f32_16x16x32_bf16 v[32:35], v[228:231], a[4:7], v[32:35]\n"
"v_mfma_f32_16x16x32_bf16 v[36:39], v[224:227], a[8:11], v[36:39]\n"
"s_waitcnt vmcnt(10)\n"
"v_cmp_ne_u32 vcc, s33, v241\n"
"v_mfma_f32_16x16x32_bf16 v[36:39], v[228:231], a[12:15], v[36:39]\n"
"v_mfma_f32_16x16x32_bf16 v[40:43], v[224:227], a[16:19], v[40:43]\n"
"s_cmp_eq_u64 vcc, 0\n"
"s_cbranch_scc0 .Lslow7_%=\n"
".Lback7_%=:\n"
"v_mfma_f32_16x16x32_bf16 v[40:43], v[228:231], a[20:23], v[40:43]\n"
"v_mfma_f32_16x16x32_bf16 v[44:47], v[224:227], a[24:27], v[44:47]\n"
"buffer_load_dwordx4 a[108:111], v240, s[36:39], s80 offen offset:3072 sc1\n"
"v_mfma_f32_16x16x32_bf16 v[44:47], v[228:231], a[28:31], v[44:47]\n"
"s_add_u32 s80, s80, 0x1000\n"
"v_mfma_f32_16x16x32_bf16 v[48:51], v[224:227], a[32:35], v[48:51]\n"
"ds_read_b128 v[232:235], v249 offset:14336\n"
"v_mfma_f32_16x16x32_bf16 v[48:51], v[228:231], a[36:39], v[48:51]\n"
"v_mfma_f32_16x16x32_bf16 v[52:55], v[224:227], a[40:43], v[52:55]\n"
"v_mfma_f32_16x16x32_bf16 v[52:55], v[228:231], a[44:47], v[52:55]\n"
"v_mfma_f32_16x16x32_bf16 v[56:59], v[224:227], a[48:51], v[56:59]\n"
"v_mfma_f32_16x16x32_bf16 v[56:59], v[228:231], a[52:55], v[56:59]\n"
"v_mfma_f32_16x16x32_bf16 v[60:63], v[224:227], a[56:59], v[60:63]\n"
"buffer_load_dwordx4 v[224:227], v240, s[36:39], s80 offen offset:0 sc1\n"
"v_mfma_f32_16x16x32_bf16 v[60:63], v[228:231], a[60:63], v[60:63]\n"
"s_waitcnt vmcnt(11)\n"
"v_mfma_f32_16x16x32_bf16 v[64:67], a[64:67], a[0:3], v[64:67]\n"
"v_mfma_f32_16x16x32_bf16 v[64:67], a[68:71], a[4:7], v[64:67]\n"
"v_mfma_f32_16x16x32_bf16 v[68:71], a[64:67], a[8:11], v[68:71]\n"
"v_mfma_f32_16x16x32_bf16 v[68:71], a[68:71], a[12:15], v[68:71]\n"
"v_mfma_f32_16x16x32_bf16 v[72:75], a[64:67], a[16:19], v[72:75]\n"
"s_waitcnt lgkmcnt(0)\n"
"ds_read_b128 v[236:239], v249 offset:0\n"
"v_lshlrev_b32 v251, 16, v232\n"
"v_mfma_f32_16x16x32_bf16 v[72:75], a[68:71], a[20:23], v[72:75]\n"
"v_fmac_f32 v251, v251, v28\n"
"v_and_b32 v232, s82, v232\n"
"v_mfma_f32_16x16x32_bf16 v[76:79], a[64:67], a[24:27], v[76:79]\n"
"buffer_load_dwordx4 v[228:231], v240, s[36:39], s80 offen offset:1024 sc1\n"
"v_fmac_f32 v232, v232, v29\n"
"v_mfma_f32_16x16x32_bf16 v[76:79], a[68:71], a[28:31], v[76:79]\n"
"v_cvt_pk_bf16_f32 v28, v251, v232\n"
"v_lshlrev_b32 v251, 16, v233\n"
"v_mfma_f32_16x16x32_bf16 v[80:83], a[64:67], a[32:35], v[80:83]\n"
"v_fmac_f32 v251, v251, v30\n"
"v_and_b32 v233, s82, v233\n"
"v_mfma_f32_16x16x32_bf16 v[80:83], a[68:71], a[36:39], v[80:83]\n"
"v_fmac_f32 v233, v233, v31\n"
"v_cvt_pk_bf16_f32 v29, v251, v233\n"
"v_mfma_f32_16x16x32_bf16 v[84:87], a[64:67], a[40:43], v[84:87]\n"
"v_lshlrev_b32 v251, 16, v234\n"
"v_fmac_f32 v251, v251, v60\n"
"v_mfma_f32_16x16x32_bf16 v[84:87], a[68:71], a[44:47], v[84:87]\n"
"v_and_b32 v234, s82, v234\n"
"v_fmac_f32 v234, v234, v61\n"
"v_mfma_f32_16x16x32_bf16 v[88:91], a[64:67], a[48:51], v[88:91]\n"
"v_cvt_pk_bf16_f32 v30, v251, v234\n"
"v_lshlrev_b32 v251, 16, v235\n"
"v_mfma_f32_16x16x32_bf16 v[88:91], a[68:71], a[52:55], v[88:91]\n"
"v_fmac_f32 v251, v251, v62\n"
"v_and_b32 v235, s82, v235\n"
"v_mfma_f32_16x16x32_bf16 v[92:95], a[64:67], a[56:59], v[92:95]\n"
"buffer_load_dwordx4 a[64:67], v240, s[36:39], s80 offen offset:2048 sc1\n"
"v_fmac_f32 v235, v235, v63\n"
"v_mfma_f32_16x16x32_bf16 v[92:95], a[68:71], a[60:63], v[92:95]\n"
"s_waitcnt vmcnt(11)\n"
"v_cvt_pk_bf16_f32 v31, v251, v235\n"
"v_mfma_f32_16x16x32_bf16 v[96:99], a[72:75], a[0:3], v[96:99]\n"
"buffer_store_dwordx4 v[28:31], v240, s[52:55], s79 offen offset:0 sc1\n"
"v_mfma_f32_16x16x32_bf16 v[96:99], a[76:79], a[4:7], v[96:99]\n"
"v_mfma_f32_16x16x32_bf16 v[100:103], a[72:75], a[8:11], v[100:103]\n"
"v_mfma_f32_16x16x32_bf16 v[100:103], a[76:79], a[12:15], v[100:103]\n"
"v_mfma_f32_16x16x32_bf16 v[104:107], a[72:75], a[16:19], v[104:107]\n"
"v_mfma_f32_16x16x32_bf16 v[104:107], a[76:79], a[20:23], v[104:107]\n"
"v_mfma_f32_16x16x32_bf16 v[108:111], a[72:75], a[24:27], v[108:111]\n"
"buffer_load_dwordx4 a[68:71], v240, s[36:39], s80 offen offset:3072 sc1\n"
"v_mfma_f32_16x16x32_bf16 v[108:111], a[76:79], a[28:31], v[108:111]\n"
"s_add_u32 s80, s80, 0x1000\n"
"v_mfma_f32_16x16x32_bf16 v[112:115], a[72:75], a[32:35], v[112:115]\n"
"v_mfma_f32_16x16x32_bf16 v[112:115], a[76:79], a[36:39], v[112:115]\n"
"v_mfma_f32_16x16x32_bf16 v[116:119], a[72:75], a[40:43], v[116:119]\n"
"v_mfma_f32_16x16x32_bf16 v[116:119], a[76:79], a[44:47], v[116:119]\n"
"v_mfma_f32_16x16x32_bf16 v[120:123], a[72:75], a[48:51], v[120:123]\n"
"v_mfma_f32_16x16x32_bf16 v[120:123], a[76:79], a[52:55], v[120:123]\n"
"v_mfma_f32_16x16x32_bf16 v[124:127], a[72:75], a[56:59], v[124:127]\n"
"buffer_load_dwordx4 a[72:75], v240, s[36:39], s80 offen offset:0 sc1\n"
"v_mfma_f32_16x16x32_bf16 v[124:127], a[76:79], a[60:63], v[124:127]\n"
"s_waitcnt vmcnt(12) lgkmcnt(0)\n"
"ds_read_b128 v[232:235], v249 offset:2048\n"
"v_lshlrev_b32 v251, 16, v236\n"
"v_mfma_f32_16x16x32_bf16 v[128:131], a[80:83], a[0:3], v[128:131]\n"
"v_fmac_f32 v251, v251, v0\n"
"v_and_b32 v236, s82, v236\n"
"v_mfma_f32_16x16x32_bf16 v[128:131], a[84:87], a[4:7], v[128:131]\n"
"v_fmac_f32 v236, v236, v1\n"
"v_cvt_pk_bf16_f32 v0, v251, v236\n"
"v_mfma_f32_16x16x32_bf16 v[132:135], a[80:83], a[8:11], v[132:135]\n"
"v_lshlrev_b32 v251, 16, v237\n"
"v_fmac_f32 v251, v251, v2\n"
"v_mfma_f32_16x16x32_bf16 v[132:135], a[84:87], a[12:15], v[132:135]\n"
"v_and_b32 v237, s82, v237\n"
"v_fmac_f32 v237, v237, v3\n"
"v_mfma_f32_16x16x32_bf16 v[136:139], a[80:83], a[16:19], v[136:139]\n"
"v_cvt_pk_bf16_f32 v1, v251, v237\n"
"v_lshlrev_b32 v251, 16, v238\n"
"v_mfma_f32_16x16x32_bf16 v[136:139], a[84:87], a[20:23], v[136:139]\n"
"v_fmac_f32 v251, v251, v32\n"
"v_and_b32 v238, s82, v238\n"
"v_mfma_f32_16x16x32_bf16 v[140:143], a[80:83], a[24:27], v[140:143]\n"
"buffer_load_dwordx4 a[76:79], v240, s[36:39], s80 offen offset:1024 sc1\n"
"v_fmac_f32 v238, v238, v33\n"
"v_mfma_f32_16x16x32_bf16 v[140:143], a[84:87], a[28:31], v[140:143]\n"
"v_cvt_pk_bf16_f32 v2, v251, v238\n"
"v_lshlrev_b32 v251, 16, v239\n"
"v_mfma_f32_16x16x32_bf16 v[144:147], a[80:83], a[32:35], v[144:147]\n"
"v_fmac_f32 v251, v251, v34\n"
"v_and_b32 v239, s82, v239\n"
"v_mfma_f32_16x16x32_bf16 v[144:147], a[84:87], a[36:39], v[144:147]\n"
"v_fmac_f32 v239, v239, v35\n"
"v_cvt_pk_bf16_f32 v3, v251, v239\n"
"v_mfma_f32_16x16x32_bf16 v[148:151], a[80:83], a[40:43], v[148:151]\n"
"buffer_store_dwordx4 v[0:3], v240, s[52:55], s68 offen offset:0 sc1\n"
"v_mfma_f32_16x16x32_bf16 v[148:151], a[84:87], a[44:47], v[148:151]\n"
"v_mfma_f32_16x16x32_bf16 v[152:155], a[80:83], a[48:51], v[152:155]\n"
"v_mfma_f32_16x16x32_bf16 v[152:155], a[84:87], a[52:55], v[152:155]\n"
"v_mfma_f32_16x16x32_bf16 v[156:159], a[80:83], a[56:59], v[156:159]\n"
"buffer_load_dwordx4 a[80:83], v240, s[36:39], s80 offen offset:2048 sc1\n"
"v_mfma_f32_16x16x32_bf16 v[156:159], a[84:87], a[60:63], v[156:159]\n"
"s_waitcnt vmcnt(13)\n"
"v_mfma_f32_16x16x32_bf16 v[160:163], a[88:91], a[0:3], v[160:163]\n"
"v_mfma_f32_16x16x32_bf16 v[160:163], a[92:95], a[4:7], v[160:163]\n"
"v_mfma_f32_16x16x32_bf16 v[164:167], a[88:91], a[8:11], v[164:167]\n"
"v_mfma_f32_16x16x32_bf16 v[164:167], a[92:95], a[12:15], v[164:167]\n"
"v_mfma_f32_16x16x32_bf16 v[168:171], a[88:91], a[16:19], v[168:171]\n"
"v_mfma_f32_16x16x32_bf16 v[168:171], a[92:95], a[20:23], v[168:171]\n"
"v_mfma_f32_16x16x32_bf16 v[172:175], a[88:91], a[24:27], v[172:175]\n"
"buffer_load_dwordx4 a[84:87], v240, s[36:39], s80 offen offset:3072 sc1\n"
"v_mfma_f32_16x16x32_bf16 v[172:175], a[92:95], a[28:31], v[172:175]\n"
"s_add_u32 s80, s80, 0x1000\n"
"v_mfma_f32_16x16x32_bf16 v[176:179], a[88:91], a[32:35], v[176:179]\n"
"v_mfma_f32_16x16x32_bf16 v[176:179], a[92:95], a[36:39], v[176:179]\n"
"v_mfma_f32_16x16x32_bf16 v[180:183], a[88:91], a[40:43], v[180:183]\n"
"s_waitcnt lgkmcnt(0)\n"
"ds_read_b128 v[236:239], v249 offset:4096\n"
"v_lshlrev_b32 v251, 16, v232\n"
"v_mfma_f32_16x16x32_bf16 v[180:183], a[92:95], a[44:47], v[180:183]\n"
"v_fmac_f32 v251, v251, v4\n"
"v_and_b32 v232, s82, v232\n"
"v_mfma_f32_16x16x32_bf16 v[184:187], a[88:91], a[48:51], v[184:187]\n"
"v_fmac_f32 v232, v232, v5\n"
"v_cvt_pk_bf16_f32 v4, v251, v232\n"
"v_mfma_f32_16x16x32_bf16 v[184:187], a[92:95], a[52:55], v[184:187]\n"
"v_lshlrev_b32 v251, 16, v233\n"
"v_fmac_f32 v251, v251, v6\n"
"v_mfma_f32_16x16x32_bf16 v[188:191], a[88:91], a[56:59], v[188:191]\n"
"buffer_load_dwordx4 a[88:91], v240, s[36:39], s80 offen offset:0 sc1\n"
"v_and_b32 v233, s82, v233\n"
"v_mfma_f32_16x16x32_bf16 v[188:191], a[92:95], a[60:63], v[188:191]\n"
"s_waitcnt vmcnt(13)\n"
"v_fmac_f32 v233, v233, v7\n"
"v_cvt_pk_bf16_f32 v5, v251, v233\n"
"v_mfma_f32_16x16x32_bf16 v[192:195], a[96:99], a[0:3], v[192:195]\n"
"v_lshlrev_b32 v251, 16, v234\n"
"v_fmac_f32 v251, v251, v36\n"
"v_mfma_f32_16x16x32_bf16 v[192:195], a[100:103], a[4:7], v[192:195]\n"
"v_and_b32 v234, s82, v234\n"
"v_fmac_f32 v234, v234, v37\n"
"v_mfma_f32_16x16x32_bf16 v[196:199], a[96:99], a[8:11], v[196:199]\n"
"v_cvt_pk_bf16_f32 v6, v251, v234\n"
"v_lshlrev_b32 v251, 16, v235\n"
"v_mfma_f32_16x16x32_bf16 v[196:199], a[100:103], a[12:15], v[196:199]\n"
"v_fmac_f32 v251, v251, v38\n"
"v_and_b32 v235, s82, v235\n"
"v_mfma_f32_16x16x32_bf16 v[200:203], a[96:99], a[16:19], v[200:203]\n"
"v_fmac_f32 v235, v235, v39\n"
"v_cvt_pk_bf16_f32 v7, v251, v235\n"
"v_mfma_f32_16x16x32_bf16 v[200:203], a[100:103], a[20:23], v[200:203]\n"
"v_mfma_f32_16x16x32_bf16 v[204:207], a[96:99], a[24:27], v[204:207]\n"
"buffer_load_dwordx4 a[92:95], v240, s[36:39], s80 offen offset:1024 sc1\n"
"v_mfma_f32_16x16x32_bf16 v[204:207], a[100:103], a[28:31], v[204:207]\n"
"buffer_store_dwordx4 v[4:7], v240, s[52:55], s69 offen offset:0 sc1\n"
"v_mfma_f32_16x16x32_bf16 v[208:211], a[96:99], a[32:35], v[208:211]\n"
"v_mfma_f32_16x16x32_bf16 v[208:211], a[100:103], a[36:39], v[208:211]\n"
"v_mfma_f32_16x16x32_bf16 v[212:215], a[96:99], a[40:43], v[212:215]\n"
"v_mfma_f32_16x16x32_bf16 v[212:215], a[100:103], a[44:47], v[212:215]\n"
"v_mfma_f32_16x16x32_bf16 v[216:219], a[96:99], a[48:51], v[216:219]\n"
"v_mfma_f32_16x16x32_bf16 v[216:219], a[100:103], a[52:55], v[216:219]\n"
"v_mfma_f32_16x16x32_bf16 v[220:223], a[96:99], a[56:59], v[220:223]\n"
"buffer_load_dwordx4 a[96:99], v240, s[36:39], s80 offen offset:2048 sc1\n"
"v_mfma_f32_16x16x32_bf16 v[220:223], a[100:103], a[60:63], v[220:223]\n"
"s_waitcnt vmcnt(14)\n"
"v_mfma_f32_16x16x32_bf16 a[112:115], a[104:107], a[0:3], a[112:115]\n"
"ds_read_b128 a[0:3], v243 offset:0\n"
"v_mfma_f32_16x16x32_bf16 a[112:115], a[108:111], a[4:7], a[112:115]\n"
"ds_read_b128 a[4:7], v244 offset:0\n"
"v_mfma_f32_16x16x32_bf16 a[116:119], a[104:107], a[8:11], a[116:119]\n"
"ds_read_b128 a[8:11], v243 offset:2048\n"
"v_mfma_f32_16x16x32_bf16 a[116:119], a[108:111], a[12:15], a[116:119]\n"
"ds_read_b128 a[12:15], v244 offset:2048\n"
"v_mfma_f32_16x16x32_bf16 a[120:123], a[104:107], a[16:19], a[120:123]\n"
"ds_read_b128 a[16:19], v243 offset:4096\n"
"v_mfma_f32_16x16x32_bf16 a[120:123], a[108:111], a[20:23], a[120:123]\n"
"s_waitcnt lgkmcnt(5)\n"
"ds_read_b128 a[20:23], v244 offset:4096\n"
"v_lshlrev_b32 v251, 16, v236\n"
"v_mfma_f32_16x16x32_bf16 a[124:127], a[104:107], a[24:27], a[124:127]\n"
"ds_read_b128 a[24:27], v243 offset:6144\n"
"buffer_load_dwordx4 a[100:103], v240, s[36:39], s80 offen offset:3072 sc1\n"
"v_mfma_f32_16x16x32_bf16 a[124:127], a[108:111], a[28:31], a[124:127]\n"
"s_add_u32 s80, s80, 0x1000\n"
"ds_read_b128 a[28:31], v244 offset:6144\n"
"v_fmac_f32 v251, v251, v8\n"
"v_mfma_f32_16x16x32_bf16 a[128:131], a[104:107], a[32:35], a[128:131]\n"
"ds_read_b128 a[32:35], v243 offset:8192\n"
"v_and_b32 v236, s82, v236\n"
"v_mfma_f32_16x16x32_bf16 a[128:131], a[108:111], a[36:39], a[128:131]\n"
"ds_read_b128 a[36:39], v244 offset:8192\n"
"v_fmac_f32 v236, v236, v9\n"
"v_mfma_f32_16x16x32_bf16 a[132:135], a[104:107], a[40:43], a[132:135]\n"
"ds_read_b128 a[40:43], v243 offset:10240\n"
"v_cvt_pk_bf16_f32 v8, v251, v236\n"
"v_mfma_f32_16x16x32_bf16 a[132:135], a[108:111], a[44:47], a[132:135]\n"
"ds_read_b128 a[44:47], v244 offset:10240\n"
"v_lshlrev_b32 v251, 16, v237\n"
"v_mfma_f32_16x16x32_bf16 a[136:139], a[104:107], a[48:51], a[136:139]\n"
"ds_read_b128 a[48:51], v243 offset:12288\n"
"v_fmac_f32 v251, v251, v10\n"
"v_mfma_f32_16x16x32_bf16 a[136:139], a[108:111], a[52:55], a[136:139]\n"
"ds_read_b128 a[52:55], v244 offset:12288\n"
"v_and_b32 v237, s82, v237\n"
"v_mfma_f32_16x16x32_bf16 a[140:143], a[104:107], a[56:59], a[140:143]\n"
"ds_read_b128 a[56:59], v243 offset:14336\n"
"buffer_load_dwordx4 a[104:107], v240, s[36:39], s80 offen offset:0 sc1\n"
"v_mfma_f32_16x16x32_bf16 a[140:143], a[108:111], a[60:63], a[140:143]\n"
"s_waitcnt vmcnt(14) lgkmcnt(11)\n"
"ds_read_b128 a[60:63], v244 offset:14336\n"
"v_fmac_f32 v237, v237, v11\n"
"v_mfma_f32_16x16x32_bf16 v[128:131], v[224:227], a[0:3], v[128:131]\n"
"ds_read_b128 v[232:235], v249 offset:6144\n"
"v_cvt_pk_bf16_f32 v9, v251, v237\n"
"v_mfma_f32_16x16x32_bf16 v[128:131], v[228:231], a[4:7], v[128:131]\n"
"v_lshlrev_b32 v251, 16, v238\n"
"v_fmac_f32 v251, v251, v40\n"
"v_mfma_f32_16x16x32_bf16 v[132:135], v[224:227], a[8:11], v[132:135]\n"
"v_and_b32 v238, s82, v238\n"
"v_fmac_f32 v238, v238, v41\n"
"v_mfma_f32_16x16x32_bf16 v[132:135], v[228:231], a[12:15], v[132:135]\n"
"s_waitcnt lgkmcnt(9)\n"
"v_cvt_pk_bf16_f32 v10, v251, v238\n"
"v_lshlrev_b32 v251, 16, v239\n"
"v_mfma_f32_16x16x32_bf16 v[136:139], v[224:227], a[16:19], v[136:139]\n"
"v_fmac_f32 v251, v251, v42\n"
"v_and_b32 v239, s82, v239\n"
"v_mfma_f32_16x16x32_bf16 v[136:139], v[228:231], a[20:23], v[136:139]\n"
"v_fmac_f32 v239, v239, v43\n"
"v_cvt_pk_bf16_f32 v11, v251, v239\n"
"v_mfma_f32_16x16x32_bf16 v[140:143], v[224:227], a[24:27], v[140:143]\n"
"buffer_load_dwordx4 a[108:111], v240, s[36:39], s80 offen offset:1024 sc1\n"
"v_mfma_f32_16x16x32_bf16 v[140:143], v[228:231], a[28:31], v[140:143]\n"
"s_waitcnt lgkmcnt(5)\n"
"buffer_store_dwordx4 v[8:11], v240, s[52:55], s70 offen offset:0 sc1\n"
"v_mfma_f32_16x16x32_bf16 v[144:147], v[224:227], a[32:35], v[144:147]\n"
"v_mfma_f32_16x16x32_bf16 v[144:147], v[228:231], a[36:39], v[144:147]\n"
"v_mfma_f32_16x16x32_bf16 v[148:151], v[224:227], a[40:43], v[148:151]\n"
"v_mfma_f32_16x16x32_bf16 v[148:151], v[228:231], a[44:47], v[148:151]\n"
"s_waitcnt lgkmcnt(1)\n"
"v_mfma_f32_16x16x32_bf16 v[152:155], v[224:227], a[48:51], v[152:155]\n"
"v_mfma_f32_16x16x32_bf16 v[152:155], v[228:231], a[52:55], v[152:155]\n"
"v_mfma_f32_16x16x32_bf16 v[156:159], v[224:227], a[56:59], v[156:159]\n"
"buffer_load_dwordx4 v[224:227], v240, s[36:39], s80 offen offset:2048 sc1\n"
"v_mfma_f32_16x16x32_bf16 v[156:159], v[228:231], a[60:63], v[156:159]\n"
"s_waitcnt vmcnt(14)\n"
"v_mfma_f32_16x16x32_bf16 v[160:163], a[64:67], a[0:3], v[160:163]\n"
"s_waitcnt lgkmcnt(0)\n"
"ds_read_b128 v[236:239], v249 offset:8192\n"
"v_lshlrev_b32 v251, 16, v232\n"
"v_mfma_f32_16x16x32_bf16 v[160:163], a[68:71], a[4:7], v[160:163]\n"
"v_fmac_f32 v251, v251, v12\n"
"v_and_b32 v232, s82, v232\n"
"v_mfma_f32_16x16x32_bf16 v[164:167], a[64:67], a[8:11], v[164:167]\n"
"v_fmac_f32 v232, v232, v13\n"
"v_cvt_pk_bf16_f32 v12, v251, v232\n"
"v_mfma_f32_16x16x32_bf16 v[164:167], a[68:71], a[12:15], v[164:167]\n"
"v_lshlrev_b32 v251, 16, v233\n"
"v_fmac_f32 v251, v251, v14\n"
"v_mfma_f32_16x16x32_bf16 v[168:171], a[64:67], a[16:19], v[168:171]\n"
"v_and_b32 v233, s82, v233\n"
"v_fmac_f32 v233, v233, v15\n"
"v_mfma_f32_16x16x32_bf16 v[168:171], a[68:71], a[20:23], v[168:171]\n"
"v_cvt_pk_bf16_f32 v13, v251, v233\n"
"v_lshlrev_b32 v251, 16, v234\n"
"v_mfma_f32_16x16x32_bf16 v[172:175], a[64:67], a[24:27], v[172:175]\n"
"buffer_load_dwordx4 v[228:231], v240, s[36:39], s80 offen offset:3072 sc1\n"
"v_fmac_f32 v251, v251, v44\n"
"v_mfma_f32_16x16x32_bf16 v[172:175], a[68:71], a[28:31], v[172:175]\n"
"s_add_u32 s80, s80, 0x1000\n"
"v_and_b32 v234, s82, v234\n"
"v_fmac_f32 v234, v234, v45\n"
"v_mfma_f32_16x16x32_bf16 v[176:179], a[64:67], a[32:35], v[176:179]\n"
"v_cvt_pk_bf16_f32 v14, v251, v234\n"
"v_lshlrev_b32 v251, 16, v235\n"
"v_mfma_f32_16x16x32_bf16 v[176:179], a[68:71], a[36:39], v[176:179]\n"
"v_fmac_f32 v251, v251, v46\n"
"v_and_b32 v235, s82, v235\n"
"v_mfma_f32_16x16x32_bf16 v[180:183], a[64:67], a[40:43], v[180:183]\n"
"v_fmac_f32 v235, v235, v47\n"
"v_cvt_pk_bf16_f32 v15, v251, v235\n"
"v_mfma_f32_16x16x32_bf16 v[180:183], a[68:71], a[44:47], v[180:183]\n"
"buffer_store_dwordx4 v[12:15], v240, s[52:55], s71 offen offset:0 sc1\n"
"v_mfma_f32_16x16x32_bf16 v[184:187], a[64:67], a[48:51], v[184:187]\n"
"v_mfma_f32_16x16x32_bf16 v[184:187], a[68:71], a[52:55], v[184:187]\n"
"v_mfma_f32_16x16x32_bf16 v[188:191], a[64:67], a[56:59], v[188:191]\n"
"buffer_load_dwordx4 a[64:67], v240, s[36:39], s80 offen offset:0 sc1\n"
"v_mfma_f32_16x16x32_bf16 v[188:191], a[68:71], a[60:63], v[188:191]\n"
"s_waitcnt vmcnt(15)\n"
"v_mfma_f32_16x16x32_bf16 v[192:195], a[72:75], a[0:3], v[192:195]\n"
"v_mfma_f32_16x16x32_bf16 v[192:195], a[76:79], a[4:7], v[192:195]\n"
"v_mfma_f32_16x16x32_bf16 v[196:199], a[72:75], a[8:11], v[196:199]\n"
"v_mfma_f32_16x16x32_bf16 v[196:199], a[76:79], a[12:15], v[196:199]\n"
"v_mfma_f32_16x16x32_bf16 v[200:203], a[72:75], a[16:19], v[200:203]\n"
"v_mfma_f32_16x16x32_bf16 v[200:203], a[76:79], a[20:23], v[200:203]\n"
"v_mfma_f32_16x16x32_bf16 v[204:207], a[72:75], a[24:27], v[204:207]\n"
"buffer_load_dwordx4 a[68:71], v240, s[36:39], s80 offen offset:1024 sc1\n"
"v_mfma_f32_16x16x32_bf16 v[204:207], a[76:79], a[28:31], v[204:207]\n"
"v_mfma_f32_16x16x32_bf16 v[208:211], a[72:75], a[32:35], v[208:211]\n"
"v_mfma_f32_16x16x32_bf16 v[208:211], a[76:79], a[36:39], v[208:211]\n"
"v_mfma_f32_16x16x32_bf16 v[212:215], a[72:75], a[40:43], v[212:215]\n"
"v_mfma_f32_16x16x32_bf16 v[212:215], a[76:79], a[44:47], v[212:215]\n"
"s_waitcnt lgkmcnt(0)\n"
"ds_read_b128 v[232:235], v249 offset:10240\n"
"v_lshlrev_b32 v251, 16, v236\n"
"v_mfma_f32_16x16x32_bf16 v[216:219], a[72:75], a[48:51], v[216:219]\n"
"v_fmac_f32 v251, v251, v16\n"
"v_and_b32 v236, s82, v236\n"
"v_mfma_f32_16x16x32_bf16 v[216:219], a[76:79], a[52:55], v[216:219]\n"
"v_fmac_f32 v236, v236, v17\n"
"v_cvt_pk_bf16_f32 v16, v251, v236\n"
"v_mfma_f32_16x16x32_bf16 v[220:223], a[72:75], a[56:59], v[220:223]\n"
"buffer_load_dwordx4 a[72:75], v240, s[36:39], s80 offen offset:2048 sc1\n"
"v_lshlrev_b32 v251, 16, v237\n"
"v_mfma_f32_16x16x32_bf16 v[220:223], a[76:79], a[60:63], v[220:223]\n"
"s_waitcnt vmcnt(14)\n"
"v_fmac_f32 v251, v251, v18\n"
"v_and_b32 v237, s82, v237\n"
"v_mfma_f32_16x16x32_bf16 a[112:115], a[80:83], a[0:3], a[112:115]\n"
"ds_read_b128 a[0:3], v243 offset:16384\n"
"v_fmac_f32 v237, v237, v19\n"
"v_mfma_f32_16x16x32_bf16 a[112:115], a[84:87], a[4:7], a[112:115]\n"
"ds_read_b128 a[4:7], v244 offset:16384\n"
"v_cvt_pk_bf16_f32 v17, v251, v237\n"
"v_mfma_f32_16x16x32_bf16 a[116:119], a[80:83], a[8:11], a[116:119]\n"
"ds_read_b128 a[8:11], v243 offset:18432\n"
"v_lshlrev_b32 v251, 16, v238\n"
"v_mfma_f32_16x16x32_bf16 a[116:119], a[84:87], a[12:15], a[116:119]\n"
"ds_read_b128 a[12:15], v244 offset:18432\n"
"v_fmac_f32 v251, v251, v48\n"
"v_mfma_f32_16x16x32_bf16 a[120:123], a[80:83], a[16:19], a[120:123]\n"
"ds_read_b128 a[16:19], v243 offset:20480\n"
"v_and_b32 v238, s82, v238\n"
"v_mfma_f32_16x16x32_bf16 a[120:123], a[84:87], a[20:23], a[120:123]\n"
"ds_read_b128 a[20:23], v244 offset:20480\n"
"v_fmac_f32 v238, v238, v49\n"
"v_mfma_f32_16x16x32_bf16 a[124:127], a[80:83], a[24:27], a[124:127]\n"
"ds_read_b128 a[24:27], v243 offset:22528\n"
"buffer_load_dwordx4 a[76:79], v240, s[36:39], s80 offen offset:3072 sc1\n"
"v_mfma_f32_16x16x32_bf16 a[124:127], a[84:87], a[28:31], a[124:127]\n"
"s_add_u32 s80, s80, 0x1000\n"
"ds_read_b128 a[28:31], v244 offset:22528\n"
"v_cvt_pk_bf16_f32 v18, v251, v238\n"
"v_mfma_f32_16x16x32_bf16 a[128:131], a[80:83], a[32:35], a[128:131]\n"
"ds_read_b128 a[32:35], v243 offset:24576\n"
"v_lshlrev_b32 v251, 16, v239\n"
"v_mfma_f32_16x16x32_bf16 a[128:131], a[84:87], a[36:39], a[128:131]\n"
"ds_read_b128 a[36:39], v244 offset:24576\n"
"v_fmac_f32 v251, v251, v50\n"
"v_mfma_f32_16x16x32_bf16 a[132:135], a[80:83], a[40:43], a[132:135]\n"
"ds_read_b128 a[40:43], v243 offset:26624\n"
"v_and_b32 v239, s82, v239\n"
"v_mfma_f32_16x16x32_bf16 a[132:135], a[84:87], a[44:47], a[132:135]\n"
"ds_read_b128 a[44:47], v244 offset:26624\n"
"v_fmac_f32 v239, v239, v51\n"
"v_mfma_f32_16x16x32_bf16 a[136:139], a[80:83], a[48:51], a[136:139]\n"
"ds_read_b128 a[48:51], v243 offset:28672\n"
"v_cvt_pk_bf16_f32 v19, v251, v239\n"
"v_mfma_f32_16x16x32_bf16 a[136:139], a[84:87], a[52:55], a[136:139]\n"
"ds_read_b128 a[52:55], v244 offset:28672\n"
"v_mfma_f32_16x16x32_bf16 a[140:143], a[80:83], a[56:59], a[140:143]\n"
"ds_read_b128 a[56:59], v243 offset:30720\n"
"buffer_load_dwordx4 a[80:83], v240, s[36:39], s80 offen offset:0 sc1\n"
"v_mfma_f32_16x16x32_bf16 a[140:143], a[84:87], a[60:63], a[140:143]\n"
"s_waitcnt vmcnt(14) lgkmcnt(11)\n"
"ds_read_b128 a[60:63], v244 offset:30720\n"
"v_mfma_f32_16x16x32_bf16 v[128:131], a[88:91], a[0:3], v[128:131]\n"
"buffer_store_dwordx4 v[16:19], v240, s[52:55], s76 offen offset:0 sc1\n"
"v_mfma_f32_16x16x32_bf16 v[128:131], a[92:95], a[4:7], v[128:131]\n"
"v_mfma_f32_16x16x32_bf16 v[132:135], a[88:91], a[8:11], v[132:135]\n"
"v_mfma_f32_16x16x32_bf16 v[132:135], a[92:95], a[12:15], v[132:135]\n"
"s_waitcnt lgkmcnt(8)\n"
"v_mfma_f32_16x16x32_bf16 v[136:139], a[88:91], a[16:19], v[136:139]\n"
"v_mfma_f32_16x16x32_bf16 v[136:139], a[92:95], a[20:23], v[136:139]\n"
"v_mfma_f32_16x16x32_bf16 v[140:143], a[88:91], a[24:27], v[140:143]\n"
"ds_read_b128 v[236:239], v249 offset:12288\n"
"buffer_load_dwordx4 a[84:87], v240, s[36:39], s80 offen offset:1024 sc1\n"
"v_mfma_f32_16x16x32_bf16 v[140:143], a[92:95], a[28:31], v[140:143]\n"
"s_waitcnt lgkmcnt(5)\n"
"v_lshlrev_b32 v251, 16, v232\n"
"v_fmac_f32 v251, v251, v20\n"
"v_mfma_f32_16x16x32_bf16 v[144:147], a[88:91], a[32:35], v[144:147]\n"
"v_and_b32 v232, s82, v232\n"
"v_fmac_f32 v232, v232, v21\n"
"v_mfma_f32_16x16x32_bf16 v[144:147], a[92:95], a[36:39], v[144:147]\n"
"s_waitcnt lgkmcnt(1)\n"
"s_barrier\n"
"v_cvt_pk_bf16_f32 v20, v251, v232\n"
"v_mfma_f32_16x16x32_bf16 v[148:151], a[88:91], a[40:43], v[148:151]\n"
".Lpad6_%=:\n"
"s_cbranch_execz .Lpad10_%=\n"
"v_lshlrev_b32 v251, 16, v233\n"
"v_fmac_f32 v251, v251, v22\n"
"v_mfma_f32_16x16x32_bf16 v[148:151], a[92:95], a[44:47], v[148:151]\n"
"v_and_b32 v233, s82, v233\n"
"v_fmac_f32 v233, v233, v23\n"
"v_mfma_f32_16x16x32_bf16 v[152:155], a[88:91], a[48:51], v[152:155]\n"
"v_cvt_pk_bf16_f32 v21, v251, v233\n"
"v_lshlrev_b32 v251, 16, v234\n"
"v_mfma_f32_16x16x32_bf16 v[152:155], a[92:95], a[52:55], v[152:155]\n"
"v_fmac_f32 v251, v251, v52\n"
"v_and_b32 v234, s82, v234\n"
"v_mfma_f32_16x16x32_bf16 v[156:159], a[88:91], a[56:59], v[156:159]\n"
"buffer_load_dwordx4 a[88:91], v240, s[36:39], s80 offen offset:2048 sc1\n"
"v_fmac_f32 v234, v234, v53\n"
"v_mfma_f32_16x16x32_bf16 v[156:159], a[92:95], a[60:63], v[156:159]\n"
"s_waitcnt vmcnt(14)\n"
"v_cvt_pk_bf16_f32 v22, v251, v234\n"
"v_lshlrev_b32 v251, 16, v235\n"
"v_mfma_f32_16x16x32_bf16 v[160:163], a[96:99], a[0:3], v[160:163]\n"
"v_fmac_f32 v251, v251, v54\n"
"v_and_b32 v235, s82, v235\n"
"v_mfma_f32_16x16x32_bf16 v[160:163], a[100:103], a[4:7], v[160:163]\n"
"v_fmac_f32 v235, v235, v55\n"
"v_cvt_pk_bf16_f32 v23, v251, v235\n"
"v_mfma_f32_16x16x32_bf16 v[164:167], a[96:99], a[8:11], v[164:167]\n"
"buffer_store_dwordx4 v[20:23], v240, s[52:55], s77 offen offset:0 sc1\n"
"v_mfma_f32_16x16x32_bf16 v[164:167], a[100:103], a[12:15], v[164:167]\n"
"ds_write_b128 v249, v[28:31] offset:14336\n"
"v_mfma_f32_16x16x32_bf16 v[168:171], a[96:99], a[16:19], v[168:171]\n"
"v_mfma_f32_16x16x32_bf16 v[168:171], a[100:103], a[20:23], v[168:171]\n"
"v_mfma_f32_16x16x32_bf16 v[172:175], a[96:99], a[24:27], v[172:175]\n"
"buffer_load_dwordx4 a[92:95], v240, s[36:39], s80 offen offset:3072 sc1\n"
"v_mfma_f32_16x16x32_bf16 v[172:175], a[100:103], a[28:31], v[172:175]\n"
"s_add_u32 s80, s80, 0x1000\n"
"v_mfma_f32_16x16x32_bf16 v[176:179], a[96:99], a[32:35], v[176:179]\n"
"ds_write_b128 v249, v[0:3] offset:0\n"
"v_mfma_f32_16x16x32_bf16 v[176:179], a[100:103], a[36:39], v[176:179]\n"
"v_mfma_f32_16x16x32_bf16 v[180:183], a[96:99], a[40:43], v[180:183]\n"
"v_mfma_f32_16x16x32_bf16 v[180:183], a[100:103], a[44:47], v[180:183]\n"
"v_mfma_f32_16x16x32_bf16 v[184:187], a[96:99], a[48:51], v[184:187]\n"
"v_mfma_f32_16x16x32_bf16 v[184:187], a[100:103], a[52:55], v[184:187]\n"
"ds_write_b128 v249, v[4:7] offset:2048\n"
"v_mfma_f32_16x16x32_bf16 v[188:191], a[96:99], a[56:59], v[188:191]\n"
"buffer_load_dwordx4 a[96:99], v240, s[36:39], s80 offen offset:0 sc1\n"
"v_mfma_f32_16x16x32_bf16 v[188:191], a[100:103], a[60:63], v[188:191]\n"
"s_waitcnt vmcnt(15)\n"
"v_mfma_f32_16x16x32_bf16 v[192:195], a[104:107], a[0:3], v[192:195]\n"
"v_mfma_f32_16x16x32_bf16 v[192:195], a[108:111], a[4:7], v[192:195]\n"
"s_waitcnt lgkmcnt(3)\n"
"ds_read_b128 v[232:235], v249 offset:47104\n"
"v_lshlrev_b32 v251, 16, v236\n"
"v_mfma_f32_16x16x32_bf16 v[196:199], a[104:107], a[8:11], v[196:199]\n"
"ds_write_b128 v249, v[8:11] offset:4096\n"
"v_mfma_f32_16x16x32_bf16 v[196:199], a[108:111], a[12:15], v[196:199]\n"
"v_fmac_f32 v251, v251, v24\n"
"v_and_b32 v236, s82, v236\n"
"v_mfma_f32_16x16x32_bf16 v[200:203], a[104:107], a[16:19], v[200:203]\n"
"v_fmac_f32 v236, v236, v25\n"
"v_cvt_pk_bf16_f32 v24, v251, v236\n"
"v_mfma_f32_16x16x32_bf16 v[200:203], a[108:111], a[20:23], v[200:203]\n"
"v_lshlrev_b32 v251, 16, v237\n"
"v_fmac_f32 v251, v251, v26\n"
"v_mfma_f32_16x16x32_bf16 v[204:207], a[104:107], a[24:27], v[204:207]\n"
"buffer_load_dwordx4 a[100:103], v240, s[36:39], s80 offen offset:1024 sc1\n"
"v_and_b32 v237, s82, v237\n"
"v_mfma_f32_16x16x32_bf16 v[204:207], a[108:111], a[28:31], v[204:207]\n"
"ds_write_b128 v249, v[12:15] offset:6144\n"
"v_mfma_f32_16x16x32_bf16 v[208:211], a[104:107], a[32:35], v[208:211]\n"
"v_fmac_f32 v237, v237, v27\n"
"v_cvt_pk_bf16_f32 v25, v251, v237\n"
"v_mfma_f32_16x16x32_bf16 v[208:211], a[108:111], a[36:39], v[208:211]\n"
"v_lshlrev_b32 v251, 16, v238\n"
"v_fmac_f32 v251, v251, v56\n"
"v_mfma_f32_16x16x32_bf16 v[212:215], a[104:107], a[40:43], v[212:215]\n"
"v_and_b32 v238, s82, v238\n"
"v_fmac_f32 v238, v238, v57\n"
"v_mfma_f32_16x16x32_bf16 v[212:215], a[108:111], a[44:47], v[212:215]\n"
"v_cvt_pk_bf16_f32 v26, v251, v238\n"
"v_lshlrev_b32 v251, 16, v239\n"
"v_mfma_f32_16x16x32_bf16 v[216:219], a[104:107], a[48:51], v[216:219]\n"
"ds_write_b128 v249, v[16:19] offset:8192\n"
"v_mfma_f32_16x16x32_bf16 v[216:219], a[108:111], a[52:55], v[216:219]\n"
"v_fmac_f32 v251, v251, v58\n"
"v_and_b32 v239, s82, v239\n"
"v_mfma_f32_16x16x32_bf16 v[220:223], a[104:107], a[56:59], v[220:223]\n"
"buffer_load_dwordx4 a[104:107], v240, s[36:39], s80 offen offset:2048 sc1\n"
"v_fmac_f32 v239, v239, v59\n"
"v_mfma_f32_16x16x32_bf16 v[220:223], a[108:111], a[60:63], v[220:223]\n"
"s_waitcnt vmcnt(14)\n"
"v_cvt_pk_bf16_f32 v27, v251, v239\n"
"v_mfma_f32_16x16x32_bf16 a[112:115], v[224:227], a[0:3], a[112:115]\n"
"ds_read_b128 a[0:3], v243 offset:32768\n"
"v_mfma_f32_16x16x32_bf16 a[112:115], v[228:231], a[4:7], a[112:115]\n"
"ds_read_b128 a[4:7], v244 offset:32768\n"
"v_mfma_f32_16x16x32_bf16 a[116:119], v[224:227], a[8:11], a[116:119]\n"
"ds_read_b128 a[8:11], v243 offset:34816\n"
"v_mfma_f32_16x16x32_bf16 a[116:119], v[228:231], a[12:15], a[116:119]\n"
"ds_read_b128 a[12:15], v244 offset:34816\n"
"v_mfma_f32_16x16x32_bf16 a[120:123], v[224:227], a[16:19], a[120:123]\n"
"ds_read_b128 a[16:19], v243 offset:36864\n"
"v_mfma_f32_16x16x32_bf16 a[120:123], v[228:231], a[20:23], a[120:123]\n"
"ds_read_b128 a[20:23], v244 offset:36864\n"
"v_mfma_f32_16x16x32_bf16 a[124:127], v[224:227], a[24:27], a[124:127]\n"
"ds_read_b128 a[24:27], v243 offset:38912\n"
"buffer_load_dwordx4 a[108:111], v240, s[36:39], s80 offen offset:3072 sc1\n"
"v_mfma_f32_16x16x32_bf16 a[124:127], v[228:231], a[28:31], a[124:127]\n"
"s_add_u32 s80, s80, 0x1000\n"
"ds_read_b128 a[28:31], v244 offset:38912\n"
"v_mfma_f32_16x16x32_bf16 a[128:131], v[224:227], a[32:35], a[128:131]\n"
"ds_read_b128 a[32:35], v243 offset:40960\n"
"v_mfma_f32_16x16x32_bf16 a[128:131], v[228:231], a[36:39], a[128:131]\n"
"ds_read_b128 a[36:39], v244 offset:40960\n"
"v_mfma_f32_16x16x32_bf16 a[132:135], v[224:227], a[40:43], a[132:135]\n"
"ds_read_b128 a[40:43], v243 offset:43008\n"
"v_mfma_f32_16x16x32_bf16 a[132:135], v[228:231], a[44:47], a[132:135]\n"
"ds_read_b128 a[44:47], v244 offset:43008\n"
"v_mfma_f32_16x16x32_bf16 a[136:139], v[224:227], a[48:51], a[136:139]\n"
"s_waitcnt lgkmcnt(15)\n"
"ds_read_b128 a[48:51], v243 offset:45056\n"
"v_lshlrev_b32 v251, 16, v232\n"
"v_mfma_f32_16x16x32_bf16 a[136:139], v[228:231], a[52:55], a[136:139]\n"
"ds_read_b128 a[52:55], v244 offset:45056\n"
"v_fmac_f32 v251, v251, v92\n"
"v_mfma_f32_16x16x32_bf16 a[140:143], v[224:227], a[56:59], a[140:143]\n"
"ds_read_b128 a[56:59], v243 offset:47104\n"
"buffer_load_dwordx4 v[224:227], v240, s[36:39], s80 offen offset:0 sc1\n"
"v_mfma_f32_16x16x32_bf16 a[140:143], v[228:231], a[60:63], a[140:143]\n"
"s_waitcnt vmcnt(13) lgkmcnt(11)\n"
"ds_read_b128 a[60:63], v244 offset:47104\n"
"v_and_b32 v232, s82, v232\n"
"v_mfma_f32_16x16x32_bf16 v[128:131], a[64:67], a[0:3], v[128:131]\n"
"ds_write_b128 v249, v[20:23] offset:10240\n"
"v_mfma_f32_16x16x32_bf16 v[128:131], a[68:71], a[4:7], v[128:131]\n"
"buffer_store_dwordx4 v[24:27], v240, s[52:55], s78 offen offset:0 sc1\n"
"v_mfma_f32_16x16x32_bf16 v[132:135], a[64:67], a[8:11], v[132:135]\n"
"ds_read_b128 v[236:239], v249 offset:32768\n"
"v_fmac_f32 v232, v232, v93\n"
"v_mfma_f32_16x16x32_bf16 v[132:135], a[68:71], a[12:15], v[132:135]\n"
"s_waitcnt lgkmcnt(10)\n"
"v_cvt_pk_bf16_f32 v92, v251, v232\n"
"v_lshlrev_b32 v251, 16, v233\n"
"v_mfma_f32_16x16x32_bf16 v[136:139], a[64:67], a[16:19], v[136:139]\n"
"v_fmac_f32 v251, v251, v94\n"
"v_and_b32 v233, s82, v233\n"
"v_mfma_f32_16x16x32_bf16 v[136:139], a[68:71], a[20:23], v[136:139]\n"
"ds_write_b128 v249, v[24:27] offset:12288\n"
"v_mfma_f32_16x16x32_bf16 v[140:143], a[64:67], a[24:27], v[140:143]\n"
"buffer_load_dwordx4 v[228:231], v240, s[36:39], s80 offen offset:1024 sc1\n"
"v_fmac_f32 v233, v233, v95\n"
"v_mfma_f32_16x16x32_bf16 v[140:143], a[68:71], a[28:31], v[140:143]\n"
"s_waitcnt lgkmcnt(7)\n"
"v_cvt_pk_bf16_f32 v93, v251, v233\n"
"v_lshlrev_b32 v251, 16, v234\n"
"v_mfma_f32_16x16x32_bf16 v[144:147], a[64:67], a[32:35], v[144:147]\n"
"v_fmac_f32 v251, v251, v124\n"
"v_and_b32 v234, s82, v234\n"
"v_mfma_f32_16x16x32_bf16 v[144:147], a[68:71], a[36:39], v[144:147]\n"
"v_fmac_f32 v234, v234, v125\n"
"v_cvt_pk_bf16_f32 v94, v251, v234\n"
"v_mfma_f32_16x16x32_bf16 v[148:151], a[64:67], a[40:43], v[148:151]\n"
"v_lshlrev_b32 v251, 16, v235\n"
"v_fmac_f32 v251, v251, v126\n"
"v_mfma_f32_16x16x32_bf16 v[148:151], a[68:71], a[44:47], v[148:151]\n"
"s_waitcnt lgkmcnt(3)\n"
"v_and_b32 v235, s82, v235\n"
"v_fmac_f32 v235, v235, v127\n"
"v_mfma_f32_16x16x32_bf16 v[152:155], a[64:67], a[48:51], v[152:155]\n"
"v_cvt_pk_bf16_f32 v95, v251, v235\n"
"v_mfma_f32_16x16x32_bf16 v[152:155], a[68:71], a[52:55], v[152:155]\n"
"v_mfma_f32_16x16x32_bf16 v[156:159], a[64:67], a[56:59], v[156:159]\n"
"buffer_load_dwordx4 a[64:67], v240, s[36:39], s80 offen offset:2048 sc1\n"
"v_mfma_f32_16x16x32_bf16 v[156:159], a[68:71], a[60:63], v[156:159]\n"
"s_waitcnt vmcnt(14)\n"
"buffer_store_dwordx4 v[92:95], v240, s[52:55], s79 offen offset:1024 sc1\n"
"v_mfma_f32_16x16x32_bf16 v[160:163], a[72:75], a[0:3], v[160:163]\n"
"v_mfma_f32_16x16x32_bf16 v[160:163], a[76:79], a[4:7], v[160:163]\n"
"v_mfma_f32_16x16x32_bf16 v[164:167], a[72:75], a[8:11], v[164:167]\n"
"v_mfma_f32_16x16x32_bf16 v[164:167], a[76:79], a[12:15], v[164:167]\n"
"v_mfma_f32_16x16x32_bf16 v[168:171], a[72:75], a[16:19], v[168:171]\n"
"v_mfma_f32_16x16x32_bf16 v[168:171], a[76:79], a[20:23], v[168:171]\n"
"v_mfma_f32_16x16x32_bf16 v[172:175], a[72:75], a[24:27], v[172:175]\n"
"buffer_load_dwordx4 a[68:71], v240, s[36:39], s80 offen offset:3072 sc1\n"
"v_mfma_f32_16x16x32_bf16 v[172:175], a[76:79], a[28:31], v[172:175]\n"
"s_add_u32 s80, s80, 0x1000\n"
"ds_read_b128 v[232:235], v249 offset:34816\n"
"v_mfma_f32_16x16x32_bf16 v[176:179], a[72:75], a[32:35], v[176:179]\n"
"s_waitcnt lgkmcnt(2)\n"
"v_lshlrev_b32 v251, 16, v236\n"
"v_fmac_f32 v251, v251, v64\n"
"v_mfma_f32_16x16x32_bf16 v[176:179], a[76:79], a[36:39], v[176:179]\n"
"v_and_b32 v236, s82, v236\n"
"v_fmac_f32 v236, v236, v65\n"
"v_mfma_f32_16x16x32_bf16 v[180:183], a[72:75], a[40:43], v[180:183]\n"
"v_cvt_pk_bf16_f32 v64, v251, v236\n"
"v_lshlrev_b32 v251, 16, v237\n"
"v_mfma_f32_16x16x32_bf16 v[180:183], a[76:79], a[44:47], v[180:183]\n"
"v_fmac_f32 v251, v251, v66\n"
"v_and_b32 v237, s82, v237\n"
"v_mfma_f32_16x16x32_bf16 v[184:187], a[72:75], a[48:51], v[184:187]\n"
"v_fmac_f32 v237, v237, v67\n"
"v_cvt_pk_bf16_f32 v65, v251, v237\n"
"v_mfma_f32_16x16x32_bf16 v[184:187], a[76:79], a[52:55], v[184:187]\n"
"v_lshlrev_b32 v251, 16, v238\n"
"v_fmac_f32 v251, v251, v96\n"
"v_mfma_f32_16x16x32_bf16 v[188:191], a[72:75], a[56:59], v[188:191]\n"
"buffer_load_dwordx4 a[72:75], v240, s[36:39], s80 offen offset:0 sc1\n"
"v_and_b32 v238, s82, v238\n"
"v_mfma_f32_16x16x32_bf16 v[188:191], a[76:79], a[60:63], v[188:191]\n"
"s_waitcnt vmcnt(14)\n"
"v_fmac_f32 v238, v238, v97\n"
"v_cvt_pk_bf16_f32 v66, v251, v238\n"
"v_mfma_f32_16x16x32_bf16 v[192:195], a[80:83], a[0:3], v[192:195]\n"
"v_lshlrev_b32 v251, 16, v239\n"
"v_fmac_f32 v251, v251, v98\n"
"v_mfma_f32_16x16x32_bf16 v[192:195], a[84:87], a[4:7], v[192:195]\n"
"v_and_b32 v239, s82, v239\n"
"v_fmac_f32 v239, v239, v99\n"
"v_mfma_f32_16x16x32_bf16 v[196:199], a[80:83], a[8:11], v[196:199]\n"
"v_cvt_pk_bf16_f32 v67, v251, v239\n"
"v_mfma_f32_16x16x32_bf16 v[196:199], a[84:87], a[12:15], v[196:199]\n"
"buffer_store_dwordx4 v[64:67], v240, s[52:55], s68 offen offset:1024 sc1\n"
"v_mfma_f32_16x16x32_bf16 v[200:203], a[80:83], a[16:19], v[200:203]\n"
"v_mfma_f32_16x16x32_bf16 v[200:203], a[84:87], a[20:23], v[200:203]\n"
"v_mfma_f32_16x16x32_bf16 v[204:207], a[80:83], a[24:27], v[204:207]\n"
"buffer_load_dwordx4 a[76:79], v240, s[36:39], s80 offen offset:1024 sc1\n"
"v_mfma_f32_16x16x32_bf16 v[204:207], a[84:87], a[28:31], v[204:207]\n"
"v_mfma_f32_16x16x32_bf16 v[208:211], a[80:83], a[32:35], v[208:211]\n"
"v_mfma_f32_16x16x32_bf16 v[208:211], a[84:87], a[36:39], v[208:211]\n"
"v_mfma_f32_16x16x32_bf16 v[212:215], a[80:83], a[40:43], v[212:215]\n"
"v_mfma_f32_16x16x32_bf16 v[212:215], a[84:87], a[44:47], v[212:215]\n"
"v_mfma_f32_16x16x32_bf16 v[216:219], a[80:83], a[48:51], v[216:219]\n"
"v_mfma_f32_16x16x32_bf16 v[216:219], a[84:87], a[52:55], v[216:219]\n"
"v_mfma_f32_16x16x32_bf16 v[220:223], a[80:83], a[56:59], v[220:223]\n"
"buffer_load_dwordx4 a[80:83], v240, s[36:39], s80 offen offset:2048 sc1\n"
"v_mfma_f32_16x16x32_bf16 v[220:223], a[84:87], a[60:63], v[220:223]\n"
"s_waitcnt vmcnt(14)\n"
"v_mfma_f32_16x16x32_bf16 a[112:115], a[88:91], a[0:3], a[112:115]\n"
"ds_read_b128 a[0:3], v243 offset:49152\n"
"v_mfma_f32_16x16x32_bf16 a[112:115], a[92:95], a[4:7], a[112:115]\n"
"ds_read_b128 a[4:7], v244 offset:49152\n"
"v_mfma_f32_16x16x32_bf16 a[116:119], a[88:91], a[8:11], a[116:119]\n"
"s_waitcnt lgkmcnt(2)\n"
"ds_read_b128 a[8:11], v243 offset:51200\n"
"v_lshlrev_b32 v251, 16, v232\n"
"v_mfma_f32_16x16x32_bf16 a[116:119], a[92:95], a[12:15], a[116:119]\n"
"ds_read_b128 a[12:15], v244 offset:51200\n"
"v_fmac_f32 v251, v251, v68\n"
"v_mfma_f32_16x16x32_bf16 a[120:123], a[88:91], a[16:19], a[120:123]\n"
"ds_read_b128 a[16:19], v243 offset:53248\n"
"v_and_b32 v232, s82, v232\n"
"v_mfma_f32_16x16x32_bf16 a[120:123], a[92:95], a[20:23], a[120:123]\n"
"ds_read_b128 a[20:23], v244 offset:53248\n"
"v_fmac_f32 v232, v232, v69\n"
"v_mfma_f32_16x16x32_bf16 a[124:127], a[88:91], a[24:27], a[124:127]\n"
"ds_read_b128 a[24:27], v243 offset:55296\n"
"buffer_load_dwordx4 a[84:87], v240, s[36:39], s80 offen offset:3072 sc1\n"
"v_mfma_f32_16x16x32_bf16 a[124:127], a[92:95], a[28:31], a[124:127]\n"
"s_add_u32 s80, s80, 0x1000\n"
"ds_read_b128 a[28:31], v244 offset:55296\n"
"v_cvt_pk_bf16_f32 v68, v251, v232\n"
"v_mfma_f32_16x16x32_bf16 a[128:131], a[88:91], a[32:35], a[128:131]\n"
"ds_read_b128 a[32:35], v243 offset:57344\n"
"v_lshlrev_b32 v251, 16, v233\n"
"v_mfma_f32_16x16x32_bf16 a[128:131], a[92:95], a[36:39], a[128:131]\n"
"ds_read_b128 a[36:39], v244 offset:57344\n"
"v_fmac_f32 v251, v251, v70\n"
"v_mfma_f32_16x16x32_bf16 a[132:135], a[88:91], a[40:43], a[132:135]\n"
"ds_read_b128 a[40:43], v243 offset:59392\n"
"v_and_b32 v233, s82, v233\n"
"v_mfma_f32_16x16x32_bf16 a[132:135], a[92:95], a[44:47], a[132:135]\n"
"ds_read_b128 a[44:47], v244 offset:59392\n"
"v_fmac_f32 v233, v233, v71\n"
"v_mfma_f32_16x16x32_bf16 a[136:139], a[88:91], a[48:51], a[136:139]\n"
"ds_read_b128 a[48:51], v243 offset:61440\n"
"v_cvt_pk_bf16_f32 v69, v251, v233\n"
"v_mfma_f32_16x16x32_bf16 a[136:139], a[92:95], a[52:55], a[136:139]\n"
"ds_read_b128 a[52:55], v244 offset:61440\n"
"v_lshlrev_b32 v251, 16, v234\n"
"v_mfma_f32_16x16x32_bf16 a[140:143], a[88:91], a[56:59], a[140:143]\n"
"ds_read_b128 a[56:59], v243 offset:63488\n"
"buffer_load_dwordx4 a[88:91], v240, s[36:39], s80 offen offset:0 sc1\n"
"v_mfma_f32_16x16x32_bf16 a[140:143], a[92:95], a[60:63], a[140:143]\n"
"s_waitcnt vmcnt(14) lgkmcnt(11)\n"
"ds_read_b128 a[60:63], v244 offset:63488\n"
"v_fmac_f32 v251, v251, v100\n"
"v_mfma_f32_16x16x32_bf16 v[128:131], a[96:99], a[0:3], v[128:131]\n"
"ds_read_b128 v[236:239], v249 offset:36864\n"
"v_and_b32 v234, s82, v234\n"
"v_mfma_f32_16x16x32_bf16 v[128:131], a[100:103], a[4:7], v[128:131]\n"
"v_fmac_f32 v234, v234, v101\n"
"v_cvt_pk_bf16_f32 v70, v251, v234\n"
"v_mfma_f32_16x16x32_bf16 v[132:135], a[96:99], a[8:11], v[132:135]\n"
"v_lshlrev_b32 v251, 16, v235\n"
"v_fmac_f32 v251, v251, v102\n"
"v_mfma_f32_16x16x32_bf16 v[132:135], a[100:103], a[12:15], v[132:135]\n"
"s_waitcnt lgkmcnt(9)\n"
"v_and_b32 v235, s82, v235\n"
"v_fmac_f32 v235, v235, v103\n"
"v_mfma_f32_16x16x32_bf16 v[136:139], a[96:99], a[16:19], v[136:139]\n"
"v_cvt_pk_bf16_f32 v71, v251, v235\n"
"v_mfma_f32_16x16x32_bf16 v[136:139], a[100:103], a[20:23], v[136:139]\n"
"v_mfma_f32_16x16x32_bf16 v[140:143], a[96:99], a[24:27], v[140:143]\n"
"buffer_load_dwordx4 a[92:95], v240, s[36:39], s80 offen offset:1024 sc1\n"
"v_mfma_f32_16x16x32_bf16 v[140:143], a[100:103], a[28:31], v[140:143]\n"
"s_waitcnt lgkmcnt(5)\n"
"buffer_store_dwordx4 v[68:71], v240, s[52:55], s69 offen offset:1024 sc1\n"
"v_mfma_f32_16x16x32_bf16 v[144:147], a[96:99], a[32:35], v[144:147]\n"
"v_mfma_f32_16x16x32_bf16 v[144:147], a[100:103], a[36:39], v[144:147]\n"
"s_waitcnt lgkmcnt(1)\n"
"s_barrier\n"
"v_mfma_f32_16x16x32_bf16 v[148:151], a[96:99], a[40:43], v[148:151]\n"
"ds_write_b128 v249, v[92:95] offset:47104\n"
"v_mfma_f32_16x16x32_bf16 v[148:151], a[100:103], a[44:47], v[148:151]\n"
"v_mfma_f32_16x16x32_bf16 v[152:155], a[96:99], a[48:51], v[152:155]\n"
"v_mfma_f32_16x16x32_bf16 v[152:155], a[100:103], a[52:55], v[152:155]\n"
"s_waitcnt lgkmcnt(1)\n"
"ds_read_b128 v[232:235], v249 offset:38912\n"
"v_lshlrev_b32 v251, 16, v236\n"
"v_mfma_f32_16x16x32_bf16 v[156:159], a[96:99], a[56:59], v[156:159]\n"
"buffer_load_dwordx4 a[96:99], v240, s[36:39], s80 offen offset:2048 sc1\n"
"v_fmac_f32 v251, v251, v72\n"
"v_mfma_f32_16x16x32_bf16 v[156:159], a[100:103], a[60:63], v[156:159]\n"
"s_waitcnt vmcnt(15)\n"
"ds_write_b128 v249, v[64:67] offset:32768\n"
"v_mfma_f32_16x16x32_bf16 v[160:163], a[104:107], a[0:3], v[160:163]\n"
"v_and_b32 v236, s82, v236\n"
"v_fmac_f32 v236, v236, v73\n"
"v_mfma_f32_16x16x32_bf16 v[160:163], a[108:111], a[4:7], v[160:163]\n"
"v_cvt_pk_bf16_f32 v72, v251, v236\n"
"v_lshlrev_b32 v251, 16, v237\n"
"v_mfma_f32_16x16x32_bf16 v[164:167], a[104:107], a[8:11], v[164:167]\n"
"v_fmac_f32 v251, v251, v74\n"
"v_and_b32 v237, s82, v237\n"
"v_mfma_f32_16x16x32_bf16 v[164:167], a[108:111], a[12:15], v[164:167]\n"
"v_fmac_f32 v237, v237, v75\n"
"v_cvt_pk_bf16_f32 v73, v251, v237\n"
"v_mfma_f32_16x16x32_bf16 v[168:171], a[104:107], a[16:19], v[168:171]\n"
"ds_write_b128 v249, v[68:71] offset:34816\n"
"v_mfma_f32_16x16x32_bf16 v[168:171], a[108:111], a[20:23], v[168:171]\n"
"v_lshlrev_b32 v251, 16, v238\n"
"v_fmac_f32 v251, v251, v104\n"
"v_mfma_f32_16x16x32_bf16 v[172:175], a[104:107], a[24:27], v[172:175]\n"
"buffer_load_dwordx4 a[100:103], v240, s[36:39], s80 offen offset:3072 sc1\n"
"v_and_b32 v238, s82, v238\n"
"v_mfma_f32_16x16x32_bf16 v[172:175], a[108:111], a[28:31], v[172:175]\n"
"s_add_u32 s80, s80, 0x1000\n"
"v_fmac_f32 v238, v238, v105\n"
"v_cvt_pk_bf16_f32 v74, v251, v238\n"
"v_mfma_f32_16x16x32_bf16 v[176:179], a[104:107], a[32:35], v[176:179]\n"
"v_lshlrev_b32 v251, 16, v239\n"
"v_fmac_f32 v251, v251, v106\n"
"v_mfma_f32_16x16x32_bf16 v[176:179], a[108:111], a[36:39], v[176:179]\n"
"v_and_b32 v239, s82, v239\n"
"v_fmac_f32 v239, v239, v107\n"
"v_mfma_f32_16x16x32_bf16 v[180:183], a[104:107], a[40:43], v[180:183]\n"
"v_cvt_pk_bf16_f32 v75, v251, v239\n"
"v_mfma_f32_16x16x32_bf16 v[180:183], a[108:111], a[44:47], v[180:183]\n"
"buffer_store_dwordx4 v[72:75], v240, s[52:55], s70 offen offset:1024 sc1\n"
"v_mfma_f32_16x16x32_bf16 v[184:187], a[104:107], a[48:51], v[184:187]\n"
"ds_write_b128 v249, v[72:75] offset:36864\n"
"v_mfma_f32_16x16x32_bf16 v[184:187], a[108:111], a[52:55], v[184:187]\n"
"v_mfma_f32_16x16x32_bf16 v[188:191], a[104:107], a[56:59], v[188:191]\n"
"buffer_load_dwordx4 a[104:107], v240, s[36:39], s80 offen offset:0 sc1\n"
"v_mfma_f32_16x16x32_bf16 v[188:191], a[108:111], a[60:63], v[188:191]\n"
"s_waitcnt vmcnt(15)\n"
"v_mfma_f32_16x16x32_bf16 v[192:195], v[224:227], a[0:3], v[192:195]\n"
"v_mfma_f32_16x16x32_bf16 v[192:195], v[228:231], a[4:7], v[192:195]\n"
"v_mfma_f32_16x16x32_bf16 v[196:199], v[224:227], a[8:11], v[196:199]\n"
"v_mfma_f32_16x16x32_bf16 v[196:199], v[228:231], a[12:15], v[196:199]\n"
"v_mfma_f32_16x16x32_bf16 v[200:203], v[224:227], a[16:19], v[200:203]\n"
"v_mfma_f32_16x16x32_bf16 v[200:203], v[228:231], a[20:23], v[200:203]\n"
"v_mfma_f32_16x16x32_bf16 v[204:207], v[224:227], a[24:27], v[204:207]\n"
"buffer_load_dwordx4 a[108:111], v240, s[36:39], s80 offen offset:1024 sc1\n"
"v_mfma_f32_16x16x32_bf16 v[204:207], v[228:231], a[28:31], v[204:207]\n"
"v_mfma_f32_16x16x32_bf16 v[208:211], v[224:227], a[32:35], v[208:211]\n"
"s_waitcnt lgkmcnt(3)\n"
"ds_read_b128 v[236:239], v249 offset:40960\n"
"v_lshlrev_b32 v251, 16, v232\n"
"v_mfma_f32_16x16x32_bf16 v[208:211], v[228:231], a[36:39], v[208:211]\n"
"v_fmac_f32 v251, v251, v76\n"
"v_and_b32 v232, s82, v232\n"
"v_mfma_f32_16x16x32_bf16 v[212:215], v[224:227], a[40:43], v[212:215]\n"
"v_fmac_f32 v232, v232, v77\n"
"v_cvt_pk_bf16_f32 v76, v251, v232\n"
"v_mfma_f32_16x16x32_bf16 v[212:215], v[228:231], a[44:47], v[212:215]\n"
"v_lshlrev_b32 v251, 16, v233\n"
"v_fmac_f32 v251, v251, v78\n"
"v_mfma_f32_16x16x32_bf16 v[216:219], v[224:227], a[48:51], v[216:219]\n"
"v_and_b32 v233, s82, v233\n"
"v_fmac_f32 v233, v233, v79\n"
"v_mfma_f32_16x16x32_bf16 v[216:219], v[228:231], a[52:55], v[216:219]\n"
"v_cvt_pk_bf16_f32 v77, v251, v233\n"
"v_lshlrev_b32 v251, 16, v234\n"
"v_mfma_f32_16x16x32_bf16 v[220:223], v[224:227], a[56:59], v[220:223]\n"
"buffer_load_dwordx4 v[224:227], v240, s[36:39], s80 offen offset:2048 sc1\n"
"v_fmac_f32 v251, v251, v108\n"
"v_mfma_f32_16x16x32_bf16 v[220:223], v[228:231], a[60:63], v[220:223]\n"
"s_waitcnt vmcnt(14)\n"
"v_and_b32 v234, s82, v234\n"
"v_fmac_f32 v234, v234, v109\n"
"v_mfma_f32_16x16x32_bf16 a[112:115], a[64:67], a[0:3], a[112:115]\n"
"ds_read_b128 a[0:3], v245 offset:0\n"
"v_cvt_pk_bf16_f32 v78, v251, v234\n"
"v_mfma_f32_16x16x32_bf16 a[112:115], a[68:71], a[4:7], a[112:115]\n"
"ds_read_b128 a[4:7], v246 offset:0\n"
"v_lshlrev_b32 v251, 16, v235\n"
"v_mfma_f32_16x16x32_bf16 a[116:119], a[64:67], a[8:11], a[116:119]\n"
"ds_read_b128 a[8:11], v245 offset:2048\n"
"v_fmac_f32 v251, v251, v110\n"
"v_mfma_f32_16x16x32_bf16 a[116:119], a[68:71], a[12:15], a[116:119]\n"
"ds_read_b128 a[12:15], v246 offset:2048\n"
"v_and_b32 v235, s82, v235\n"
"v_mfma_f32_16x16x32_bf16 a[120:123], a[64:67], a[16:19], a[120:123]\n"
"ds_read_b128 a[16:19], v245 offset:4096\n"
"v_fmac_f32 v235, v235, v111\n"
"v_mfma_f32_16x16x32_bf16 a[120:123], a[68:71], a[20:23], a[120:123]\n"
"ds_read_b128 a[20:23], v246 offset:4096\n"
"v_cvt_pk_bf16_f32 v79, v251, v235\n"
"v_mfma_f32_16x16x32_bf16 a[124:127], a[64:67], a[24:27], a[124:127]\n"
"ds_read_b128 a[24:27], v245 offset:6144\n"
"buffer_load_dwordx4 v[228:231], v240, s[36:39], s80 offen offset:3072 sc1\n"
"v_mfma_f32_16x16x32_bf16 a[124:127], a[68:71], a[28:31], a[124:127]\n"
"s_add_u32 s80, s80, 0x1000\n"
"ds_read_b128 a[28:31], v246 offset:6144\n"
"v_mfma_f32_16x16x32_bf16 a[128:131], a[64:67], a[32:35], a[128:131]\n"
"ds_read_b128 a[32:35], v245 offset:8192\n"
"v_mfma_f32_16x16x32_bf16 a[128:131], a[68:71], a[36:39], a[128:131]\n"
"ds_read_b128 a[36:39], v246 offset:8192\n"
"v_mfma_f32_16x16x32_bf16 a[132:135], a[64:67], a[40:43], a[132:135]\n"
"ds_read_b128 a[40:43], v245 offset:10240\n"
"v_mfma_f32_16x16x32_bf16 a[132:135], a[68:71], a[44:47], a[132:135]\n"
"ds_read_b128 a[44:47], v246 offset:10240\n"
"v_mfma_f32_16x16x32_bf16 a[136:139], a[64:67], a[48:51], a[136:139]\n"
"ds_read_b128 a[48:51], v245 offset:12288\n"
"v_mfma_f32_16x16x32_bf16 a[136:139], a[68:71], a[52:55], a[136:139]\n"
"ds_read_b128 a[52:55], v246 offset:12288\n"
"v_mfma_f32_16x16x32_bf16 a[140:143], a[64:67], a[56:59], a[140:143]\n"
"ds_read_b128 a[56:59], v245 offset:14336\n"
"buffer_load_dwordx4 a[64:67], v240, s[36:39], s80 offen offset:0 sc1\n"
"v_mfma_f32_16x16x32_bf16 a[140:143], a[68:71], a[60:63], a[140:143]\n"
"s_waitcnt vmcnt(13) lgkmcnt(11)\n"
"ds_read_b128 a[60:63], v246 offset:14336\n"
"v_mfma_f32_16x16x32_bf16 v[128:131], a[72:75], a[0:3], v[128:131]\n"
"buffer_store_dwordx4 v[76:79], v240, s[52:55], s71 offen offset:1024 sc1\n"
"v_mfma_f32_16x16x32_bf16 v[128:131], a[76:79], a[4:7], v[128:131]\n"
"ds_write_b128 v249, v[76:79] offset:38912\n"
"v_mfma_f32_16x16x32_bf16 v[132:135], a[72:75], a[8:11], v[132:135]\n"
"v_mfma_f32_16x16x32_bf16 v[132:135], a[76:79], a[12:15], v[132:135]\n"
"s_waitcnt lgkmcnt(9)\n"
"ds_read_b128 v[232:235], v249 offset:43008\n"
"v_lshlrev_b32 v251, 16, v236\n"
"v_mfma_f32_16x16x32_bf16 v[136:139], a[72:75], a[16:19], v[136:139]\n"
"v_fmac_f32 v251, v251, v80\n"
"v_and_b32 v236, s82, v236\n"
"v_mfma_f32_16x16x32_bf16 v[136:139], a[76:79], a[20:23], v[136:139]\n"
"v_fmac_f32 v236, v236, v81\n"
"v_cvt_pk_bf16_f32 v80, v251, v236\n"
"v_mfma_f32_16x16x32_bf16 v[140:143], a[72:75], a[24:27], v[140:143]\n"
"buffer_load_dwordx4 a[68:71], v240, s[36:39], s80 offen offset:1024 sc1\n"
"v_lshlrev_b32 v251, 16, v237\n"
"v_mfma_f32_16x16x32_bf16 v[140:143], a[76:79], a[28:31], v[140:143]\n"
"s_waitcnt lgkmcnt(6)\n"
"v_fmac_f32 v251, v251, v82\n"
"v_and_b32 v237, s82, v237\n"
"v_mfma_f32_16x16x32_bf16 v[144:147], a[72:75], a[32:35], v[144:147]\n"
"v_fmac_f32 v237, v237, v83\n"
"v_cvt_pk_bf16_f32 v81, v251, v237\n"
"v_mfma_f32_16x16x32_bf16 v[144:147], a[76:79], a[36:39], v[144:147]\n"
"v_lshlrev_b32 v251, 16, v238\n"
"v_fmac_f32 v251, v251, v112\n"
"v_mfma_f32_16x16x32_bf16 v[148:151], a[72:75], a[40:43], v[148:151]\n"
"v_and_b32 v238, s82, v238\n"
"v_fmac_f32 v238, v238, v113\n"
"v_mfma_f32_16x16x32_bf16 v[148:151], a[76:79], a[44:47], v[148:151]\n"
"s_waitcnt lgkmcnt(2)\n"
"v_cvt_pk_bf16_f32 v82, v251, v238\n"
"v_lshlrev_b32 v251, 16, v239\n"
"v_mfma_f32_16x16x32_bf16 v[152:155], a[72:75], a[48:51], v[152:155]\n"
"v_fmac_f32 v251, v251, v114\n"
"v_and_b32 v239, s82, v239\n"
"v_mfma_f32_16x16x32_bf16 v[152:155], a[76:79], a[52:55], v[152:155]\n"
"v_fmac_f32 v239, v239, v115\n"
"v_cvt_pk_bf16_f32 v83, v251, v239\n"
"v_mfma_f32_16x16x32_bf16 v[156:159], a[72:75], a[56:59], v[156:159]\n"
"buffer_load_dwordx4 a[72:75], v240, s[36:39], s80 offen offset:2048 sc1\n"
"v_mfma_f32_16x16x32_bf16 v[156:159], a[76:79], a[60:63], v[156:159]\n"
"s_waitcnt vmcnt(14)\n"
"buffer_store_dwordx4 v[80:83], v240, s[52:55], s76 offen offset:1024 sc1\n"
"v_mfma_f32_16x16x32_bf16 v[160:163], a[80:83], a[0:3], v[160:163]\n"
"ds_write_b128 v249, v[80:83] offset:40960\n"
"v_mfma_f32_16x16x32_bf16 v[160:163], a[84:87], a[4:7], v[160:163]\n"
"v_mfma_f32_16x16x32_bf16 v[164:167], a[80:83], a[8:11], v[164:167]\n"
"v_mfma_f32_16x16x32_bf16 v[164:167], a[84:87], a[12:15], v[164:167]\n"
"v_mfma_f32_16x16x32_bf16 v[168:171], a[80:83], a[16:19], v[168:171]\n"
"v_mfma_f32_16x16x32_bf16 v[168:171], a[84:87], a[20:23], v[168:171]\n"
"v_mfma_f32_16x16x32_bf16 v[172:175], a[80:83], a[24:27], v[172:175]\n"
"buffer_load_dwordx4 a[76:79], v240, s[36:39], s80 offen offset:3072 sc1\n"
"v_mfma_f32_16x16x32_bf16 v[172:175], a[84:87], a[28:31], v[172:175]\n"
"s_add_u32 s80, s80, 0x1000\n"
"v_mfma_f32_16x16x32_bf16 v[176:179], a[80:83], a[32:35], v[176:179]\n"
"v_mfma_f32_16x16x32_bf16 v[176:179], a[84:87], a[36:39], v[176:179]\n"
"v_mfma_f32_16x16x32_bf16 v[180:183], a[80:83], a[40:43], v[180:183]\n"
"v_mfma_f32_16x16x32_bf16 v[180:183], a[84:87], a[44:47], v[180:183]\n"
"v_mfma_f32_16x16x32_bf16 v[184:187], a[80:83], a[48:51], v[184:187]\n"
"v_mfma_f32_16x16x32_bf16 v[184:187], a[84:87], a[52:55], v[184:187]\n"
"v_mfma_f32_16x16x32_bf16 v[188:191], a[80:83], a[56:59], v[188:191]\n"
"ds_read_b128 v[236:239], v249 offset:45056\n"
"buffer_load_dwordx4 a[80:83], v240, s[36:39], s80 offen offset:0 sc1\n"
"v_mfma_f32_16x16x32_bf16 v[188:191], a[84:87], a[60:63], v[188:191]\n"
"s_waitcnt vmcnt(15) lgkmcnt(2)\n"
"v_lshlrev_b32 v251, 16, v232\n"
"v_fmac_f32 v251, v251, v84\n"
"v_mfma_f32_16x16x32_bf16 v[192:195], a[88:91], a[0:3], v[192:195]\n"
"v_and_b32 v232, s82, v232\n"
"v_fmac_f32 v232, v232, v85\n"
"v_mfma_f32_16x16x32_bf16 v[192:195], a[92:95], a[4:7], v[192:195]\n"
"v_cvt_pk_bf16_f32 v84, v251, v232\n"
"v_lshlrev_b32 v251, 16, v233\n"
"v_mfma_f32_16x16x32_bf16 v[196:199], a[88:91], a[8:11], v[196:199]\n"
"v_fmac_f32 v251, v251, v86\n"
"v_and_b32 v233, s82, v233\n"
"v_mfma_f32_16x16x32_bf16 v[196:199], a[92:95], a[12:15], v[196:199]\n"
"v_fmac_f32 v233, v233, v87\n"
"v_cvt_pk_bf16_f32 v85, v251, v233\n"
"v_mfma_f32_16x16x32_bf16 v[200:203], a[88:91], a[16:19], v[200:203]\n"
"v_lshlrev_b32 v251, 16, v234\n"
"v_fmac_f32 v251, v251, v116\n"
"v_mfma_f32_16x16x32_bf16 v[200:203], a[92:95], a[20:23], v[200:203]\n"
"v_and_b32 v234, s82, v234\n"
"v_fmac_f32 v234, v234, v117\n"
"v_mfma_f32_16x16x32_bf16 v[204:207], a[88:91], a[24:27], v[204:207]\n"
"buffer_load_dwordx4 a[84:87], v240, s[36:39], s80 offen offset:1024 sc1\n"
"v_cvt_pk_bf16_f32 v86, v251, v234\n"
"v_mfma_f32_16x16x32_bf16 v[204:207], a[92:95], a[28:31], v[204:207]\n"
"v_lshlrev_b32 v251, 16, v235\n"
"v_fmac_f32 v251, v251, v118\n"
"v_mfma_f32_16x16x32_bf16 v[208:211], a[88:91], a[32:35], v[208:211]\n"
"v_and_b32 v235, s82, v235\n"
"v_fmac_f32 v235, v235, v119\n"
"v_mfma_f32_16x16x32_bf16 v[208:211], a[92:95], a[36:39], v[208:211]\n"
"v_cvt_pk_bf16_f32 v87, v251, v235\n"
"v_mfma_f32_16x16x32_bf16 v[212:215], a[88:91], a[40:43], v[212:215]\n"
"buffer_store_dwordx4 v[84:87], v240, s[52:55], s77 offen offset:1024 sc1\n"
"v_mfma_f32_16x16x32_bf16 v[212:215], a[92:95], a[44:47], v[212:215]\n"
"ds_write_b128 v249, v[84:87] offset:43008\n"
"v_mfma_f32_16x16x32_bf16 v[216:219], a[88:91], a[48:51], v[216:219]\n"
"v_mfma_f32_16x16x32_bf16 v[216:219], a[92:95], a[52:55], v[216:219]\n"
"v_mfma_f32_16x16x32_bf16 v[220:223], a[88:91], a[56:59], v[220:223]\n"
"buffer_load_dwordx4 a[88:91], v240, s[36:39], s80 offen offset:2048 sc1\n"
"v_mfma_f32_16x16x32_bf16 v[220:223], a[92:95], a[60:63], v[220:223]\n"
"s_waitcnt vmcnt(15)\n"
"v_mfma_f32_16x16x32_bf16 a[112:115], a[96:99], a[0:3], a[112:115]\n"
"ds_read_b128 a[0:3], v245 offset:16384\n"
"v_mfma_f32_16x16x32_bf16 a[112:115], a[100:103], a[4:7], a[112:115]\n"
"ds_read_b128 a[4:7], v246 offset:16384\n"
"v_mfma_f32_16x16x32_bf16 a[116:119], a[96:99], a[8:11], a[116:119]\n"
"ds_read_b128 a[8:11], v245 offset:18432\n"
"v_mfma_f32_16x16x32_bf16 a[116:119], a[100:103], a[12:15], a[116:119]\n"
"ds_read_b128 a[12:15], v246 offset:18432\n"
"v_mfma_f32_16x16x32_bf16 a[120:123], a[96:99], a[16:19], a[120:123]\n"
"ds_read_b128 a[16:19], v245 offset:20480\n"
"v_mfma_f32_16x16x32_bf16 a[120:123], a[100:103], a[20:23], a[120:123]\n"
"ds_read_b128 a[20:23], v246 offset:20480\n"
"v_mfma_f32_16x16x32_bf16 a[124:127], a[96:99], a[24:27], a[124:127]\n"
"ds_read_b128 a[24:27], v245 offset:22528\n"
"buffer_load_dwordx4 a[92:95], v240, s[36:39], s80 offen offset:3072 sc1\n"
"v_mfma_f32_16x16x32_bf16 a[124:127], a[100:103], a[28:31], a[124:127]\n"
"s_add_u32 s80, s80, 0x1000\n"
"ds_read_b128 a[28:31], v246 offset:22528\n"
"v_mfma_f32_16x16x32_bf16 a[128:131], a[96:99], a[32:35], a[128:131]\n"
"ds_read_b128 a[32:35], v245 offset:24576\n"
"v_mfma_f32_16x16x32_bf16 a[128:131], a[100:103], a[36:39], a[128:131]\n"
"s_waitcnt lgkmcnt(10)\n"
"ds_read_b128 a[36:39], v246 offset:24576\n"
"v_lshlrev_b32 v251, 16, v236\n"
"v_mfma_f32_16x16x32_bf16 a[132:135], a[96:99], a[40:43], a[132:135]\n"
"ds_read_b128 a[40:43], v245 offset:26624\n"
"v_fmac_f32 v251, v251, v88\n"
"v_mfma_f32_16x16x32_bf16 a[132:135], a[100:103], a[44:47], a[132:135]\n"
"ds_read_b128 a[44:47], v246 offset:26624\n"
"v_and_b32 v236, s82, v236\n"
"v_mfma_f32_16x16x32_bf16 a[136:139], a[96:99], a[48:51], a[136:139]\n"
"ds_read_b128 a[48:51], v245 offset:28672\n"
"v_fmac_f32 v236, v236, v89\n"
"v_mfma_f32_16x16x32_bf16 a[136:139], a[100:103], a[52:55], a[136:139]\n"
"ds_read_b128 a[52:55], v246 offset:28672\n"
"v_cvt_pk_bf16_f32 v88, v251, v236\n"
"v_mfma_f32_16x16x32_bf16 a[140:143], a[96:99], a[56:59], a[140:143]\n"
"ds_read_b128 a[56:59], v245 offset:30720\n"
"buffer_load_dwordx4 a[96:99], v240, s[36:39], s80 offen offset:0 sc1\n"
"v_mfma_f32_16x16x32_bf16 a[140:143], a[100:103], a[60:63], a[140:143]\n"
"s_waitcnt vmcnt(14) lgkmcnt(11)\n"
"ds_read_b128 a[60:63], v246 offset:30720\n"
"v_lshlrev_b32 v251, 16, v237\n"
"v_mfma_f32_16x16x32_bf16 v[128:131], a[104:107], a[0:3], v[128:131]\n"
"ds_read_b128 v[232:235], v250 offset:14336\n"
"v_fmac_f32 v251, v251, v90\n"
"v_mfma_f32_16x16x32_bf16 v[128:131], a[108:111], a[4:7], v[128:131]\n"
"v_and_b32 v237, s82, v237\n"
"v_fmac_f32 v237, v237, v91\n"
"v_mfma_f32_16x16x32_bf16 v[132:135], a[104:107], a[8:11], v[132:135]\n"
".Lpad7_%=:\n"
"s_cbranch_execz .Lpad11_%=\n"
"v_cvt_pk_bf16_f32 v89, v251, v237\n"
"v_lshlrev_b32 v251, 16, v238\n"
"v_mfma_f32_16x16x32_bf16 v[132:135], a[108:111], a[12:15], v[132:135]\n"
"s_waitcnt lgkmcnt(9)\n"
"v_fmac_f32 v251, v251, v120\n"
"v_and_b32 v238, s82, v238\n"
"v_mfma_f32_16x16x32_bf16 v[136:139], a[104:107], a[16:19], v[136:139]\n"
"buffer_load_dwordx4 v[28:31], v240, s[36:39], s81 offen offset:0 sc1\n"
"v_fmac_f32 v238, v238, v121\n"
"v_mfma_f32_16x16x32_bf16 v[136:139], a[108:111], a[20:23], v[136:139]\n"
"v_cvt_pk_bf16_f32 v90, v251, v238\n"
"v_lshlrev_b32 v251, 16, v239\n"
"v_mfma_f32_16x16x32_bf16 v[140:143], a[104:107], a[24:27], v[140:143]\n"
"buffer_load_dwordx4 a[100:103], v240, s[36:39], s80 offen offset:1024 sc1\n"
"v_fmac_f32 v251, v251, v122\n"
"v_mfma_f32_16x16x32_bf16 v[140:143], a[108:111], a[28:31], v[140:143]\n"
"s_waitcnt lgkmcnt(5)\n"
"v_and_b32 v239, s82, v239\n"
"v_fmac_f32 v239, v239, v123\n"
"v_mfma_f32_16x16x32_bf16 v[144:147], a[104:107], a[32:35], v[144:147]\n"
"v_cvt_pk_bf16_f32 v91, v251, v239\n"
"v_mfma_f32_16x16x32_bf16 v[144:147], a[108:111], a[36:39], v[144:147]\n"
"s_waitcnt lgkmcnt(1)\n"
"s_barrier\n"
"v_mfma_f32_16x16x32_bf16 v[148:151], a[104:107], a[40:43], v[148:151]\n"
"buffer_store_dwordx4 v[88:91], v240, s[52:55], s78 offen offset:1024 sc1\n"
"v_mfma_f32_16x16x32_bf16 v[148:151], a[108:111], a[44:47], v[148:151]\n"
"ds_write_b128 v249, v[88:91] offset:45056\n"
"v_mfma_f32_16x16x32_bf16 v[152:155], a[104:107], a[48:51], v[152:155]\n"
"v_mfma_f32_16x16x32_bf16 v[152:155], a[108:111], a[52:55], v[152:155]\n"
"v_mfma_f32_16x16x32_bf16 v[156:159], a[104:107], a[56:59], v[156:159]\n"
"buffer_load_dwordx4 a[104:107], v240, s[36:39], s80 offen offset:2048 sc1\n"
"v_mfma_f32_16x16x32_bf16 v[156:159], a[108:111], a[60:63], v[156:159]\n"
"s_waitcnt vmcnt(16)\n"
"v_mfma_f32_16x16x32_bf16 v[160:163], v[224:227], a[0:3], v[160:163]\n"
"buffer_load_dwordx4 v[60:63], v240, s[36:39], s81 offen offset:1024 sc1\n"
"v_mfma_f32_16x16x32_bf16 v[160:163], v[228:231], a[4:7], v[160:163]\n"
"v_mfma_f32_16x16x32_bf16 v[164:167], v[224:227], a[8:11], v[164:167]\n"
"v_mfma_f32_16x16x32_bf16 v[164:167], v[228:231], a[12:15], v[164:167]\n"
"v_mfma_f32_16x16x32_bf16 v[168:171], v[224:227], a[16:19], v[168:171]\n"
"v_mfma_f32_16x16x32_bf16 v[168:171], v[228:231], a[20:23], v[168:171]\n"
"v_mfma_f32_16x16x32_bf16 v[172:175], v[224:227], a[24:27], v[172:175]\n"
"buffer_load_dwordx4 a[108:111], v240, s[36:39], s80 offen offset:3072 sc1\n"
"v_mfma_f32_16x16x32_bf16 v[172:175], v[228:231], a[28:31], v[172:175]\n"
"s_add_u32 s80, s80, 0x1000\n"
"v_mfma_f32_16x16x32_bf16 v[176:179], v[224:227], a[32:35], v[176:179]\n"
"v_mfma_f32_16x16x32_bf16 v[176:179], v[228:231], a[36:39], v[176:179]\n"
"v_mfma_f32_16x16x32_bf16 v[180:183], v[224:227], a[40:43], v[180:183]\n"
"v_mfma_f32_16x16x32_bf16 v[180:183], v[228:231], a[44:47], v[180:183]\n"
"v_mfma_f32_16x16x32_bf16 v[184:187], v[224:227], a[48:51], v[184:187]\n"
"v_mfma_f32_16x16x32_bf16 v[184:187], v[228:231], a[52:55], v[184:187]\n"
"v_mfma_f32_16x16x32_bf16 v[188:191], v[224:227], a[56:59], v[188:191]\n"
"buffer_load_dwordx4 v[224:227], v240, s[36:39], s80 offen offset:0 sc1\n"
"v_mfma_f32_16x16x32_bf16 v[188:191], v[228:231], a[60:63], v[188:191]\n"
"s_waitcnt vmcnt(16)\n"
"v_mfma_f32_16x16x32_bf16 v[192:195], a[64:67], a[0:3], v[192:195]\n"
"buffer_load_dwordx4 v[124:127], v240, s[36:39], s81 offen offset:3072 sc1\n"
"v_mfma_f32_16x16x32_bf16 v[192:195], a[68:71], a[4:7], v[192:195]\n"
"v_mfma_f32_16x16x32_bf16 v[196:199], a[64:67], a[8:11], v[196:199]\n"
"v_mfma_f32_16x16x32_bf16 v[196:199], a[68:71], a[12:15], v[196:199]\n"
"v_mfma_f32_16x16x32_bf16 v[200:203], a[64:67], a[16:19], v[200:203]\n"
"ds_read_b128 v[236:239], v250 offset:0\n"
"v_mfma_f32_16x16x32_bf16 v[200:203], a[68:71], a[20:23], v[200:203]\n"
"s_waitcnt lgkmcnt(2)\n"
"v_lshlrev_b32 v251, 16, v232\n"
"v_fmac_f32 v251, v251, v156\n"
"v_mfma_f32_16x16x32_bf16 v[204:207], a[64:67], a[24:27], v[204:207]\n"
"buffer_load_dwordx4 v[228:231], v240, s[36:39], s80 offen offset:1024 sc1\n"
"v_and_b32 v232, s82, v232\n"
"v_mfma_f32_16x16x32_bf16 v[204:207], a[68:71], a[28:31], v[204:207]\n"
"v_fmac_f32 v232, v232, v157\n"
"v_cvt_pk_bf16_f32 v156, v251, v232\n"
"v_mfma_f32_16x16x32_bf16 v[208:211], a[64:67], a[32:35], v[208:211]\n"
"v_lshlrev_b32 v251, 16, v233\n"
"v_fmac_f32 v251, v251, v158\n"
"v_mfma_f32_16x16x32_bf16 v[208:211], a[68:71], a[36:39], v[208:211]\n"
"v_and_b32 v233, s82, v233\n"
"v_fmac_f32 v233, v233, v159\n"
"v_mfma_f32_16x16x32_bf16 v[212:215], a[64:67], a[40:43], v[212:215]\n"
"v_cvt_pk_bf16_f32 v157, v251, v233\n"
"v_lshlrev_b32 v251, 16, v234\n"
"v_mfma_f32_16x16x32_bf16 v[212:215], a[68:71], a[44:47], v[212:215]\n"
"v_fmac_f32 v251, v251, v188\n"
"v_and_b32 v234, s82, v234\n"
"v_mfma_f32_16x16x32_bf16 v[216:219], a[64:67], a[48:51], v[216:219]\n"
"v_fmac_f32 v234, v234, v189\n"
"v_cvt_pk_bf16_f32 v158, v251, v234\n"
"v_mfma_f32_16x16x32_bf16 v[216:219], a[68:71], a[52:55], v[216:219]\n"
"v_lshlrev_b32 v251, 16, v235\n"
"v_fmac_f32 v251, v251, v190\n"
"v_mfma_f32_16x16x32_bf16 v[220:223], a[64:67], a[56:59], v[220:223]\n"
"buffer_load_dwordx4 a[64:67], v240, s[36:39], s80 offen offset:2048 sc1\n"
"v_and_b32 v235, s82, v235\n"
"v_mfma_f32_16x16x32_bf16 v[220:223], a[68:71], a[60:63], v[220:223]\n"
"s_waitcnt vmcnt(16)\n"
"v_fmac_f32 v235, v235, v191\n"
"v_cvt_pk_bf16_f32 v159, v251, v235\n"
"v_mfma_f32_16x16x32_bf16 a[112:115], a[72:75], a[0:3], a[112:115]\n"
"ds_read_b128 a[0:3], v243 offset:0\n"
"buffer_load_dwordx4 v[92:95], v240, s[36:39], s81 offen offset:2048 sc1\n"
"v_mfma_f32_16x16x32_bf16 a[112:115], a[76:79], a[4:7], a[112:115]\n"
"s_add_u32 s81, s81, 4096\n"
"ds_read_b128 a[4:7], v244 offset:0\n"
"v_mfma_f32_16x16x32_bf16 a[116:119], a[72:75], a[8:11], a[116:119]\n"
"ds_read_b128 a[8:11], v243 offset:2048\n"
"v_mfma_f32_16x16x32_bf16 a[116:119], a[76:79], a[12:15], a[116:119]\n"
"ds_read_b128 a[12:15], v244 offset:2048\n"
"v_mfma_f32_16x16x32_bf16 a[120:123], a[72:75], a[16:19], a[120:123]\n"
"ds_read_b128 a[16:19], v243 offset:4096\n"
"v_mfma_f32_16x16x32_bf16 a[120:123], a[76:79], a[20:23], a[120:123]\n"
"ds_read_b128 a[20:23], v244 offset:4096\n"
"v_mfma_f32_16x16x32_bf16 a[124:127], a[72:75], a[24:27], a[124:127]\n"
"ds_read_b128 a[24:27], v243 offset:6144\n"
"buffer_load_dwordx4 a[68:71], v240, s[36:39], s80 offen offset:3072 sc1\n"
"v_mfma_f32_16x16x32_bf16 a[124:127], a[76:79], a[28:31], a[124:127]\n"
"s_add_u32 s80, s80, 0x1000\n"
"ds_read_b128 a[28:31], v244 offset:6144\n"
"v_mfma_f32_16x16x32_bf16 a[128:131], a[72:75], a[32:35], a[128:131]\n"
"ds_read_b128 a[32:35], v243 offset:8192\n"
"v_mfma_f32_16x16x32_bf16 a[128:131], a[76:79], a[36:39], a[128:131]\n"
"ds_read_b128 a[36:39], v244 offset:8192\n"
"v_mfma_f32_16x16x32_bf16 a[132:135], a[72:75], a[40:43], a[132:135]\n"
"ds_read_b128 a[40:43], v243 offset:10240\n"
"v_mfma_f32_16x16x32_bf16 a[132:135], a[76:79], a[44:47], a[132:135]\n"
"ds_read_b128 a[44:47], v244 offset:10240\n"
"v_mfma_f32_16x16x32_bf16 a[136:139], a[72:75], a[48:51], a[136:139]\n"
"ds_read_b128 a[48:51], v243 offset:12288\n"
"v_mfma_f32_16x16x32_bf16 a[136:139], a[76:79], a[52:55], a[136:139]\n"
"ds_read_b128 a[52:55], v244 offset:12288\n"
"v_mfma_f32_16x16x32_bf16 a[140:143], a[72:75], a[56:59], a[140:143]\n"
"ds_read_b128 a[56:59], v243 offset:14336\n"
"buffer_load_dwordx4 a[72:75], v240, s[36:39], s80 offen offset:0 sc1\n"
"v_mfma_f32_16x16x32_bf16 a[140:143], a[76:79], a[60:63], a[140:143]\n"
"s_waitcnt vmcnt(12) lgkmcnt(11)\n"
"ds_read_b128 a[60:63], v244 offset:14336\n"
"v_lshlrev_b32 v251, 16, v236\n"
"v_mfma_f32_16x16x32_bf16 v[0:3], a[80:83], a[0:3], v[28:31]\n"
"ds_read_b128 v[232:235], v250 offset:2048\n"
"v_mfma_f32_16x16x32_bf16 v[0:3], a[84:87], a[4:7], v[0:3]\n"
"buffer_store_dwordx4 v[156:159], v240, s[52:55], s79 offen offset:2048 sc1\n"
"v_mfma_f32_16x16x32_bf16 v[4:7], a[80:83], a[8:11], v[28:31]\n"
"ds_write_b128 v250, v[156:159] offset:14336\n"
"v_mfma_f32_16x16x32_bf16 v[4:7], a[84:87], a[12:15], v[4:7]\n"
"s_waitcnt lgkmcnt(10)\n"
"v_fmac_f32 v251, v251, v128\n"
"v_and_b32 v236, s82, v236\n"
"v_mfma_f32_16x16x32_bf16 v[8:11], a[80:83], a[16:19], v[28:31]\n"
"v_fmac_f32 v236, v236, v129\n"
"v_cvt_pk_bf16_f32 v128, v251, v236\n"
"v_mfma_f32_16x16x32_bf16 v[8:11], a[84:87], a[20:23], v[8:11]\n"
"v_lshlrev_b32 v251, 16, v237\n"
"v_fmac_f32 v251, v251, v130\n"
"v_mfma_f32_16x16x32_bf16 v[12:15], a[80:83], a[24:27], v[28:31]\n"
"buffer_load_dwordx4 a[76:79], v240, s[36:39], s80 offen offset:1024 sc1\n"
"v_and_b32 v237, s82, v237\n"
"v_mfma_f32_16x16x32_bf16 v[12:15], a[84:87], a[28:31], v[12:15]\n"
"s_waitcnt lgkmcnt(6)\n"
"v_fmac_f32 v237, v237, v131\n"
"v_cvt_pk_bf16_f32 v129, v251, v237\n"
"v_mfma_f32_16x16x32_bf16 v[16:19], a[80:83], a[32:35], v[28:31]\n"
"v_lshlrev_b32 v251, 16, v238\n"
"v_fmac_f32 v251, v251, v160\n"
"v_mfma_f32_16x16x32_bf16 v[16:19], a[84:87], a[36:39], v[16:19]\n"
"v_and_b32 v238, s82, v238\n"
"v_fmac_f32 v238, v238, v161\n"
"v_mfma_f32_16x16x32_bf16 v[20:23], a[80:83], a[40:43], v[28:31]\n"
"v_cvt_pk_bf16_f32 v130, v251, v238\n"
"v_lshlrev_b32 v251, 16, v239\n"
"v_mfma_f32_16x16x32_bf16 v[20:23], a[84:87], a[44:47], v[20:23]\n"
"s_waitcnt lgkmcnt(2)\n"
"v_fmac_f32 v251, v251, v162\n"
"v_and_b32 v239, s82, v239\n"
"v_mfma_f32_16x16x32_bf16 v[24:27], a[80:83], a[48:51], v[28:31]\n"
"v_fmac_f32 v239, v239, v163\n"
"v_cvt_pk_bf16_f32 v131, v251, v239\n"
"v_mfma_f32_16x16x32_bf16 v[24:27], a[84:87], a[52:55], v[24:27]\n"
"ds_write_b128 v250, v[128:131] offset:0\n"
"v_mfma_f32_16x16x32_bf16 v[28:31], a[80:83], a[56:59], v[28:31]\n"
"buffer_load_dwordx4 a[80:83], v240, s[36:39], s80 offen offset:2048 sc1\n"
"v_mfma_f32_16x16x32_bf16 v[28:31], a[84:87], a[60:63], v[28:31]\n"
"s_waitcnt vmcnt(11)\n"
"buffer_store_dwordx4 v[128:131], v240, s[52:55], s68 offen offset:2048 sc1\n"
"v_mfma_f32_16x16x32_bf16 v[32:35], a[88:91], a[0:3], v[60:63]\n"
"v_mfma_f32_16x16x32_bf16 v[32:35], a[92:95], a[4:7], v[32:35]\n"
"v_mfma_f32_16x16x32_bf16 v[36:39], a[88:91], a[8:11], v[60:63]\n"
"v_mfma_f32_16x16x32_bf16 v[36:39], a[92:95], a[12:15], v[36:39]\n"
"v_mfma_f32_16x16x32_bf16 v[40:43], a[88:91], a[16:19], v[60:63]\n"
"v_mfma_f32_16x16x32_bf16 v[40:43], a[92:95], a[20:23], v[40:43]\n"
"v_mfma_f32_16x16x32_bf16 v[44:47], a[88:91], a[24:27], v[60:63]\n"
"buffer_load_dwordx4 a[84:87], v240, s[36:39], s80 offen offset:3072 sc1\n"
"v_mfma_f32_16x16x32_bf16 v[44:47], a[92:95], a[28:31], v[44:47]\n"
"s_add_u32 s80, s80, 0x1000\n"
"v_mfma_f32_16x16x32_bf16 v[48:51], a[88:91], a[32:35], v[60:63]\n"
"v_mfma_f32_16x16x32_bf16 v[48:51], a[92:95], a[36:39], v[48:51]\n"
"v_mfma_f32_16x16x32_bf16 v[52:55], a[88:91], a[40:43], v[60:63]\n"
"ds_read_b128 v[236:239], v250 offset:4096\n"
"v_lshrrev_b32 v241, 2, v240\n"
"v_mfma_f32_16x16x32_bf16 v[52:55], a[92:95], a[44:47], v[52:55]\n"
"buffer_store_dword v242, v241, s[60:63], s85 offen offset:0 sc1\n"
"v_mfma_f32_16x16x32_bf16 v[56:59], a[88:91], a[48:51], v[60:63]\n"
"s_waitcnt lgkmcnt(3)\n"
"v_lshlrev_b32 v251, 16, v232\n"
"v_fmac_f32 v251, v251, v132\n"
"v_mfma_f32_16x16x32_bf16 v[56:59], a[92:95], a[52:55], v[56:59]\n"
"v_and_b32 v232, s82, v232\n"
"v_fmac_f32 v232, v232, v133\n"
"v_mfma_f32_16x16x32_bf16 v[60:63], a[88:91], a[56:59], v[60:63]\n"
"buffer_load_dwordx4 a[88:91], v240, s[36:39], s80 offen offset:0 sc1\n"
"v_cvt_pk_bf16_f32 v132, v251, v232\n"
"v_mfma_f32_16x16x32_bf16 v[60:63], a[92:95], a[60:63], v[60:63]\n"
"s_waitcnt vmcnt(12)\n"
"v_lshlrev_b32 v251, 16, v233\n"
"v_fmac_f32 v251, v251, v134\n"
"v_mfma_f32_16x16x32_bf16 v[96:99], a[96:99], a[0:3], v[124:127]\n"
"v_and_b32 v233, s82, v233\n"
"v_fmac_f32 v233, v233, v135\n"
"v_mfma_f32_16x16x32_bf16 v[96:99], a[100:103], a[4:7], v[96:99]\n"
"v_cvt_pk_bf16_f32 v133, v251, v233\n"
"v_lshlrev_b32 v251, 16, v234\n"
"v_mfma_f32_16x16x32_bf16 v[100:103], a[96:99], a[8:11], v[124:127]\n"
"v_fmac_f32 v251, v251, v164\n"
"v_and_b32 v234, s82, v234\n"
"v_mfma_f32_16x16x32_bf16 v[100:103], a[100:103], a[12:15], v[100:103]\n"
"v_fmac_f32 v234, v234, v165\n"
"v_cvt_pk_bf16_f32 v134, v251, v234\n"
"v_mfma_f32_16x16x32_bf16 v[104:107], a[96:99], a[16:19], v[124:127]\n"
"v_lshlrev_b32 v251, 16, v235\n"
"v_fmac_f32 v251, v251, v166\n"
"v_mfma_f32_16x16x32_bf16 v[104:107], a[100:103], a[20:23], v[104:107]\n"
"v_and_b32 v235, s82, v235\n"
"v_fmac_f32 v235, v235, v167\n"
"v_mfma_f32_16x16x32_bf16 v[108:111], a[96:99], a[24:27], v[124:127]\n"
"buffer_load_dwordx4 a[92:95], v240, s[36:39], s80 offen offset:1024 sc1\n"
"v_cvt_pk_bf16_f32 v135, v251, v235\n"
"v_mfma_f32_16x16x32_bf16 v[108:111], a[100:103], a[28:31], v[108:111]\n"
"buffer_store_dwordx4 v[132:135], v240, s[52:55], s69 offen offset:2048 sc1\n"
"v_mfma_f32_16x16x32_bf16 v[112:115], a[96:99], a[32:35], v[124:127]\n"
"ds_write_b128 v250, v[132:135] offset:2048\n"
"v_mfma_f32_16x16x32_bf16 v[112:115], a[100:103], a[36:39], v[112:115]\n"
"v_mfma_f32_16x16x32_bf16 v[116:119], a[96:99], a[40:43], v[124:127]\n"
"v_mfma_f32_16x16x32_bf16 v[116:119], a[100:103], a[44:47], v[116:119]\n"
"v_mfma_f32_16x16x32_bf16 v[120:123], a[96:99], a[48:51], v[124:127]\n"
"v_mfma_f32_16x16x32_bf16 v[120:123], a[100:103], a[52:55], v[120:123]\n"
"v_mfma_f32_16x16x32_bf16 v[124:127], a[96:99], a[56:59], v[124:127]\n"
"buffer_load_dwordx4 a[96:99], v240, s[36:39], s80 offen offset:2048 sc1\n"
"v_mfma_f32_16x16x32_bf16 v[124:127], a[100:103], a[60:63], v[124:127]\n"
"s_waitcnt vmcnt(12)\n"
"v_mfma_f32_16x16x32_bf16 v[64:67], a[104:107], a[0:3], v[92:95]\n"
"ds_read_b128 a[0:3], v243 offset:16384\n"
"v_mfma_f32_16x16x32_bf16 v[64:67], a[108:111], a[4:7], v[64:67]\n"
"ds_read_b128 a[4:7], v244 offset:16384\n"
"v_mfma_f32_16x16x32_bf16 v[68:71], a[104:107], a[8:11], v[92:95]\n"
"ds_read_b128 a[8:11], v243 offset:18432\n"
"v_mfma_f32_16x16x32_bf16 v[68:71], a[108:111], a[12:15], v[68:71]\n"
"ds_read_b128 a[12:15], v244 offset:18432\n"
"v_mfma_f32_16x16x32_bf16 v[72:75], a[104:107], a[16:19], v[92:95]\n"
"ds_read_b128 a[16:19], v243 offset:20480\n"
"v_mfma_f32_16x16x32_bf16 v[72:75], a[108:111], a[20:23], v[72:75]\n"
"ds_read_b128 a[20:23], v244 offset:20480\n"
"v_mfma_f32_16x16x32_bf16 v[76:79], a[104:107], a[24:27], v[92:95]\n"
"ds_read_b128 a[24:27], v243 offset:22528\n"
"buffer_load_dwordx4 a[100:103], v240, s[36:39], s80 offen offset:3072 sc1\n"
"v_mfma_f32_16x16x32_bf16 v[76:79], a[108:111], a[28:31], v[76:79]\n"
"s_add_u32 s80, s80, 0x1000\n"
"ds_read_b128 a[28:31], v244 offset:22528\n"
"v_mfma_f32_16x16x32_bf16 v[80:83], a[104:107], a[32:35], v[92:95]\n"
"ds_read_b128 a[32:35], v243 offset:24576\n"
"v_mfma_f32_16x16x32_bf16 v[80:83], a[108:111], a[36:39], v[80:83]\n"
"ds_read_b128 a[36:39], v244 offset:24576\n"
"v_mfma_f32_16x16x32_bf16 v[84:87], a[104:107], a[40:43], v[92:95]\n"
"ds_read_b128 a[40:43], v243 offset:26624\n"
"v_mfma_f32_16x16x32_bf16 v[84:87], a[108:111], a[44:47], v[84:87]\n"
"ds_read_b128 a[44:47], v244 offset:26624\n"
"v_mfma_f32_16x16x32_bf16 v[88:91], a[104:107], a[48:51], v[92:95]\n"
"ds_read_b128 a[48:51], v243 offset:28672\n"
"v_mfma_f32_16x16x32_bf16 v[88:91], a[108:111], a[52:55], v[88:91]\n"
"ds_read_b128 a[52:55], v244 offset:28672\n"
"v_mfma_f32_16x16x32_bf16 v[92:95], a[104:107], a[56:59], v[92:95]\n"
"ds_read_b128 a[56:59], v243 offset:30720\n"
"buffer_load_dwordx4 a[104:107], v240, s[36:39], s80 offen offset:0 sc1\n"
"v_mfma_f32_16x16x32_bf16 v[92:95], a[108:111], a[60:63], v[92:95]\n"
"s_waitcnt lgkmcnt(11)\n"
"ds_read_b128 a[60:63], v244 offset:30720\n"
"v_mfma_f32_16x16x32_bf16 v[0:3], v[224:227], a[0:3], v[0:3]\n"
"ds_read_b128 v[232:235], v250 offset:6144\n"
"v_mfma_f32_16x16x32_bf16 v[0:3], v[228:231], a[4:7], v[0:3]\n"
"ds_write_b128 v254, v[236:239] offset:0\n"
"v_mfma_f32_16x16x32_bf16 v[4:7], v[224:227], a[8:11], v[4:7]\n"
"v_lshlrev_b32 v251, 16, v236\n"
"v_fmac_f32 v251, v251, v136\n"
"v_mfma_f32_16x16x32_bf16 v[4:7], v[228:231], a[12:15], v[4:7]\n"
"s_waitcnt lgkmcnt(10)\n"
"v_and_b32 v236, s82, v236\n"
"v_fmac_f32 v236, v236, v137\n"
"v_mfma_f32_16x16x32_bf16 v[8:11], v[224:227], a[16:19], v[8:11]\n"
"v_cvt_pk_bf16_f32 v136, v251, v236\n"
"v_lshlrev_b32 v251, 16, v237\n"
"v_mfma_f32_16x16x32_bf16 v[8:11], v[228:231], a[20:23], v[8:11]\n"
"v_fmac_f32 v251, v251, v138\n"
"v_and_b32 v237, s82, v237\n"
"v_mfma_f32_16x16x32_bf16 v[12:15], v[224:227], a[24:27], v[12:15]\n"
"buffer_load_dwordx4 a[108:111], v240, s[36:39], s80 offen offset:1024 sc1\n"
"v_fmac_f32 v237, v237, v139\n"
"v_mfma_f32_16x16x32_bf16 v[12:15], v[228:231], a[28:31], v[12:15]\n"
"s_waitcnt lgkmcnt(6)\n"
"v_cvt_pk_bf16_f32 v137, v251, v237\n"
"v_lshlrev_b32 v251, 16, v238\n"
"v_mfma_f32_16x16x32_bf16 v[16:19], v[224:227], a[32:35], v[16:19]\n"
"v_fmac_f32 v251, v251, v168\n"
"v_and_b32 v238, s82, v238\n"
"v_mfma_f32_16x16x32_bf16 v[16:19], v[228:231], a[36:39], v[16:19]\n"
"v_fmac_f32 v238, v238, v169\n"
"v_cvt_pk_bf16_f32 v138, v251, v238\n"
"v_mfma_f32_16x16x32_bf16 v[20:23], v[224:227], a[40:43], v[20:23]\n"
"v_lshlrev_b32 v251, 16, v239\n"
"v_fmac_f32 v251, v251, v170\n"
"v_mfma_f32_16x16x32_bf16 v[20:23], v[228:231], a[44:47], v[20:23]\n"
"s_waitcnt lgkmcnt(2)\n"
"v_and_b32 v239, s82, v239\n"
"v_fmac_f32 v239, v239, v171\n"
"v_mfma_f32_16x16x32_bf16 v[24:27], v[224:227], a[48:51], v[24:27]\n"
"v_cvt_pk_bf16_f32 v139, v251, v239\n"
"v_mfma_f32_16x16x32_bf16 v[24:27], v[228:231], a[52:55], v[24:27]\n"
"ds_write_b128 v250, v[136:139] offset:4096\n"
"v_mfma_f32_16x16x32_bf16 v[28:31], v[224:227], a[56:59], v[28:31]\n"
"buffer_load_dwordx4 v[224:227], v240, s[36:39], s80 offen offset:2048 sc1\n"
"v_mfma_f32_16x16x32_bf16 v[28:31], v[228:231], a[60:63], v[28:31]\n"
"s_waitcnt vmcnt(15)\n"
"buffer_store_dwordx4 v[136:139], v240, s[52:55], s70 offen offset:2048 sc1\n"
"v_mfma_f32_16x16x32_bf16 v[32:35], a[64:67], a[0:3], v[32:35]\n"
"ds_read_b128 v[236:239], v250 offset:8192\n"
"v_mfma_f32_16x16x32_bf16 v[32:35], a[68:71], a[4:7], v[32:35]\n"
"s_waitcnt lgkmcnt(3)\n"
"ds_write_b128 v254, v[232:235] offset:1024\n"
"v_mfma_f32_16x16x32_bf16 v[36:39], a[64:67], a[8:11], v[36:39]\n"
"v_lshlrev_b32 v251, 16, v232\n"
"v_fmac_f32 v251, v251, v140\n"
"v_mfma_f32_16x16x32_bf16 v[36:39], a[68:71], a[12:15], v[36:39]\n"
"v_and_b32 v232, s82, v232\n"
"v_fmac_f32 v232, v232, v141\n"
"v_mfma_f32_16x16x32_bf16 v[40:43], a[64:67], a[16:19], v[40:43]\n"
"v_cvt_pk_bf16_f32 v140, v251, v232\n"
"v_lshlrev_b32 v251, 16, v233\n"
"v_mfma_f32_16x16x32_bf16 v[40:43], a[68:71], a[20:23], v[40:43]\n"
"v_fmac_f32 v251, v251, v142\n"
"v_and_b32 v233, s82, v233\n"
"v_mfma_f32_16x16x32_bf16 v[44:47], a[64:67], a[24:27], v[44:47]\n"
"buffer_load_dwordx4 v[228:231], v240, s[36:39], s80 offen offset:3072 sc1\n"
"v_fmac_f32 v233, v233, v143\n"
"v_mfma_f32_16x16x32_bf16 v[44:47], a[68:71], a[28:31], v[44:47]\n"
"s_add_u32 s80, s80, 0x1000\n"
"v_cvt_pk_bf16_f32 v141, v251, v233\n"
"v_lshlrev_b32 v251, 16, v234\n"
"v_mfma_f32_16x16x32_bf16 v[48:51], a[64:67], a[32:35], v[48:51]\n"
"v_fmac_f32 v251, v251, v172\n"
"v_and_b32 v234, s82, v234\n"
"v_mfma_f32_16x16x32_bf16 v[48:51], a[68:71], a[36:39], v[48:51]\n"
"v_fmac_f32 v234, v234, v173\n"
"v_cvt_pk_bf16_f32 v142, v251, v234\n"
"v_mfma_f32_16x16x32_bf16 v[52:55], a[64:67], a[40:43], v[52:55]\n"
"v_lshlrev_b32 v251, 16, v235\n"
"v_fmac_f32 v251, v251, v174\n"
"v_mfma_f32_16x16x32_bf16 v[52:55], a[68:71], a[44:47], v[52:55]\n"
"v_and_b32 v235, s82, v235\n"
"v_fmac_f32 v235, v235, v175\n"
"v_mfma_f32_16x16x32_bf16 v[56:59], a[64:67], a[48:51], v[56:59]\n"
"v_cvt_pk_bf16_f32 v143, v251, v235\n"
"v_mfma_f32_16x16x32_bf16 v[56:59], a[68:71], a[52:55], v[56:59]\n"
"ds_write_b128 v250, v[140:143] offset:6144\n"
"v_mfma_f32_16x16x32_bf16 v[60:63], a[64:67], a[56:59], v[60:63]\n"
"buffer_load_dwordx4 a[64:67], v240, s[36:39], s80 offen offset:0 sc1\n"
"v_mfma_f32_16x16x32_bf16 v[60:63], a[68:71], a[60:63], v[60:63]\n"
"s_waitcnt vmcnt(15)\n"
"buffer_store_dwordx4 v[140:143], v240, s[52:55], s71 offen offset:2048 sc1\n"
"v_mfma_f32_16x16x32_bf16 v[64:67], a[72:75], a[0:3], v[64:67]\n"
"v_mfma_f32_16x16x32_bf16 v[64:67], a[76:79], a[4:7], v[64:67]\n"
"v_mfma_f32_16x16x32_bf16 v[68:71], a[72:75], a[8:11], v[68:71]\n"
"v_mfma_f32_16x16x32_bf16 v[68:71], a[76:79], a[12:15], v[68:71]\n"
"v_mfma_f32_16x16x32_bf16 v[72:75], a[72:75], a[16:19], v[72:75]\n"
"v_mfma_f32_16x16x32_bf16 v[72:75], a[76:79], a[20:23], v[72:75]\n"
"v_mfma_f32_16x16x32_bf16 v[76:79], a[72:75], a[24:27], v[76:79]\n"
"buffer_load_dwordx4 a[68:71], v240, s[36:39], s80 offen offset:1024 sc1\n"
"v_mfma_f32_16x16x32_bf16 v[76:79], a[76:79], a[28:31], v[76:79]\n"
"v_mfma_f32_16x16x32_bf16 v[80:83], a[72:75], a[32:35], v[80:83]\n"
"v_mfma_f32_16x16x32_bf16 v[80:83], a[76:79], a[36:39], v[80:83]\n"
"v_mfma_f32_16x16x32_bf16 v[84:87], a[72:75], a[40:43], v[84:87]\n"
"v_mfma_f32_16x16x32_bf16 v[84:87], a[76:79], a[44:47], v[84:87]\n"
"ds_read_b128 v[232:235], v250 offset:10240\n"
"v_mfma_f32_16x16x32_bf16 v[88:91], a[72:75], a[48:51], v[88:91]\n"
"s_waitcnt lgkmcnt(3)\n"
"ds_write_b128 v254, v[236:239] offset:2048\n"
"v_mfma_f32_16x16x32_bf16 v[88:91], a[76:79], a[52:55], v[88:91]\n"
"s_barrier\n"
"v_mfma_f32_16x16x32_bf16 v[92:95], a[72:75], a[56:59], v[92:95]\n"
"buffer_load_dwordx4 a[72:75], v240, s[36:39], s80 offen offset:2048 sc1\n"
"v_lshlrev_b32 v251, 16, v236\n"
"v_mfma_f32_16x16x32_bf16 v[92:95], a[76:79], a[60:63], v[92:95]\n"
"s_waitcnt vmcnt(15)\n"
"v_fmac_f32 v251, v251, v144\n"
"v_and_b32 v236, s82, v236\n"
"v_mfma_f32_16x16x32_bf16 v[96:99], a[80:83], a[0:3], v[96:99]\n"
"ds_read_b128 a[0:3], v243 offset:32768\n"
"v_fmac_f32 v236, v236, v145\n"
"v_mfma_f32_16x16x32_bf16 v[96:99], a[84:87], a[4:7], v[96:99]\n"
"ds_read_b128 a[4:7], v244 offset:32768\n"
"v_cvt_pk_bf16_f32 v144, v251, v236\n"
"v_mfma_f32_16x16x32_bf16 v[100:103], a[80:83], a[8:11], v[100:103]\n"
"ds_read_b128 a[8:11], v243 offset:34816\n"
"v_lshlrev_b32 v251, 16, v237\n"
"v_mfma_f32_16x16x32_bf16 v[100:103], a[84:87], a[12:15], v[100:103]\n"
"ds_read_b128 a[12:15], v244 offset:34816\n"
"v_fmac_f32 v251, v251, v146\n"
"v_mfma_f32_16x16x32_bf16 v[104:107], a[80:83], a[16:19], v[104:107]\n"
"ds_read_b128 a[16:19], v243 offset:36864\n"
"v_and_b32 v237, s82, v237\n"
"v_mfma_f32_16x16x32_bf16 v[104:107], a[84:87], a[20:23], v[104:107]\n"
"ds_read_b128 a[20:23], v244 offset:36864\n"
"v_fmac_f32 v237, v237, v147\n"
"v_mfma_f32_16x16x32_bf16 v[108:111], a[80:83], a[24:27], v[108:111]\n"
"ds_read_b128 a[24:27], v243 offset:38912\n"
"buffer_load_dwordx4 a[76:79], v240, s[36:39], s80 offen offset:3072 sc1\n"
"v_mfma_f32_16x16x32_bf16 v[108:111], a[84:87], a[28:31], v[108:111]\n"
"s_add_u32 s80, s80, 0x1000\n"
"ds_read_b128 a[28:31], v244 offset:38912\n"
"v_cvt_pk_bf16_f32 v145, v251, v237\n"
"v_mfma_f32_16x16x32_bf16 v[112:115], a[80:83], a[32:35], v[112:115]\n"
"ds_read_b128 a[32:35], v243 offset:40960\n"
"v_lshlrev_b32 v251, 16, v238\n"
"v_mfma_f32_16x16x32_bf16 v[112:115], a[84:87], a[36:39], v[112:115]\n"
"ds_read_b128 a[36:39], v244 offset:40960\n"
"v_fmac_f32 v251, v251, v176\n"
"v_mfma_f32_16x16x32_bf16 v[116:119], a[80:83], a[40:43], v[116:119]\n"
"ds_read_b128 a[40:43], v243 offset:43008\n"
"v_and_b32 v238, s82, v238\n"
"v_mfma_f32_16x16x32_bf16 v[116:119], a[84:87], a[44:47], v[116:119]\n"
"ds_read_b128 a[44:47], v244 offset:43008\n"
"v_fmac_f32 v238, v238, v177\n"
"v_mfma_f32_16x16x32_bf16 v[120:123], a[80:83], a[48:51], v[120:123]\n"
"ds_read_b128 a[48:51], v243 offset:45056\n"
"v_cvt_pk_bf16_f32 v146, v251, v238\n"
"v_mfma_f32_16x16x32_bf16 v[120:123], a[84:87], a[52:55], v[120:123]\n"
"ds_read_b128 a[52:55], v244 offset:45056\n"
"v_lshlrev_b32 v251, 16, v239\n"
"v_mfma_f32_16x16x32_bf16 v[124:127], a[80:83], a[56:59], v[124:127]\n"
"ds_read_b128 a[56:59], v243 offset:47104\n"
"buffer_load_dwordx4 a[80:83], v240, s[36:39], s80 offen offset:0 sc1\n"
"v_mfma_f32_16x16x32_bf16 v[124:127], a[84:87], a[60:63], v[124:127]\n"
"s_waitcnt vmcnt(14) lgkmcnt(11)\n"
"ds_read_b128 a[60:63], v244 offset:47104\n"
"v_fmac_f32 v251, v251, v178\n"
"v_mfma_f32_16x16x32_bf16 v[0:3], a[88:91], a[0:3], v[0:3]\n"
"v_and_b32 v239, s82, v239\n"
"v_fmac_f32 v239, v239, v179\n"
"v_mfma_f32_16x16x32_bf16 v[0:3], a[92:95], a[4:7], v[0:3]\n"
"v_cvt_pk_bf16_f32 v147, v251, v239\n"
"v_mfma_f32_16x16x32_bf16 v[4:7], a[88:91], a[8:11], v[4:7]\n"
"buffer_store_dwordx4 v[144:147], v240, s[52:55], s76 offen offset:2048 sc1\n"
"v_mfma_f32_16x16x32_bf16 v[4:7], a[92:95], a[12:15], v[4:7]\n"
"s_waitcnt lgkmcnt(8)\n"
"ds_write_b128 v250, v[144:147] offset:8192\n"
"v_mfma_f32_16x16x32_bf16 v[8:11], a[88:91], a[16:19], v[8:11]\n"
"v_mfma_f32_16x16x32_bf16 v[8:11], a[92:95], a[20:23], v[8:11]\n"
"v_mfma_f32_16x16x32_bf16 v[12:15], a[88:91], a[24:27], v[12:15]\n"
"ds_read_b128 v[236:239], v250 offset:12288\n"
"buffer_load_dwordx4 a[84:87], v240, s[36:39], s80 offen offset:1024 sc1\n"
"v_mfma_f32_16x16x32_bf16 v[12:15], a[92:95], a[28:31], v[12:15]\n"
"s_waitcnt lgkmcnt(6)\n"
"ds_write_b128 v254, v[232:235] offset:3072\n"
"v_mfma_f32_16x16x32_bf16 v[16:19], a[88:91], a[32:35], v[16:19]\n"
"v_lshlrev_b32 v251, 16, v232\n"
"v_fmac_f32 v251, v251, v148\n"
"v_mfma_f32_16x16x32_bf16 v[16:19], a[92:95], a[36:39], v[16:19]\n"
"v_and_b32 v232, s82, v232\n"
"v_fmac_f32 v232, v232, v149\n"
"v_mfma_f32_16x16x32_bf16 v[20:23], a[88:91], a[40:43], v[20:23]\n"
"v_cvt_pk_bf16_f32 v148, v251, v232\n"
"v_lshlrev_b32 v251, 16, v233\n"
"v_mfma_f32_16x16x32_bf16 v[20:23], a[92:95], a[44:47], v[20:23]\n"
"s_waitcnt lgkmcnt(3)\n"
"v_fmac_f32 v251, v251, v150\n"
"v_and_b32 v233, s82, v233\n"
"v_mfma_f32_16x16x32_bf16 v[24:27], a[88:91], a[48:51], v[24:27]\n"
"v_fmac_f32 v233, v233, v151\n"
"v_cvt_pk_bf16_f32 v149, v251, v233\n"
"v_mfma_f32_16x16x32_bf16 v[24:27], a[92:95], a[52:55], v[24:27]\n"
"v_lshlrev_b32 v251, 16, v234\n"
"v_fmac_f32 v251, v251, v180\n"
"v_mfma_f32_16x16x32_bf16 v[28:31], a[88:91], a[56:59], v[28:31]\n"
"buffer_load_dwordx4 a[88:91], v240, s[36:39], s80 offen offset:2048 sc1\n"
"v_and_b32 v234, s82, v234\n"
"v_mfma_f32_16x16x32_bf16 v[28:31], a[92:95], a[60:63], v[28:31]\n"
"s_waitcnt vmcnt(14)\n"
"v_fmac_f32 v234, v234, v181\n"
"v_cvt_pk_bf16_f32 v150, v251, v234\n"
"v_mfma_f32_16x16x32_bf16 v[32:35], a[96:99], a[0:3], v[32:35]\n"
"v_lshlrev_b32 v251, 16, v235\n"
"v_fmac_f32 v251, v251, v182\n"
"v_mfma_f32_16x16x32_bf16 v[32:35], a[100:103], a[4:7], v[32:35]\n"
"v_and_b32 v235, s82, v235\n"
"v_fmac_f32 v235, v235, v183\n"
"v_mfma_f32_16x16x32_bf16 v[36:39], a[96:99], a[8:11], v[36:39]\n"
"v_cvt_pk_bf16_f32 v151, v251, v235\n"
"v_mfma_f32_16x16x32_bf16 v[36:39], a[100:103], a[12:15], v[36:39]\n"
"buffer_store_dwordx4 v[148:151], v240, s[52:55], s77 offen offset:2048 sc1\n"
"v_mfma_f32_16x16x32_bf16 v[40:43], a[96:99], a[16:19], v[40:43]\n"
"ds_write_b128 v250, v[148:151] offset:10240\n"
"v_mfma_f32_16x16x32_bf16 v[40:43], a[100:103], a[20:23], v[40:43]\n"
"v_mfma_f32_16x16x32_bf16 v[44:47], a[96:99], a[24:27], v[44:47]\n"
"buffer_load_dwordx4 a[92:95], v240, s[36:39], s80 offen offset:3072 sc1\n"
"v_mfma_f32_16x16x32_bf16 v[44:47], a[100:103], a[28:31], v[44:47]\n"
"s_add_u32 s80, s80, 0x1000\n"
"v_mfma_f32_16x16x32_bf16 v[48:51], a[96:99], a[32:35], v[48:51]\n"
"v_mfma_f32_16x16x32_bf16 v[48:51], a[100:103], a[36:39], v[48:51]\n"
"v_mfma_f32_16x16x32_bf16 v[52:55], a[96:99], a[40:43], v[52:55]\n"
"v_mfma_f32_16x16x32_bf16 v[52:55], a[100:103], a[44:47], v[52:55]\n"
"v_mfma_f32_16x16x32_bf16 v[56:59], a[96:99], a[48:51], v[56:59]\n"
"v_mfma_f32_16x16x32_bf16 v[56:59], a[100:103], a[52:55], v[56:59]\n"
"v_mfma_f32_16x16x32_bf16 v[60:63], a[96:99], a[56:59], v[60:63]\n"
"buffer_load_dwordx4 a[96:99], v240, s[36:39], s80 offen offset:0 sc1\n"
"v_mfma_f32_16x16x32_bf16 v[60:63], a[100:103], a[60:63], v[60:63]\n"
"s_waitcnt vmcnt(15)\n"
"v_mfma_f32_16x16x32_bf16 v[64:67], a[104:107], a[0:3], v[64:67]\n"
"v_mfma_f32_16x16x32_bf16 v[64:67], a[108:111], a[4:7], v[64:67]\n"
"s_waitcnt lgkmcnt(2)\n"
"ds_read_b128 v[232:235], v250 offset:47104\n"
"v_lshlrev_b32 v251, 16, v236\n"
"v_mfma_f32_16x16x32_bf16 v[68:71], a[104:107], a[8:11], v[68:71]\n"
"v_fmac_f32 v251, v251, v152\n"
"v_and_b32 v236, s82, v236\n"
"v_mfma_f32_16x16x32_bf16 v[68:71], a[108:111], a[12:15], v[68:71]\n"
"v_fmac_f32 v236, v236, v153\n"
"v_cvt_pk_bf16_f32 v152, v251, v236\n"
"v_mfma_f32_16x16x32_bf16 v[72:75], a[104:107], a[16:19], v[72:75]\n"
"v_lshlrev_b32 v251, 16, v237\n"
"v_fmac_f32 v251, v251, v154\n"
"v_mfma_f32_16x16x32_bf16 v[72:75], a[108:111], a[20:23], v[72:75]\n"
"v_and_b32 v237, s82, v237\n"
"v_fmac_f32 v237, v237, v155\n"
"v_mfma_f32_16x16x32_bf16 v[76:79], a[104:107], a[24:27], v[76:79]\n"
"buffer_load_dwordx4 a[100:103], v240, s[36:39], s80 offen offset:1024 sc1\n"
"v_cvt_pk_bf16_f32 v153, v251, v237\n"
"v_mfma_f32_16x16x32_bf16 v[76:79], a[108:111], a[28:31], v[76:79]\n"
"v_lshlrev_b32 v251, 16, v238\n"
"v_fmac_f32 v251, v251, v184\n"
"v_mfma_f32_16x16x32_bf16 v[80:83], a[104:107], a[32:35], v[80:83]\n"
"v_and_b32 v238, s82, v238\n"
"v_fmac_f32 v238, v238, v185\n"
"v_mfma_f32_16x16x32_bf16 v[80:83], a[108:111], a[36:39], v[80:83]\n"
"v_cvt_pk_bf16_f32 v154, v251, v238\n"
"v_lshlrev_b32 v251, 16, v239\n"
"v_mfma_f32_16x16x32_bf16 v[84:87], a[104:107], a[40:43], v[84:87]\n"
"v_fmac_f32 v251, v251, v186\n"
"v_and_b32 v239, s82, v239\n"
"v_mfma_f32_16x16x32_bf16 v[84:87], a[108:111], a[44:47], v[84:87]\n"
"v_fmac_f32 v239, v239, v187\n"
"v_cvt_pk_bf16_f32 v155, v251, v239\n"
"v_mfma_f32_16x16x32_bf16 v[88:91], a[104:107], a[48:51], v[88:91]\n"
"ds_write_b128 v250, v[152:155] offset:12288\n"
"v_mfma_f32_16x16x32_bf16 v[88:91], a[108:111], a[52:55], v[88:91]\n"
"v_mfma_f32_16x16x32_bf16 v[92:95], a[104:107], a[56:59], v[92:95]\n"
"buffer_load_dwordx4 a[104:107], v240, s[36:39], s80 offen offset:2048 sc1\n"
"v_mfma_f32_16x16x32_bf16 v[92:95], a[108:111], a[60:63], v[92:95]\n"
"s_waitcnt vmcnt(14)\n"
"buffer_load_dword v241, v240, s[64:67], 0 offen offset:0 sc1\n"
"v_mfma_f32_16x16x32_bf16 v[96:99], v[224:227], a[0:3], v[96:99]\n"
"ds_read_b128 a[0:3], v243 offset:49152\n"
"v_mfma_f32_16x16x32_bf16 v[96:99], v[228:231], a[4:7], v[96:99]\n"
"ds_read_b128 a[4:7], v244 offset:49152\n"
"v_mfma_f32_16x16x32_bf16 v[100:103], v[224:227], a[8:11], v[100:103]\n"
"ds_read_b128 a[8:11], v243 offset:51200\n"
"v_mfma_f32_16x16x32_bf16 v[100:103], v[228:231], a[12:15], v[100:103]\n"
"ds_read_b128 a[12:15], v244 offset:51200\n"
"v_mfma_f32_16x16x32_bf16 v[104:107], v[224:227], a[16:19], v[104:107]\n"
"ds_read_b128 a[16:19], v243 offset:53248\n"
"v_mfma_f32_16x16x32_bf16 v[104:107], v[228:231], a[20:23], v[104:107]\n"
"ds_read_b128 a[20:23], v244 offset:53248\n"
"v_mfma_f32_16x16x32_bf16 v[108:111], v[224:227], a[24:27], v[108:111]\n"
"ds_read_b128 a[24:27], v243 offset:55296\n"
"buffer_load_dwordx4 a[108:111], v240, s[36:39], s80 offen offset:3072 sc1\n"
"v_mfma_f32_16x16x32_bf16 v[108:111], v[228:231], a[28:31], v[108:111]\n"
"s_add_u32 s80, s80, 0x1000\n"
"ds_read_b128 a[28:31], v244 offset:55296\n"
"v_mfma_f32_16x16x32_bf16 v[112:115], v[224:227], a[32:35], v[112:115]\n"
"ds_read_b128 a[32:35], v243 offset:57344\n"
"v_mfma_f32_16x16x32_bf16 v[112:115], v[228:231], a[36:39], v[112:115]\n"
"ds_read_b128 a[36:39], v244 offset:57344\n"
"v_mfma_f32_16x16x32_bf16 v[116:119], v[224:227], a[40:43], v[116:119]\n"
"ds_read_b128 a[40:43], v243 offset:59392\n"
"v_mfma_f32_16x16x32_bf16 v[116:119], v[228:231], a[44:47], v[116:119]\n"
"ds_read_b128 a[44:47], v244 offset:59392\n"
"v_mfma_f32_16x16x32_bf16 v[120:123], v[224:227], a[48:51], v[120:123]\n"
"s_waitcnt lgkmcnt(13)\n"
"ds_read_b128 a[48:51], v243 offset:61440\n"
"v_lshlrev_b32 v251, 16, v232\n"
"v_mfma_f32_16x16x32_bf16 v[120:123], v[228:231], a[52:55], v[120:123]\n"
"ds_read_b128 a[52:55], v244 offset:61440\n"
"v_fmac_f32 v251, v251, v220\n"
"v_mfma_f32_16x16x32_bf16 v[124:127], v[224:227], a[56:59], v[124:127]\n"
"ds_read_b128 a[56:59], v243 offset:63488\n"
"buffer_load_dwordx4 v[224:227], v240, s[36:39], s80 offen offset:0 sc1\n"
"v_mfma_f32_16x16x32_bf16 v[124:127], v[228:231], a[60:63], v[124:127]\n"
".Lpad8_%=:\n"
"s_cbranch_execz .Lpad12_%=\n"
"s_waitcnt vmcnt(14) lgkmcnt(11)\n"
"ds_read_b128 a[60:63], v244 offset:63488\n"
"v_and_b32 v232, s82, v232\n"
"v_mfma_f32_16x16x32_bf16 v[0:3], a[64:67], a[0:3], v[0:3]\n"
"buffer_store_dwordx4 v[152:155], v240, s[52:55], s78 offen offset:2048 sc1\n"
"v_mfma_f32_16x16x32_bf16 v[0:3], a[68:71], a[4:7], v[0:3]\n"
"ds_read_b128 v[236:239], v250 offset:32768\n"
"v_fmac_f32 v232, v232, v221\n"
"v_mfma_f32_16x16x32_bf16 v[4:7], a[64:67], a[8:11], v[4:7]\n"
"v_cvt_pk_bf16_f32 v220, v251, v232\n"
"v_lshlrev_b32 v251, 16, v233\n"
"v_mfma_f32_16x16x32_bf16 v[4:7], a[68:71], a[12:15], v[4:7]\n"
"s_waitcnt lgkmcnt(9)\n"
"v_fmac_f32 v251, v251, v222\n"
"v_and_b32 v233, s82, v233\n"
"v_mfma_f32_16x16x32_bf16 v[8:11], a[64:67], a[16:19], v[8:11]\n"
"v_fmac_f32 v233, v233, v223\n"
"v_cvt_pk_bf16_f32 v221, v251, v233\n"
"v_mfma_f32_16x16x32_bf16 v[8:11], a[68:71], a[20:23], v[8:11]\n"
"v_lshlrev_b32 v251, 16, v234\n"
"v_accvgpr_read_b32 v252, a140\n"
"v_mfma_f32_16x16x32_bf16 v[12:15], a[64:67], a[24:27], v[12:15]\n"
"buffer_load_dwordx4 v[228:231], v240, s[36:39], s80 offen offset:1024 sc1\n"
"v_fmac_f32 v251, v251, v252\n"
"v_mfma_f32_16x16x32_bf16 v[12:15], a[68:71], a[28:31], v[12:15]\n"
"s_waitcnt lgkmcnt(5)\n"
"v_and_b32 v234, s82, v234\n"
"v_accvgpr_read_b32 v252, a141\n"
"v_mfma_f32_16x16x32_bf16 v[16:19], a[64:67], a[32:35], v[16:19]\n"
"v_fmac_f32 v234, v234, v252\n"
"v_cvt_pk_bf16_f32 v222, v251, v234\n"
"v_mfma_f32_16x16x32_bf16 v[16:19], a[68:71], a[36:39], v[16:19]\n"
"v_lshlrev_b32 v251, 16, v235\n"
"v_accvgpr_read_b32 v252, a142\n"
"v_mfma_f32_16x16x32_bf16 v[20:23], a[64:67], a[40:43], v[20:23]\n"
"v_fmac_f32 v251, v251, v252\n"
"v_and_b32 v235, s82, v235\n"
"v_mfma_f32_16x16x32_bf16 v[20:23], a[68:71], a[44:47], v[20:23]\n"
"s_waitcnt lgkmcnt(1)\n"
"v_accvgpr_read_b32 v252, a143\n"
"v_fmac_f32 v235, v235, v252\n"
"v_mfma_f32_16x16x32_bf16 v[24:27], a[64:67], a[48:51], v[24:27]\n"
"v_cvt_pk_bf16_f32 v223, v251, v235\n"
"v_mfma_f32_16x16x32_bf16 v[24:27], a[68:71], a[52:55], v[24:27]\n"
"ds_write_b128 v250, v[220:223] offset:47104\n"
"v_mfma_f32_16x16x32_bf16 v[28:31], a[64:67], a[56:59], v[28:31]\n"
"buffer_load_dwordx4 a[64:67], v240, s[36:39], s80 offen offset:2048 sc1\n"
"v_mfma_f32_16x16x32_bf16 v[28:31], a[68:71], a[60:63], v[28:31]\n"
"s_waitcnt vmcnt(15)\n"
"buffer_store_dwordx4 v[220:223], v240, s[52:55], s79 offen offset:3072 sc1\n"
"v_mfma_f32_16x16x32_bf16 v[32:35], a[72:75], a[0:3], v[32:35]\n"
"v_mfma_f32_16x16x32_bf16 v[32:35], a[76:79], a[4:7], v[32:35]\n"
"v_mfma_f32_16x16x32_bf16 v[36:39], a[72:75], a[8:11], v[36:39]\n"
"v_mfma_f32_16x16x32_bf16 v[36:39], a[76:79], a[12:15], v[36:39]\n"
"v_mfma_f32_16x16x32_bf16 v[40:43], a[72:75], a[16:19], v[40:43]\n"
"v_mfma_f32_16x16x32_bf16 v[40:43], a[76:79], a[20:23], v[40:43]\n"
"v_mfma_f32_16x16x32_bf16 v[44:47], a[72:75], a[24:27], v[44:47]\n"
"buffer_load_dwordx4 a[68:71], v240, s[36:39], s80 offen offset:3072 sc1\n"
"v_mfma_f32_16x16x32_bf16 v[44:47], a[76:79], a[28:31], v[44:47]\n"
"s_add_u32 s80, s80, 0x1000\n"
"ds_read_b128 v[232:235], v250 offset:34816\n"
"v_mfma_f32_16x16x32_bf16 v[48:51], a[72:75], a[32:35], v[48:51]\n"
"s_waitcnt lgkmcnt(2)\n"
"v_lshlrev_b32 v251, 16, v236\n"
"v_fmac_f32 v251, v251, v192\n"
"v_mfma_f32_16x16x32_bf16 v[48:51], a[76:79], a[36:39], v[48:51]\n"
"v_and_b32 v236, s82, v236\n"
"v_fmac_f32 v236, v236, v193\n"
"v_mfma_f32_16x16x32_bf16 v[52:55], a[72:75], a[40:43], v[52:55]\n"
"v_cvt_pk_bf16_f32 v192, v251, v236\n"
"v_lshlrev_b32 v251, 16, v237\n"
"v_mfma_f32_16x16x32_bf16 v[52:55], a[76:79], a[44:47], v[52:55]\n"
"v_fmac_f32 v251, v251, v194\n"
"v_and_b32 v237, s82, v237\n"
"v_mfma_f32_16x16x32_bf16 v[56:59], a[72:75], a[48:51], v[56:59]\n"
"v_fmac_f32 v237, v237, v195\n"
"v_cvt_pk_bf16_f32 v193, v251, v237\n"
"v_mfma_f32_16x16x32_bf16 v[56:59], a[76:79], a[52:55], v[56:59]\n"
"v_lshlrev_b32 v251, 16, v238\n"
"v_accvgpr_read_b32 v252, a112\n"
"v_mfma_f32_16x16x32_bf16 v[60:63], a[72:75], a[56:59], v[60:63]\n"
"buffer_load_dwordx4 a[72:75], v240, s[36:39], s80 offen offset:0 sc1\n"
"v_fmac_f32 v251, v251, v252\n"
"v_mfma_f32_16x16x32_bf16 v[60:63], a[76:79], a[60:63], v[60:63]\n"
"s_waitcnt vmcnt(15)\n"
"v_and_b32 v238, s82, v238\n"
"v_accvgpr_read_b32 v252, a113\n"
"v_mfma_f32_16x16x32_bf16 v[64:67], a[80:83], a[0:3], v[64:67]\n"
"v_fmac_f32 v238, v238, v252\n"
"v_cvt_pk_bf16_f32 v194, v251, v238\n"
"v_mfma_f32_16x16x32_bf16 v[64:67], a[84:87], a[4:7], v[64:67]\n"
"v_lshlrev_b32 v251, 16, v239\n"
"v_accvgpr_read_b32 v252, a114\n"
"v_mfma_f32_16x16x32_bf16 v[68:71], a[80:83], a[8:11], v[68:71]\n"
"v_fmac_f32 v251, v251, v252\n"
"v_and_b32 v239, s82, v239\n"
"v_mfma_f32_16x16x32_bf16 v[68:71], a[84:87], a[12:15], v[68:71]\n"
"v_accvgpr_read_b32 v252, a115\n"
"v_fmac_f32 v239, v239, v252\n"
"v_mfma_f32_16x16x32_bf16 v[72:75], a[80:83], a[16:19], v[72:75]\n"
"v_cvt_pk_bf16_f32 v195, v251, v239\n"
"v_mfma_f32_16x16x32_bf16 v[72:75], a[84:87], a[20:23], v[72:75]\n"
"ds_write_b128 v250, v[192:195] offset:32768\n"
"v_mfma_f32_16x16x32_bf16 v[76:79], a[80:83], a[24:27], v[76:79]\n"
"buffer_load_dwordx4 a[76:79], v240, s[36:39], s80 offen offset:1024 sc1\n"
"v_mfma_f32_16x16x32_bf16 v[76:79], a[84:87], a[28:31], v[76:79]\n"
"buffer_store_dwordx4 v[192:195], v240, s[52:55], s68 offen offset:3072 sc1\n"
"v_mfma_f32_16x16x32_bf16 v[80:83], a[80:83], a[32:35], v[80:83]\n"
"v_mfma_f32_16x16x32_bf16 v[80:83], a[84:87], a[36:39], v[80:83]\n"
"v_mfma_f32_16x16x32_bf16 v[84:87], a[80:83], a[40:43], v[84:87]\n"
"v_mfma_f32_16x16x32_bf16 v[84:87], a[84:87], a[44:47], v[84:87]\n"
"s_waitcnt vmcnt(10)\n"
"v_cmp_ne_u32 vcc, s33, v241\n"
"v_mfma_f32_16x16x32_bf16 v[88:91], a[80:83], a[48:51], v[88:91]\n"
"v_mfma_f32_16x16x32_bf16 v[88:91], a[84:87], a[52:55], v[88:91]\n"
"s_barrier\n"
"v_mfma_f32_16x16x32_bf16 v[92:95], a[80:83], a[56:59], v[92:95]\n"
"s_cmp_eq_u64 vcc, 0\n"
"s_cbranch_scc0 .Lslow0_%=\n"
".Lback0_%=:\n"
"buffer_load_dwordx4 a[80:83], v240, s[36:39], s80 offen offset:2048 sc1\n"
"v_mfma_f32_16x16x32_bf16 v[92:95], a[84:87], a[60:63], v[92:95]\n"
"v_mfma_f32_16x16x32_bf16 v[96:99], a[88:91], a[0:3], v[96:99]\n"
"ds_read_b128 a[0:3], v245 offset:0\n"
"v_mfma_f32_16x16x32_bf16 v[96:99], a[92:95], a[4:7], v[96:99]\n"
"ds_read_b128 a[4:7], v246 offset:0\n"
"v_mfma_f32_16x16x32_bf16 v[100:103], a[88:91], a[8:11], v[100:103]\n"
"s_waitcnt lgkmcnt(3)\n"
"ds_read_b128 a[8:11], v245 offset:2048\n"
"v_lshlrev_b32 v251, 16, v232\n"
"v_mfma_f32_16x16x32_bf16 v[100:103], a[92:95], a[12:15], v[100:103]\n"
"ds_read_b128 a[12:15], v246 offset:2048\n"
"v_fmac_f32 v251, v251, v196\n"
"v_mfma_f32_16x16x32_bf16 v[104:107], a[88:91], a[16:19], v[104:107]\n"
"ds_read_b128 a[16:19], v245 offset:4096\n"
"v_and_b32 v232, s82, v232\n"
"v_mfma_f32_16x16x32_bf16 v[104:107], a[92:95], a[20:23], v[104:107]\n"
"ds_read_b128 a[20:23], v246 offset:4096\n"
"v_fmac_f32 v232, v232, v197\n"
"v_mfma_f32_16x16x32_bf16 v[108:111], a[88:91], a[24:27], v[108:111]\n"
"ds_read_b128 a[24:27], v245 offset:6144\n"
"buffer_load_dwordx4 a[84:87], v240, s[36:39], s80 offen offset:3072 sc1\n"
"v_mfma_f32_16x16x32_bf16 v[108:111], a[92:95], a[28:31], v[108:111]\n"
"s_add_u32 s80, s80, 0x1000\n"
"ds_read_b128 a[28:31], v246 offset:6144\n"
"v_cvt_pk_bf16_f32 v196, v251, v232\n"
"v_mfma_f32_16x16x32_bf16 v[112:115], a[88:91], a[32:35], v[112:115]\n"
"ds_read_b128 a[32:35], v245 offset:8192\n"
"v_lshlrev_b32 v251, 16, v233\n"
"v_mfma_f32_16x16x32_bf16 v[112:115], a[92:95], a[36:39], v[112:115]\n"
"ds_read_b128 a[36:39], v246 offset:8192\n"
"v_fmac_f32 v251, v251, v198\n"
"v_mfma_f32_16x16x32_bf16 v[116:119], a[88:91], a[40:43], v[116:119]\n"
"ds_read_b128 a[40:43], v245 offset:10240\n"
"v_and_b32 v233, s82, v233\n"
"v_mfma_f32_16x16x32_bf16 v[116:119], a[92:95], a[44:47], v[116:119]\n"
"ds_read_b128 a[44:47], v246 offset:10240\n"
"v_fmac_f32 v233, v233, v199\n"
"v_mfma_f32_16x16x32_bf16 v[120:123], a[88:91], a[48:51], v[120:123]\n"
"ds_read_b128 a[48:51], v245 offset:12288\n"
"v_cvt_pk_bf16_f32 v197, v251, v233\n"
"v_mfma_f32_16x16x32_bf16 v[120:123], a[92:95], a[52:55], v[120:123]\n"
"ds_read_b128 a[52:55], v246 offset:12288\n"
"v_lshlrev_b32 v251, 16, v234\n"
"v_mfma_f32_16x16x32_bf16 v[124:127], a[88:91], a[56:59], v[124:127]\n"
"ds_read_b128 a[56:59], v245 offset:14336\n"
"buffer_load_dwordx4 a[88:91], v240, s[36:39], s80 offen offset:0 sc1\n"
"v_mfma_f32_16x16x32_bf16 v[124:127], a[92:95], a[60:63], v[124:127]\n"
"s_waitcnt lgkmcnt(11)\n"
"ds_read_b128 a[60:63], v246 offset:14336\n"
"v_accvgpr_read_b32 v252, a116\n"
"v_mfma_f32_16x16x32_bf16 v[0:3], a[96:99], a[0:3], v[0:3]\n"
"s_add_u32 m0, s83, 131072\n"
"ds_read_b128 v[236:239], v250 offset:36864\n"
"v_fmac_f32 v251, v251, v252\n"
"v_mfma_f32_16x16x32_bf16 v[0:3], a[100:103], a[4:7], v[0:3]\n"
"buffer_load_dwordx4 v240, s[56:59], s68 offen offset:0 sc1 lds\n"
"v_mfma_f32_16x16x32_bf16 v[4:7], a[96:99], a[8:11], v[4:7]\n"
"v_and_b32 v234, s82, v234\n"
"v_accvgpr_read_b32 v252, a117\n"
"v_mfma_f32_16x16x32_bf16 v[4:7], a[100:103], a[12:15], v[4:7]\n"
"s_waitcnt lgkmcnt(9)\n"
"v_fmac_f32 v234, v234, v252\n"
"v_cvt_pk_bf16_f32 v198, v251, v234\n"
"v_mfma_f32_16x16x32_bf16 v[8:11], a[96:99], a[16:19], v[8:11]\n"
"v_lshlrev_b32 v251, 16, v235\n"
"v_accvgpr_read_b32 v252, a118\n"
"v_mfma_f32_16x16x32_bf16 v[8:11], a[100:103], a[20:23], v[8:11]\n"
"v_fmac_f32 v251, v251, v252\n"
"v_and_b32 v235, s82, v235\n"
"v_mfma_f32_16x16x32_bf16 v[12:15], a[96:99], a[24:27], v[12:15]\n"
"s_add_u32 m0, s83, 133120\n"
"buffer_load_dwordx4 a[92:95], v240, s[36:39], s80 offen offset:1024 sc1\n"
"v_accvgpr_read_b32 v252, a119\n"
"v_mfma_f32_16x16x32_bf16 v[12:15], a[100:103], a[28:31], v[12:15]\n"
"s_waitcnt lgkmcnt(5)\n"
"buffer_load_dwordx4 v240, s[56:59], s69 offen offset:0 sc1 lds\n"
"v_mfma_f32_16x16x32_bf16 v[16:19], a[96:99], a[32:35], v[16:19]\n"
"v_fmac_f32 v235, v235, v252\n"
"v_cvt_pk_bf16_f32 v199, v251, v235\n"
"v_mfma_f32_16x16x32_bf16 v[16:19], a[100:103], a[36:39], v[16:19]\n"
"buffer_store_dwordx4 v[196:199], v240, s[52:55], s69 offen offset:3072 sc1\n"
"v_mfma_f32_16x16x32_bf16 v[20:23], a[96:99], a[40:43], v[20:23]\n"
"ds_write_b128 v250, v[196:199] offset:34816\n"
"v_mfma_f32_16x16x32_bf16 v[20:23], a[100:103], a[44:47], v[20:23]\n"
"s_waitcnt lgkmcnt(2)\n"
"v_mfma_f32_16x16x32_bf16 v[24:27], a[96:99], a[48:51], v[24:27]\n"
"s_add_u32 m0, s83, 135168\n"
"v_mfma_f32_16x16x32_bf16 v[24:27], a[100:103], a[52:55], v[24:27]\n"
"buffer_load_dwordx4 v240, s[56:59], s70 offen offset:0 sc1 lds\n"
"v_mfma_f32_16x16x32_bf16 v[28:31], a[96:99], a[56:59], v[28:31]\n"
"ds_read_b128 v[232:235], v250 offset:38912\n"
"buffer_load_dwordx4 a[96:99], v240, s[36:39], s80 offen offset:2048 sc1\n"
"v_mfma_f32_16x16x32_bf16 v[28:31], a[100:103], a[60:63], v[28:31]\n"
"s_waitcnt vmcnt(18) lgkmcnt(2)\n"
"v_lshlrev_b32 v251, 16, v236\n"
"v_fmac_f32 v251, v251, v200\n"
"v_mfma_f32_16x16x32_bf16 v[32:35], a[104:107], a[0:3], v[32:35]\n"
"v_and_b32 v236, s82, v236\n"
"v_fmac_f32 v236, v236, v201\n"
"v_mfma_f32_16x16x32_bf16 v[32:35], a[108:111], a[4:7], v[32:35]\n"
"v_cvt_pk_bf16_f32 v200, v251, v236\n"
"v_lshlrev_b32 v251, 16, v237\n"
"v_mfma_f32_16x16x32_bf16 v[36:39], a[104:107], a[8:11], v[36:39]\n"
"s_add_u32 m0, s83, 137216\n"
"v_fmac_f32 v251, v251, v202\n"
"v_and_b32 v237, s82, v237\n"
"v_mfma_f32_16x16x32_bf16 v[36:39], a[108:111], a[12:15], v[36:39]\n"
"buffer_load_dwordx4 v240, s[56:59], s71 offen offset:0 sc1 lds\n"
"v_mfma_f32_16x16x32_bf16 v[40:43], a[104:107], a[16:19], v[40:43]\n"
"v_fmac_f32 v237, v237, v203\n"
"v_cvt_pk_bf16_f32 v201, v251, v237\n"
"v_mfma_f32_16x16x32_bf16 v[40:43], a[108:111], a[20:23], v[40:43]\n"
"v_lshlrev_b32 v251, 16, v238\n"
"v_accvgpr_read_b32 v252, a120\n"
"v_mfma_f32_16x16x32_bf16 v[44:47], a[104:107], a[24:27], v[44:47]\n"
"buffer_load_dwordx4 a[100:103], v240, s[36:39], s80 offen offset:3072 sc1\n"
"v_fmac_f32 v251, v251, v252\n"
"v_mfma_f32_16x16x32_bf16 v[44:47], a[108:111], a[28:31], v[44:47]\n"
"s_add_u32 s80, s80, 0x1000\n"
"v_and_b32 v238, s82, v238\n"
"v_accvgpr_read_b32 v252, a121\n"
"v_mfma_f32_16x16x32_bf16 v[48:51], a[104:107], a[32:35], v[48:51]\n"
"v_fmac_f32 v238, v238, v252\n"
"v_cvt_pk_bf16_f32 v202, v251, v238\n"
"v_mfma_f32_16x16x32_bf16 v[48:51], a[108:111], a[36:39], v[48:51]\n"
"v_lshlrev_b32 v251, 16, v239\n"
"v_accvgpr_read_b32 v252, a122\n"
"v_mfma_f32_16x16x32_bf16 v[52:55], a[104:107], a[40:43], v[52:55]\n"
"v_fmac_f32 v251, v251, v252\n"
"v_and_b32 v239, s82, v239\n"
"v_mfma_f32_16x16x32_bf16 v[52:55], a[108:111], a[44:47], v[52:55]\n"
"v_accvgpr_read_b32 v252, a123\n"
"v_fmac_f32 v239, v239, v252\n"
"v_mfma_f32_16x16x32_bf16 v[56:59], a[104:107], a[48:51], v[56:59]\n"
"v_cvt_pk_bf16_f32 v203, v251, v239\n"
"v_mfma_f32_16x16x32_bf16 v[56:59], a[108:111], a[52:55], v[56:59]\n"
"ds_write_b128 v250, v[200:203] offset:36864\n"
"v_mfma_f32_16x16x32_bf16 v[60:63], a[104:107], a[56:59], v[60:63]\n"
"buffer_load_dwordx4 a[104:107], v240, s[36:39], s80 offen offset:0 sc1\n"
"v_mfma_f32_16x16x32_bf16 v[60:63], a[108:111], a[60:63], v[60:63]\n"
"s_waitcnt vmcnt(18)\n"
"buffer_store_dwordx4 v[200:203], v240, s[52:55], s70 offen offset:3072 sc1\n"
"v_mfma_f32_16x16x32_bf16 v[64:67], v[224:227], a[0:3], v[64:67]\n"
"v_mfma_f32_16x16x32_bf16 v[64:67], v[228:231], a[4:7], v[64:67]\n"
"v_mfma_f32_16x16x32_bf16 v[68:71], v[224:227], a[8:11], v[68:71]\n"
"v_mfma_f32_16x16x32_bf16 v[68:71], v[228:231], a[12:15], v[68:71]\n"
"v_mfma_f32_16x16x32_bf16 v[72:75], v[224:227], a[16:19], v[72:75]\n"
"v_mfma_f32_16x16x32_bf16 v[72:75], v[228:231], a[20:23], v[72:75]\n"
"v_mfma_f32_16x16x32_bf16 v[76:79], v[224:227], a[24:27], v[76:79]\n"
"buffer_load_dwordx4 a[108:111], v240, s[36:39], s80 offen offset:1024 sc1\n"
"v_mfma_f32_16x16x32_bf16 v[76:79], v[228:231], a[28:31], v[76:79]\n"
"v_mfma_f32_16x16x32_bf16 v[80:83], v[224:227], a[32:35], v[80:83]\n"
"s_waitcnt lgkmcnt(1)\n"
"ds_read_b128 v[236:239], v250 offset:40960\n"
"v_lshlrev_b32 v251, 16, v232\n"
"v_mfma_f32_16x16x32_bf16 v[80:83], v[228:231], a[36:39], v[80:83]\n"
"v_fmac_f32 v251, v251, v204\n"
"v_and_b32 v232, s82, v232\n"
"v_mfma_f32_16x16x32_bf16 v[84:87], v[224:227], a[40:43], v[84:87]\n"
"v_fmac_f32 v232, v232, v205\n"
"v_cvt_pk_bf16_f32 v204, v251, v232\n"
"v_mfma_f32_16x16x32_bf16 v[84:87], v[228:231], a[44:47], v[84:87]\n"
"v_lshlrev_b32 v251, 16, v233\n"
"v_fmac_f32 v251, v251, v206\n"
"v_mfma_f32_16x16x32_bf16 v[88:91], v[224:227], a[48:51], v[88:91]\n"
"v_and_b32 v233, s82, v233\n"
"v_fmac_f32 v233, v233, v207\n"
"v_mfma_f32_16x16x32_bf16 v[88:91], v[228:231], a[52:55], v[88:91]\n"
"v_cvt_pk_bf16_f32 v205, v251, v233\n"
"v_lshlrev_b32 v251, 16, v234\n"
"v_mfma_f32_16x16x32_bf16 v[92:95], v[224:227], a[56:59], v[92:95]\n"
"buffer_load_dwordx4 v[224:227], v240, s[36:39], s80 offen offset:2048 sc1\n"
"v_accvgpr_read_b32 v252, a124\n"
"v_mfma_f32_16x16x32_bf16 v[92:95], v[228:231], a[60:63], v[92:95]\n"
"s_waitcnt vmcnt(18)\n"
"v_fmac_f32 v251, v251, v252\n"
"v_and_b32 v234, s82, v234\n"
"v_mfma_f32_16x16x32_bf16 v[96:99], a[64:67], a[0:3], v[96:99]\n"
"ds_read_b128 a[0:3], v245 offset:16384\n"
"v_accvgpr_read_b32 v252, a125\n"
"v_mfma_f32_16x16x32_bf16 v[96:99], a[68:71], a[4:7], v[96:99]\n"
"ds_read_b128 a[4:7], v246 offset:16384\n"
"v_fmac_f32 v234, v234, v252\n"
"v_mfma_f32_16x16x32_bf16 v[100:103], a[64:67], a[8:11], v[100:103]\n"
"ds_read_b128 a[8:11], v245 offset:18432\n"
"v_cvt_pk_bf16_f32 v206, v251, v234\n"
"v_mfma_f32_16x16x32_bf16 v[100:103], a[68:71], a[12:15], v[100:103]\n"
"ds_read_b128 a[12:15], v246 offset:18432\n"
"v_lshlrev_b32 v251, 16, v235\n"
"v_mfma_f32_16x16x32_bf16 v[104:107], a[64:67], a[16:19], v[104:107]\n"
"ds_read_b128 a[16:19], v245 offset:20480\n"
"v_accvgpr_read_b32 v252, a126\n"
"v_mfma_f32_16x16x32_bf16 v[104:107], a[68:71], a[20:23], v[104:107]\n"
"ds_read_b128 a[20:23], v246 offset:20480\n"
"v_fmac_f32 v251, v251, v252\n"
"v_mfma_f32_16x16x32_bf16 v[108:111], a[64:67], a[24:27], v[108:111]\n"
"ds_read_b128 a[24:27], v245 offset:22528\n"
"buffer_load_dwordx4 v[228:231], v240, s[36:39], s80 offen offset:3072 sc1\n"
"v_mfma_f32_16x16x32_bf16 v[108:111], a[68:71], a[28:31], v[108:111]\n"
"s_add_u32 s80, s80, 0x1000\n"
"ds_read_b128 a[28:31], v246 offset:22528\n"
"v_and_b32 v235, s82, v235\n"
"v_mfma_f32_16x16x32_bf16 v[112:115], a[64:67], a[32:35], v[112:115]\n"
"ds_read_b128 a[32:35], v245 offset:24576\n"
"v_accvgpr_read_b32 v252, a127\n"
"v_mfma_f32_16x16x32_bf16 v[112:115], a[68:71], a[36:39], v[112:115]\n"
"ds_read_b128 a[36:39], v246 offset:24576\n"
"v_fmac_f32 v235, v235, v252\n"
"v_mfma_f32_16x16x32_bf16 v[116:119], a[64:67], a[40:43], v[116:119]\n"
"ds_read_b128 a[40:43], v245 offset:26624\n"
"v_cvt_pk_bf16_f32 v207, v251, v235\n"
"v_mfma_f32_16x16x32_bf16 v[116:119], a[68:71], a[44:47], v[116:119]\n"
"ds_read_b128 a[44:47], v246 offset:26624\n"
"v_mfma_f32_16x16x32_bf16 v[120:123], a[64:67], a[48:51], v[120:123]\n"
"ds_read_b128 a[48:51], v245 offset:28672\n"
"v_mfma_f32_16x16x32_bf16 v[120:123], a[68:71], a[52:55], v[120:123]\n"
"ds_read_b128 a[52:55], v246 offset:28672\n"
"v_mfma_f32_16x16x32_bf16 v[124:127], a[64:67], a[56:59], v[124:127]\n"
"ds_read_b128 a[56:59], v245 offset:30720\n"
"buffer_load_dwordx4 a[64:67], v240, s[36:39], s80 offen offset:0 sc1\n"
"v_mfma_f32_16x16x32_bf16 v[124:127], a[68:71], a[60:63], v[124:127]\n"
"s_waitcnt vmcnt(18) lgkmcnt(11)\n"
"ds_read_b128 a[60:63], v246 offset:30720\n"
"v_mfma_f32_16x16x32_bf16 v[0:3], a[72:75], a[0:3], v[0:3]\n"
"buffer_store_dwordx4 v[204:207], v240, s[52:55], s71 offen offset:3072 sc1\n"
"v_mfma_f32_16x16x32_bf16 v[0:3], a[76:79], a[4:7], v[0:3]\n"
"ds_write_b128 v250, v[204:207] offset:38912\n"
"v_mfma_f32_16x16x32_bf16 v[4:7], a[72:75], a[8:11], v[4:7]\n"
"v_mfma_f32_16x16x32_bf16 v[4:7], a[76:79], a[12:15], v[4:7]\n"
"s_waitcnt lgkmcnt(9)\n"
"ds_read_b128 v[232:235], v250 offset:43008\n"
"v_lshlrev_b32 v251, 16, v236\n"
"v_mfma_f32_16x16x32_bf16 v[8:11], a[72:75], a[16:19], v[8:11]\n"
"v_fmac_f32 v251, v251, v208\n"
"v_and_b32 v236, s82, v236\n"
"v_mfma_f32_16x16x32_bf16 v[8:11], a[76:79], a[20:23], v[8:11]\n"
"v_fmac_f32 v236, v236, v209\n"
"v_cvt_pk_bf16_f32 v208, v251, v236\n"
"v_mfma_f32_16x16x32_bf16 v[12:15], a[72:75], a[24:27], v[12:15]\n"
"buffer_load_dwordx4 a[68:71], v240, s[36:39], s80 offen offset:1024 sc1\n"
"v_lshlrev_b32 v251, 16, v237\n"
"v_mfma_f32_16x16x32_bf16 v[12:15], a[76:79], a[28:31], v[12:15]\n"
"s_waitcnt lgkmcnt(6)\n"
"v_fmac_f32 v251, v251, v210\n"
"v_and_b32 v237, s82, v237\n"
"v_mfma_f32_16x16x32_bf16 v[16:19], a[72:75], a[32:35], v[16:19]\n"
"v_fmac_f32 v237, v237, v211\n"
"v_cvt_pk_bf16_f32 v209, v251, v237\n"
"v_mfma_f32_16x16x32_bf16 v[16:19], a[76:79], a[36:39], v[16:19]\n"
"v_lshlrev_b32 v251, 16, v238\n"
"v_accvgpr_read_b32 v252, a128\n"
"v_mfma_f32_16x16x32_bf16 v[20:23], a[72:75], a[40:43], v[20:23]\n"
"v_fmac_f32 v251, v251, v252\n"
"v_and_b32 v238, s82, v238\n"
"v_mfma_f32_16x16x32_bf16 v[20:23], a[76:79], a[44:47], v[20:23]\n"
"s_waitcnt lgkmcnt(2)\n"
"v_accvgpr_read_b32 v252, a129\n"
"v_fmac_f32 v238, v238, v252\n"
"v_mfma_f32_16x16x32_bf16 v[24:27], a[72:75], a[48:51], v[24:27]\n"
"v_cvt_pk_bf16_f32 v210, v251, v238\n"
"v_lshlrev_b32 v251, 16, v239\n"
"v_mfma_f32_16x16x32_bf16 v[24:27], a[76:79], a[52:55], v[24:27]\n"
"v_accvgpr_read_b32 v252, a130\n"
"v_fmac_f32 v251, v251, v252\n"
"v_mfma_f32_16x16x32_bf16 v[28:31], a[72:75], a[56:59], v[28:31]\n"
"buffer_load_dwordx4 a[72:75], v240, s[36:39], s80 offen offset:2048 sc1\n"
"v_and_b32 v239, s82, v239\n"
"v_mfma_f32_16x16x32_bf16 v[28:31], a[76:79], a[60:63], v[28:31]\n"
"s_waitcnt vmcnt(18)\n"
"v_accvgpr_read_b32 v252, a131\n"
"v_fmac_f32 v239, v239, v252\n"
"v_mfma_f32_16x16x32_bf16 v[32:35], a[80:83], a[0:3], v[32:35]\n"
"v_cvt_pk_bf16_f32 v211, v251, v239\n"
"v_mfma_f32_16x16x32_bf16 v[32:35], a[84:87], a[4:7], v[32:35]\n"
"buffer_store_dwordx4 v[208:211], v240, s[52:55], s76 offen offset:3072 sc1\n"
"v_mfma_f32_16x16x32_bf16 v[36:39], a[80:83], a[8:11], v[36:39]\n"
"ds_write_b128 v250, v[208:211] offset:40960\n"
"v_mfma_f32_16x16x32_bf16 v[36:39], a[84:87], a[12:15], v[36:39]\n"
"v_mfma_f32_16x16x32_bf16 v[40:43], a[80:83], a[16:19], v[40:43]\n"
"v_mfma_f32_16x16x32_bf16 v[40:43], a[84:87], a[20:23], v[40:43]\n"
"v_mfma_f32_16x16x32_bf16 v[44:47], a[80:83], a[24:27], v[44:47]\n"
"buffer_load_dwordx4 a[76:79], v240, s[36:39], s80 offen offset:3072 sc1\n"
"v_mfma_f32_16x16x32_bf16 v[44:47], a[84:87], a[28:31], v[44:47]\n"
"s_add_u32 s80, s80, 0x1000\n"
"v_mfma_f32_16x16x32_bf16 v[48:51], a[80:83], a[32:35], v[48:51]\n"
"v_mfma_f32_16x16x32_bf16 v[48:51], a[84:87], a[36:39], v[48:51]\n"
"v_mfma_f32_16x16x32_bf16 v[52:55], a[80:83], a[40:43], v[52:55]\n"
"v_mfma_f32_16x16x32_bf16 v[52:55], a[84:87], a[44:47], v[52:55]\n"
"v_mfma_f32_16x16x32_bf16 v[56:59], a[80:83], a[48:51], v[56:59]\n"
"v_mfma_f32_16x16x32_bf16 v[56:59], a[84:87], a[52:55], v[56:59]\n"
"v_mfma_f32_16x16x32_bf16 v[60:63], a[80:83], a[56:59], v[60:63]\n"
"ds_read_b128 v[236:239], v250 offset:45056\n"
"buffer_load_dwordx4 a[80:83], v240, s[36:39], s80 offen offset:0 sc1\n"
"v_mfma_f32_16x16x32_bf16 v[60:63], a[84:87], a[60:63], v[60:63]\n"
"s_waitcnt vmcnt(18) lgkmcnt(2)\n"
"v_lshlrev_b32 v251, 16, v232\n"
"v_fmac_f32 v251, v251, v212\n"
"v_mfma_f32_16x16x32_bf16 v[64:67], a[88:91], a[0:3], v[64:67]\n"
"v_and_b32 v232, s82, v232\n"
"v_fmac_f32 v232, v232, v213\n"
"v_mfma_f32_16x16x32_bf16 v[64:67], a[92:95], a[4:7], v[64:67]\n"
"v_cvt_pk_bf16_f32 v212, v251, v232\n"
"v_lshlrev_b32 v251, 16, v233\n"
"v_mfma_f32_16x16x32_bf16 v[68:71], a[88:91], a[8:11], v[68:71]\n"
"v_fmac_f32 v251, v251, v214\n"
"v_and_b32 v233, s82, v233\n"
"v_mfma_f32_16x16x32_bf16 v[68:71], a[92:95], a[12:15], v[68:71]\n"
"v_fmac_f32 v233, v233, v215\n"
"v_cvt_pk_bf16_f32 v213, v251, v233\n"
"v_mfma_f32_16x16x32_bf16 v[72:75], a[88:91], a[16:19], v[72:75]\n"
"v_lshlrev_b32 v251, 16, v234\n"
"v_accvgpr_read_b32 v252, a132\n"
"v_mfma_f32_16x16x32_bf16 v[72:75], a[92:95], a[20:23], v[72:75]\n"
"v_fmac_f32 v251, v251, v252\n"
"v_and_b32 v234, s82, v234\n"
"v_mfma_f32_16x16x32_bf16 v[76:79], a[88:91], a[24:27], v[76:79]\n"
"buffer_load_dwordx4 a[84:87], v240, s[36:39], s80 offen offset:1024 sc1\n"
"v_accvgpr_read_b32 v252, a133\n"
"v_mfma_f32_16x16x32_bf16 v[76:79], a[92:95], a[28:31], v[76:79]\n"
"v_fmac_f32 v234, v234, v252\n"
"v_cvt_pk_bf16_f32 v214, v251, v234\n"
"v_mfma_f32_16x16x32_bf16 v[80:83], a[88:91], a[32:35], v[80:83]\n"
"v_lshlrev_b32 v251, 16, v235\n"
"v_accvgpr_read_b32 v252, a134\n"
"v_mfma_f32_16x16x32_bf16 v[80:83], a[92:95], a[36:39], v[80:83]\n"
"v_fmac_f32 v251, v251, v252\n"
"v_and_b32 v235, s82, v235\n"
"v_mfma_f32_16x16x32_bf16 v[84:87], a[88:91], a[40:43], v[84:87]\n"
"v_accvgpr_read_b32 v252, a135\n"
"v_fmac_f32 v235, v235, v252\n"
"v_mfma_f32_16x16x32_bf16 v[84:87], a[92:95], a[44:47], v[84:87]\n"
"v_cvt_pk_bf16_f32 v215, v251, v235\n"
"v_mfma_f32_16x16x32_bf16 v[88:91], a[88:91], a[48:51], v[88:91]\n"
"ds_write_b128 v250, v[212:215] offset:43008\n"
"v_mfma_f32_16x16x32_bf16 v[88:91], a[92:95], a[52:55], v[88:91]\n"
"s_waitcnt vmcnt(14)\n"
"s_barrier\n"
"v_mfma_f32_16x16x32_bf16 v[92:95], a[88:91], a[56:59], v[92:95]\n"
"buffer_load_dwordx4 a[88:91], v240, s[36:39], s80 offen offset:2048 sc1\n"
"v_mfma_f32_16x16x32_bf16 v[92:95], a[92:95], a[60:63], v[92:95]\n"
"s_waitcnt vmcnt(14)\n"
"buffer_store_dwordx4 v[212:215], v240, s[52:55], s77 offen offset:3072 sc1\n"
"v_mfma_f32_16x16x32_bf16 v[96:99], a[96:99], a[0:3], v[96:99]\n"
"ds_read_b128 a[0:3], v247 offset:0\n"
"v_mfma_f32_16x16x32_bf16 v[96:99], a[100:103], a[4:7], v[96:99]\n"
"ds_read_b128 a[4:7], v248 offset:0\n"
"v_mfma_f32_16x16x32_bf16 v[100:103], a[96:99], a[8:11], v[100:103]\n"
"ds_read_b128 a[8:11], v247 offset:2048\n"
"v_mfma_f32_16x16x32_bf16 v[100:103], a[100:103], a[12:15], v[100:103]\n"
"ds_read_b128 a[12:15], v248 offset:2048\n"
"v_mfma_f32_16x16x32_bf16 v[104:107], a[96:99], a[16:19], v[104:107]\n"
"ds_read_b128 a[16:19], v247 offset:4096\n"
"v_mfma_f32_16x16x32_bf16 v[104:107], a[100:103], a[20:23], v[104:107]\n"
"ds_read_b128 a[20:23], v248 offset:4096\n"
"v_mfma_f32_16x16x32_bf16 v[108:111], a[96:99], a[24:27], v[108:111]\n"
"ds_read_b128 a[24:27], v247 offset:6144\n"
"buffer_load_dwordx4 a[92:95], v240, s[36:39], s80 offen offset:3072 sc1\n"
"v_mfma_f32_16x16x32_bf16 v[108:111], a[100:103], a[28:31], v[108:111]\n"
"s_add_u32 s80, s80, 0x1000\n"
"ds_read_b128 a[28:31], v248 offset:6144\n"
"v_mfma_f32_16x16x32_bf16 v[112:115], a[96:99], a[32:35], v[112:115]\n"
"ds_read_b128 a[32:35], v247 offset:8192\n"
"v_mfma_f32_16x16x32_bf16 v[112:115], a[100:103], a[36:39], v[112:115]\n"
"s_waitcnt lgkmcnt(10)\n"
"ds_read_b128 a[36:39], v248 offset:8192\n"
"v_lshlrev_b32 v251, 16, v236\n"
"v_mfma_f32_16x16x32_bf16 v[116:119], a[96:99], a[40:43], v[116:119]\n"
"ds_read_b128 a[40:43], v247 offset:10240\n"
"v_fmac_f32 v251, v251, v216\n"
"v_mfma_f32_16x16x32_bf16 v[116:119], a[100:103], a[44:47], v[116:119]\n"
"ds_read_b128 a[44:47], v248 offset:10240\n"
"v_and_b32 v236, s82, v236\n"
"v_mfma_f32_16x16x32_bf16 v[120:123], a[96:99], a[48:51], v[120:123]\n"
"ds_read_b128 a[48:51], v247 offset:12288\n"
"v_fmac_f32 v236, v236, v217\n"
"v_mfma_f32_16x16x32_bf16 v[120:123], a[100:103], a[52:55], v[120:123]\n"
"ds_read_b128 a[52:55], v248 offset:12288\n"
"v_cvt_pk_bf16_f32 v216, v251, v236\n"
"v_mfma_f32_16x16x32_bf16 v[124:127], a[96:99], a[56:59], v[124:127]\n"
"ds_read_b128 a[56:59], v247 offset:14336\n"
"buffer_load_dwordx4 a[96:99], v240, s[36:39], s80 offen offset:0 sc1\n"
"v_mfma_f32_16x16x32_bf16 v[124:127], a[100:103], a[60:63], v[124:127]\n"
"s_waitcnt vmcnt(14) lgkmcnt(11)\n"
"ds_read_b128 a[60:63], v248 offset:14336\n"
"v_lshlrev_b32 v251, 16, v237\n"
"v_mfma_f32_16x16x32_bf16 v[0:3], a[104:107], a[0:3], v[0:3]\n"
"v_fmac_f32 v251, v251, v218\n"
"v_and_b32 v237, s82, v237\n"
"v_mfma_f32_16x16x32_bf16 v[0:3], a[108:111], a[4:7], v[0:3]\n"
"v_fmac_f32 v237, v237, v219\n"
"v_cvt_pk_bf16_f32 v217, v251, v237\n"
"v_mfma_f32_16x16x32_bf16 v[4:7], a[104:107], a[8:11], v[4:7]\n"
"v_lshlrev_b32 v251, 16, v238\n"
"v_accvgpr_read_b32 v252, a136\n"
"v_mfma_f32_16x16x32_bf16 v[4:7], a[108:111], a[12:15], v[4:7]\n"
"s_waitcnt lgkmcnt(8)\n"
"v_fmac_f32 v251, v251, v252\n"
"v_and_b32 v238, s82, v238\n"
"v_mfma_f32_16x16x32_bf16 v[8:11], a[104:107], a[16:19], v[8:11]\n"
"buffer_load_dwordx4 v[156:159], v240, s[36:39], s81 offen offset:0 sc1\n"
"v_accvgpr_read_b32 v252, a137\n"
"v_mfma_f32_16x16x32_bf16 v[8:11], a[108:111], a[20:23], v[8:11]\n"
"v_fmac_f32 v238, v238, v252\n"
"v_cvt_pk_bf16_f32 v218, v251, v238\n"
"v_mfma_f32_16x16x32_bf16 v[12:15], a[104:107], a[24:27], v[12:15]\n"
"buffer_load_dwordx4 a[100:103], v240, s[36:39], s80 offen offset:1024 sc1\n"
"v_lshlrev_b32 v251, 16, v239\n"
"v_mfma_f32_16x16x32_bf16 v[12:15], a[108:111], a[28:31], v[12:15]\n"
"s_waitcnt lgkmcnt(4)\n"
"v_accvgpr_read_b32 v252, a138\n"
"v_fmac_f32 v251, v251, v252\n"
"v_mfma_f32_16x16x32_bf16 v[16:19], a[104:107], a[32:35], v[16:19]\n"
"v_and_b32 v239, s82, v239\n"
"v_accvgpr_read_b32 v252, a139\n"
"v_mfma_f32_16x16x32_bf16 v[16:19], a[108:111], a[36:39], v[16:19]\n"
"s_waitcnt lgkmcnt(0)\n"
"s_barrier\n"
"v_fmac_f32 v239, v239, v252\n"
"v_mfma_f32_16x16x32_bf16 v[20:23], a[104:107], a[40:43], v[20:23]\n"
"s_add_u32 m0, s83, 131072\n"
"v_cvt_pk_bf16_f32 v219, v251, v239\n"
"v_mfma_f32_16x16x32_bf16 v[20:23], a[108:111], a[44:47], v[20:23]\n"
"buffer_load_dwordx4 v240, s[56:59], s72 offen offset:0 sc1 lds\n"
"v_mfma_f32_16x16x32_bf16 v[24:27], a[104:107], a[48:51], v[24:27]\n"
"ds_write_b128 v250, v[216:219] offset:45056\n"
"v_mfma_f32_16x16x32_bf16 v[24:27], a[108:111], a[52:55], v[24:27]\n"
"v_mfma_f32_16x16x32_bf16 v[28:31], a[104:107], a[56:59], v[28:31]\n"
"buffer_load_dwordx4 a[104:107], v240, s[36:39], s80 offen offset:2048 sc1\n"
"v_mfma_f32_16x16x32_bf16 v[28:31], a[108:111], a[60:63], v[28:31]\n"
"s_waitcnt vmcnt(16)\n"
"v_mfma_f32_16x16x32_bf16 v[32:35], v[224:227], a[0:3], v[32:35]\n"
"s_add_u32 m0, s83, 133120\n"
"buffer_load_dwordx4 v[188:191], v240, s[36:39], s81 offen offset:1024 sc1\n"
"v_mfma_f32_16x16x32_bf16 v[32:35], v[228:231], a[4:7], v[32:35]\n"
"buffer_load_dwordx4 v240, s[56:59], s73 offen offset:0 sc1 lds\n"
"v_mfma_f32_16x16x32_bf16 v[36:39], v[224:227], a[8:11], v[36:39]\n"
"v_mfma_f32_16x16x32_bf16 v[36:39], v[228:231], a[12:15], v[36:39]\n"
"v_mfma_f32_16x16x32_bf16 v[40:43], v[224:227], a[16:19], v[40:43]\n"
"v_mfma_f32_16x16x32_bf16 v[40:43], v[228:231], a[20:23], v[40:43]\n"
"v_mfma_f32_16x16x32_bf16 v[44:47], v[224:227], a[24:27], v[44:47]\n"
"s_add_u32 m0, s83, 135168\n"
"buffer_load_dwordx4 a[108:111], v240, s[36:39], s80 offen offset:3072 sc1\n"
"v_mfma_f32_16x16x32_bf16 v[44:47], v[228:231], a[28:31], v[44:47]\n"
"s_add_u32 s80, s80, 0x1000\n"
"buffer_load_dwordx4 v240, s[56:59], s74 offen offset:0 sc1 lds\n"
"v_mfma_f32_16x16x32_bf16 v[48:51], v[224:227], a[32:35], v[48:51]\n"
"buffer_store_dwordx4 v[216:219], v240, s[52:55], s78 offen offset:3072 sc1\n"
"v_mfma_f32_16x16x32_bf16 v[48:51], v[228:231], a[36:39], v[48:51]\n"
"s_add_u32 s52, s52, 0x4000\n"
"s_addc_u32 s53, s53, 0\n"
"v_mfma_f32_16x16x32_bf16 v[52:55], v[224:227], a[40:43], v[52:55]\n"
"v_mfma_f32_16x16x32_bf16 v[52:55], v[228:231], a[44:47], v[52:55]\n"
"v_mfma_f32_16x16x32_bf16 v[56:59], v[224:227], a[48:51], v[56:59]\n"
"s_add_u32 m0, s83, 137216\n"
"v_mfma_f32_16x16x32_bf16 v[56:59], v[228:231], a[52:55], v[56:59]\n"
"buffer_load_dwordx4 v240, s[56:59], s75 offen offset:0 sc1 lds\n"
"v_mfma_f32_16x16x32_bf16 v[60:63], v[224:227], a[56:59], v[60:63]\n"
"buffer_load_dwordx4 v[224:227], v240, s[36:39], s80 offen offset:0 sc1\n"
"v_mfma_f32_16x16x32_bf16 v[60:63], v[228:231], a[60:63], v[60:63]\n"
"s_waitcnt vmcnt(20)\n"
"v_mfma_f32_16x16x32_bf16 v[96:99], a[64:67], a[0:3], v[96:99]\n"
"buffer_load_dwordx4 a[140:143], v240, s[36:39], s81 offen offset:3072 sc1\n"
"v_mfma_f32_16x16x32_bf16 v[96:99], a[68:71], a[4:7], v[96:99]\n"
"v_mfma_f32_16x16x32_bf16 v[100:103], a[64:67], a[8:11], v[100:103]\n"
"v_mfma_f32_16x16x32_bf16 v[100:103], a[68:71], a[12:15], v[100:103]\n"
"v_mfma_f32_16x16x32_bf16 v[104:107], a[64:67], a[16:19], v[104:107]\n"
"v_mfma_f32_16x16x32_bf16 v[104:107], a[68:71], a[20:23], v[104:107]\n"
"v_mfma_f32_16x16x32_bf16 v[108:111], a[64:67], a[24:27], v[108:111]\n"
"buffer_load_dwordx4 v[228:231], v240, s[36:39], s80 offen offset:1024 sc1\n"
"v_mfma_f32_16x16x32_bf16 v[108:111], a[68:71], a[28:31], v[108:111]\n"
"v_mfma_f32_16x16x32_bf16 v[112:115], a[64:67], a[32:35], v[112:115]\n"
"v_mfma_f32_16x16x32_bf16 v[112:115], a[68:71], a[36:39], v[112:115]\n"
".Lpad9_%=:\n"
"s_cbranch_execz .Lpad13_%=\n"
"v_mfma_f32_16x16x32_bf16 v[116:119], a[64:67], a[40:43], v[116:119]\n"
"v_mfma_f32_16x16x32_bf16 v[116:119], a[68:71], a[44:47], v[116:119]\n"
"v_mfma_f32_16x16x32_bf16 v[120:123], a[64:67], a[48:51], v[120:123]\n"
"v_mfma_f32_16x16x32_bf16 v[120:123], a[68:71], a[52:55], v[120:123]\n"
"v_mfma_f32_16x16x32_bf16 v[124:127], a[64:67], a[56:59], v[124:127]\n"
"buffer_load_dwordx4 a[64:67], v240, s[36:39], s80 offen offset:2048 sc1\n"
"v_mfma_f32_16x16x32_bf16 v[124:127], a[68:71], a[60:63], v[124:127]\n"
"s_waitcnt vmcnt(20)\n"
"v_mfma_f32_16x16x32_bf16 v[64:67], a[72:75], a[0:3], v[64:67]\n"
"buffer_load_dwordx4 v[220:223], v240, s[36:39], s81 offen offset:2048 sc1\n"
"v_mfma_f32_16x16x32_bf16 v[64:67], a[76:79], a[4:7], v[64:67]\n"
"s_add_u32 s81, s81, 61440\n"
"v_mfma_f32_16x16x32_bf16 v[68:71], a[72:75], a[8:11], v[68:71]\n"
"v_mfma_f32_16x16x32_bf16 v[68:71], a[76:79], a[12:15], v[68:71]\n"
"v_mfma_f32_16x16x32_bf16 v[72:75], a[72:75], a[16:19], v[72:75]\n"
"v_mfma_f32_16x16x32_bf16 v[72:75], a[76:79], a[20:23], v[72:75]\n"
"v_mfma_f32_16x16x32_bf16 v[76:79], a[72:75], a[24:27], v[76:79]\n"
"buffer_load_dwordx4 a[68:71], v240, s[36:39], s80 offen offset:3072 sc1\n"
"v_mfma_f32_16x16x32_bf16 v[76:79], a[76:79], a[28:31], v[76:79]\n"
"s_add_u32 s80, s80, 0x1000\n"
"v_mfma_f32_16x16x32_bf16 v[80:83], a[72:75], a[32:35], v[80:83]\n"
"v_mfma_f32_16x16x32_bf16 v[80:83], a[76:79], a[36:39], v[80:83]\n"
"v_mfma_f32_16x16x32_bf16 v[84:87], a[72:75], a[40:43], v[84:87]\n"
"v_mfma_f32_16x16x32_bf16 v[84:87], a[76:79], a[44:47], v[84:87]\n"
"v_mfma_f32_16x16x32_bf16 v[88:91], a[72:75], a[48:51], v[88:91]\n"
"v_mfma_f32_16x16x32_bf16 v[88:91], a[76:79], a[52:55], v[88:91]\n"
"v_mfma_f32_16x16x32_bf16 v[92:95], a[72:75], a[56:59], v[92:95]\n"
"buffer_load_dwordx4 a[72:75], v240, s[36:39], s80 offen offset:0 sc1\n"
"v_mfma_f32_16x16x32_bf16 v[92:95], a[76:79], a[60:63], v[92:95]\n"
"s_waitcnt vmcnt(16)\n"
"v_mfma_f32_16x16x32_bf16 v[128:131], a[80:83], a[0:3], v[156:159]\n"
"v_mfma_f32_16x16x32_bf16 v[128:131], a[84:87], a[4:7], v[128:131]\n"
"v_mfma_f32_16x16x32_bf16 v[132:135], a[80:83], a[8:11], v[156:159]\n"
"v_mfma_f32_16x16x32_bf16 v[132:135], a[84:87], a[12:15], v[132:135]\n"
"v_mfma_f32_16x16x32_bf16 v[136:139], a[80:83], a[16:19], v[156:159]\n"
"v_mfma_f32_16x16x32_bf16 v[136:139], a[84:87], a[20:23], v[136:139]\n"
"v_mfma_f32_16x16x32_bf16 v[140:143], a[80:83], a[24:27], v[156:159]\n"
"buffer_load_dwordx4 a[76:79], v240, s[36:39], s80 offen offset:1024 sc1\n"
"v_mfma_f32_16x16x32_bf16 v[140:143], a[84:87], a[28:31], v[140:143]\n"
"v_mfma_f32_16x16x32_bf16 v[144:147], a[80:83], a[32:35], v[156:159]\n"
"v_mfma_f32_16x16x32_bf16 v[144:147], a[84:87], a[36:39], v[144:147]\n"
"v_mfma_f32_16x16x32_bf16 v[148:151], a[80:83], a[40:43], v[156:159]\n"
"v_mfma_f32_16x16x32_bf16 v[148:151], a[84:87], a[44:47], v[148:151]\n"
"v_mfma_f32_16x16x32_bf16 v[152:155], a[80:83], a[48:51], v[156:159]\n"
"v_mfma_f32_16x16x32_bf16 v[152:155], a[84:87], a[52:55], v[152:155]\n"
"v_mfma_f32_16x16x32_bf16 v[156:159], a[80:83], a[56:59], v[156:159]\n"
"buffer_load_dwordx4 a[80:83], v240, s[36:39], s80 offen offset:2048 sc1\n"
"v_mfma_f32_16x16x32_bf16 v[156:159], a[84:87], a[60:63], v[156:159]\n"
"s_waitcnt vmcnt(14)\n"
"v_mfma_f32_16x16x32_bf16 v[160:163], a[88:91], a[0:3], v[188:191]\n"
"v_mfma_f32_16x16x32_bf16 v[160:163], a[92:95], a[4:7], v[160:163]\n"
"v_mfma_f32_16x16x32_bf16 v[164:167], a[88:91], a[8:11], v[188:191]\n"
"v_mfma_f32_16x16x32_bf16 v[164:167], a[92:95], a[12:15], v[164:167]\n"
"v_mfma_f32_16x16x32_bf16 v[168:171], a[88:91], a[16:19], v[188:191]\n"
"v_mfma_f32_16x16x32_bf16 v[168:171], a[92:95], a[20:23], v[168:171]\n"
"v_mfma_f32_16x16x32_bf16 v[172:175], a[88:91], a[24:27], v[188:191]\n"
"buffer_load_dwordx4 a[84:87], v240, s[36:39], s80 offen offset:3072 sc1\n"
"v_mfma_f32_16x16x32_bf16 v[172:175], a[92:95], a[28:31], v[172:175]\n"
"s_add_u32 s80, s80, 0x1000\n"
"v_mfma_f32_16x16x32_bf16 v[176:179], a[88:91], a[32:35], v[188:191]\n"
"v_mfma_f32_16x16x32_bf16 v[176:179], a[92:95], a[36:39], v[176:179]\n"
"v_mfma_f32_16x16x32_bf16 v[180:183], a[88:91], a[40:43], v[188:191]\n"
"v_mfma_f32_16x16x32_bf16 v[180:183], a[92:95], a[44:47], v[180:183]\n"
"v_mfma_f32_16x16x32_bf16 v[184:187], a[88:91], a[48:51], v[188:191]\n"
"v_mfma_f32_16x16x32_bf16 v[184:187], a[92:95], a[52:55], v[184:187]\n"
"v_mfma_f32_16x16x32_bf16 v[188:191], a[88:91], a[56:59], v[188:191]\n"
"buffer_load_dwordx4 a[88:91], v240, s[36:39], s80 offen offset:0 sc1\n"
"v_mfma_f32_16x16x32_bf16 v[188:191], a[92:95], a[60:63], v[188:191]\n"
"s_waitcnt vmcnt(9)\n"
"v_mfma_f32_16x16x32_bf16 a[112:115], a[96:99], a[0:3], a[140:143]\n"
"v_mfma_f32_16x16x32_bf16 a[112:115], a[100:103], a[4:7], a[112:115]\n"
"v_mfma_f32_16x16x32_bf16 a[116:119], a[96:99], a[8:11], a[140:143]\n"
"v_mfma_f32_16x16x32_bf16 a[116:119], a[100:103], a[12:15], a[116:119]\n"
"v_mfma_f32_16x16x32_bf16 a[120:123], a[96:99], a[16:19], a[140:143]\n"
"v_mfma_f32_16x16x32_bf16 a[120:123], a[100:103], a[20:23], a[120:123]\n"
"v_mfma_f32_16x16x32_bf16 a[124:127], a[96:99], a[24:27], a[140:143]\n"
"buffer_load_dwordx4 a[92:95], v240, s[36:39], s80 offen offset:1024 sc1\n"
"v_mfma_f32_16x16x32_bf16 a[124:127], a[100:103], a[28:31], a[124:127]\n"
"v_mfma_f32_16x16x32_bf16 a[128:131], a[96:99], a[32:35], a[140:143]\n"
"v_lshrrev_b32 v241, 2, v240\n"
"v_mfma_f32_16x16x32_bf16 a[128:131], a[100:103], a[36:39], a[128:131]\n"
"buffer_store_dword v242, v241, s[60:63], s85 offen offset:1024 sc1\n"
"v_mfma_f32_16x16x32_bf16 a[132:135], a[96:99], a[40:43], a[140:143]\n"
"v_mfma_f32_16x16x32_bf16 a[132:135], a[100:103], a[44:47], a[132:135]\n"
"v_mfma_f32_16x16x32_bf16 a[136:139], a[96:99], a[48:51], a[140:143]\n"
"v_mfma_f32_16x16x32_bf16 a[136:139], a[100:103], a[52:55], a[136:139]\n"
"s_barrier\n"
"v_mfma_f32_16x16x32_bf16 a[140:143], a[96:99], a[56:59], a[140:143]\n"
"buffer_load_dwordx4 a[96:99], v240, s[36:39], s80 offen offset:2048 sc1\n"
"v_mfma_f32_16x16x32_bf16 a[140:143], a[100:103], a[60:63], a[140:143]\n"
"s_waitcnt vmcnt(9)\n"
"v_mfma_f32_16x16x32_bf16 v[192:195], a[104:107], a[0:3], v[220:223]\n"
"ds_read_b128 a[0:3], v247 offset:0\n"
"v_mfma_f32_16x16x32_bf16 v[192:195], a[108:111], a[4:7], v[192:195]\n"
"ds_read_b128 a[4:7], v248 offset:0\n"
"v_mfma_f32_16x16x32_bf16 v[196:199], a[104:107], a[8:11], v[220:223]\n"
"ds_read_b128 a[8:11], v247 offset:2048\n"
"v_mfma_f32_16x16x32_bf16 v[196:199], a[108:111], a[12:15], v[196:199]\n"
"ds_read_b128 a[12:15], v248 offset:2048\n"
"v_mfma_f32_16x16x32_bf16 v[200:203], a[104:107], a[16:19], v[220:223]\n"
"ds_read_b128 a[16:19], v247 offset:4096\n"
"v_mfma_f32_16x16x32_bf16 v[200:203], a[108:111], a[20:23], v[200:203]\n"
"ds_read_b128 a[20:23], v248 offset:4096\n"
"v_mfma_f32_16x16x32_bf16 v[204:207], a[104:107], a[24:27], v[220:223]\n"
"ds_read_b128 a[24:27], v247 offset:6144\n"
"buffer_load_dwordx4 a[100:103], v240, s[36:39], s80 offen offset:3072 sc1\n"
"v_mfma_f32_16x16x32_bf16 v[204:207], a[108:111], a[28:31], v[204:207]\n"
"s_add_u32 s80, s80, 0x1000\n"
"ds_read_b128 a[28:31], v248 offset:6144\n"
"v_mfma_f32_16x16x32_bf16 v[208:211], a[104:107], a[32:35], v[220:223]\n"
"ds_read_b128 a[32:35], v247 offset:8192\n"
"v_mfma_f32_16x16x32_bf16 v[208:211], a[108:111], a[36:39], v[208:211]\n"
"ds_read_b128 a[36:39], v248 offset:8192\n"
"v_mfma_f32_16x16x32_bf16 v[212:215], a[104:107], a[40:43], v[220:223]\n"
"ds_read_b128 a[40:43], v247 offset:10240\n"
"v_mfma_f32_16x16x32_bf16 v[212:215], a[108:111], a[44:47], v[212:215]\n"
"ds_read_b128 a[44:47], v248 offset:10240\n"
"v_mfma_f32_16x16x32_bf16 v[216:219], a[104:107], a[48:51], v[220:223]\n"
"ds_read_b128 a[48:51], v247 offset:12288\n"
"v_mfma_f32_16x16x32_bf16 v[216:219], a[108:111], a[52:55], v[216:219]\n"
"ds_read_b128 a[52:55], v248 offset:12288\n"
"v_mfma_f32_16x16x32_bf16 v[220:223], a[104:107], a[56:59], v[220:223]\n"
"ds_read_b128 a[56:59], v247 offset:14336\n"
"buffer_load_dwordx4 a[104:107], v240, s[36:39], s80 offen offset:0 sc1\n"
"v_mfma_f32_16x16x32_bf16 v[220:223], a[108:111], a[60:63], v[220:223]\n"
"s_waitcnt lgkmcnt(11)\n"
"ds_read_b128 a[60:63], v248 offset:14336\n"
"v_mfma_f32_16x16x32_bf16 v[0:3], v[224:227], a[0:3], v[0:3]\n"
"v_mfma_f32_16x16x32_bf16 v[0:3], v[228:231], a[4:7], v[0:3]\n"
"v_mfma_f32_16x16x32_bf16 v[4:7], v[224:227], a[8:11], v[4:7]\n"
"v_mfma_f32_16x16x32_bf16 v[4:7], v[228:231], a[12:15], v[4:7]\n"
"s_waitcnt lgkmcnt(8)\n"
"v_mfma_f32_16x16x32_bf16 v[8:11], v[224:227], a[16:19], v[8:11]\n"
"v_mfma_f32_16x16x32_bf16 v[8:11], v[228:231], a[20:23], v[8:11]\n"
"v_mfma_f32_16x16x32_bf16 v[12:15], v[224:227], a[24:27], v[12:15]\n"
"buffer_load_dwordx4 a[108:111], v240, s[36:39], s80 offen offset:1024 sc1\n"
"v_mfma_f32_16x16x32_bf16 v[12:15], v[228:231], a[28:31], v[12:15]\n"
"s_waitcnt lgkmcnt(4)\n"
"v_mfma_f32_16x16x32_bf16 v[16:19], v[224:227], a[32:35], v[16:19]\n"
"v_mfma_f32_16x16x32_bf16 v[16:19], v[228:231], a[36:39], v[16:19]\n"
"s_waitcnt lgkmcnt(0)\n"
"s_barrier\n"
"v_mfma_f32_16x16x32_bf16 v[20:23], v[224:227], a[40:43], v[20:23]\n"
"s_add_u32 m0, s83, 130048\n"
"v_mfma_f32_16x16x32_bf16 v[20:23], v[228:231], a[44:47], v[20:23]\n"
"buffer_load_dwordx4 v240, s[56:59], s68 offen offset:1024 sc1 lds\n"
"v_mfma_f32_16x16x32_bf16 v[24:27], v[224:227], a[48:51], v[24:27]\n"
"v_mfma_f32_16x16x32_bf16 v[24:27], v[228:231], a[52:55], v[24:27]\n"
"v_mfma_f32_16x16x32_bf16 v[28:31], v[224:227], a[56:59], v[28:31]\n"
"buffer_load_dwordx4 v[224:227], v240, s[36:39], s80 offen offset:2048 sc1\n"
"v_mfma_f32_16x16x32_bf16 v[28:31], v[228:231], a[60:63], v[28:31]\n"
"s_waitcnt vmcnt(13)\n"
"v_mfma_f32_16x16x32_bf16 v[32:35], a[64:67], a[0:3], v[32:35]\n"
"s_add_u32 m0, s83, 132096\n"
"v_mfma_f32_16x16x32_bf16 v[32:35], a[68:71], a[4:7], v[32:35]\n"
"buffer_load_dwordx4 v240, s[56:59], s69 offen offset:1024 sc1 lds\n"
"v_mfma_f32_16x16x32_bf16 v[36:39], a[64:67], a[8:11], v[36:39]\n"
"v_mfma_f32_16x16x32_bf16 v[36:39], a[68:71], a[12:15], v[36:39]\n"
"v_mfma_f32_16x16x32_bf16 v[40:43], a[64:67], a[16:19], v[40:43]\n"
"v_mfma_f32_16x16x32_bf16 v[40:43], a[68:71], a[20:23], v[40:43]\n"
"v_mfma_f32_16x16x32_bf16 v[44:47], a[64:67], a[24:27], v[44:47]\n"
"s_add_u32 m0, s83, 134144\n"
"buffer_load_dwordx4 v[228:231], v240, s[36:39], s80 offen offset:3072 sc1\n"
"v_mfma_f32_16x16x32_bf16 v[44:47], a[68:71], a[28:31], v[44:47]\n"
"s_add_u32 s80, s80, 0x1000\n"
"buffer_load_dwordx4 v240, s[56:59], s70 offen offset:1024 sc1 lds\n"
"v_mfma_f32_16x16x32_bf16 v[48:51], a[64:67], a[32:35], v[48:51]\n"
"v_mfma_f32_16x16x32_bf16 v[48:51], a[68:71], a[36:39], v[48:51]\n"
"v_mfma_f32_16x16x32_bf16 v[52:55], a[64:67], a[40:43], v[52:55]\n"
"v_mfma_f32_16x16x32_bf16 v[52:55], a[68:71], a[44:47], v[52:55]\n"
"v_mfma_f32_16x16x32_bf16 v[56:59], a[64:67], a[48:51], v[56:59]\n"
"s_add_u32 m0, s83, 136192\n"
"v_mfma_f32_16x16x32_bf16 v[56:59], a[68:71], a[52:55], v[56:59]\n"
"buffer_load_dwordx4 v240, s[56:59], s71 offen offset:1024 sc1 lds\n"
"v_mfma_f32_16x16x32_bf16 v[60:63], a[64:67], a[56:59], v[60:63]\n"
"buffer_load_dwordx4 a[64:67], v240, s[36:39], s80 offen offset:0 sc1\n"
"v_mfma_f32_16x16x32_bf16 v[60:63], a[68:71], a[60:63], v[60:63]\n"
"s_waitcnt vmcnt(16)\n"
"v_mfma_f32_16x16x32_bf16 v[64:67], a[72:75], a[0:3], v[64:67]\n"
"v_mfma_f32_16x16x32_bf16 v[64:67], a[76:79], a[4:7], v[64:67]\n"
"v_mfma_f32_16x16x32_bf16 v[68:71], a[72:75], a[8:11], v[68:71]\n"
"v_mfma_f32_16x16x32_bf16 v[68:71], a[76:79], a[12:15], v[68:71]\n"
"v_mfma_f32_16x16x32_bf16 v[72:75], a[72:75], a[16:19], v[72:75]\n"
"v_mfma_f32_16x16x32_bf16 v[72:75], a[76:79], a[20:23], v[72:75]\n"
"v_mfma_f32_16x16x32_bf16 v[76:79], a[72:75], a[24:27], v[76:79]\n"
"buffer_load_dwordx4 a[68:71], v240, s[36:39], s80 offen offset:1024 sc1\n"
"v_mfma_f32_16x16x32_bf16 v[76:79], a[76:79], a[28:31], v[76:79]\n"
"v_mfma_f32_16x16x32_bf16 v[80:83], a[72:75], a[32:35], v[80:83]\n"
"v_mfma_f32_16x16x32_bf16 v[80:83], a[76:79], a[36:39], v[80:83]\n"
"v_mfma_f32_16x16x32_bf16 v[84:87], a[72:75], a[40:43], v[84:87]\n"
"v_mfma_f32_16x16x32_bf16 v[84:87], a[76:79], a[44:47], v[84:87]\n"
"v_mfma_f32_16x16x32_bf16 v[88:91], a[72:75], a[48:51], v[88:91]\n"
"v_mfma_f32_16x16x32_bf16 v[88:91], a[76:79], a[52:55], v[88:91]\n"
"v_mfma_f32_16x16x32_bf16 v[92:95], a[72:75], a[56:59], v[92:95]\n"
"buffer_load_dwordx4 a[72:75], v240, s[36:39], s80 offen offset:2048 sc1\n"
"v_mfma_f32_16x16x32_bf16 v[92:95], a[76:79], a[60:63], v[92:95]\n"
"s_waitcnt vmcnt(16)\n"
"v_mfma_f32_16x16x32_bf16 v[96:99], a[80:83], a[0:3], v[96:99]\n"
"v_mfma_f32_16x16x32_bf16 v[96:99], a[84:87], a[4:7], v[96:99]\n"
"v_mfma_f32_16x16x32_bf16 v[100:103], a[80:83], a[8:11], v[100:103]\n"
"v_mfma_f32_16x16x32_bf16 v[100:103], a[84:87], a[12:15], v[100:103]\n"
"v_mfma_f32_16x16x32_bf16 v[104:107], a[80:83], a[16:19], v[104:107]\n"
"v_mfma_f32_16x16x32_bf16 v[104:107], a[84:87], a[20:23], v[104:107]\n"
"v_mfma_f32_16x16x32_bf16 v[108:111], a[80:83], a[24:27], v[108:111]\n"
"buffer_load_dwordx4 a[76:79], v240, s[36:39], s80 offen offset:3072 sc1\n"
"v_mfma_f32_16x16x32_bf16 v[108:111], a[84:87], a[28:31], v[108:111]\n"
"s_add_u32 s80, s80, 0x1000\n"
"v_mfma_f32_16x16x32_bf16 v[112:115], a[80:83], a[32:35], v[112:115]\n"
"v_mfma_f32_16x16x32_bf16 v[112:115], a[84:87], a[36:39], v[112:115]\n"
"v_mfma_f32_16x16x32_bf16 v[116:119], a[80:83], a[40:43], v[116:119]\n"
"v_mfma_f32_16x16x32_bf16 v[116:119], a[84:87], a[44:47], v[116:119]\n"
"v_mfma_f32_16x16x32_bf16 v[120:123], a[80:83], a[48:51], v[120:123]\n"
"v_mfma_f32_16x16x32_bf16 v[120:123], a[84:87], a[52:55], v[120:123]\n"
"v_mfma_f32_16x16x32_bf16 v[124:127], a[80:83], a[56:59], v[124:127]\n"
"buffer_load_dwordx4 a[80:83], v240, s[36:39], s80 offen offset:0 sc1\n"
"v_mfma_f32_16x16x32_bf16 v[124:127], a[84:87], a[60:63], v[124:127]\n"
"s_waitcnt vmcnt(16)\n"
"v_mfma_f32_16x16x32_bf16 v[128:131], a[88:91], a[0:3], v[128:131]\n"
"v_mfma_f32_16x16x32_bf16 v[128:131], a[92:95], a[4:7], v[128:131]\n"
"v_mfma_f32_16x16x32_bf16 v[132:135], a[88:91], a[8:11], v[132:135]\n"
"v_mfma_f32_16x16x32_bf16 v[132:135], a[92:95], a[12:15], v[132:135]\n"
"v_mfma_f32_16x16x32_bf16 v[136:139], a[88:91], a[16:19], v[136:139]\n"
"v_mfma_f32_16x16x32_bf16 v[136:139], a[92:95], a[20:23], v[136:139]\n"
"v_mfma_f32_16x16x32_bf16 v[140:143], a[88:91], a[24:27], v[140:143]\n"
"buffer_load_dwordx4 a[84:87], v240, s[36:39], s80 offen offset:1024 sc1\n"
"v_mfma_f32_16x16x32_bf16 v[140:143], a[92:95], a[28:31], v[140:143]\n"
"v_mfma_f32_16x16x32_bf16 v[144:147], a[88:91], a[32:35], v[144:147]\n"
"v_mfma_f32_16x16x32_bf16 v[144:147], a[92:95], a[36:39], v[144:147]\n"
"v_mfma_f32_16x16x32_bf16 v[148:151], a[88:91], a[40:43], v[148:151]\n"
"v_mfma_f32_16x16x32_bf16 v[148:151], a[92:95], a[44:47], v[148:151]\n"
"v_mfma_f32_16x16x32_bf16 v[152:155], a[88:91], a[48:51], v[152:155]\n"
"v_mfma_f32_16x16x32_bf16 v[152:155], a[92:95], a[52:55], v[152:155]\n"
"v_mfma_f32_16x16x32_bf16 v[156:159], a[88:91], a[56:59], v[156:159]\n"
"buffer_load_dwordx4 a[88:91], v240, s[36:39], s80 offen offset:2048 sc1\n"
"v_mfma_f32_16x16x32_bf16 v[156:159], a[92:95], a[60:63], v[156:159]\n"
"s_waitcnt vmcnt(15)\n"
"v_mfma_f32_16x16x32_bf16 v[160:163], a[96:99], a[0:3], v[160:163]\n"
"v_mfma_f32_16x16x32_bf16 v[160:163], a[100:103], a[4:7], v[160:163]\n"
"v_mfma_f32_16x16x32_bf16 v[164:167], a[96:99], a[8:11], v[164:167]\n"
"v_mfma_f32_16x16x32_bf16 v[164:167], a[100:103], a[12:15], v[164:167]\n"
"v_mfma_f32_16x16x32_bf16 v[168:171], a[96:99], a[16:19], v[168:171]\n"
"v_mfma_f32_16x16x32_bf16 v[168:171], a[100:103], a[20:23], v[168:171]\n"
"v_mfma_f32_16x16x32_bf16 v[172:175], a[96:99], a[24:27], v[172:175]\n"
"buffer_load_dwordx4 a[92:95], v240, s[36:39], s80 offen offset:3072 sc1\n"
"v_mfma_f32_16x16x32_bf16 v[172:175], a[100:103], a[28:31], v[172:175]\n"
"s_add_u32 s80, s80, 0x1000\n"
"v_mfma_f32_16x16x32_bf16 v[176:179], a[96:99], a[32:35], v[176:179]\n"
"v_mfma_f32_16x16x32_bf16 v[176:179], a[100:103], a[36:39], v[176:179]\n"
"v_mfma_f32_16x16x32_bf16 v[180:183], a[96:99], a[40:43], v[180:183]\n"
"v_mfma_f32_16x16x32_bf16 v[180:183], a[100:103], a[44:47], v[180:183]\n"
"v_mfma_f32_16x16x32_bf16 v[184:187], a[96:99], a[48:51], v[184:187]\n"
"v_mfma_f32_16x16x32_bf16 v[184:187], a[100:103], a[52:55], v[184:187]\n"
"v_mfma_f32_16x16x32_bf16 v[188:191], a[96:99], a[56:59], v[188:191]\n"
"buffer_load_dwordx4 a[96:99], v240, s[36:39], s80 offen offset:0 sc1\n"
"v_mfma_f32_16x16x32_bf16 v[188:191], a[100:103], a[60:63], v[188:191]\n"
"s_waitcnt vmcnt(15)\n"
"v_mfma_f32_16x16x32_bf16 v[192:195], a[104:107], a[0:3], v[192:195]\n"
"v_mfma_f32_16x16x32_bf16 v[192:195], a[108:111], a[4:7], v[192:195]\n"
"v_mfma_f32_16x16x32_bf16 v[196:199], a[104:107], a[8:11], v[196:199]\n"
"v_mfma_f32_16x16x32_bf16 v[196:199], a[108:111], a[12:15], v[196:199]\n"
"v_mfma_f32_16x16x32_bf16 v[200:203], a[104:107], a[16:19], v[200:203]\n"
"v_mfma_f32_16x16x32_bf16 v[200:203], a[108:111], a[20:23], v[200:203]\n"
"v_mfma_f32_16x16x32_bf16 v[204:207], a[104:107], a[24:27], v[204:207]\n"
"buffer_load_dwordx4 a[100:103], v240, s[36:39], s80 offen offset:1024 sc1\n"
"v_mfma_f32_16x16x32_bf16 v[204:207], a[108:111], a[28:31], v[204:207]\n"
"v_mfma_f32_16x16x32_bf16 v[208:211], a[104:107], a[32:35], v[208:211]\n"
"v_mfma_f32_16x16x32_bf16 v[208:211], a[108:111], a[36:39], v[208:211]\n"
"v_mfma_f32_16x16x32_bf16 v[212:215], a[104:107], a[40:43], v[212:215]\n"
"v_mfma_f32_16x16x32_bf16 v[212:215], a[108:111], a[44:47], v[212:215]\n"
"v_mfma_f32_16x16x32_bf16 v[216:219], a[104:107], a[48:51], v[216:219]\n"
"v_mfma_f32_16x16x32_bf16 v[216:219], a[108:111], a[52:55], v[216:219]\n"
"s_waitcnt vmcnt(10)\n"
"s_barrier\n"
"v_mfma_f32_16x16x32_bf16 v[220:223], a[104:107], a[56:59], v[220:223]\n"
"buffer_load_dwordx4 a[104:107], v240, s[36:39], s80 offen offset:2048 sc1\n"
"v_mfma_f32_16x16x32_bf16 v[220:223], a[108:111], a[60:63], v[220:223]\n"
"v_mfma_f32_16x16x32_bf16 a[112:115], v[224:227], a[0:3], a[112:115]\n"
"ds_read_b128 a[0:3], v247 offset:0\n"
"v_mfma_f32_16x16x32_bf16 a[112:115], v[228:231], a[4:7], a[112:115]\n"
"ds_read_b128 a[4:7], v248 offset:0\n"
"v_mfma_f32_16x16x32_bf16 a[116:119], v[224:227], a[8:11], a[116:119]\n"
"ds_read_b128 a[8:11], v247 offset:2048\n"
"v_mfma_f32_16x16x32_bf16 a[116:119], v[228:231], a[12:15], a[116:119]\n"
"ds_read_b128 a[12:15], v248 offset:2048\n"
"v_mfma_f32_16x16x32_bf16 a[120:123], v[224:227], a[16:19], a[120:123]\n"
"ds_read_b128 a[16:19], v247 offset:4096\n"
"v_mfma_f32_16x16x32_bf16 a[120:123], v[228:231], a[20:23], a[120:123]\n"
"ds_read_b128 a[20:23], v248 offset:4096\n"
"v_mfma_f32_16x16x32_bf16 a[124:127], v[224:227], a[24:27], a[124:127]\n"
"ds_read_b128 a[24:27], v247 offset:6144\n"
"buffer_load_dwordx4 a[108:111], v240, s[36:39], s80 offen offset:3072 sc1\n"
"v_mfma_f32_16x16x32_bf16 a[124:127], v[228:231], a[28:31], a[124:127]\n"
"s_add_u32 s80, s80, 0x1000\n"
"ds_read_b128 a[28:31], v248 offset:6144\n"
"v_mfma_f32_16x16x32_bf16 a[128:131], v[224:227], a[32:35], a[128:131]\n"
"ds_read_b128 a[32:35], v247 offset:8192\n"
"v_mfma_f32_16x16x32_bf16 a[128:131], v[228:231], a[36:39], a[128:131]\n"
"ds_read_b128 a[36:39], v248 offset:8192\n"
"v_mfma_f32_16x16x32_bf16 a[132:135], v[224:227], a[40:43], a[132:135]\n"
"ds_read_b128 a[40:43], v247 offset:10240\n"
"v_mfma_f32_16x16x32_bf16 a[132:135], v[228:231], a[44:47], a[132:135]\n"
"ds_read_b128 a[44:47], v248 offset:10240\n"
"v_mfma_f32_16x16x32_bf16 a[136:139], v[224:227], a[48:51], a[136:139]\n"
"ds_read_b128 a[48:51], v247 offset:12288\n"
"v_mfma_f32_16x16x32_bf16 a[136:139], v[228:231], a[52:55], a[136:139]\n"
"ds_read_b128 a[52:55], v248 offset:12288\n"
"v_mfma_f32_16x16x32_bf16 a[140:143], v[224:227], a[56:59], a[140:143]\n"
"ds_read_b128 a[56:59], v247 offset:14336\n"
"buffer_load_dwordx4 v[224:227], v240, s[36:39], s80 offen offset:0 sc1\n"
"v_mfma_f32_16x16x32_bf16 a[140:143], v[228:231], a[60:63], a[140:143]\n"
"s_waitcnt vmcnt(11) lgkmcnt(11)\n"
"ds_read_b128 a[60:63], v248 offset:14336\n"
"v_mfma_f32_16x16x32_bf16 v[0:3], a[64:67], a[0:3], v[0:3]\n"
"v_mfma_f32_16x16x32_bf16 v[0:3], a[68:71], a[4:7], v[0:3]\n"
"v_mfma_f32_16x16x32_bf16 v[4:7], a[64:67], a[8:11], v[4:7]\n"
"v_mfma_f32_16x16x32_bf16 v[4:7], a[68:71], a[12:15], v[4:7]\n"
"s_waitcnt lgkmcnt(8)\n"
"v_mfma_f32_16x16x32_bf16 v[8:11], a[64:67], a[16:19], v[8:11]\n"
"v_mfma_f32_16x16x32_bf16 v[8:11], a[68:71], a[20:23], v[8:11]\n"
"v_mfma_f32_16x16x32_bf16 v[12:15], a[64:67], a[24:27], v[12:15]\n"
"buffer_load_dwordx4 v[228:231], v240, s[36:39], s80 offen offset:1024 sc1\n"
"v_mfma_f32_16x16x32_bf16 v[12:15], a[68:71], a[28:31], v[12:15]\n"
"s_waitcnt lgkmcnt(4)\n"
"v_mfma_f32_16x16x32_bf16 v[16:19], a[64:67], a[32:35], v[16:19]\n"
"v_mfma_f32_16x16x32_bf16 v[16:19], a[68:71], a[36:39], v[16:19]\n"
"s_waitcnt lgkmcnt(0)\n"
"s_barrier\n"
"v_mfma_f32_16x16x32_bf16 v[20:23], a[64:67], a[40:43], v[20:23]\n"
"s_add_u32 m0, s83, 130048\n"
"v_mfma_f32_16x16x32_bf16 v[20:23], a[68:71], a[44:47], v[20:23]\n"
"buffer_load_dwordx4 v240, s[56:59], s72 offen offset:1024 sc1 lds\n"
"v_mfma_f32_16x16x32_bf16 v[24:27], a[64:67], a[48:51], v[24:27]\n"
"v_mfma_f32_16x16x32_bf16 v[24:27], a[68:71], a[52:55], v[24:27]\n"
"v_mfma_f32_16x16x32_bf16 v[28:31], a[64:67], a[56:59], v[28:31]\n"
"buffer_load_dwordx4 a[64:67], v240, s[36:39], s80 offen offset:2048 sc1\n"
"v_mfma_f32_16x16x32_bf16 v[28:31], a[68:71], a[60:63], v[28:31]\n"
"s_waitcnt vmcnt(12)\n"
"v_mfma_f32_16x16x32_bf16 v[32:35], a[72:75], a[0:3], v[32:35]\n"
"s_add_u32 m0, s83, 132096\n"
"v_mfma_f32_16x16x32_bf16 v[32:35], a[76:79], a[4:7], v[32:35]\n"
"buffer_load_dwordx4 v240, s[56:59], s73 offen offset:1024 sc1 lds\n"
"v_mfma_f32_16x16x32_bf16 v[36:39], a[72:75], a[8:11], v[36:39]\n"
"v_mfma_f32_16x16x32_bf16 v[36:39], a[76:79], a[12:15], v[36:39]\n"
"v_mfma_f32_16x16x32_bf16 v[40:43], a[72:75], a[16:19], v[40:43]\n"
"v_mfma_f32_16x16x32_bf16 v[40:43], a[76:79], a[20:23], v[40:43]\n"
"v_mfma_f32_16x16x32_bf16 v[44:47], a[72:75], a[24:27], v[44:47]\n"
"s_add_u32 m0, s83, 134144\n"
"buffer_load_dwordx4 a[68:71], v240, s[36:39], s80 offen offset:3072 sc1\n"
"v_mfma_f32_16x16x32_bf16 v[44:47], a[76:79], a[28:31], v[44:47]\n"
"s_add_u32 s80, s80, 0x1000\n"
"buffer_load_dwordx4 v240, s[56:59], s74 offen offset:1024 sc1 lds\n"
"v_mfma_f32_16x16x32_bf16 v[48:51], a[72:75], a[32:35], v[48:51]\n"
"v_mfma_f32_16x16x32_bf16 v[48:51], a[76:79], a[36:39], v[48:51]\n"
"v_mfma_f32_16x16x32_bf16 v[52:55], a[72:75], a[40:43], v[52:55]\n"
"v_mfma_f32_16x16x32_bf16 v[52:55], a[76:79], a[44:47], v[52:55]\n"
"v_mfma_f32_16x16x32_bf16 v[56:59], a[72:75], a[48:51], v[56:59]\n"
"s_add_u32 m0, s83, 136192\n"
"v_mfma_f32_16x16x32_bf16 v[56:59], a[76:79], a[52:55], v[56:59]\n"
"buffer_load_dwordx4 v240, s[56:59], s75 offen offset:1024 sc1 lds\n"
"v_mfma_f32_16x16x32_bf16 v[60:63], a[72:75], a[56:59], v[60:63]\n"
"buffer_load_dwordx4 a[72:75], v240, s[36:39], s80 offen offset:0 sc1\n"
"v_mfma_f32_16x16x32_bf16 v[60:63], a[76:79], a[60:63], v[60:63]\n"
"s_waitcnt vmcnt(15)\n"
"v_mfma_f32_16x16x32_bf16 v[64:67], a[80:83], a[0:3], v[64:67]\n"
"v_mfma_f32_16x16x32_bf16 v[64:67], a[84:87], a[4:7], v[64:67]\n"
"v_mfma_f32_16x16x32_bf16 v[68:71], a[80:83], a[8:11], v[68:71]\n"
"v_mfma_f32_16x16x32_bf16 v[68:71], a[84:87], a[12:15], v[68:71]\n"
"v_mfma_f32_16x16x32_bf16 v[72:75], a[80:83], a[16:19], v[72:75]\n"
"v_mfma_f32_16x16x32_bf16 v[72:75], a[84:87], a[20:23], v[72:75]\n"
"v_mfma_f32_16x16x32_bf16 v[76:79], a[80:83], a[24:27], v[76:79]\n"
"buffer_load_dwordx4 a[76:79], v240, s[36:39], s80 offen offset:1024 sc1\n"
"v_mfma_f32_16x16x32_bf16 v[76:79], a[84:87], a[28:31], v[76:79]\n"
"v_mfma_f32_16x16x32_bf16 v[80:83], a[80:83], a[32:35], v[80:83]\n"
"v_mfma_f32_16x16x32_bf16 v[80:83], a[84:87], a[36:39], v[80:83]\n"
"v_mfma_f32_16x16x32_bf16 v[84:87], a[80:83], a[40:43], v[84:87]\n"
"v_mfma_f32_16x16x32_bf16 v[84:87], a[84:87], a[44:47], v[84:87]\n"
"v_mfma_f32_16x16x32_bf16 v[88:91], a[80:83], a[48:51], v[88:91]\n"
"v_mfma_f32_16x16x32_bf16 v[88:91], a[84:87], a[52:55], v[88:91]\n"
"v_mfma_f32_16x16x32_bf16 v[92:95], a[80:83], a[56:59], v[92:95]\n"
"buffer_load_dwordx4 a[80:83], v240, s[36:39], s80 offen offset:2048 sc1\n"
"v_mfma_f32_16x16x32_bf16 v[92:95], a[84:87], a[60:63], v[92:95]\n"
"s_waitcnt vmcnt(15)\n"
"v_mfma_f32_16x16x32_bf16 v[96:99], a[88:91], a[0:3], v[96:99]\n"
"v_mfma_f32_16x16x32_bf16 v[96:99], a[92:95], a[4:7], v[96:99]\n"
"v_mfma_f32_16x16x32_bf16 v[100:103], a[88:91], a[8:11], v[100:103]\n"
"v_mfma_f32_16x16x32_bf16 v[100:103], a[92:95], a[12:15], v[100:103]\n"
"v_mfma_f32_16x16x32_bf16 v[104:107], a[88:91], a[16:19], v[104:107]\n"
"v_mfma_f32_16x16x32_bf16 v[104:107], a[92:95], a[20:23], v[104:107]\n"
"v_mfma_f32_16x16x32_bf16 v[108:111], a[88:91], a[24:27], v[108:111]\n"
"buffer_load_dwordx4 a[84:87], v240, s[36:39], s80 offen offset:3072 sc1\n"
"v_mfma_f32_16x16x32_bf16 v[108:111], a[92:95], a[28:31], v[108:111]\n"
"s_add_u32 s80, s80, 0x1000\n"
"v_mfma_f32_16x16x32_bf16 v[112:115], a[88:91], a[32:35], v[112:115]\n"
"v_mfma_f32_16x16x32_bf16 v[112:115], a[92:95], a[36:39], v[112:115]\n"
"v_mfma_f32_16x16x32_bf16 v[116:119], a[88:91], a[40:43], v[116:119]\n"
"v_mfma_f32_16x16x32_bf16 v[116:119], a[92:95], a[44:47], v[116:119]\n"
"v_mfma_f32_16x16x32_bf16 v[120:123], a[88:91], a[48:51], v[120:123]\n"
"v_mfma_f32_16x16x32_bf16 v[120:123], a[92:95], a[52:55], v[120:123]\n"
"v_mfma_f32_16x16x32_bf16 v[124:127], a[88:91], a[56:59], v[124:127]\n"
"buffer_load_dwordx4 a[88:91], v240, s[36:39], s80 offen offset:0 sc1\n"
"v_mfma_f32_16x16x32_bf16 v[124:127], a[92:95], a[60:63], v[124:127]\n"
"s_waitcnt vmcnt(15)\n"
"v_mfma_f32_16x16x32_bf16 v[128:131], a[96:99], a[0:3], v[128:131]\n"
"v_mfma_f32_16x16x32_bf16 v[128:131], a[100:103], a[4:7], v[128:131]\n"
"v_mfma_f32_16x16x32_bf16 v[132:135], a[96:99], a[8:11], v[132:135]\n"
"v_mfma_f32_16x16x32_bf16 v[132:135], a[100:103], a[12:15], v[132:135]\n"
"v_mfma_f32_16x16x32_bf16 v[136:139], a[96:99], a[16:19], v[136:139]\n"
"v_mfma_f32_16x16x32_bf16 v[136:139], a[100:103], a[20:23], v[136:139]\n"
"v_mfma_f32_16x16x32_bf16 v[140:143], a[96:99], a[24:27], v[140:143]\n"
"buffer_load_dwordx4 a[92:95], v240, s[36:39], s80 offen offset:1024 sc1\n"
"v_mfma_f32_16x16x32_bf16 v[140:143], a[100:103], a[28:31], v[140:143]\n"
"v_mfma_f32_16x16x32_bf16 v[144:147], a[96:99], a[32:35], v[144:147]\n"
"v_mfma_f32_16x16x32_bf16 v[144:147], a[100:103], a[36:39], v[144:147]\n"
"v_mfma_f32_16x16x32_bf16 v[148:151], a[96:99], a[40:43], v[148:151]\n"
"v_mfma_f32_16x16x32_bf16 v[148:151], a[100:103], a[44:47], v[148:151]\n"
"v_mfma_f32_16x16x32_bf16 v[152:155], a[96:99], a[48:51], v[152:155]\n"
"v_mfma_f32_16x16x32_bf16 v[152:155], a[100:103], a[52:55], v[152:155]\n"
"v_mfma_f32_16x16x32_bf16 v[156:159], a[96:99], a[56:59], v[156:159]\n"
"buffer_load_dwordx4 a[96:99], v240, s[36:39], s80 offen offset:2048 sc1\n"
"v_mfma_f32_16x16x32_bf16 v[156:159], a[100:103], a[60:63], v[156:159]\n"
"s_waitcnt vmcnt(15)\n"
"v_mfma_f32_16x16x32_bf16 v[160:163], a[104:107], a[0:3], v[160:163]\n"
"v_mfma_f32_16x16x32_bf16 v[160:163], a[108:111], a[4:7], v[160:163]\n"
"v_mfma_f32_16x16x32_bf16 v[164:167], a[104:107], a[8:11], v[164:167]\n"
"v_mfma_f32_16x16x32_bf16 v[164:167], a[108:111], a[12:15], v[164:167]\n"
"v_mfma_f32_16x16x32_bf16 v[168:171], a[104:107], a[16:19], v[168:171]\n"
"v_mfma_f32_16x16x32_bf16 v[168:171], a[108:111], a[20:23], v[168:171]\n"
"v_mfma_f32_16x16x32_bf16 v[172:175], a[104:107], a[24:27], v[172:175]\n"
"buffer_load_dwordx4 a[100:103], v240, s[36:39], s80 offen offset:3072 sc1\n"
"v_mfma_f32_16x16x32_bf16 v[172:175], a[108:111], a[28:31], v[172:175]\n"
"s_add_u32 s80, s80, 0x1000\n"
"v_mfma_f32_16x16x32_bf16 v[176:179], a[104:107], a[32:35], v[176:179]\n"
"v_mfma_f32_16x16x32_bf16 v[176:179], a[108:111], a[36:39], v[176:179]\n"
"v_mfma_f32_16x16x32_bf16 v[180:183], a[104:107], a[40:43], v[180:183]\n"
"v_mfma_f32_16x16x32_bf16 v[180:183], a[108:111], a[44:47], v[180:183]\n"
"v_mfma_f32_16x16x32_bf16 v[184:187], a[104:107], a[48:51], v[184:187]\n"
"v_mfma_f32_16x16x32_bf16 v[184:187], a[108:111], a[52:55], v[184:187]\n"
"v_mfma_f32_16x16x32_bf16 v[188:191], a[104:107], a[56:59], v[188:191]\n"
"buffer_load_dwordx4 a[104:107], v240, s[36:39], s80 offen offset:0 sc1\n"
"v_mfma_f32_16x16x32_bf16 v[188:191], a[108:111], a[60:63], v[188:191]\n"
"s_waitcnt vmcnt(15)\n"
"v_mfma_f32_16x16x32_bf16 v[192:195], v[224:227], a[0:3], v[192:195]\n"
"v_mfma_f32_16x16x32_bf16 v[192:195], v[228:231], a[4:7], v[192:195]\n"
"v_mfma_f32_16x16x32_bf16 v[196:199], v[224:227], a[8:11], v[196:199]\n"
"v_mfma_f32_16x16x32_bf16 v[196:199], v[228:231], a[12:15], v[196:199]\n"
"v_mfma_f32_16x16x32_bf16 v[200:203], v[224:227], a[16:19], v[200:203]\n"
"v_mfma_f32_16x16x32_bf16 v[200:203], v[228:231], a[20:23], v[200:203]\n"
"v_mfma_f32_16x16x32_bf16 v[204:207], v[224:227], a[24:27], v[204:207]\n"
"buffer_load_dwordx4 a[108:111], v240, s[36:39], s80 offen offset:1024 sc1\n"
"v_mfma_f32_16x16x32_bf16 v[204:207], v[228:231], a[28:31], v[204:207]\n"
"v_mfma_f32_16x16x32_bf16 v[208:211], v[224:227], a[32:35], v[208:211]\n"
"v_mfma_f32_16x16x32_bf16 v[208:211], v[228:231], a[36:39], v[208:211]\n"
"v_mfma_f32_16x16x32_bf16 v[212:215], v[224:227], a[40:43], v[212:215]\n"
"v_mfma_f32_16x16x32_bf16 v[212:215], v[228:231], a[44:47], v[212:215]\n"
"v_mfma_f32_16x16x32_bf16 v[216:219], v[224:227], a[48:51], v[216:219]\n"
"v_mfma_f32_16x16x32_bf16 v[216:219], v[228:231], a[52:55], v[216:219]\n"
"s_barrier\n"
"v_mfma_f32_16x16x32_bf16 v[220:223], v[224:227], a[56:59], v[220:223]\n"
"buffer_load_dwordx4 v[224:227], v240, s[36:39], s80 offen offset:2048 sc1\n"
"v_mfma_f32_16x16x32_bf16 v[220:223], v[228:231], a[60:63], v[220:223]\n"
"s_waitcnt vmcnt(13)\n"
"v_mfma_f32_16x16x32_bf16 a[112:115], a[64:67], a[0:3], a[112:115]\n"
"ds_read_b128 a[0:3], v245 offset:32768\n"
"v_mfma_f32_16x16x32_bf16 a[112:115], a[68:71], a[4:7], a[112:115]\n"
"ds_read_b128 a[4:7], v246 offset:32768\n"
"v_mfma_f32_16x16x32_bf16 a[116:119], a[64:67], a[8:11], a[116:119]\n"
"ds_read_b128 a[8:11], v245 offset:34816\n"
"v_mfma_f32_16x16x32_bf16 a[116:119], a[68:71], a[12:15], a[116:119]\n"
"ds_read_b128 a[12:15], v246 offset:34816\n"
"v_mfma_f32_16x16x32_bf16 a[120:123], a[64:67], a[16:19], a[120:123]\n"
"ds_read_b128 a[16:19], v245 offset:36864\n"
"v_mfma_f32_16x16x32_bf16 a[120:123], a[68:71], a[20:23], a[120:123]\n"
"ds_read_b128 a[20:23], v246 offset:36864\n"
"v_mfma_f32_16x16x32_bf16 a[124:127], a[64:67], a[24:27], a[124:127]\n"
"ds_read_b128 a[24:27], v245 offset:38912\n"
"buffer_load_dwordx4 v[228:231], v240, s[36:39], s80 offen offset:3072 sc1\n"
"v_mfma_f32_16x16x32_bf16 a[124:127], a[68:71], a[28:31], a[124:127]\n"
"s_add_u32 s80, s80, 0x1000\n"
"ds_read_b128 a[28:31], v246 offset:38912\n"
"v_mfma_f32_16x16x32_bf16 a[128:131], a[64:67], a[32:35], a[128:131]\n"
"ds_read_b128 a[32:35], v245 offset:40960\n"
"v_mfma_f32_16x16x32_bf16 a[128:131], a[68:71], a[36:39], a[128:131]\n"
"ds_read_b128 a[36:39], v246 offset:40960\n"
"v_mfma_f32_16x16x32_bf16 a[132:135], a[64:67], a[40:43], a[132:135]\n"
"ds_read_b128 a[40:43], v245 offset:43008\n"
"v_mfma_f32_16x16x32_bf16 a[132:135], a[68:71], a[44:47], a[132:135]\n"
"ds_read_b128 a[44:47], v246 offset:43008\n"
"v_mfma_f32_16x16x32_bf16 a[136:139], a[64:67], a[48:51], a[136:139]\n"
"ds_read_b128 a[48:51], v245 offset:45056\n"
"v_mfma_f32_16x16x32_bf16 a[136:139], a[68:71], a[52:55], a[136:139]\n"
"ds_read_b128 a[52:55], v246 offset:45056\n"
"v_mfma_f32_16x16x32_bf16 a[140:143], a[64:67], a[56:59], a[140:143]\n"
"ds_read_b128 a[56:59], v245 offset:47104\n"
"buffer_load_dwordx4 a[64:67], v240, s[36:39], s80 offen offset:0 sc1\n"
"v_mfma_f32_16x16x32_bf16 a[140:143], a[68:71], a[60:63], a[140:143]\n"
"s_waitcnt vmcnt(11) lgkmcnt(11)\n"
"ds_read_b128 a[60:63], v246 offset:47104\n"
"v_mfma_f32_16x16x32_bf16 v[0:3], a[72:75], a[0:3], v[0:3]\n"
"v_mfma_f32_16x16x32_bf16 v[0:3], a[76:79], a[4:7], v[0:3]\n"
"v_mfma_f32_16x16x32_bf16 v[4:7], a[72:75], a[8:11], v[4:7]\n"
"v_mfma_f32_16x16x32_bf16 v[4:7], a[76:79], a[12:15], v[4:7]\n"
"s_waitcnt lgkmcnt(8)\n"
"v_mfma_f32_16x16x32_bf16 v[8:11], a[72:75], a[16:19], v[8:11]\n"
"v_mfma_f32_16x16x32_bf16 v[8:11], a[76:79], a[20:23], v[8:11]\n"
"v_mfma_f32_16x16x32_bf16 v[12:15], a[72:75], a[24:27], v[12:15]\n"
"buffer_load_dwordx4 a[68:71], v240, s[36:39], s80 offen offset:1024 sc1\n"
"v_mfma_f32_16x16x32_bf16 v[12:15], a[76:79], a[28:31], v[12:15]\n"
"s_waitcnt lgkmcnt(4)\n"
"v_mfma_f32_16x16x32_bf16 v[16:19], a[72:75], a[32:35], v[16:19]\n"
"v_mfma_f32_16x16x32_bf16 v[16:19], a[76:79], a[36:39], v[16:19]\n"
"v_mfma_f32_16x16x32_bf16 v[20:23], a[72:75], a[40:43], v[20:23]\n"
"v_mfma_f32_16x16x32_bf16 v[20:23], a[76:79], a[44:47], v[20:23]\n"
"s_waitcnt lgkmcnt(0)\n"
"v_mfma_f32_16x16x32_bf16 v[24:27], a[72:75], a[48:51], v[24:27]\n"
"v_mfma_f32_16x16x32_bf16 v[24:27], a[76:79], a[52:55], v[24:27]\n"
"v_mfma_f32_16x16x32_bf16 v[28:31], a[72:75], a[56:59], v[28:31]\n"
"buffer_load_dwordx4 a[72:75], v240, s[36:39], s80 offen offset:2048 sc1\n"
"v_mfma_f32_16x16x32_bf16 v[28:31], a[76:79], a[60:63], v[28:31]\n"
"s_waitcnt vmcnt(11)\n"
"v_mfma_f32_16x16x32_bf16 v[32:35], a[80:83], a[0:3], v[32:35]\n"
"v_mfma_f32_16x16x32_bf16 v[32:35], a[84:87], a[4:7], v[32:35]\n"
"v_mfma_f32_16x16x32_bf16 v[36:39], a[80:83], a[8:11], v[36:39]\n"
"v_mfma_f32_16x16x32_bf16 v[36:39], a[84:87], a[12:15], v[36:39]\n"
"v_mfma_f32_16x16x32_bf16 v[40:43], a[80:83], a[16:19], v[40:43]\n"
"v_mfma_f32_16x16x32_bf16 v[40:43], a[84:87], a[20:23], v[40:43]\n"
"v_mfma_f32_16x16x32_bf16 v[44:47], a[80:83], a[24:27], v[44:47]\n"
"buffer_load_dwordx4 a[76:79], v240, s[36:39], s80 offen offset:3072 sc1\n"
"v_mfma_f32_16x16x32_bf16 v[44:47], a[84:87], a[28:31], v[44:47]\n"
"s_add_u32 s80, s80, 0x1000\n"
"v_mfma_f32_16x16x32_bf16 v[48:51], a[80:83], a[32:35], v[48:51]\n"
".Lpad10_%=:\n"
"s_cbranch_execz .Lpad14_%=\n"
"v_mfma_f32_16x16x32_bf16 v[48:51], a[84:87], a[36:39], v[48:51]\n"
"v_mfma_f32_16x16x32_bf16 v[52:55], a[80:83], a[40:43], v[52:55]\n"
"v_mfma_f32_16x16x32_bf16 v[52:55], a[84:87], a[44:47], v[52:55]\n"
"v_mfma_f32_16x16x32_bf16 v[56:59], a[80:83], a[48:51], v[56:59]\n"
"v_mfma_f32_16x16x32_bf16 v[56:59], a[84:87], a[52:55], v[56:59]\n"
"v_mfma_f32_16x16x32_bf16 v[60:63], a[80:83], a[56:59], v[60:63]\n"
"buffer_load_dwordx4 a[80:83], v240, s[36:39], s80 offen offset:0 sc1\n"
"v_mfma_f32_16x16x32_bf16 v[60:63], a[84:87], a[60:63], v[60:63]\n"
"s_waitcnt vmcnt(11)\n"
"v_mfma_f32_16x16x32_bf16 v[64:67], a[88:91], a[0:3], v[64:67]\n"
"v_mfma_f32_16x16x32_bf16 v[64:67], a[92:95], a[4:7], v[64:67]\n"
"v_mfma_f32_16x16x32_bf16 v[68:71], a[88:91], a[8:11], v[68:71]\n"
"v_mfma_f32_16x16x32_bf16 v[68:71], a[92:95], a[12:15], v[68:71]\n"
"v_mfma_f32_16x16x32_bf16 v[72:75], a[88:91], a[16:19], v[72:75]\n"
"v_mfma_f32_16x16x32_bf16 v[72:75], a[92:95], a[20:23], v[72:75]\n"
"v_mfma_f32_16x16x32_bf16 v[76:79], a[88:91], a[24:27], v[76:79]\n"
"buffer_load_dwordx4 a[84:87], v240, s[36:39], s80 offen offset:1024 sc1\n"
"v_mfma_f32_16x16x32_bf16 v[76:79], a[92:95], a[28:31], v[76:79]\n"
"v_mfma_f32_16x16x32_bf16 v[80:83], a[88:91], a[32:35], v[80:83]\n"
"v_mfma_f32_16x16x32_bf16 v[80:83], a[92:95], a[36:39], v[80:83]\n"
"v_mfma_f32_16x16x32_bf16 v[84:87], a[88:91], a[40:43], v[84:87]\n"
"v_mfma_f32_16x16x32_bf16 v[84:87], a[92:95], a[44:47], v[84:87]\n"
"v_mfma_f32_16x16x32_bf16 v[88:91], a[88:91], a[48:51], v[88:91]\n"
"v_mfma_f32_16x16x32_bf16 v[88:91], a[92:95], a[52:55], v[88:91]\n"
"v_mfma_f32_16x16x32_bf16 v[92:95], a[88:91], a[56:59], v[92:95]\n"
"buffer_load_dwordx4 a[88:91], v240, s[36:39], s80 offen offset:2048 sc1\n"
"v_mfma_f32_16x16x32_bf16 v[92:95], a[92:95], a[60:63], v[92:95]\n"
"s_waitcnt vmcnt(11)\n"
"v_mfma_f32_16x16x32_bf16 v[96:99], a[96:99], a[0:3], v[96:99]\n"
"v_mfma_f32_16x16x32_bf16 v[96:99], a[100:103], a[4:7], v[96:99]\n"
"v_mfma_f32_16x16x32_bf16 v[100:103], a[96:99], a[8:11], v[100:103]\n"
"v_mfma_f32_16x16x32_bf16 v[100:103], a[100:103], a[12:15], v[100:103]\n"
"v_mfma_f32_16x16x32_bf16 v[104:107], a[96:99], a[16:19], v[104:107]\n"
"v_mfma_f32_16x16x32_bf16 v[104:107], a[100:103], a[20:23], v[104:107]\n"
"v_mfma_f32_16x16x32_bf16 v[108:111], a[96:99], a[24:27], v[108:111]\n"
"buffer_load_dwordx4 a[92:95], v240, s[36:39], s80 offen offset:3072 sc1\n"
"v_mfma_f32_16x16x32_bf16 v[108:111], a[100:103], a[28:31], v[108:111]\n"
"s_add_u32 s80, s80, 0x1000\n"
"v_mfma_f32_16x16x32_bf16 v[112:115], a[96:99], a[32:35], v[112:115]\n"
"v_mfma_f32_16x16x32_bf16 v[112:115], a[100:103], a[36:39], v[112:115]\n"
"v_mfma_f32_16x16x32_bf16 v[116:119], a[96:99], a[40:43], v[116:119]\n"
"v_mfma_f32_16x16x32_bf16 v[116:119], a[100:103], a[44:47], v[116:119]\n"
"v_mfma_f32_16x16x32_bf16 v[120:123], a[96:99], a[48:51], v[120:123]\n"
"v_mfma_f32_16x16x32_bf16 v[120:123], a[100:103], a[52:55], v[120:123]\n"
"v_mfma_f32_16x16x32_bf16 v[124:127], a[96:99], a[56:59], v[124:127]\n"
"buffer_load_dwordx4 a[96:99], v240, s[36:39], s80 offen offset:0 sc1\n"
"v_mfma_f32_16x16x32_bf16 v[124:127], a[100:103], a[60:63], v[124:127]\n"
"s_waitcnt vmcnt(11)\n"
"v_mfma_f32_16x16x32_bf16 v[128:131], a[104:107], a[0:3], v[128:131]\n"
"v_mfma_f32_16x16x32_bf16 v[128:131], a[108:111], a[4:7], v[128:131]\n"
"v_mfma_f32_16x16x32_bf16 v[132:135], a[104:107], a[8:11], v[132:135]\n"
"v_mfma_f32_16x16x32_bf16 v[132:135], a[108:111], a[12:15], v[132:135]\n"
"buffer_load_dword v241, v240, s[64:67], 0 offen offset:1024 sc1\n"
"v_mfma_f32_16x16x32_bf16 v[136:139], a[104:107], a[16:19], v[136:139]\n"
"v_mfma_f32_16x16x32_bf16 v[136:139], a[108:111], a[20:23], v[136:139]\n"
"v_mfma_f32_16x16x32_bf16 v[140:143], a[104:107], a[24:27], v[140:143]\n"
"buffer_load_dwordx4 a[100:103], v240, s[36:39], s80 offen offset:1024 sc1\n"
"v_mfma_f32_16x16x32_bf16 v[140:143], a[108:111], a[28:31], v[140:143]\n"
"v_mfma_f32_16x16x32_bf16 v[144:147], a[104:107], a[32:35], v[144:147]\n"
"v_mfma_f32_16x16x32_bf16 v[144:147], a[108:111], a[36:39], v[144:147]\n"
"v_mfma_f32_16x16x32_bf16 v[148:151], a[104:107], a[40:43], v[148:151]\n"
"v_mfma_f32_16x16x32_bf16 v[148:151], a[108:111], a[44:47], v[148:151]\n"
"v_mfma_f32_16x16x32_bf16 v[152:155], a[104:107], a[48:51], v[152:155]\n"
"v_mfma_f32_16x16x32_bf16 v[152:155], a[108:111], a[52:55], v[152:155]\n"
"v_mfma_f32_16x16x32_bf16 v[156:159], a[104:107], a[56:59], v[156:159]\n"
"buffer_load_dwordx4 a[104:107], v240, s[36:39], s80 offen offset:2048 sc1\n"
"v_mfma_f32_16x16x32_bf16 v[156:159], a[108:111], a[60:63], v[156:159]\n"
"s_waitcnt vmcnt(12)\n"
"v_mfma_f32_16x16x32_bf16 v[160:163], v[224:227], a[0:3], v[160:163]\n"
"v_mfma_f32_16x16x32_bf16 v[160:163], v[228:231], a[4:7], v[160:163]\n"
"v_mfma_f32_16x16x32_bf16 v[164:167], v[224:227], a[8:11], v[164:167]\n"
"v_mfma_f32_16x16x32_bf16 v[164:167], v[228:231], a[12:15], v[164:167]\n"
"v_mfma_f32_16x16x32_bf16 v[168:171], v[224:227], a[16:19], v[168:171]\n"
"v_mfma_f32_16x16x32_bf16 v[168:171], v[228:231], a[20:23], v[168:171]\n"
"v_mfma_f32_16x16x32_bf16 v[172:175], v[224:227], a[24:27], v[172:175]\n"
"buffer_load_dwordx4 a[108:111], v240, s[36:39], s80 offen offset:3072 sc1\n"
"v_mfma_f32_16x16x32_bf16 v[172:175], v[228:231], a[28:31], v[172:175]\n"
"s_add_u32 s80, s80, 0x1000\n"
"v_mfma_f32_16x16x32_bf16 v[176:179], v[224:227], a[32:35], v[176:179]\n"
"v_mfma_f32_16x16x32_bf16 v[176:179], v[228:231], a[36:39], v[176:179]\n"
"v_mfma_f32_16x16x32_bf16 v[180:183], v[224:227], a[40:43], v[180:183]\n"
"v_mfma_f32_16x16x32_bf16 v[180:183], v[228:231], a[44:47], v[180:183]\n"
"v_mfma_f32_16x16x32_bf16 v[184:187], v[224:227], a[48:51], v[184:187]\n"
"v_mfma_f32_16x16x32_bf16 v[184:187], v[228:231], a[52:55], v[184:187]\n"
"v_mfma_f32_16x16x32_bf16 v[188:191], v[224:227], a[56:59], v[188:191]\n"
"buffer_load_dwordx4 v[224:227], v240, s[36:39], s80 offen offset:0 sc1\n"
"v_mfma_f32_16x16x32_bf16 v[188:191], v[228:231], a[60:63], v[188:191]\n"
"s_waitcnt vmcnt(12)\n"
"v_mfma_f32_16x16x32_bf16 v[192:195], a[64:67], a[0:3], v[192:195]\n"
"v_mfma_f32_16x16x32_bf16 v[192:195], a[68:71], a[4:7], v[192:195]\n"
"v_mfma_f32_16x16x32_bf16 v[196:199], a[64:67], a[8:11], v[196:199]\n"
"v_mfma_f32_16x16x32_bf16 v[196:199], a[68:71], a[12:15], v[196:199]\n"
"v_mfma_f32_16x16x32_bf16 v[200:203], a[64:67], a[16:19], v[200:203]\n"
"v_mfma_f32_16x16x32_bf16 v[200:203], a[68:71], a[20:23], v[200:203]\n"
"v_mfma_f32_16x16x32_bf16 v[204:207], a[64:67], a[24:27], v[204:207]\n"
"buffer_load_dwordx4 v[228:231], v240, s[36:39], s80 offen offset:1024 sc1\n"
"v_mfma_f32_16x16x32_bf16 v[204:207], a[68:71], a[28:31], v[204:207]\n"
"v_mfma_f32_16x16x32_bf16 v[208:211], a[64:67], a[32:35], v[208:211]\n"
"v_mfma_f32_16x16x32_bf16 v[208:211], a[68:71], a[36:39], v[208:211]\n"
"v_mfma_f32_16x16x32_bf16 v[212:215], a[64:67], a[40:43], v[212:215]\n"
"v_mfma_f32_16x16x32_bf16 v[212:215], a[68:71], a[44:47], v[212:215]\n"
"v_mfma_f32_16x16x32_bf16 v[216:219], a[64:67], a[48:51], v[216:219]\n"
"v_mfma_f32_16x16x32_bf16 v[216:219], a[68:71], a[52:55], v[216:219]\n"
"s_barrier\n"
"v_mfma_f32_16x16x32_bf16 v[220:223], a[64:67], a[56:59], v[220:223]\n"
"buffer_load_dwordx4 a[64:67], v240, s[36:39], s80 offen offset:2048 sc1\n"
"v_mfma_f32_16x16x32_bf16 v[220:223], a[68:71], a[60:63], v[220:223]\n"
"s_waitcnt vmcnt(12)\n"
"v_mfma_f32_16x16x32_bf16 a[112:115], a[72:75], a[0:3], a[112:115]\n"
"ds_read_b128 a[0:3], v247 offset:0\n"
"v_mfma_f32_16x16x32_bf16 a[112:115], a[76:79], a[4:7], a[112:115]\n"
"ds_read_b128 a[4:7], v248 offset:0\n"
"v_mfma_f32_16x16x32_bf16 a[116:119], a[72:75], a[8:11], a[116:119]\n"
"ds_read_b128 a[8:11], v247 offset:2048\n"
"v_mfma_f32_16x16x32_bf16 a[116:119], a[76:79], a[12:15], a[116:119]\n"
"ds_read_b128 a[12:15], v248 offset:2048\n"
"v_mfma_f32_16x16x32_bf16 a[120:123], a[72:75], a[16:19], a[120:123]\n"
"ds_read_b128 a[16:19], v247 offset:4096\n"
"v_mfma_f32_16x16x32_bf16 a[120:123], a[76:79], a[20:23], a[120:123]\n"
"ds_read_b128 a[20:23], v248 offset:4096\n"
"v_mfma_f32_16x16x32_bf16 a[124:127], a[72:75], a[24:27], a[124:127]\n"
"ds_read_b128 a[24:27], v247 offset:6144\n"
"buffer_load_dwordx4 a[68:71], v240, s[36:39], s80 offen offset:3072 sc1\n"
"v_mfma_f32_16x16x32_bf16 a[124:127], a[76:79], a[28:31], a[124:127]\n"
"s_add_u32 s80, s80, 0x1000\n"
"ds_read_b128 a[28:31], v248 offset:6144\n"
"v_mfma_f32_16x16x32_bf16 a[128:131], a[72:75], a[32:35], a[128:131]\n"
"ds_read_b128 a[32:35], v247 offset:8192\n"
"v_mfma_f32_16x16x32_bf16 a[128:131], a[76:79], a[36:39], a[128:131]\n"
"ds_read_b128 a[36:39], v248 offset:8192\n"
"v_mfma_f32_16x16x32_bf16 a[132:135], a[72:75], a[40:43], a[132:135]\n"
"ds_read_b128 a[40:43], v247 offset:10240\n"
"v_mfma_f32_16x16x32_bf16 a[132:135], a[76:79], a[44:47], a[132:135]\n"
"ds_read_b128 a[44:47], v248 offset:10240\n"
"v_mfma_f32_16x16x32_bf16 a[136:139], a[72:75], a[48:51], a[136:139]\n"
"ds_read_b128 a[48:51], v247 offset:12288\n"
"v_mfma_f32_16x16x32_bf16 a[136:139], a[76:79], a[52:55], a[136:139]\n"
"ds_read_b128 a[52:55], v248 offset:12288\n"
"v_mfma_f32_16x16x32_bf16 a[140:143], a[72:75], a[56:59], a[140:143]\n"
"ds_read_b128 a[56:59], v247 offset:14336\n"
"buffer_load_dwordx4 a[72:75], v240, s[36:39], s80 offen offset:0 sc1\n"
"v_mfma_f32_16x16x32_bf16 a[140:143], a[76:79], a[60:63], a[140:143]\n"
"s_waitcnt vmcnt(12) lgkmcnt(11)\n"
"ds_read_b128 a[60:63], v248 offset:14336\n"
"v_mfma_f32_16x16x32_bf16 v[0:3], a[80:83], a[0:3], v[0:3]\n"
"v_mfma_f32_16x16x32_bf16 v[0:3], a[84:87], a[4:7], v[0:3]\n"
"s_waitcnt vmcnt(8)\n"
"v_cmp_ne_u32 vcc, s33, v241\n"
"v_mfma_f32_16x16x32_bf16 v[4:7], a[80:83], a[8:11], v[4:7]\n"
"v_mfma_f32_16x16x32_bf16 v[4:7], a[84:87], a[12:15], v[4:7]\n"
"s_waitcnt lgkmcnt(8)\n"
"v_mfma_f32_16x16x32_bf16 v[8:11], a[80:83], a[16:19], v[8:11]\n"
"s_cmp_eq_u64 vcc, 0\n"
"s_cbranch_scc0 .Lslow1_%=\n"
".Lback1_%=:\n"
"v_mfma_f32_16x16x32_bf16 v[8:11], a[84:87], a[20:23], v[8:11]\n"
"v_mfma_f32_16x16x32_bf16 v[12:15], a[80:83], a[24:27], v[12:15]\n"
"buffer_load_dwordx4 a[76:79], v240, s[36:39], s80 offen offset:1024 sc1\n"
"v_mfma_f32_16x16x32_bf16 v[12:15], a[84:87], a[28:31], v[12:15]\n"
"s_waitcnt lgkmcnt(4)\n"
"v_mfma_f32_16x16x32_bf16 v[16:19], a[80:83], a[32:35], v[16:19]\n"
"v_mfma_f32_16x16x32_bf16 v[16:19], a[84:87], a[36:39], v[16:19]\n"
"s_waitcnt lgkmcnt(0)\n"
"s_barrier\n"
"v_mfma_f32_16x16x32_bf16 v[20:23], a[80:83], a[40:43], v[20:23]\n"
"s_add_u32 m0, s83, 129024\n"
"v_mfma_f32_16x16x32_bf16 v[20:23], a[84:87], a[44:47], v[20:23]\n"
"buffer_load_dwordx4 v240, s[56:59], s68 offen offset:2048 sc1 lds\n"
"v_mfma_f32_16x16x32_bf16 v[24:27], a[80:83], a[48:51], v[24:27]\n"
"v_mfma_f32_16x16x32_bf16 v[24:27], a[84:87], a[52:55], v[24:27]\n"
"v_mfma_f32_16x16x32_bf16 v[28:31], a[80:83], a[56:59], v[28:31]\n"
"buffer_load_dwordx4 a[80:83], v240, s[36:39], s80 offen offset:2048 sc1\n"
"v_mfma_f32_16x16x32_bf16 v[28:31], a[84:87], a[60:63], v[28:31]\n"
"v_mfma_f32_16x16x32_bf16 v[32:35], a[88:91], a[0:3], v[32:35]\n"
"s_add_u32 m0, s83, 131072\n"
"v_mfma_f32_16x16x32_bf16 v[32:35], a[92:95], a[4:7], v[32:35]\n"
"buffer_load_dwordx4 v240, s[56:59], s69 offen offset:2048 sc1 lds\n"
"v_mfma_f32_16x16x32_bf16 v[36:39], a[88:91], a[8:11], v[36:39]\n"
"v_mfma_f32_16x16x32_bf16 v[36:39], a[92:95], a[12:15], v[36:39]\n"
"v_mfma_f32_16x16x32_bf16 v[40:43], a[88:91], a[16:19], v[40:43]\n"
"v_mfma_f32_16x16x32_bf16 v[40:43], a[92:95], a[20:23], v[40:43]\n"
"v_mfma_f32_16x16x32_bf16 v[44:47], a[88:91], a[24:27], v[44:47]\n"
"s_add_u32 m0, s83, 133120\n"
"buffer_load_dwordx4 a[84:87], v240, s[36:39], s80 offen offset:3072 sc1\n"
"v_mfma_f32_16x16x32_bf16 v[44:47], a[92:95], a[28:31], v[44:47]\n"
"s_add_u32 s80, s80, 0x1000\n"
"buffer_load_dwordx4 v240, s[56:59], s70 offen offset:2048 sc1 lds\n"
"v_mfma_f32_16x16x32_bf16 v[48:51], a[88:91], a[32:35], v[48:51]\n"
"v_mfma_f32_16x16x32_bf16 v[48:51], a[92:95], a[36:39], v[48:51]\n"
"v_mfma_f32_16x16x32_bf16 v[52:55], a[88:91], a[40:43], v[52:55]\n"
"v_mfma_f32_16x16x32_bf16 v[52:55], a[92:95], a[44:47], v[52:55]\n"
"v_mfma_f32_16x16x32_bf16 v[56:59], a[88:91], a[48:51], v[56:59]\n"
"s_add_u32 m0, s83, 135168\n"
"v_mfma_f32_16x16x32_bf16 v[56:59], a[92:95], a[52:55], v[56:59]\n"
"buffer_load_dwordx4 v240, s[56:59], s71 offen offset:2048 sc1 lds\n"
"v_mfma_f32_16x16x32_bf16 v[60:63], a[88:91], a[56:59], v[60:63]\n"
"buffer_load_dwordx4 a[88:91], v240, s[36:39], s80 offen offset:0 sc1\n"
"v_mfma_f32_16x16x32_bf16 v[60:63], a[92:95], a[60:63], v[60:63]\n"
"s_waitcnt vmcnt(15)\n"
"v_mfma_f32_16x16x32_bf16 v[64:67], a[96:99], a[0:3], v[64:67]\n"
"v_mfma_f32_16x16x32_bf16 v[64:67], a[100:103], a[4:7], v[64:67]\n"
"v_mfma_f32_16x16x32_bf16 v[68:71], a[96:99], a[8:11], v[68:71]\n"
"v_mfma_f32_16x16x32_bf16 v[68:71], a[100:103], a[12:15], v[68:71]\n"
"v_mfma_f32_16x16x32_bf16 v[72:75], a[96:99], a[16:19], v[72:75]\n"
"v_mfma_f32_16x16x32_bf16 v[72:75], a[100:103], a[20:23], v[72:75]\n"
"v_mfma_f32_16x16x32_bf16 v[76:79], a[96:99], a[24:27], v[76:79]\n"
"buffer_load_dwordx4 a[92:95], v240, s[36:39], s80 offen offset:1024 sc1\n"
"v_mfma_f32_16x16x32_bf16 v[76:79], a[100:103], a[28:31], v[76:79]\n"
"v_mfma_f32_16x16x32_bf16 v[80:83], a[96:99], a[32:35], v[80:83]\n"
"v_mfma_f32_16x16x32_bf16 v[80:83], a[100:103], a[36:39], v[80:83]\n"
"v_mfma_f32_16x16x32_bf16 v[84:87], a[96:99], a[40:43], v[84:87]\n"
"v_mfma_f32_16x16x32_bf16 v[84:87], a[100:103], a[44:47], v[84:87]\n"
"v_mfma_f32_16x16x32_bf16 v[88:91], a[96:99], a[48:51], v[88:91]\n"
"v_mfma_f32_16x16x32_bf16 v[88:91], a[100:103], a[52:55], v[88:91]\n"
"v_mfma_f32_16x16x32_bf16 v[92:95], a[96:99], a[56:59], v[92:95]\n"
"buffer_load_dwordx4 a[96:99], v240, s[36:39], s80 offen offset:2048 sc1\n"
"v_mfma_f32_16x16x32_bf16 v[92:95], a[100:103], a[60:63], v[92:95]\n"
"s_waitcnt vmcnt(15)\n"
"v_mfma_f32_16x16x32_bf16 v[96:99], a[104:107], a[0:3], v[96:99]\n"
"v_mfma_f32_16x16x32_bf16 v[96:99], a[108:111], a[4:7], v[96:99]\n"
"v_mfma_f32_16x16x32_bf16 v[100:103], a[104:107], a[8:11], v[100:103]\n"
"v_mfma_f32_16x16x32_bf16 v[100:103], a[108:111], a[12:15], v[100:103]\n"
"v_mfma_f32_16x16x32_bf16 v[104:107], a[104:107], a[16:19], v[104:107]\n"
"v_mfma_f32_16x16x32_bf16 v[104:107], a[108:111], a[20:23], v[104:107]\n"
"v_mfma_f32_16x16x32_bf16 v[108:111], a[104:107], a[24:27], v[108:111]\n"
"buffer_load_dwordx4 a[100:103], v240, s[36:39], s80 offen offset:3072 sc1\n"
"v_mfma_f32_16x16x32_bf16 v[108:111], a[108:111], a[28:31], v[108:111]\n"
"s_add_u32 s80, s80, 0x1000\n"
"v_mfma_f32_16x16x32_bf16 v[112:115], a[104:107], a[32:35], v[112:115]\n"
"v_mfma_f32_16x16x32_bf16 v[112:115], a[108:111], a[36:39], v[112:115]\n"
"v_mfma_f32_16x16x32_bf16 v[116:119], a[104:107], a[40:43], v[116:119]\n"
"v_mfma_f32_16x16x32_bf16 v[116:119], a[108:111], a[44:47], v[116:119]\n"
"v_mfma_f32_16x16x32_bf16 v[120:123], a[104:107], a[48:51], v[120:123]\n"
"v_mfma_f32_16x16x32_bf16 v[120:123], a[108:111], a[52:55], v[120:123]\n"
"v_mfma_f32_16x16x32_bf16 v[124:127], a[104:107], a[56:59], v[124:127]\n"
"buffer_load_dwordx4 a[104:107], v240, s[36:39], s80 offen offset:0 sc1\n"
"v_mfma_f32_16x16x32_bf16 v[124:127], a[108:111], a[60:63], v[124:127]\n"
"s_waitcnt vmcnt(15)\n"
"v_mfma_f32_16x16x32_bf16 v[128:131], v[224:227], a[0:3], v[128:131]\n"
"v_mfma_f32_16x16x32_bf16 v[128:131], v[228:231], a[4:7], v[128:131]\n"
"v_mfma_f32_16x16x32_bf16 v[132:135], v[224:227], a[8:11], v[132:135]\n"
"v_mfma_f32_16x16x32_bf16 v[132:135], v[228:231], a[12:15], v[132:135]\n"
"v_mfma_f32_16x16x32_bf16 v[136:139], v[224:227], a[16:19], v[136:139]\n"
"v_mfma_f32_16x16x32_bf16 v[136:139], v[228:231], a[20:23], v[136:139]\n"
"v_mfma_f32_16x16x32_bf16 v[140:143], v[224:227], a[24:27], v[140:143]\n"
"buffer_load_dwordx4 a[108:111], v240, s[36:39], s80 offen offset:1024 sc1\n"
"v_mfma_f32_16x16x32_bf16 v[140:143], v[228:231], a[28:31], v[140:143]\n"
"v_mfma_f32_16x16x32_bf16 v[144:147], v[224:227], a[32:35], v[144:147]\n"
"v_mfma_f32_16x16x32_bf16 v[144:147], v[228:231], a[36:39], v[144:147]\n"
"v_mfma_f32_16x16x32_bf16 v[148:151], v[224:227], a[40:43], v[148:151]\n"
"v_mfma_f32_16x16x32_bf16 v[148:151], v[228:231], a[44:47], v[148:151]\n"
"v_mfma_f32_16x16x32_bf16 v[152:155], v[224:227], a[48:51], v[152:155]\n"
"v_mfma_f32_16x16x32_bf16 v[152:155], v[228:231], a[52:55], v[152:155]\n"
"v_mfma_f32_16x16x32_bf16 v[156:159], v[224:227], a[56:59], v[156:159]\n"
"buffer_load_dwordx4 v[224:227], v240, s[36:39], s80 offen offset:2048 sc1\n"
"v_mfma_f32_16x16x32_bf16 v[156:159], v[228:231], a[60:63], v[156:159]\n"
"s_waitcnt vmcnt(15)\n"
"v_mfma_f32_16x16x32_bf16 v[160:163], a[64:67], a[0:3], v[160:163]\n"
"v_mfma_f32_16x16x32_bf16 v[160:163], a[68:71], a[4:7], v[160:163]\n"
"v_mfma_f32_16x16x32_bf16 v[164:167], a[64:67], a[8:11], v[164:167]\n"
"v_mfma_f32_16x16x32_bf16 v[164:167], a[68:71], a[12:15], v[164:167]\n"
"v_mfma_f32_16x16x32_bf16 v[168:171], a[64:67], a[16:19], v[168:171]\n"
"v_mfma_f32_16x16x32_bf16 v[168:171], a[68:71], a[20:23], v[168:171]\n"
"v_mfma_f32_16x16x32_bf16 v[172:175], a[64:67], a[24:27], v[172:175]\n"
"buffer_load_dwordx4 v[228:231], v240, s[36:39], s80 offen offset:3072 sc1\n"
"v_mfma_f32_16x16x32_bf16 v[172:175], a[68:71], a[28:31], v[172:175]\n"
"s_add_u32 s80, s80, 0x1000\n"
"v_mfma_f32_16x16x32_bf16 v[176:179], a[64:67], a[32:35], v[176:179]\n"
"v_mfma_f32_16x16x32_bf16 v[176:179], a[68:71], a[36:39], v[176:179]\n"
"v_mfma_f32_16x16x32_bf16 v[180:183], a[64:67], a[40:43], v[180:183]\n"
"v_mfma_f32_16x16x32_bf16 v[180:183], a[68:71], a[44:47], v[180:183]\n"
"v_mfma_f32_16x16x32_bf16 v[184:187], a[64:67], a[48:51], v[184:187]\n"
"v_mfma_f32_16x16x32_bf16 v[184:187], a[68:71], a[52:55], v[184:187]\n"
"v_mfma_f32_16x16x32_bf16 v[188:191], a[64:67], a[56:59], v[188:191]\n"
"buffer_load_dwordx4 a[64:67], v240, s[36:39], s80 offen offset:0 sc1\n"
"v_mfma_f32_16x16x32_bf16 v[188:191], a[68:71], a[60:63], v[188:191]\n"
"s_waitcnt vmcnt(15)\n"
"v_mfma_f32_16x16x32_bf16 v[192:195], a[72:75], a[0:3], v[192:195]\n"
"v_mfma_f32_16x16x32_bf16 v[192:195], a[76:79], a[4:7], v[192:195]\n"
"v_mfma_f32_16x16x32_bf16 v[196:199], a[72:75], a[8:11], v[196:199]\n"
"v_mfma_f32_16x16x32_bf16 v[196:199], a[76:79], a[12:15], v[196:199]\n"
"v_mfma_f32_16x16x32_bf16 v[200:203], a[72:75], a[16:19], v[200:203]\n"
"v_mfma_f32_16x16x32_bf16 v[200:203], a[76:79], a[20:23], v[200:203]\n"
"v_mfma_f32_16x16x32_bf16 v[204:207], a[72:75], a[24:27], v[204:207]\n"
"buffer_load_dwordx4 a[68:71], v240, s[36:39], s80 offen offset:1024 sc1\n"
"v_mfma_f32_16x16x32_bf16 v[204:207], a[76:79], a[28:31], v[204:207]\n"
"v_mfma_f32_16x16x32_bf16 v[208:211], a[72:75], a[32:35], v[208:211]\n"
"v_mfma_f32_16x16x32_bf16 v[208:211], a[76:79], a[36:39], v[208:211]\n"
"v_mfma_f32_16x16x32_bf16 v[212:215], a[72:75], a[40:43], v[212:215]\n"
"v_mfma_f32_16x16x32_bf16 v[212:215], a[76:79], a[44:47], v[212:215]\n"
"v_mfma_f32_16x16x32_bf16 v[216:219], a[72:75], a[48:51], v[216:219]\n"
"v_mfma_f32_16x16x32_bf16 v[216:219], a[76:79], a[52:55], v[216:219]\n"
"s_waitcnt vmcnt(10)\n"
"s_barrier\n"
"v_mfma_f32_16x16x32_bf16 v[220:223], a[72:75], a[56:59], v[220:223]\n"
"buffer_load_dwordx4 a[72:75], v240, s[36:39], s80 offen offset:2048 sc1\n"
"v_mfma_f32_16x16x32_bf16 v[220:223], a[76:79], a[60:63], v[220:223]\n"
"v_mfma_f32_16x16x32_bf16 a[112:115], a[80:83], a[0:3], a[112:115]\n"
"ds_read_b128 a[0:3], v247 offset:0\n"
"v_mfma_f32_16x16x32_bf16 a[112:115], a[84:87], a[4:7], a[112:115]\n"
"ds_read_b128 a[4:7], v248 offset:0\n"
"v_mfma_f32_16x16x32_bf16 a[116:119], a[80:83], a[8:11], a[116:119]\n"
"ds_read_b128 a[8:11], v247 offset:2048\n"
"v_mfma_f32_16x16x32_bf16 a[116:119], a[84:87], a[12:15], a[116:119]\n"
"ds_read_b128 a[12:15], v248 offset:2048\n"
"v_mfma_f32_16x16x32_bf16 a[120:123], a[80:83], a[16:19], a[120:123]\n"
"ds_read_b128 a[16:19], v247 offset:4096\n"
"v_mfma_f32_16x16x32_bf16 a[120:123], a[84:87], a[20:23], a[120:123]\n"
"ds_read_b128 a[20:23], v248 offset:4096\n"
"v_mfma_f32_16x16x32_bf16 a[124:127], a[80:83], a[24:27], a[124:127]\n"
"ds_read_b128 a[24:27], v247 offset:6144\n"
"buffer_load_dwordx4 a[76:79], v240, s[36:39], s80 offen offset:3072 sc1\n"
"v_mfma_f32_16x16x32_bf16 a[124:127], a[84:87], a[28:31], a[124:127]\n"
"s_add_u32 s80, s80, 0x1000\n"
"ds_read_b128 a[28:31], v248 offset:6144\n"
"v_mfma_f32_16x16x32_bf16 a[128:131], a[80:83], a[32:35], a[128:131]\n"
"ds_read_b128 a[32:35], v247 offset:8192\n"
"v_mfma_f32_16x16x32_bf16 a[128:131], a[84:87], a[36:39], a[128:131]\n"
"ds_read_b128 a[36:39], v248 offset:8192\n"
"v_mfma_f32_16x16x32_bf16 a[132:135], a[80:83], a[40:43], a[132:135]\n"
"ds_read_b128 a[40:43], v247 offset:10240\n"
"v_mfma_f32_16x16x32_bf16 a[132:135], a[84:87], a[44:47], a[132:135]\n"
"ds_read_b128 a[44:47], v248 offset:10240\n"
"v_mfma_f32_16x16x32_bf16 a[136:139], a[80:83], a[48:51], a[136:139]\n"
"ds_read_b128 a[48:51], v247 offset:12288\n"
"v_mfma_f32_16x16x32_bf16 a[136:139], a[84:87], a[52:55], a[136:139]\n"
"ds_read_b128 a[52:55], v248 offset:12288\n"
"v_mfma_f32_16x16x32_bf16 a[140:143], a[80:83], a[56:59], a[140:143]\n"
"ds_read_b128 a[56:59], v247 offset:14336\n"
"buffer_load_dwordx4 a[80:83], v240, s[36:39], s80 offen offset:0 sc1\n"
"v_mfma_f32_16x16x32_bf16 a[140:143], a[84:87], a[60:63], a[140:143]\n"
"s_waitcnt vmcnt(11) lgkmcnt(11)\n"
"ds_read_b128 a[60:63], v248 offset:14336\n"
"v_mfma_f32_16x16x32_bf16 v[0:3], a[88:91], a[0:3], v[0:3]\n"
"v_mfma_f32_16x16x32_bf16 v[0:3], a[92:95], a[4:7], v[0:3]\n"
"v_mfma_f32_16x16x32_bf16 v[4:7], a[88:91], a[8:11], v[4:7]\n"
"v_mfma_f32_16x16x32_bf16 v[4:7], a[92:95], a[12:15], v[4:7]\n"
"s_waitcnt lgkmcnt(8)\n"
"v_mfma_f32_16x16x32_bf16 v[8:11], a[88:91], a[16:19], v[8:11]\n"
"v_mfma_f32_16x16x32_bf16 v[8:11], a[92:95], a[20:23], v[8:11]\n"
"v_mfma_f32_16x16x32_bf16 v[12:15], a[88:91], a[24:27], v[12:15]\n"
"buffer_load_dwordx4 a[84:87], v240, s[36:39], s80 offen offset:1024 sc1\n"
"v_mfma_f32_16x16x32_bf16 v[12:15], a[92:95], a[28:31], v[12:15]\n"
"s_waitcnt lgkmcnt(4)\n"
"v_mfma_f32_16x16x32_bf16 v[16:19], a[88:91], a[32:35], v[16:19]\n"
"v_mfma_f32_16x16x32_bf16 v[16:19], a[92:95], a[36:39], v[16:19]\n"
"s_waitcnt lgkmcnt(0)\n"
"s_barrier\n"
"v_mfma_f32_16x16x32_bf16 v[20:23], a[88:91], a[40:43], v[20:23]\n"
"s_add_u32 m0, s83, 129024\n"
"v_mfma_f32_16x16x32_bf16 v[20:23], a[92:95], a[44:47], v[20:23]\n"
"buffer_load_dwordx4 v240, s[56:59], s72 offen offset:2048 sc1 lds\n"
"v_mfma_f32_16x16x32_bf16 v[24:27], a[88:91], a[48:51], v[24:27]\n"
"v_mfma_f32_16x16x32_bf16 v[24:27], a[92:95], a[52:55], v[24:27]\n"
"v_mfma_f32_16x16x32_bf16 v[28:31], a[88:91], a[56:59], v[28:31]\n"
"buffer_load_dwordx4 a[88:91], v240, s[36:39], s80 offen offset:2048 sc1\n"
"v_mfma_f32_16x16x32_bf16 v[28:31], a[92:95], a[60:63], v[28:31]\n"
"s_waitcnt vmcnt(12)\n"
"v_mfma_f32_16x16x32_bf16 v[32:35], a[96:99], a[0:3], v[32:35]\n"
"s_add_u32 m0, s83, 131072\n"
"v_mfma_f32_16x16x32_bf16 v[32:35], a[100:103], a[4:7], v[32:35]\n"
"buffer_load_dwordx4 v240, s[56:59], s73 offen offset:2048 sc1 lds\n"
"v_mfma_f32_16x16x32_bf16 v[36:39], a[96:99], a[8:11], v[36:39]\n"
"v_mfma_f32_16x16x32_bf16 v[36:39], a[100:103], a[12:15], v[36:39]\n"
"v_mfma_f32_16x16x32_bf16 v[40:43], a[96:99], a[16:19], v[40:43]\n"
"v_mfma_f32_16x16x32_bf16 v[40:43], a[100:103], a[20:23], v[40:43]\n"
"v_mfma_f32_16x16x32_bf16 v[44:47], a[96:99], a[24:27], v[44:47]\n"
"s_add_u32 m0, s83, 133120\n"
"buffer_load_dwordx4 a[92:95], v240, s[36:39], s80 offen offset:3072 sc1\n"
"v_mfma_f32_16x16x32_bf16 v[44:47], a[100:103], a[28:31], v[44:47]\n"
"s_add_u32 s80, s80, 0x1000\n"
"buffer_load_dwordx4 v240, s[56:59], s74 offen offset:2048 sc1 lds\n"
"v_mfma_f32_16x16x32_bf16 v[48:51], a[96:99], a[32:35], v[48:51]\n"
"v_mfma_f32_16x16x32_bf16 v[48:51], a[100:103], a[36:39], v[48:51]\n"
"v_mfma_f32_16x16x32_bf16 v[52:55], a[96:99], a[40:43], v[52:55]\n"
"v_mfma_f32_16x16x32_bf16 v[52:55], a[100:103], a[44:47], v[52:55]\n"
"v_mfma_f32_16x16x32_bf16 v[56:59], a[96:99], a[48:51], v[56:59]\n"
"s_add_u32 m0, s83, 135168\n"
"v_mfma_f32_16x16x32_bf16 v[56:59], a[100:103], a[52:55], v[56:59]\n"
"buffer_load_dwordx4 v240, s[56:59], s75 offen offset:2048 sc1 lds\n"
"v_mfma_f32_16x16x32_bf16 v[60:63], a[96:99], a[56:59], v[60:63]\n"
"buffer_load_dwordx4 a[96:99], v240, s[36:39], s80 offen offset:0 sc1\n"
"v_mfma_f32_16x16x32_bf16 v[60:63], a[100:103], a[60:63], v[60:63]\n"
"s_waitcnt vmcnt(15)\n"
"v_mfma_f32_16x16x32_bf16 v[64:67], a[104:107], a[0:3], v[64:67]\n"
"v_mfma_f32_16x16x32_bf16 v[64:67], a[108:111], a[4:7], v[64:67]\n"
"v_mfma_f32_16x16x32_bf16 v[68:71], a[104:107], a[8:11], v[68:71]\n"
"v_mfma_f32_16x16x32_bf16 v[68:71], a[108:111], a[12:15], v[68:71]\n"
"v_mfma_f32_16x16x32_bf16 v[72:75], a[104:107], a[16:19], v[72:75]\n"
"v_mfma_f32_16x16x32_bf16 v[72:75], a[108:111], a[20:23], v[72:75]\n"
"v_mfma_f32_16x16x32_bf16 v[76:79], a[104:107], a[24:27], v[76:79]\n"
"buffer_load_dwordx4 a[100:103], v240, s[36:39], s80 offen offset:1024 sc1\n"
"v_mfma_f32_16x16x32_bf16 v[76:79], a[108:111], a[28:31], v[76:79]\n"
"v_mfma_f32_16x16x32_bf16 v[80:83], a[104:107], a[32:35], v[80:83]\n"
"v_mfma_f32_16x16x32_bf16 v[80:83], a[108:111], a[36:39], v[80:83]\n"
"v_mfma_f32_16x16x32_bf16 v[84:87], a[104:107], a[40:43], v[84:87]\n"
"v_mfma_f32_16x16x32_bf16 v[84:87], a[108:111], a[44:47], v[84:87]\n"
"v_mfma_f32_16x16x32_bf16 v[88:91], a[104:107], a[48:51], v[88:91]\n"
"v_mfma_f32_16x16x32_bf16 v[88:91], a[108:111], a[52:55], v[88:91]\n"
"v_mfma_f32_16x16x32_bf16 v[92:95], a[104:107], a[56:59], v[92:95]\n"
"buffer_load_dwordx4 a[104:107], v240, s[36:39], s80 offen offset:2048 sc1\n"
"v_mfma_f32_16x16x32_bf16 v[92:95], a[108:111], a[60:63], v[92:95]\n"
"s_waitcnt vmcnt(15)\n"
"v_mfma_f32_16x16x32_bf16 v[96:99], v[224:227], a[0:3], v[96:99]\n"
"v_mfma_f32_16x16x32_bf16 v[96:99], v[228:231], a[4:7], v[96:99]\n"
"v_mfma_f32_16x16x32_bf16 v[100:103], v[224:227], a[8:11], v[100:103]\n"
"v_mfma_f32_16x16x32_bf16 v[100:103], v[228:231], a[12:15], v[100:103]\n"
"v_mfma_f32_16x16x32_bf16 v[104:107], v[224:227], a[16:19], v[104:107]\n"
"v_mfma_f32_16x16x32_bf16 v[104:107], v[228:231], a[20:23], v[104:107]\n"
"v_mfma_f32_16x16x32_bf16 v[108:111], v[224:227], a[24:27], v[108:111]\n"
"buffer_load_dwordx4 a[108:111], v240, s[36:39], s80 offen offset:3072 sc1\n"
"v_mfma_f32_16x16x32_bf16 v[108:111], v[228:231], a[28:31], v[108:111]\n"
"s_add_u32 s80, s80, 0x1000\n"
"v_mfma_f32_16x16x32_bf16 v[112:115], v[224:227], a[32:35], v[112:115]\n"
"v_mfma_f32_16x16x32_bf16 v[112:115], v[228:231], a[36:39], v[112:115]\n"
"v_mfma_f32_16x16x32_bf16 v[116:119], v[224:227], a[40:43], v[116:119]\n"
"v_mfma_f32_16x16x32_bf16 v[116:119], v[228:231], a[44:47], v[116:119]\n"
"v_mfma_f32_16x16x32_bf16 v[120:123], v[224:227], a[48:51], v[120:123]\n"
"v_mfma_f32_16x16x32_bf16 v[120:123], v[228:231], a[52:55], v[120:123]\n"
"v_mfma_f32_16x16x32_bf16 v[124:127], v[224:227], a[56:59], v[124:127]\n"
"buffer_load_dwordx4 v[224:227], v240, s[36:39], s80 offen offset:0 sc1\n"
"v_mfma_f32_16x16x32_bf16 v[124:127], v[228:231], a[60:63], v[124:127]\n"
"s_waitcnt vmcnt(15)\n"
"v_mfma_f32_16x16x32_bf16 v[128:131], a[64:67], a[0:3], v[128:131]\n"
"v_mfma_f32_16x16x32_bf16 v[128:131], a[68:71], a[4:7], v[128:131]\n"
"v_mfma_f32_16x16x32_bf16 v[132:135], a[64:67], a[8:11], v[132:135]\n"
"v_mfma_f32_16x16x32_bf16 v[132:135], a[68:71], a[12:15], v[132:135]\n"
"v_mfma_f32_16x16x32_bf16 v[136:139], a[64:67], a[16:19], v[136:139]\n"
"v_mfma_f32_16x16x32_bf16 v[136:139], a[68:71], a[20:23], v[136:139]\n"
"v_mfma_f32_16x16x32_bf16 v[140:143], a[64:67], a[24:27], v[140:143]\n"
"buffer_load_dwordx4 v[228:231], v240, s[36:39], s80 offen offset:1024 sc1\n"
"v_mfma_f32_16x16x32_bf16 v[140:143], a[68:71], a[28:31], v[140:143]\n"
"v_mfma_f32_16x16x32_bf16 v[144:147], a[64:67], a[32:35], v[144:147]\n"
"v_mfma_f32_16x16x32_bf16 v[144:147], a[68:71], a[36:39], v[144:147]\n"
"v_mfma_f32_16x16x32_bf16 v[148:151], a[64:67], a[40:43], v[148:151]\n"
"v_mfma_f32_16x16x32_bf16 v[148:151], a[68:71], a[44:47], v[148:151]\n"
"v_mfma_f32_16x16x32_bf16 v[152:155], a[64:67], a[48:51], v[152:155]\n"
"v_mfma_f32_16x16x32_bf16 v[152:155], a[68:71], a[52:55], v[152:155]\n"
"v_mfma_f32_16x16x32_bf16 v[156:159], a[64:67], a[56:59], v[156:159]\n"
"buffer_load_dwordx4 a[64:67], v240, s[36:39], s80 offen offset:2048 sc1\n"
"v_mfma_f32_16x16x32_bf16 v[156:159], a[68:71], a[60:63], v[156:159]\n"
"s_waitcnt vmcnt(15)\n"
"v_mfma_f32_16x16x32_bf16 v[160:163], a[72:75], a[0:3], v[160:163]\n"
"v_mfma_f32_16x16x32_bf16 v[160:163], a[76:79], a[4:7], v[160:163]\n"
"v_mfma_f32_16x16x32_bf16 v[164:167], a[72:75], a[8:11], v[164:167]\n"
"v_mfma_f32_16x16x32_bf16 v[164:167], a[76:79], a[12:15], v[164:167]\n"
"v_mfma_f32_16x16x32_bf16 v[168:171], a[72:75], a[16:19], v[168:171]\n"
"v_mfma_f32_16x16x32_bf16 v[168:171], a[76:79], a[20:23], v[168:171]\n"
"v_mfma_f32_16x16x32_bf16 v[172:175], a[72:75], a[24:27], v[172:175]\n"
"buffer_load_dwordx4 a[68:71], v240, s[36:39], s80 offen offset:3072 sc1\n"
"v_mfma_f32_16x16x32_bf16 v[172:175], a[76:79], a[28:31], v[172:175]\n"
"s_add_u32 s80, s80, 0x1000\n"
"v_mfma_f32_16x16x32_bf16 v[176:179], a[72:75], a[32:35], v[176:179]\n"
"v_mfma_f32_16x16x32_bf16 v[176:179], a[76:79], a[36:39], v[176:179]\n"
"v_mfma_f32_16x16x32_bf16 v[180:183], a[72:75], a[40:43], v[180:183]\n"
"v_mfma_f32_16x16x32_bf16 v[180:183], a[76:79], a[44:47], v[180:183]\n"
"v_mfma_f32_16x16x32_bf16 v[184:187], a[72:75], a[48:51], v[184:187]\n"
"v_mfma_f32_16x16x32_bf16 v[184:187], a[76:79], a[52:55], v[184:187]\n"
"v_mfma_f32_16x16x32_bf16 v[188:191], a[72:75], a[56:59], v[188:191]\n"
"buffer_load_dwordx4 a[72:75], v240, s[36:39], s80 offen offset:0 sc1\n"
"v_mfma_f32_16x16x32_bf16 v[188:191], a[76:79], a[60:63], v[188:191]\n"
"s_waitcnt vmcnt(15)\n"
"v_mfma_f32_16x16x32_bf16 v[192:195], a[80:83], a[0:3], v[192:195]\n"
"v_mfma_f32_16x16x32_bf16 v[192:195], a[84:87], a[4:7], v[192:195]\n"
"v_mfma_f32_16x16x32_bf16 v[196:199], a[80:83], a[8:11], v[196:199]\n"
"v_mfma_f32_16x16x32_bf16 v[196:199], a[84:87], a[12:15], v[196:199]\n"
"v_mfma_f32_16x16x32_bf16 v[200:203], a[80:83], a[16:19], v[200:203]\n"
"v_mfma_f32_16x16x32_bf16 v[200:203], a[84:87], a[20:23], v[200:203]\n"
"v_mfma_f32_16x16x32_bf16 v[204:207], a[80:83], a[24:27], v[204:207]\n"
"buffer_load_dwordx4 a[76:79], v240, s[36:39], s80 offen offset:1024 sc1\n"
"v_mfma_f32_16x16x32_bf16 v[204:207], a[84:87], a[28:31], v[204:207]\n"
"v_mfma_f32_16x16x32_bf16 v[208:211], a[80:83], a[32:35], v[208:211]\n"
"v_mfma_f32_16x16x32_bf16 v[208:211], a[84:87], a[36:39], v[208:211]\n"
"v_mfma_f32_16x16x32_bf16 v[212:215], a[80:83], a[40:43], v[212:215]\n"
"v_mfma_f32_16x16x32_bf16 v[212:215], a[84:87], a[44:47], v[212:215]\n"
"v_mfma_f32_16x16x32_bf16 v[216:219], a[80:83], a[48:51], v[216:219]\n"
"v_mfma_f32_16x16x32_bf16 v[216:219], a[84:87], a[52:55], v[216:219]\n"
"v_mfma_f32_16x16x32_bf16 v[220:223], a[80:83], a[56:59], v[220:223]\n"
"buffer_load_dwordx4 a[80:83], v240, s[36:39], s80 offen offset:2048 sc1\n"
"v_mfma_f32_16x16x32_bf16 v[220:223], a[84:87], a[60:63], v[220:223]\n"
"s_waitcnt vmcnt(13)\n"
"v_mfma_f32_16x16x32_bf16 a[112:115], a[88:91], a[0:3], a[112:115]\n"
"ds_read_b128 a[0:3], v245 offset:49152\n"
"v_mfma_f32_16x16x32_bf16 a[112:115], a[92:95], a[4:7], a[112:115]\n"
"ds_read_b128 a[4:7], v246 offset:49152\n"
"v_mfma_f32_16x16x32_bf16 a[116:119], a[88:91], a[8:11], a[116:119]\n"
"ds_read_b128 a[8:11], v245 offset:51200\n"
"v_mfma_f32_16x16x32_bf16 a[116:119], a[92:95], a[12:15], a[116:119]\n"
"ds_read_b128 a[12:15], v246 offset:51200\n"
"v_mfma_f32_16x16x32_bf16 a[120:123], a[88:91], a[16:19], a[120:123]\n"
"ds_read_b128 a[16:19], v245 offset:53248\n"
"v_mfma_f32_16x16x32_bf16 a[120:123], a[92:95], a[20:23], a[120:123]\n"
"ds_read_b128 a[20:23], v246 offset:53248\n"
"v_mfma_f32_16x16x32_bf16 a[124:127], a[88:91], a[24:27], a[124:127]\n"
"ds_read_b128 a[24:27], v245 offset:55296\n"
"buffer_load_dwordx4 a[84:87], v240, s[36:39], s80 offen offset:3072 sc1\n"
"v_mfma_f32_16x16x32_bf16 a[124:127], a[92:95], a[28:31], a[124:127]\n"
"s_add_u32 s80, s80, 0x1000\n"
"ds_read_b128 a[28:31], v246 offset:55296\n"
"v_mfma_f32_16x16x32_bf16 a[128:131], a[88:91], a[32:35], a[128:131]\n"
"ds_read_b128 a[32:35], v245 offset:57344\n"
"v_mfma_f32_16x16x32_bf16 a[128:131], a[92:95], a[36:39], a[128:131]\n"
"ds_read_b128 a[36:39], v246 offset:57344\n"
"v_mfma_f32_16x16x32_bf16 a[132:135], a[88:91], a[40:43], a[132:135]\n"
"ds_read_b128 a[40:43], v245 offset:59392\n"
"v_mfma_f32_16x16x32_bf16 a[132:135], a[92:95], a[44:47], a[132:135]\n"
"ds_read_b128 a[44:47], v246 offset:59392\n"
"v_mfma_f32_16x16x32_bf16 a[136:139], a[88:91], a[48:51], a[136:139]\n"
"ds_read_b128 a[48:51], v245 offset:61440\n"
"v_mfma_f32_16x16x32_bf16 a[136:139], a[92:95], a[52:55], a[136:139]\n"
"ds_read_b128 a[52:55], v246 offset:61440\n"
"v_mfma_f32_16x16x32_bf16 a[140:143], a[88:91], a[56:59], a[140:143]\n"
"ds_read_b128 a[56:59], v245 offset:63488\n"
"buffer_load_dwordx4 a[88:91], v240, s[36:39], s80 offen offset:0 sc1\n"
"v_mfma_f32_16x16x32_bf16 a[140:143], a[92:95], a[60:63], a[140:143]\n"
"s_waitcnt vmcnt(11) lgkmcnt(11)\n"
"ds_read_b128 a[60:63], v246 offset:63488\n"
"v_mfma_f32_16x16x32_bf16 v[0:3], a[96:99], a[0:3], v[0:3]\n"
"v_mfma_f32_16x16x32_bf16 v[0:3], a[100:103], a[4:7], v[0:3]\n"
"v_mfma_f32_16x16x32_bf16 v[4:7], a[96:99], a[8:11], v[4:7]\n"
"v_mfma_f32_16x16x32_bf16 v[4:7], a[100:103], a[12:15], v[4:7]\n"
"s_waitcnt lgkmcnt(8)\n"
"v_mfma_f32_16x16x32_bf16 v[8:11], a[96:99], a[16:19], v[8:11]\n"
"v_mfma_f32_16x16x32_bf16 v[8:11], a[100:103], a[20:23], v[8:11]\n"
"v_mfma_f32_16x16x32_bf16 v[12:15], a[96:99], a[24:27], v[12:15]\n"
"buffer_load_dwordx4 a[92:95], v240, s[36:39], s80 offen offset:1024 sc1\n"
"v_mfma_f32_16x16x32_bf16 v[12:15], a[100:103], a[28:31], v[12:15]\n"
"s_waitcnt lgkmcnt(4)\n"
"v_mfma_f32_16x16x32_bf16 v[16:19], a[96:99], a[32:35], v[16:19]\n"
"v_mfma_f32_16x16x32_bf16 v[16:19], a[100:103], a[36:39], v[16:19]\n"
"v_mfma_f32_16x16x32_bf16 v[20:23], a[96:99], a[40:43], v[20:23]\n"
"v_mfma_f32_16x16x32_bf16 v[20:23], a[100:103], a[44:47], v[20:23]\n"
".Lpad11_%=:\n"
"s_cbranch_execz .Lret1_%=\n"
"s_waitcnt lgkmcnt(0)\n"
"v_mfma_f32_16x16x32_bf16 v[24:27], a[96:99], a[48:51], v[24:27]\n"
"v_mfma_f32_16x16x32_bf16 v[24:27], a[100:103], a[52:55], v[24:27]\n"
"v_mfma_f32_16x16x32_bf16 v[28:31], a[96:99], a[56:59], v[28:31]\n"
"buffer_load_dwordx4 a[96:99], v240, s[36:39], s80 offen offset:2048 sc1\n"
"v_mfma_f32_16x16x32_bf16 v[28:31], a[100:103], a[60:63], v[28:31]\n"
"s_waitcnt vmcnt(11)\n"
"v_mfma_f32_16x16x32_bf16 v[32:35], a[104:107], a[0:3], v[32:35]\n"
"v_mfma_f32_16x16x32_bf16 v[32:35], a[108:111], a[4:7], v[32:35]\n"
"v_mfma_f32_16x16x32_bf16 v[36:39], a[104:107], a[8:11], v[36:39]\n"
"v_mfma_f32_16x16x32_bf16 v[36:39], a[108:111], a[12:15], v[36:39]\n"
"v_mfma_f32_16x16x32_bf16 v[40:43], a[104:107], a[16:19], v[40:43]\n"
"v_mfma_f32_16x16x32_bf16 v[40:43], a[108:111], a[20:23], v[40:43]\n"
"v_mfma_f32_16x16x32_bf16 v[44:47], a[104:107], a[24:27], v[44:47]\n"
"buffer_load_dwordx4 a[100:103], v240, s[36:39], s80 offen offset:3072 sc1\n"
"v_mfma_f32_16x16x32_bf16 v[44:47], a[108:111], a[28:31], v[44:47]\n"
"s_add_u32 s80, s80, 0x1000\n"
"v_mfma_f32_16x16x32_bf16 v[48:51], a[104:107], a[32:35], v[48:51]\n"
"v_mfma_f32_16x16x32_bf16 v[48:51], a[108:111], a[36:39], v[48:51]\n"
"v_mfma_f32_16x16x32_bf16 v[52:55], a[104:107], a[40:43], v[52:55]\n"
"v_mfma_f32_16x16x32_bf16 v[52:55], a[108:111], a[44:47], v[52:55]\n"
"v_mfma_f32_16x16x32_bf16 v[56:59], a[104:107], a[48:51], v[56:59]\n"
"v_mfma_f32_16x16x32_bf16 v[56:59], a[108:111], a[52:55], v[56:59]\n"
"v_mfma_f32_16x16x32_bf16 v[60:63], a[104:107], a[56:59], v[60:63]\n"
"buffer_load_dwordx4 a[104:107], v240, s[36:39], s80 offen offset:0 sc1\n"
"v_mfma_f32_16x16x32_bf16 v[60:63], a[108:111], a[60:63], v[60:63]\n"
"s_waitcnt vmcnt(11)\n"
"v_mfma_f32_16x16x32_bf16 v[64:67], v[224:227], a[0:3], v[64:67]\n"
"v_mfma_f32_16x16x32_bf16 v[64:67], v[228:231], a[4:7], v[64:67]\n"
"v_mfma_f32_16x16x32_bf16 v[68:71], v[224:227], a[8:11], v[68:71]\n"
"v_mfma_f32_16x16x32_bf16 v[68:71], v[228:231], a[12:15], v[68:71]\n"
"v_mfma_f32_16x16x32_bf16 v[72:75], v[224:227], a[16:19], v[72:75]\n"
"v_mfma_f32_16x16x32_bf16 v[72:75], v[228:231], a[20:23], v[72:75]\n"
"v_mfma_f32_16x16x32_bf16 v[76:79], v[224:227], a[24:27], v[76:79]\n"
"buffer_load_dwordx4 a[108:111], v240, s[36:39], s80 offen offset:1024 sc1\n"
"v_mfma_f32_16x16x32_bf16 v[76:79], v[228:231], a[28:31], v[76:79]\n"
"v_mfma_f32_16x16x32_bf16 v[80:83], v[224:227], a[32:35], v[80:83]\n"
"v_mfma_f32_16x16x32_bf16 v[80:83], v[228:231], a[36:39], v[80:83]\n"
"v_mfma_f32_16x16x32_bf16 v[84:87], v[224:227], a[40:43], v[84:87]\n"
"v_mfma_f32_16x16x32_bf16 v[84:87], v[228:231], a[44:47], v[84:87]\n"
"v_mfma_f32_16x16x32_bf16 v[88:91], v[224:227], a[48:51], v[88:91]\n"
"v_mfma_f32_16x16x32_bf16 v[88:91], v[228:231], a[52:55], v[88:91]\n"
"v_mfma_f32_16x16x32_bf16 v[92:95], v[224:227], a[56:59], v[92:95]\n"
"buffer_load_dwordx4 v[224:227], v240, s[36:39], s80 offen offset:2048 sc1\n"
"v_mfma_f32_16x16x32_bf16 v[92:95], v[228:231], a[60:63], v[92:95]\n"
"s_waitcnt vmcnt(11)\n"
"v_mfma_f32_16x16x32_bf16 v[96:99], a[64:67], a[0:3], v[96:99]\n"
"v_mfma_f32_16x16x32_bf16 v[96:99], a[68:71], a[4:7], v[96:99]\n"
"v_mfma_f32_16x16x32_bf16 v[100:103], a[64:67], a[8:11], v[100:103]\n"
"v_mfma_f32_16x16x32_bf16 v[100:103], a[68:71], a[12:15], v[100:103]\n"
"v_mfma_f32_16x16x32_bf16 v[104:107], a[64:67], a[16:19], v[104:107]\n"
"v_mfma_f32_16x16x32_bf16 v[104:107], a[68:71], a[20:23], v[104:107]\n"
"v_mfma_f32_16x16x32_bf16 v[108:111], a[64:67], a[24:27], v[108:111]\n"
"buffer_load_dwordx4 v[228:231], v240, s[36:39], s80 offen offset:3072 sc1\n"
"v_mfma_f32_16x16x32_bf16 v[108:111], a[68:71], a[28:31], v[108:111]\n"
"s_add_u32 s80, s80, 0x1000\n"
"v_mfma_f32_16x16x32_bf16 v[112:115], a[64:67], a[32:35], v[112:115]\n"
"v_mfma_f32_16x16x32_bf16 v[112:115], a[68:71], a[36:39], v[112:115]\n"
"v_mfma_f32_16x16x32_bf16 v[116:119], a[64:67], a[40:43], v[116:119]\n"
"v_mfma_f32_16x16x32_bf16 v[116:119], a[68:71], a[44:47], v[116:119]\n"
"v_mfma_f32_16x16x32_bf16 v[120:123], a[64:67], a[48:51], v[120:123]\n"
"v_mfma_f32_16x16x32_bf16 v[120:123], a[68:71], a[52:55], v[120:123]\n"
"v_mfma_f32_16x16x32_bf16 v[124:127], a[64:67], a[56:59], v[124:127]\n"
"buffer_load_dwordx4 a[64:67], v240, s[36:39], s80 offen offset:0 sc1\n"
"v_mfma_f32_16x16x32_bf16 v[124:127], a[68:71], a[60:63], v[124:127]\n"
"s_waitcnt vmcnt(11)\n"
"v_mfma_f32_16x16x32_bf16 v[128:131], a[72:75], a[0:3], v[128:131]\n"
"v_mfma_f32_16x16x32_bf16 v[128:131], a[76:79], a[4:7], v[128:131]\n"
"v_mfma_f32_16x16x32_bf16 v[132:135], a[72:75], a[8:11], v[132:135]\n"
"v_mfma_f32_16x16x32_bf16 v[132:135], a[76:79], a[12:15], v[132:135]\n"
"v_mfma_f32_16x16x32_bf16 v[136:139], a[72:75], a[16:19], v[136:139]\n"
"v_mfma_f32_16x16x32_bf16 v[136:139], a[76:79], a[20:23], v[136:139]\n"
"v_mfma_f32_16x16x32_bf16 v[140:143], a[72:75], a[24:27], v[140:143]\n"
"buffer_load_dwordx4 a[68:71], v240, s[36:39], s80 offen offset:1024 sc1\n"
"v_mfma_f32_16x16x32_bf16 v[140:143], a[76:79], a[28:31], v[140:143]\n"
"v_mfma_f32_16x16x32_bf16 v[144:147], a[72:75], a[32:35], v[144:147]\n"
"v_mfma_f32_16x16x32_bf16 v[144:147], a[76:79], a[36:39], v[144:147]\n"
"v_mfma_f32_16x16x32_bf16 v[148:151], a[72:75], a[40:43], v[148:151]\n"
"v_mfma_f32_16x16x32_bf16 v[148:151], a[76:79], a[44:47], v[148:151]\n"
"v_mfma_f32_16x16x32_bf16 v[152:155], a[72:75], a[48:51], v[152:155]\n"
"v_mfma_f32_16x16x32_bf16 v[152:155], a[76:79], a[52:55], v[152:155]\n"
"v_mfma_f32_16x16x32_bf16 v[156:159], a[72:75], a[56:59], v[156:159]\n"
"buffer_load_dwordx4 a[72:75], v240, s[36:39], s80 offen offset:2048 sc1\n"
"v_mfma_f32_16x16x32_bf16 v[156:159], a[76:79], a[60:63], v[156:159]\n"
"s_waitcnt vmcnt(11)\n"
"v_mfma_f32_16x16x32_bf16 v[160:163], a[80:83], a[0:3], v[160:163]\n"
"v_mfma_f32_16x16x32_bf16 v[160:163], a[84:87], a[4:7], v[160:163]\n"
"v_mfma_f32_16x16x32_bf16 v[164:167], a[80:83], a[8:11], v[164:167]\n"
"v_mfma_f32_16x16x32_bf16 v[164:167], a[84:87], a[12:15], v[164:167]\n"
"v_mfma_f32_16x16x32_bf16 v[168:171], a[80:83], a[16:19], v[168:171]\n"
"v_mfma_f32_16x16x32_bf16 v[168:171], a[84:87], a[20:23], v[168:171]\n"
"v_mfma_f32_16x16x32_bf16 v[172:175], a[80:83], a[24:27], v[172:175]\n"
"buffer_load_dwordx4 a[76:79], v240, s[36:39], s80 offen offset:3072 sc1\n"
"v_mfma_f32_16x16x32_bf16 v[172:175], a[84:87], a[28:31], v[172:175]\n"
"s_add_u32 s80, s80, 0x1000\n"
"v_mfma_f32_16x16x32_bf16 v[176:179], a[80:83], a[32:35], v[176:179]\n"
"v_mfma_f32_16x16x32_bf16 v[176:179], a[84:87], a[36:39], v[176:179]\n"
"v_mfma_f32_16x16x32_bf16 v[180:183], a[80:83], a[40:43], v[180:183]\n"
"v_mfma_f32_16x16x32_bf16 v[180:183], a[84:87], a[44:47], v[180:183]\n"
"v_mfma_f32_16x16x32_bf16 v[184:187], a[80:83], a[48:51], v[184:187]\n"
"v_mfma_f32_16x16x32_bf16 v[184:187], a[84:87], a[52:55], v[184:187]\n"
"v_mfma_f32_16x16x32_bf16 v[188:191], a[80:83], a[56:59], v[188:191]\n"
"buffer_load_dwordx4 a[80:83], v240, s[36:39], s80 offen offset:0 sc1\n"
"v_mfma_f32_16x16x32_bf16 v[188:191], a[84:87], a[60:63], v[188:191]\n"
"s_waitcnt vmcnt(11)\n"
"v_mfma_f32_16x16x32_bf16 v[192:195], a[88:91], a[0:3], v[192:195]\n"
"v_mfma_f32_16x16x32_bf16 v[192:195], a[92:95], a[4:7], v[192:195]\n"
"v_mfma_f32_16x16x32_bf16 v[196:199], a[88:91], a[8:11], v[196:199]\n"
"v_mfma_f32_16x16x32_bf16 v[196:199], a[92:95], a[12:15], v[196:199]\n"
"v_mfma_f32_16x16x32_bf16 v[200:203], a[88:91], a[16:19], v[200:203]\n"
"v_mfma_f32_16x16x32_bf16 v[200:203], a[92:95], a[20:23], v[200:203]\n"
"v_mfma_f32_16x16x32_bf16 v[204:207], a[88:91], a[24:27], v[204:207]\n"
"buffer_load_dwordx4 a[84:87], v240, s[36:39], s80 offen offset:1024 sc1\n"
"v_mfma_f32_16x16x32_bf16 v[204:207], a[92:95], a[28:31], v[204:207]\n"
"v_mfma_f32_16x16x32_bf16 v[208:211], a[88:91], a[32:35], v[208:211]\n"
"v_mfma_f32_16x16x32_bf16 v[208:211], a[92:95], a[36:39], v[208:211]\n"
"v_mfma_f32_16x16x32_bf16 v[212:215], a[88:91], a[40:43], v[212:215]\n"
"v_mfma_f32_16x16x32_bf16 v[212:215], a[92:95], a[44:47], v[212:215]\n"
"v_mfma_f32_16x16x32_bf16 v[216:219], a[88:91], a[48:51], v[216:219]\n"
"v_mfma_f32_16x16x32_bf16 v[216:219], a[92:95], a[52:55], v[216:219]\n"
"s_barrier\n"
"v_mfma_f32_16x16x32_bf16 v[220:223], a[88:91], a[56:59], v[220:223]\n"
"buffer_load_dwordx4 a[88:91], v240, s[36:39], s80 offen offset:2048 sc1\n"
"v_mfma_f32_16x16x32_bf16 v[220:223], a[92:95], a[60:63], v[220:223]\n"
"s_waitcnt vmcnt(11)\n"
"v_mfma_f32_16x16x32_bf16 a[112:115], a[96:99], a[0:3], a[112:115]\n"
"ds_read_b128 a[0:3], v247 offset:0\n"
"v_mfma_f32_16x16x32_bf16 a[112:115], a[100:103], a[4:7], a[112:115]\n"
"ds_read_b128 a[4:7], v248 offset:0\n"
"v_mfma_f32_16x16x32_bf16 a[116:119], a[96:99], a[8:11], a[116:119]\n"
"ds_read_b128 a[8:11], v247 offset:2048\n"
"v_mfma_f32_16x16x32_bf16 a[116:119], a[100:103], a[12:15], a[116:119]\n"
"ds_read_b128 a[12:15], v248 offset:2048\n"
"v_mfma_f32_16x16x32_bf16 a[120:123], a[96:99], a[16:19], a[120:123]\n"
"ds_read_b128 a[16:19], v247 offset:4096\n"
"v_mfma_f32_16x16x32_bf16 a[120:123], a[100:103], a[20:23], a[120:123]\n"
"ds_read_b128 a[20:23], v248 offset:4096\n"
"v_mfma_f32_16x16x32_bf16 a[124:127], a[96:99], a[24:27], a[124:127]\n"
"ds_read_b128 a[24:27], v247 offset:6144\n"
"buffer_load_dwordx4 a[92:95], v240, s[36:39], s80 offen offset:3072 sc1\n"
"v_mfma_f32_16x16x32_bf16 a[124:127], a[100:103], a[28:31], a[124:127]\n"
"s_add_u32 s80, s80, 0x1000\n"
"ds_read_b128 a[28:31], v248 offset:6144\n"
"v_mfma_f32_16x16x32_bf16 a[128:131], a[96:99], a[32:35], a[128:131]\n"
"ds_read_b128 a[32:35], v247 offset:8192\n"
"v_mfma_f32_16x16x32_bf16 a[128:131], a[100:103], a[36:39], a[128:131]\n"
"ds_read_b128 a[36:39], v248 offset:8192\n"
"v_mfma_f32_16x16x32_bf16 a[132:135], a[96:99], a[40:43], a[132:135]\n"
"ds_read_b128 a[40:43], v247 offset:10240\n"
"v_mfma_f32_16x16x32_bf16 a[132:135], a[100:103], a[44:47], a[132:135]\n"
"ds_read_b128 a[44:47], v248 offset:10240\n"
"v_mfma_f32_16x16x32_bf16 a[136:139], a[96:99], a[48:51], a[136:139]\n"
"ds_read_b128 a[48:51], v247 offset:12288\n"
"v_mfma_f32_16x16x32_bf16 a[136:139], a[100:103], a[52:55], a[136:139]\n"
"ds_read_b128 a[52:55], v248 offset:12288\n"
"v_mfma_f32_16x16x32_bf16 a[140:143], a[96:99], a[56:59], a[140:143]\n"
"ds_read_b128 a[56:59], v247 offset:14336\n"
"buffer_load_dwordx4 a[96:99], v240, s[36:39], s80 offen offset:0 sc1\n"
"v_mfma_f32_16x16x32_bf16 a[140:143], a[100:103], a[60:63], a[140:143]\n"
"s_waitcnt vmcnt(11) lgkmcnt(11)\n"
"ds_read_b128 a[60:63], v248 offset:14336\n"
"v_mfma_f32_16x16x32_bf16 v[0:3], a[104:107], a[0:3], v[0:3]\n"
"v_mfma_f32_16x16x32_bf16 v[0:3], a[108:111], a[4:7], v[0:3]\n"
"v_mfma_f32_16x16x32_bf16 v[4:7], a[104:107], a[8:11], v[4:7]\n"
"v_mfma_f32_16x16x32_bf16 v[4:7], a[108:111], a[12:15], v[4:7]\n"
"s_waitcnt lgkmcnt(8)\n"
"v_mfma_f32_16x16x32_bf16 v[8:11], a[104:107], a[16:19], v[8:11]\n"
"v_mfma_f32_16x16x32_bf16 v[8:11], a[108:111], a[20:23], v[8:11]\n"
"v_mfma_f32_16x16x32_bf16 v[12:15], a[104:107], a[24:27], v[12:15]\n"
"buffer_load_dwordx4 a[100:103], v240, s[36:39], s80 offen offset:1024 sc1\n"
"v_mfma_f32_16x16x32_bf16 v[12:15], a[108:111], a[28:31], v[12:15]\n"
"s_waitcnt lgkmcnt(4)\n"
"v_mfma_f32_16x16x32_bf16 v[16:19], a[104:107], a[32:35], v[16:19]\n"
"v_mfma_f32_16x16x32_bf16 v[16:19], a[108:111], a[36:39], v[16:19]\n"
"s_waitcnt lgkmcnt(0)\n"
"s_barrier\n"
"v_mfma_f32_16x16x32_bf16 v[20:23], a[104:107], a[40:43], v[20:23]\n"
"s_add_u32 m0, s83, 128000\n"
"v_mfma_f32_16x16x32_bf16 v[20:23], a[108:111], a[44:47], v[20:23]\n"
"buffer_load_dwordx4 v240, s[56:59], s68 offen offset:3072 sc1 lds\n"
"v_mfma_f32_16x16x32_bf16 v[24:27], a[104:107], a[48:51], v[24:27]\n"
"v_mfma_f32_16x16x32_bf16 v[24:27], a[108:111], a[52:55], v[24:27]\n"
"v_mfma_f32_16x16x32_bf16 v[28:31], a[104:107], a[56:59], v[28:31]\n"
"buffer_load_dwordx4 a[104:107], v240, s[36:39], s80 offen offset:2048 sc1\n"
"v_mfma_f32_16x16x32_bf16 v[28:31], a[108:111], a[60:63], v[28:31]\n"
"s_waitcnt vmcnt(12)\n"
"v_mfma_f32_16x16x32_bf16 v[32:35], v[224:227], a[0:3], v[32:35]\n"
"s_add_u32 m0, s83, 130048\n"
"v_mfma_f32_16x16x32_bf16 v[32:35], v[228:231], a[4:7], v[32:35]\n"
"buffer_load_dwordx4 v240, s[56:59], s69 offen offset:3072 sc1 lds\n"
"v_mfma_f32_16x16x32_bf16 v[36:39], v[224:227], a[8:11], v[36:39]\n"
"v_mfma_f32_16x16x32_bf16 v[36:39], v[228:231], a[12:15], v[36:39]\n"
"v_mfma_f32_16x16x32_bf16 v[40:43], v[224:227], a[16:19], v[40:43]\n"
"v_mfma_f32_16x16x32_bf16 v[40:43], v[228:231], a[20:23], v[40:43]\n"
"v_mfma_f32_16x16x32_bf16 v[44:47], v[224:227], a[24:27], v[44:47]\n"
"s_add_u32 m0, s83, 132096\n"
"buffer_load_dwordx4 a[108:111], v240, s[36:39], s80 offen offset:3072 sc1\n"
"v_mfma_f32_16x16x32_bf16 v[44:47], v[228:231], a[28:31], v[44:47]\n"
"s_add_u32 s80, s80, 0x1000\n"
"buffer_load_dwordx4 v240, s[56:59], s70 offen offset:3072 sc1 lds\n"
"v_mfma_f32_16x16x32_bf16 v[48:51], v[224:227], a[32:35], v[48:51]\n"
"v_mfma_f32_16x16x32_bf16 v[48:51], v[228:231], a[36:39], v[48:51]\n"
"v_mfma_f32_16x16x32_bf16 v[52:55], v[224:227], a[40:43], v[52:55]\n"
"v_mfma_f32_16x16x32_bf16 v[52:55], v[228:231], a[44:47], v[52:55]\n"
"v_mfma_f32_16x16x32_bf16 v[56:59], v[224:227], a[48:51], v[56:59]\n"
"s_add_u32 m0, s83, 134144\n"
"v_mfma_f32_16x16x32_bf16 v[56:59], v[228:231], a[52:55], v[56:59]\n"
"buffer_load_dwordx4 v240, s[56:59], s71 offen offset:3072 sc1 lds\n"
"v_mfma_f32_16x16x32_bf16 v[60:63], v[224:227], a[56:59], v[60:63]\n"
"buffer_load_dwordx4 v[224:227], v240, s[36:39], s80 offen offset:0 sc1\n"
"v_mfma_f32_16x16x32_bf16 v[60:63], v[228:231], a[60:63], v[60:63]\n"
"s_waitcnt vmcnt(15)\n"
"v_mfma_f32_16x16x32_bf16 v[64:67], a[64:67], a[0:3], v[64:67]\n"
"v_mfma_f32_16x16x32_bf16 v[64:67], a[68:71], a[4:7], v[64:67]\n"
"v_mfma_f32_16x16x32_bf16 v[68:71], a[64:67], a[8:11], v[68:71]\n"
"v_mfma_f32_16x16x32_bf16 v[68:71], a[68:71], a[12:15], v[68:71]\n"
"v_mfma_f32_16x16x32_bf16 v[72:75], a[64:67], a[16:19], v[72:75]\n"
"v_mfma_f32_16x16x32_bf16 v[72:75], a[68:71], a[20:23], v[72:75]\n"
"v_mfma_f32_16x16x32_bf16 v[76:79], a[64:67], a[24:27], v[76:79]\n"
"buffer_load_dwordx4 v[228:231], v240, s[36:39], s80 offen offset:1024 sc1\n"
"v_mfma_f32_16x16x32_bf16 v[76:79], a[68:71], a[28:31], v[76:79]\n"
"v_mfma_f32_16x16x32_bf16 v[80:83], a[64:67], a[32:35], v[80:83]\n"
"v_mfma_f32_16x16x32_bf16 v[80:83], a[68:71], a[36:39], v[80:83]\n"
"v_mfma_f32_16x16x32_bf16 v[84:87], a[64:67], a[40:43], v[84:87]\n"
"v_mfma_f32_16x16x32_bf16 v[84:87], a[68:71], a[44:47], v[84:87]\n"
"v_mfma_f32_16x16x32_bf16 v[88:91], a[64:67], a[48:51], v[88:91]\n"
"v_mfma_f32_16x16x32_bf16 v[88:91], a[68:71], a[52:55], v[88:91]\n"
"v_mfma_f32_16x16x32_bf16 v[92:95], a[64:67], a[56:59], v[92:95]\n"
"buffer_load_dwordx4 a[64:67], v240, s[36:39], s80 offen offset:2048 sc1\n"
"v_mfma_f32_16x16x32_bf16 v[92:95], a[68:71], a[60:63], v[92:95]\n"
"s_waitcnt vmcnt(15)\n"
"v_mfma_f32_16x16x32_bf16 v[96:99], a[72:75], a[0:3], v[96:99]\n"
"v_mfma_f32_16x16x32_bf16 v[96:99], a[76:79], a[4:7], v[96:99]\n"
"v_mfma_f32_16x16x32_bf16 v[100:103], a[72:75], a[8:11], v[100:103]\n"
"v_mfma_f32_16x16x32_bf16 v[100:103], a[76:79], a[12:15], v[100:103]\n"
"v_mfma_f32_16x16x32_bf16 v[104:107], a[72:75], a[16:19], v[104:107]\n"
"v_mfma_f32_16x16x32_bf16 v[104:107], a[76:79], a[20:23], v[104:107]\n"
"v_mfma_f32_16x16x32_bf16 v[108:111], a[72:75], a[24:27], v[108:111]\n"
"buffer_load_dwordx4 a[68:71], v240, s[36:39], s80 offen offset:3072 sc1\n"
"v_mfma_f32_16x16x32_bf16 v[108:111], a[76:79], a[28:31], v[108:111]\n"
"s_add_u32 s80, s80, 0x1000\n"
"v_mfma_f32_16x16x32_bf16 v[112:115], a[72:75], a[32:35], v[112:115]\n"
"v_mfma_f32_16x16x32_bf16 v[112:115], a[76:79], a[36:39], v[112:115]\n"
"v_mfma_f32_16x16x32_bf16 v[116:119], a[72:75], a[40:43], v[116:119]\n"
"v_mfma_f32_16x16x32_bf16 v[116:119], a[76:79], a[44:47], v[116:119]\n"
"v_mfma_f32_16x16x32_bf16 v[120:123], a[72:75], a[48:51], v[120:123]\n"
"v_mfma_f32_16x16x32_bf16 v[120:123], a[76:79], a[52:55], v[120:123]\n"
"v_mfma_f32_16x16x32_bf16 v[124:127], a[72:75], a[56:59], v[124:127]\n"
"buffer_load_dwordx4 a[72:75], v240, s[36:39], s80 offen offset:0 sc1\n"
"v_mfma_f32_16x16x32_bf16 v[124:127], a[76:79], a[60:63], v[124:127]\n"
"s_waitcnt vmcnt(15)\n"
"v_mfma_f32_16x16x32_bf16 v[128:131], a[80:83], a[0:3], v[128:131]\n"
"v_mfma_f32_16x16x32_bf16 v[128:131], a[84:87], a[4:7], v[128:131]\n"
"v_mfma_f32_16x16x32_bf16 v[132:135], a[80:83], a[8:11], v[132:135]\n"
"v_mfma_f32_16x16x32_bf16 v[132:135], a[84:87], a[12:15], v[132:135]\n"
"v_mfma_f32_16x16x32_bf16 v[136:139], a[80:83], a[16:19], v[136:139]\n"
"v_mfma_f32_16x16x32_bf16 v[136:139], a[84:87], a[20:23], v[136:139]\n"
"v_mfma_f32_16x16x32_bf16 v[140:143], a[80:83], a[24:27], v[140:143]\n"
"buffer_load_dwordx4 a[76:79], v240, s[36:39], s80 offen offset:1024 sc1\n"
"v_mfma_f32_16x16x32_bf16 v[140:143], a[84:87], a[28:31], v[140:143]\n"
"v_mfma_f32_16x16x32_bf16 v[144:147], a[80:83], a[32:35], v[144:147]\n"
"v_mfma_f32_16x16x32_bf16 v[144:147], a[84:87], a[36:39], v[144:147]\n"
"v_mfma_f32_16x16x32_bf16 v[148:151], a[80:83], a[40:43], v[148:151]\n"
"v_mfma_f32_16x16x32_bf16 v[148:151], a[84:87], a[44:47], v[148:151]\n"
"v_mfma_f32_16x16x32_bf16 v[152:155], a[80:83], a[48:51], v[152:155]\n"
"v_mfma_f32_16x16x32_bf16 v[152:155], a[84:87], a[52:55], v[152:155]\n"
"v_mfma_f32_16x16x32_bf16 v[156:159], a[80:83], a[56:59], v[156:159]\n"
"buffer_load_dwordx4 a[80:83], v240, s[36:39], s80 offen offset:2048 sc1\n"
"v_mfma_f32_16x16x32_bf16 v[156:159], a[84:87], a[60:63], v[156:159]\n"
"s_waitcnt vmcnt(15)\n"
"v_mfma_f32_16x16x32_bf16 v[160:163], a[88:91], a[0:3], v[160:163]\n"
"v_mfma_f32_16x16x32_bf16 v[160:163], a[92:95], a[4:7], v[160:163]\n"
"v_mfma_f32_16x16x32_bf16 v[164:167], a[88:91], a[8:11], v[164:167]\n"
"v_mfma_f32_16x16x32_bf16 v[164:167], a[92:95], a[12:15], v[164:167]\n"
"v_mfma_f32_16x16x32_bf16 v[168:171], a[88:91], a[16:19], v[168:171]\n"
"v_mfma_f32_16x16x32_bf16 v[168:171], a[92:95], a[20:23], v[168:171]\n"
"v_mfma_f32_16x16x32_bf16 v[172:175], a[88:91], a[24:27], v[172:175]\n"
"buffer_load_dwordx4 a[84:87], v240, s[36:39], s80 offen offset:3072 sc1\n"
"v_mfma_f32_16x16x32_bf16 v[172:175], a[92:95], a[28:31], v[172:175]\n"
"s_add_u32 s80, s80, 0x1000\n"
"v_mfma_f32_16x16x32_bf16 v[176:179], a[88:91], a[32:35], v[176:179]\n"
"v_mfma_f32_16x16x32_bf16 v[176:179], a[92:95], a[36:39], v[176:179]\n"
"v_mfma_f32_16x16x32_bf16 v[180:183], a[88:91], a[40:43], v[180:183]\n"
"v_mfma_f32_16x16x32_bf16 v[180:183], a[92:95], a[44:47], v[180:183]\n"
"v_mfma_f32_16x16x32_bf16 v[184:187], a[88:91], a[48:51], v[184:187]\n"
"v_mfma_f32_16x16x32_bf16 v[184:187], a[92:95], a[52:55], v[184:187]\n"
"v_mfma_f32_16x16x32_bf16 v[188:191], a[88:91], a[56:59], v[188:191]\n"
"buffer_load_dwordx4 a[88:91], v240, s[36:39], s80 offen offset:0 sc1\n"
"v_mfma_f32_16x16x32_bf16 v[188:191], a[92:95], a[60:63], v[188:191]\n"
"s_waitcnt vmcnt(15)\n"
"v_mfma_f32_16x16x32_bf16 v[192:195], a[96:99], a[0:3], v[192:195]\n"
"v_mfma_f32_16x16x32_bf16 v[192:195], a[100:103], a[4:7], v[192:195]\n"
"v_mfma_f32_16x16x32_bf16 v[196:199], a[96:99], a[8:11], v[196:199]\n"
"v_mfma_f32_16x16x32_bf16 v[196:199], a[100:103], a[12:15], v[196:199]\n"
"v_mfma_f32_16x16x32_bf16 v[200:203], a[96:99], a[16:19], v[200:203]\n"
"v_mfma_f32_16x16x32_bf16 v[200:203], a[100:103], a[20:23], v[200:203]\n"
"v_mfma_f32_16x16x32_bf16 v[204:207], a[96:99], a[24:27], v[204:207]\n"
"buffer_load_dwordx4 a[92:95], v240, s[36:39], s80 offen offset:1024 sc1\n"
"v_mfma_f32_16x16x32_bf16 v[204:207], a[100:103], a[28:31], v[204:207]\n"
"v_mfma_f32_16x16x32_bf16 v[208:211], a[96:99], a[32:35], v[208:211]\n"
"v_mfma_f32_16x16x32_bf16 v[208:211], a[100:103], a[36:39], v[208:211]\n"
"v_mfma_f32_16x16x32_bf16 v[212:215], a[96:99], a[40:43], v[212:215]\n"
"v_mfma_f32_16x16x32_bf16 v[212:215], a[100:103], a[44:47], v[212:215]\n"
"v_mfma_f32_16x16x32_bf16 v[216:219], a[96:99], a[48:51], v[216:219]\n"
"v_mfma_f32_16x16x32_bf16 v[216:219], a[100:103], a[52:55], v[216:219]\n"
"s_waitcnt vmcnt(10)\n"
"s_barrier\n"
"v_mfma_f32_16x16x32_bf16 v[220:223], a[96:99], a[56:59], v[220:223]\n"
"buffer_load_dwordx4 a[96:99], v240, s[36:39], s80 offen offset:2048 sc1\n"
"v_mfma_f32_16x16x32_bf16 v[220:223], a[100:103], a[60:63], v[220:223]\n"
"v_mfma_f32_16x16x32_bf16 a[112:115], a[104:107], a[0:3], a[112:115]\n"
"ds_read_b128 a[0:3], v247 offset:0\n"
"v_mfma_f32_16x16x32_bf16 a[112:115], a[108:111], a[4:7], a[112:115]\n"
"ds_read_b128 a[4:7], v248 offset:0\n"
"v_mfma_f32_16x16x32_bf16 a[116:119], a[104:107], a[8:11], a[116:119]\n"
"ds_read_b128 a[8:11], v247 offset:2048\n"
"v_mfma_f32_16x16x32_bf16 a[116:119], a[108:111], a[12:15], a[116:119]\n"
"ds_read_b128 a[12:15], v248 offset:2048\n"
"v_mfma_f32_16x16x32_bf16 a[120:123], a[104:107], a[16:19], a[120:123]\n"
"ds_read_b128 a[16:19], v247 offset:4096\n"
"v_mfma_f32_16x16x32_bf16 a[120:123], a[108:111], a[20:23], a[120:123]\n"
"ds_read_b128 a[20:23], v248 offset:4096\n"
"v_mfma_f32_16x16x32_bf16 a[124:127], a[104:107], a[24:27], a[124:127]\n"
"ds_read_b128 a[24:27], v247 offset:6144\n"
"buffer_load_dwordx4 a[100:103], v240, s[36:39], s80 offen offset:3072 sc1\n"
"v_mfma_f32_16x16x32_bf16 a[124:127], a[108:111], a[28:31], a[124:127]\n"
"s_add_u32 s80, s80, 0x1000\n"
"ds_read_b128 a[28:31], v248 offset:6144\n"
"v_mfma_f32_16x16x32_bf16 a[128:131], a[104:107], a[32:35], a[128:131]\n"
"ds_read_b128 a[32:35], v247 offset:8192\n"
"v_mfma_f32_16x16x32_bf16 a[128:131], a[108:111], a[36:39], a[128:131]\n"
"ds_read_b128 a[36:39], v248 offset:8192\n"
"v_mfma_f32_16x16x32_bf16 a[132:135], a[104:107], a[40:43], a[132:135]\n"
"ds_read_b128 a[40:43], v247 offset:10240\n"
"v_mfma_f32_16x16x32_bf16 a[132:135], a[108:111], a[44:47], a[132:135]\n"
"ds_read_b128 a[44:47], v248 offset:10240\n"
"v_mfma_f32_16x16x32_bf16 a[136:139], a[104:107], a[48:51], a[136:139]\n"
"ds_read_b128 a[48:51], v247 offset:12288\n"
"v_mfma_f32_16x16x32_bf16 a[136:139], a[108:111], a[52:55], a[136:139]\n"
"ds_read_b128 a[52:55], v248 offset:12288\n"
"v_mfma_f32_16x16x32_bf16 a[140:143], a[104:107], a[56:59], a[140:143]\n"
"ds_read_b128 a[56:59], v247 offset:14336\n"
"buffer_load_dwordx4 a[104:107], v240, s[36:39], s80 offen offset:0 sc1\n"
"v_mfma_f32_16x16x32_bf16 a[140:143], a[108:111], a[60:63], a[140:143]\n"
"s_waitcnt vmcnt(11) lgkmcnt(11)\n"
"ds_read_b128 a[60:63], v248 offset:14336\n"
"v_mfma_f32_16x16x32_bf16 v[0:3], v[224:227], a[0:3], v[0:3]\n"
"v_mfma_f32_16x16x32_bf16 v[0:3], v[228:231], a[4:7], v[0:3]\n"
"v_mfma_f32_16x16x32_bf16 v[4:7], v[224:227], a[8:11], v[4:7]\n"
"v_mfma_f32_16x16x32_bf16 v[4:7], v[228:231], a[12:15], v[4:7]\n"
"s_waitcnt lgkmcnt(8)\n"
"v_mfma_f32_16x16x32_bf16 v[8:11], v[224:227], a[16:19], v[8:11]\n"
"v_mfma_f32_16x16x32_bf16 v[8:11], v[228:231], a[20:23], v[8:11]\n"
"v_mfma_f32_16x16x32_bf16 v[12:15], v[224:227], a[24:27], v[12:15]\n"
"buffer_load_dwordx4 a[108:111], v240, s[36:39], s80 offen offset:1024 sc1\n"
"v_mfma_f32_16x16x32_bf16 v[12:15], v[228:231], a[28:31], v[12:15]\n"
"s_waitcnt lgkmcnt(4)\n"
"v_mfma_f32_16x16x32_bf16 v[16:19], v[224:227], a[32:35], v[16:19]\n"
"v_mfma_f32_16x16x32_bf16 v[16:19], v[228:231], a[36:39], v[16:19]\n"
"s_waitcnt lgkmcnt(0)\n"
"s_barrier\n"
"v_mfma_f32_16x16x32_bf16 v[20:23], v[224:227], a[40:43], v[20:23]\n"
"s_add_u32 m0, s83, 128000\n"
"v_mfma_f32_16x16x32_bf16 v[20:23], v[228:231], a[44:47], v[20:23]\n"
"buffer_load_dwordx4 v240, s[56:59], s72 offen offset:3072 sc1 lds\n"
"v_mfma_f32_16x16x32_bf16 v[24:27], v[224:227], a[48:51], v[24:27]\n"
"v_mfma_f32_16x16x32_bf16 v[24:27], v[228:231], a[52:55], v[24:27]\n"
"v_mfma_f32_16x16x32_bf16 v[28:31], v[224:227], a[56:59], v[28:31]\n"
"buffer_load_dwordx4 v[224:227], v240, s[36:39], s80 offen offset:2048 sc1\n"
"v_mfma_f32_16x16x32_bf16 v[28:31], v[228:231], a[60:63], v[28:31]\n"
"s_waitcnt vmcnt(12)\n"
"v_mfma_f32_16x16x32_bf16 v[32:35], a[64:67], a[0:3], v[32:35]\n"
"s_add_u32 m0, s83, 130048\n"
"v_mfma_f32_16x16x32_bf16 v[32:35], a[68:71], a[4:7], v[32:35]\n"
"buffer_load_dwordx4 v240, s[56:59], s73 offen offset:3072 sc1 lds\n"
"v_mfma_f32_16x16x32_bf16 v[36:39], a[64:67], a[8:11], v[36:39]\n"
"v_mfma_f32_16x16x32_bf16 v[36:39], a[68:71], a[12:15], v[36:39]\n"
"v_mfma_f32_16x16x32_bf16 v[40:43], a[64:67], a[16:19], v[40:43]\n"
"v_mfma_f32_16x16x32_bf16 v[40:43], a[68:71], a[20:23], v[40:43]\n"
"v_mfma_f32_16x16x32_bf16 v[44:47], a[64:67], a[24:27], v[44:47]\n"
"s_add_u32 m0, s83, 132096\n"
"buffer_load_dwordx4 v[228:231], v240, s[36:39], s80 offen offset:3072 sc1\n"
"v_mfma_f32_16x16x32_bf16 v[44:47], a[68:71], a[28:31], v[44:47]\n"
"s_add_u32 s80, s80, 0x1000\n"
"buffer_load_dwordx4 v240, s[56:59], s74 offen offset:3072 sc1 lds\n"
"v_mfma_f32_16x16x32_bf16 v[48:51], a[64:67], a[32:35], v[48:51]\n"
"v_mfma_f32_16x16x32_bf16 v[48:51], a[68:71], a[36:39], v[48:51]\n"
"v_mfma_f32_16x16x32_bf16 v[52:55], a[64:67], a[40:43], v[52:55]\n"
"v_mfma_f32_16x16x32_bf16 v[52:55], a[68:71], a[44:47], v[52:55]\n"
"v_mfma_f32_16x16x32_bf16 v[56:59], a[64:67], a[48:51], v[56:59]\n"
"s_add_u32 m0, s83, 134144\n"
"v_mfma_f32_16x16x32_bf16 v[56:59], a[68:71], a[52:55], v[56:59]\n"
"buffer_load_dwordx4 v240, s[56:59], s75 offen offset:3072 sc1 lds\n"
"v_mfma_f32_16x16x32_bf16 v[60:63], a[64:67], a[56:59], v[60:63]\n"
"s_add_u32 s56, s56, 0x4000\n"
"s_addc_u32 s57, s57, 0\n"
"buffer_load_dwordx4 a[64:67], v240, s[36:39], s80 offen offset:0 sc1\n"
"v_mfma_f32_16x16x32_bf16 v[60:63], a[68:71], a[60:63], v[60:63]\n"
"s_waitcnt vmcnt(15)\n"
"v_mfma_f32_16x16x32_bf16 v[64:67], a[72:75], a[0:3], v[64:67]\n"
"v_mfma_f32_16x16x32_bf16 v[64:67], a[76:79], a[4:7], v[64:67]\n"
"v_mfma_f32_16x16x32_bf16 v[68:71], a[72:75], a[8:11], v[68:71]\n"
"v_mfma_f32_16x16x32_bf16 v[68:71], a[76:79], a[12:15], v[68:71]\n"
"v_mfma_f32_16x16x32_bf16 v[72:75], a[72:75], a[16:19], v[72:75]\n"
"v_mfma_f32_16x16x32_bf16 v[72:75], a[76:79], a[20:23], v[72:75]\n"
"v_mfma_f32_16x16x32_bf16 v[76:79], a[72:75], a[24:27], v[76:79]\n"
"buffer_load_dwordx4 a[68:71], v240, s[36:39], s80 offen offset:1024 sc1\n"
"v_mfma_f32_16x16x32_bf16 v[76:79], a[76:79], a[28:31], v[76:79]\n"
"v_mfma_f32_16x16x32_bf16 v[80:83], a[72:75], a[32:35], v[80:83]\n"
"v_mfma_f32_16x16x32_bf16 v[80:83], a[76:79], a[36:39], v[80:83]\n"
"v_mfma_f32_16x16x32_bf16 v[84:87], a[72:75], a[40:43], v[84:87]\n"
"v_mfma_f32_16x16x32_bf16 v[84:87], a[76:79], a[44:47], v[84:87]\n"
"v_mfma_f32_16x16x32_bf16 v[88:91], a[72:75], a[48:51], v[88:91]\n"
"v_mfma_f32_16x16x32_bf16 v[88:91], a[76:79], a[52:55], v[88:91]\n"
"v_mfma_f32_16x16x32_bf16 v[92:95], a[72:75], a[56:59], v[92:95]\n"
"buffer_load_dwordx4 a[72:75], v240, s[36:39], s80 offen offset:2048 sc1\n"
"v_mfma_f32_16x16x32_bf16 v[92:95], a[76:79], a[60:63], v[92:95]\n"
"s_waitcnt vmcnt(15)\n"
"v_mfma_f32_16x16x32_bf16 v[96:99], a[80:83], a[0:3], v[96:99]\n"
"v_mfma_f32_16x16x32_bf16 v[96:99], a[84:87], a[4:7], v[96:99]\n"
"v_mfma_f32_16x16x32_bf16 v[100:103], a[80:83], a[8:11], v[100:103]\n"
"v_mfma_f32_16x16x32_bf16 v[100:103], a[84:87], a[12:15], v[100:103]\n"
"v_mfma_f32_16x16x32_bf16 v[104:107], a[80:83], a[16:19], v[104:107]\n"
"v_mfma_f32_16x16x32_bf16 v[104:107], a[84:87], a[20:23], v[104:107]\n"
"v_mfma_f32_16x16x32_bf16 v[108:111], a[80:83], a[24:27], v[108:111]\n"
"buffer_load_dwordx4 a[76:79], v240, s[36:39], s80 offen offset:3072 sc1\n"
"v_mfma_f32_16x16x32_bf16 v[108:111], a[84:87], a[28:31], v[108:111]\n"
"s_add_u32 s80, s80, 0x1000\n"
"v_mfma_f32_16x16x32_bf16 v[112:115], a[80:83], a[32:35], v[112:115]\n"
"v_mfma_f32_16x16x32_bf16 v[112:115], a[84:87], a[36:39], v[112:115]\n"
"v_mfma_f32_16x16x32_bf16 v[116:119], a[80:83], a[40:43], v[116:119]\n"
"v_mfma_f32_16x16x32_bf16 v[116:119], a[84:87], a[44:47], v[116:119]\n"
"v_mfma_f32_16x16x32_bf16 v[120:123], a[80:83], a[48:51], v[120:123]\n"
"v_mfma_f32_16x16x32_bf16 v[120:123], a[84:87], a[52:55], v[120:123]\n"
"v_mfma_f32_16x16x32_bf16 v[124:127], a[80:83], a[56:59], v[124:127]\n"
"buffer_load_dwordx4 a[80:83], v240, s[36:39], s80 offen offset:0 sc1\n"
"v_mfma_f32_16x16x32_bf16 v[124:127], a[84:87], a[60:63], v[124:127]\n"
"s_waitcnt vmcnt(15)\n"
"v_mfma_f32_16x16x32_bf16 v[128:131], a[88:91], a[0:3], v[128:131]\n"
"v_mfma_f32_16x16x32_bf16 v[128:131], a[92:95], a[4:7], v[128:131]\n"
"v_mfma_f32_16x16x32_bf16 v[132:135], a[88:91], a[8:11], v[132:135]\n"
"v_mfma_f32_16x16x32_bf16 v[132:135], a[92:95], a[12:15], v[132:135]\n"
"v_mfma_f32_16x16x32_bf16 v[136:139], a[88:91], a[16:19], v[136:139]\n"
"v_mfma_f32_16x16x32_bf16 v[136:139], a[92:95], a[20:23], v[136:139]\n"
"v_mfma_f32_16x16x32_bf16 v[140:143], a[88:91], a[24:27], v[140:143]\n"
"buffer_load_dwordx4 a[84:87], v240, s[36:39], s80 offen offset:1024 sc1\n"
"v_mfma_f32_16x16x32_bf16 v[140:143], a[92:95], a[28:31], v[140:143]\n"
"v_mfma_f32_16x16x32_bf16 v[144:147], a[88:91], a[32:35], v[144:147]\n"
"v_mfma_f32_16x16x32_bf16 v[144:147], a[92:95], a[36:39], v[144:147]\n"
"v_mfma_f32_16x16x32_bf16 v[148:151], a[88:91], a[40:43], v[148:151]\n"
"v_mfma_f32_16x16x32_bf16 v[148:151], a[92:95], a[44:47], v[148:151]\n"
"v_mfma_f32_16x16x32_bf16 v[152:155], a[88:91], a[48:51], v[152:155]\n"
"v_mfma_f32_16x16x32_bf16 v[152:155], a[92:95], a[52:55], v[152:155]\n"
"v_mfma_f32_16x16x32_bf16 v[156:159], a[88:91], a[56:59], v[156:159]\n"
"buffer_load_dwordx4 a[88:91], v240, s[36:39], s80 offen offset:2048 sc1\n"
"v_mfma_f32_16x16x32_bf16 v[156:159], a[92:95], a[60:63], v[156:159]\n"
"s_waitcnt vmcnt(15)\n"
"v_mfma_f32_16x16x32_bf16 v[160:163], a[96:99], a[0:3], v[160:163]\n"
"v_mfma_f32_16x16x32_bf16 v[160:163], a[100:103], a[4:7], v[160:163]\n"
"v_mfma_f32_16x16x32_bf16 v[164:167], a[96:99], a[8:11], v[164:167]\n"
"v_mfma_f32_16x16x32_bf16 v[164:167], a[100:103], a[12:15], v[164:167]\n"
"v_mfma_f32_16x16x32_bf16 v[168:171], a[96:99], a[16:19], v[168:171]\n"
"v_mfma_f32_16x16x32_bf16 v[168:171], a[100:103], a[20:23], v[168:171]\n"
"v_mfma_f32_16x16x32_bf16 v[172:175], a[96:99], a[24:27], v[172:175]\n"
"buffer_load_dwordx4 a[92:95], v240, s[36:39], s80 offen offset:3072 sc1\n"
"v_mfma_f32_16x16x32_bf16 v[172:175], a[100:103], a[28:31], v[172:175]\n"
"s_add_u32 s80, s80, 0x1000\n"
"v_mfma_f32_16x16x32_bf16 v[176:179], a[96:99], a[32:35], v[176:179]\n"
"v_mfma_f32_16x16x32_bf16 v[176:179], a[100:103], a[36:39], v[176:179]\n"
"v_mfma_f32_16x16x32_bf16 v[180:183], a[96:99], a[40:43], v[180:183]\n"
"v_mfma_f32_16x16x32_bf16 v[180:183], a[100:103], a[44:47], v[180:183]\n"
"v_mfma_f32_16x16x32_bf16 v[184:187], a[96:99], a[48:51], v[184:187]\n"
"v_mfma_f32_16x16x32_bf16 v[184:187], a[100:103], a[52:55], v[184:187]\n"
"v_mfma_f32_16x16x32_bf16 v[188:191], a[96:99], a[56:59], v[188:191]\n"
"buffer_load_dwordx4 a[96:99], v240, s[36:39], s80 offen offset:0 sc1\n"
"v_mfma_f32_16x16x32_bf16 v[188:191], a[100:103], a[60:63], v[188:191]\n"
"s_waitcnt vmcnt(15)\n"
"v_mfma_f32_16x16x32_bf16 v[192:195], a[104:107], a[0:3], v[192:195]\n"
"v_mfma_f32_16x16x32_bf16 v[192:195], a[108:111], a[4:7], v[192:195]\n"
"v_mfma_f32_16x16x32_bf16 v[196:199], a[104:107], a[8:11], v[196:199]\n"
"v_mfma_f32_16x16x32_bf16 v[196:199], a[108:111], a[12:15], v[196:199]\n"
"v_mfma_f32_16x16x32_bf16 v[200:203], a[104:107], a[16:19], v[200:203]\n"
"v_mfma_f32_16x16x32_bf16 v[200:203], a[108:111], a[20:23], v[200:203]\n"
"v_mfma_f32_16x16x32_bf16 v[204:207], a[104:107], a[24:27], v[204:207]\n"
"buffer_load_dwordx4 a[100:103], v240, s[36:39], s80 offen offset:1024 sc1\n"
"v_mfma_f32_16x16x32_bf16 v[204:207], a[108:111], a[28:31], v[204:207]\n"
"v_mfma_f32_16x16x32_bf16 v[208:211], a[104:107], a[32:35], v[208:211]\n"
"v_mfma_f32_16x16x32_bf16 v[208:211], a[108:111], a[36:39], v[208:211]\n"
"v_mfma_f32_16x16x32_bf16 v[212:215], a[104:107], a[40:43], v[212:215]\n"
"v_mfma_f32_16x16x32_bf16 v[212:215], a[108:111], a[44:47], v[212:215]\n"
"v_mfma_f32_16x16x32_bf16 v[216:219], a[104:107], a[48:51], v[216:219]\n"
"v_mfma_f32_16x16x32_bf16 v[216:219], a[108:111], a[52:55], v[216:219]\n"
"s_waitcnt vmcnt(10)\n"
"s_barrier\n"
"v_mfma_f32_16x16x32_bf16 v[220:223], a[104:107], a[56:59], v[220:223]\n"
"buffer_load_dwordx4 a[104:107], v240, s[36:39], s80 offen offset:2048 sc1\n"
"v_mfma_f32_16x16x32_bf16 v[220:223], a[108:111], a[60:63], v[220:223]\n"
"v_mfma_f32_16x16x32_bf16 a[112:115], v[224:227], a[0:3], a[112:115]\n"
"ds_read_b128 a[0:3], v247 offset:0\n"
"v_mfma_f32_16x16x32_bf16 a[112:115], v[228:231], a[4:7], a[112:115]\n"
"ds_read_b128 a[4:7], v248 offset:0\n"
"v_mfma_f32_16x16x32_bf16 a[116:119], v[224:227], a[8:11], a[116:119]\n"
"ds_read_b128 a[8:11], v247 offset:2048\n"
"v_mfma_f32_16x16x32_bf16 a[116:119], v[228:231], a[12:15], a[116:119]\n"
"ds_read_b128 a[12:15], v248 offset:2048\n"
"v_mfma_f32_16x16x32_bf16 a[120:123], v[224:227], a[16:19], a[120:123]\n"
"ds_read_b128 a[16:19], v247 offset:4096\n"
"v_mfma_f32_16x16x32_bf16 a[120:123], v[228:231], a[20:23], a[120:123]\n"
"ds_read_b128 a[20:23], v248 offset:4096\n"
"v_mfma_f32_16x16x32_bf16 a[124:127], v[224:227], a[24:27], a[124:127]\n"
"ds_read_b128 a[24:27], v247 offset:6144\n"
"buffer_load_dwordx4 a[108:111], v240, s[36:39], s80 offen offset:3072 sc1\n"
"v_mfma_f32_16x16x32_bf16 a[124:127], v[228:231], a[28:31], a[124:127]\n"
"s_add_u32 s80, s80, 0x1000\n"
"ds_read_b128 a[28:31], v248 offset:6144\n"
"v_mfma_f32_16x16x32_bf16 a[128:131], v[224:227], a[32:35], a[128:131]\n"
"ds_read_b128 a[32:35], v247 offset:8192\n"
"v_mfma_f32_16x16x32_bf16 a[128:131], v[228:231], a[36:39], a[128:131]\n"
"ds_read_b128 a[36:39], v248 offset:8192\n"
"v_mfma_f32_16x16x32_bf16 a[132:135], v[224:227], a[40:43], a[132:135]\n"
"ds_read_b128 a[40:43], v247 offset:10240\n"
"v_mfma_f32_16x16x32_bf16 a[132:135], v[228:231], a[44:47], a[132:135]\n"
"ds_read_b128 a[44:47], v248 offset:10240\n"
"v_mfma_f32_16x16x32_bf16 a[136:139], v[224:227], a[48:51], a[136:139]\n"
"ds_read_b128 a[48:51], v247 offset:12288\n"
"v_mfma_f32_16x16x32_bf16 a[136:139], v[228:231], a[52:55], a[136:139]\n"
"ds_read_b128 a[52:55], v248 offset:12288\n"
"v_mfma_f32_16x16x32_bf16 a[140:143], v[224:227], a[56:59], a[140:143]\n"
"ds_read_b128 a[56:59], v247 offset:14336\n"
"buffer_load_dwordx4 v[224:227], v240, s[36:39], s80 offen offset:0 sc1\n"
"v_mfma_f32_16x16x32_bf16 a[140:143], v[228:231], a[60:63], a[140:143]\n"
".Lpad12_%=:\n"
"s_cbranch_execz .Lret1_%=\n"
"s_waitcnt vmcnt(11) lgkmcnt(11)\n"
"ds_read_b128 a[60:63], v248 offset:14336\n"
"v_mfma_f32_16x16x32_bf16 v[0:3], a[64:67], a[0:3], v[0:3]\n"
"v_mfma_f32_16x16x32_bf16 v[0:3], a[68:71], a[4:7], v[0:3]\n"
"v_mfma_f32_16x16x32_bf16 v[4:7], a[64:67], a[8:11], v[4:7]\n"
"v_mfma_f32_16x16x32_bf16 v[4:7], a[68:71], a[12:15], v[4:7]\n"
"s_waitcnt lgkmcnt(8)\n"
"v_mfma_f32_16x16x32_bf16 v[8:11], a[64:67], a[16:19], v[8:11]\n"
"v_mfma_f32_16x16x32_bf16 v[8:11], a[68:71], a[20:23], v[8:11]\n"
"v_mfma_f32_16x16x32_bf16 v[12:15], a[64:67], a[24:27], v[12:15]\n"
"buffer_load_dwordx4 v[228:231], v240, s[36:39], s80 offen offset:1024 sc1\n"
"v_mfma_f32_16x16x32_bf16 v[12:15], a[68:71], a[28:31], v[12:15]\n"
"s_waitcnt lgkmcnt(4)\n"
"v_mfma_f32_16x16x32_bf16 v[16:19], a[64:67], a[32:35], v[16:19]\n"
"v_mfma_f32_16x16x32_bf16 v[16:19], a[68:71], a[36:39], v[16:19]\n"
"v_mfma_f32_16x16x32_bf16 v[20:23], a[64:67], a[40:43], v[20:23]\n"
"v_mfma_f32_16x16x32_bf16 v[20:23], a[68:71], a[44:47], v[20:23]\n"
"s_waitcnt lgkmcnt(0)\n"
"v_mfma_f32_16x16x32_bf16 v[24:27], a[64:67], a[48:51], v[24:27]\n"
"v_mfma_f32_16x16x32_bf16 v[24:27], a[68:71], a[52:55], v[24:27]\n"
"v_mfma_f32_16x16x32_bf16 v[28:31], a[64:67], a[56:59], v[28:31]\n"
"buffer_load_dwordx4 a[64:67], v240, s[36:39], s80 offen offset:2048 sc1\n"
"v_mfma_f32_16x16x32_bf16 v[28:31], a[68:71], a[60:63], v[28:31]\n"
"s_waitcnt vmcnt(11)\n"
"v_mfma_f32_16x16x32_bf16 v[32:35], a[72:75], a[0:3], v[32:35]\n"
"v_mfma_f32_16x16x32_bf16 v[32:35], a[76:79], a[4:7], v[32:35]\n"
"v_mfma_f32_16x16x32_bf16 v[36:39], a[72:75], a[8:11], v[36:39]\n"
"v_mfma_f32_16x16x32_bf16 v[36:39], a[76:79], a[12:15], v[36:39]\n"
"v_mfma_f32_16x16x32_bf16 v[40:43], a[72:75], a[16:19], v[40:43]\n"
"v_mfma_f32_16x16x32_bf16 v[40:43], a[76:79], a[20:23], v[40:43]\n"
"v_mfma_f32_16x16x32_bf16 v[44:47], a[72:75], a[24:27], v[44:47]\n"
"buffer_load_dwordx4 a[68:71], v240, s[36:39], s80 offen offset:3072 sc1\n"
"v_mfma_f32_16x16x32_bf16 v[44:47], a[76:79], a[28:31], v[44:47]\n"
"s_add_u32 s80, s80, 0x1000\n"
"v_mfma_f32_16x16x32_bf16 v[48:51], a[72:75], a[32:35], v[48:51]\n"
"ds_read_b128 v[232:235], v249 offset:14336\n"
"v_mfma_f32_16x16x32_bf16 v[48:51], a[76:79], a[36:39], v[48:51]\n"
"v_mfma_f32_16x16x32_bf16 v[52:55], a[72:75], a[40:43], v[52:55]\n"
"v_mfma_f32_16x16x32_bf16 v[52:55], a[76:79], a[44:47], v[52:55]\n"
"v_mfma_f32_16x16x32_bf16 v[56:59], a[72:75], a[48:51], v[56:59]\n"
"v_mfma_f32_16x16x32_bf16 v[56:59], a[76:79], a[52:55], v[56:59]\n"
"v_mfma_f32_16x16x32_bf16 v[60:63], a[72:75], a[56:59], v[60:63]\n"
"buffer_load_dwordx4 a[72:75], v240, s[36:39], s80 offen offset:0 sc1\n"
"v_mfma_f32_16x16x32_bf16 v[60:63], a[76:79], a[60:63], v[60:63]\n"
"s_waitcnt vmcnt(11)\n"
"v_mfma_f32_16x16x32_bf16 v[64:67], a[80:83], a[0:3], v[64:67]\n"
"v_mfma_f32_16x16x32_bf16 v[64:67], a[84:87], a[4:7], v[64:67]\n"
"v_mfma_f32_16x16x32_bf16 v[68:71], a[80:83], a[8:11], v[68:71]\n"
"v_mfma_f32_16x16x32_bf16 v[68:71], a[84:87], a[12:15], v[68:71]\n"
"v_mfma_f32_16x16x32_bf16 v[72:75], a[80:83], a[16:19], v[72:75]\n"
"s_waitcnt lgkmcnt(0)\n"
"ds_read_b128 v[236:239], v249 offset:0\n"
"v_accvgpr_read_b32 v253, a144\n"
"v_mfma_f32_16x16x32_bf16 v[72:75], a[84:87], a[20:23], v[72:75]\n"
"v_lshlrev_b32 v251, 16, v253\n"
"v_lshlrev_b32 v252, 16, v232\n"
"v_mfma_f32_16x16x32_bf16 v[76:79], a[80:83], a[24:27], v[76:79]\n"
"buffer_load_dwordx4 a[76:79], v240, s[36:39], s80 offen offset:1024 sc1\n"
"v_fmac_f32 v252, v251, v28\n"
"v_mfma_f32_16x16x32_bf16 v[76:79], a[84:87], a[28:31], v[76:79]\n"
"v_and_b32 v253, s82, v253\n"
"v_and_b32 v232, s82, v232\n"
"v_mfma_f32_16x16x32_bf16 v[80:83], a[80:83], a[32:35], v[80:83]\n"
"v_fmac_f32 v232, v253, v29\n"
"v_cvt_pk_bf16_f32 v28, v252, v232\n"
"v_mfma_f32_16x16x32_bf16 v[80:83], a[84:87], a[36:39], v[80:83]\n"
"v_accvgpr_read_b32 v253, a145\n"
"v_lshlrev_b32 v251, 16, v253\n"
"v_mfma_f32_16x16x32_bf16 v[84:87], a[80:83], a[40:43], v[84:87]\n"
"v_lshlrev_b32 v252, 16, v233\n"
"v_fmac_f32 v252, v251, v30\n"
"v_mfma_f32_16x16x32_bf16 v[84:87], a[84:87], a[44:47], v[84:87]\n"
"v_and_b32 v253, s82, v253\n"
"v_and_b32 v233, s82, v233\n"
"v_mfma_f32_16x16x32_bf16 v[88:91], a[80:83], a[48:51], v[88:91]\n"
"v_fmac_f32 v233, v253, v31\n"
"v_cvt_pk_bf16_f32 v29, v252, v233\n"
"v_mfma_f32_16x16x32_bf16 v[88:91], a[84:87], a[52:55], v[88:91]\n"
"v_accvgpr_read_b32 v253, a146\n"
"v_lshlrev_b32 v251, 16, v253\n"
"v_mfma_f32_16x16x32_bf16 v[92:95], a[80:83], a[56:59], v[92:95]\n"
"buffer_load_dwordx4 a[80:83], v240, s[36:39], s80 offen offset:2048 sc1\n"
"v_lshlrev_b32 v252, 16, v234\n"
"v_mfma_f32_16x16x32_bf16 v[92:95], a[84:87], a[60:63], v[92:95]\n"
"s_waitcnt vmcnt(11)\n"
"v_fmac_f32 v252, v251, v60\n"
"v_and_b32 v253, s82, v253\n"
"v_mfma_f32_16x16x32_bf16 v[96:99], a[88:91], a[0:3], v[96:99]\n"
"v_and_b32 v234, s82, v234\n"
"v_fmac_f32 v234, v253, v61\n"
"v_mfma_f32_16x16x32_bf16 v[96:99], a[92:95], a[4:7], v[96:99]\n"
"v_cvt_pk_bf16_f32 v30, v252, v234\n"
"v_accvgpr_read_b32 v253, a147\n"
"v_mfma_f32_16x16x32_bf16 v[100:103], a[88:91], a[8:11], v[100:103]\n"
"v_lshlrev_b32 v251, 16, v253\n"
"v_lshlrev_b32 v252, 16, v235\n"
"v_mfma_f32_16x16x32_bf16 v[100:103], a[92:95], a[12:15], v[100:103]\n"
"v_fmac_f32 v252, v251, v62\n"
"v_and_b32 v253, s82, v253\n"
"v_mfma_f32_16x16x32_bf16 v[104:107], a[88:91], a[16:19], v[104:107]\n"
"v_and_b32 v235, s82, v235\n"
"v_fmac_f32 v235, v253, v63\n"
"v_mfma_f32_16x16x32_bf16 v[104:107], a[92:95], a[20:23], v[104:107]\n"
"v_cvt_pk_bf16_f32 v31, v252, v235\n"
"v_mfma_f32_16x16x32_bf16 v[108:111], a[88:91], a[24:27], v[108:111]\n"
"buffer_load_dwordx4 a[84:87], v240, s[36:39], s80 offen offset:3072 sc1\n"
"v_mfma_f32_16x16x32_bf16 v[108:111], a[92:95], a[28:31], v[108:111]\n"
"s_add_u32 s80, s80, 0x1000\n"
"buffer_store_dwordx4 v[28:31], v240, s[52:55], s79 offen offset:0 sc1\n"
"v_mfma_f32_16x16x32_bf16 v[112:115], a[88:91], a[32:35], v[112:115]\n"
"v_mfma_f32_16x16x32_bf16 v[112:115], a[92:95], a[36:39], v[112:115]\n"
"v_mfma_f32_16x16x32_bf16 v[116:119], a[88:91], a[40:43], v[116:119]\n"
"v_mfma_f32_16x16x32_bf16 v[116:119], a[92:95], a[44:47], v[116:119]\n"
"v_mfma_f32_16x16x32_bf16 v[120:123], a[88:91], a[48:51], v[120:123]\n"
"v_mfma_f32_16x16x32_bf16 v[120:123], a[92:95], a[52:55], v[120:123]\n"
"v_mfma_f32_16x16x32_bf16 v[124:127], a[88:91], a[56:59], v[124:127]\n"
"buffer_load_dwordx4 a[88:91], v240, s[36:39], s80 offen offset:0 sc1\n"
"v_mfma_f32_16x16x32_bf16 v[124:127], a[92:95], a[60:63], v[124:127]\n"
"s_waitcnt vmcnt(12) lgkmcnt(0)\n"
"ds_read_b128 v[232:235], v249 offset:2048\n"
"v_accvgpr_read_b32 v253, a148\n"
"v_mfma_f32_16x16x32_bf16 v[128:131], a[96:99], a[0:3], v[128:131]\n"
"v_lshlrev_b32 v251, 16, v253\n"
"v_lshlrev_b32 v252, 16, v236\n"
"v_mfma_f32_16x16x32_bf16 v[128:131], a[100:103], a[4:7], v[128:131]\n"
"v_fmac_f32 v252, v251, v0\n"
"v_and_b32 v253, s82, v253\n"
"v_mfma_f32_16x16x32_bf16 v[132:135], a[96:99], a[8:11], v[132:135]\n"
"v_and_b32 v236, s82, v236\n"
"v_fmac_f32 v236, v253, v1\n"
"v_mfma_f32_16x16x32_bf16 v[132:135], a[100:103], a[12:15], v[132:135]\n"
"v_cvt_pk_bf16_f32 v0, v252, v236\n"
"v_accvgpr_read_b32 v253, a149\n"
"v_mfma_f32_16x16x32_bf16 v[136:139], a[96:99], a[16:19], v[136:139]\n"
"v_lshlrev_b32 v251, 16, v253\n"
"v_lshlrev_b32 v252, 16, v237\n"
"v_mfma_f32_16x16x32_bf16 v[136:139], a[100:103], a[20:23], v[136:139]\n"
"v_fmac_f32 v252, v251, v2\n"
"v_and_b32 v253, s82, v253\n"
"v_mfma_f32_16x16x32_bf16 v[140:143], a[96:99], a[24:27], v[140:143]\n"
"buffer_load_dwordx4 a[92:95], v240, s[36:39], s80 offen offset:1024 sc1\n"
"v_and_b32 v237, s82, v237\n"
"v_mfma_f32_16x16x32_bf16 v[140:143], a[100:103], a[28:31], v[140:143]\n"
"v_fmac_f32 v237, v253, v3\n"
"v_cvt_pk_bf16_f32 v1, v252, v237\n"
"v_mfma_f32_16x16x32_bf16 v[144:147], a[96:99], a[32:35], v[144:147]\n"
"v_accvgpr_read_b32 v253, a150\n"
"v_lshlrev_b32 v251, 16, v253\n"
"v_mfma_f32_16x16x32_bf16 v[144:147], a[100:103], a[36:39], v[144:147]\n"
"v_lshlrev_b32 v252, 16, v238\n"
"v_fmac_f32 v252, v251, v32\n"
"v_mfma_f32_16x16x32_bf16 v[148:151], a[96:99], a[40:43], v[148:151]\n"
"v_and_b32 v253, s82, v253\n"
"v_and_b32 v238, s82, v238\n"
"v_mfma_f32_16x16x32_bf16 v[148:151], a[100:103], a[44:47], v[148:151]\n"
"v_fmac_f32 v238, v253, v33\n"
"v_cvt_pk_bf16_f32 v2, v252, v238\n"
"v_mfma_f32_16x16x32_bf16 v[152:155], a[96:99], a[48:51], v[152:155]\n"
"v_accvgpr_read_b32 v253, a151\n"
"v_lshlrev_b32 v251, 16, v253\n"
"v_mfma_f32_16x16x32_bf16 v[152:155], a[100:103], a[52:55], v[152:155]\n"
"v_lshlrev_b32 v252, 16, v239\n"
"v_fmac_f32 v252, v251, v34\n"
"v_mfma_f32_16x16x32_bf16 v[156:159], a[96:99], a[56:59], v[156:159]\n"
"buffer_load_dwordx4 a[96:99], v240, s[36:39], s80 offen offset:2048 sc1\n"
"v_and_b32 v253, s82, v253\n"
"v_mfma_f32_16x16x32_bf16 v[156:159], a[100:103], a[60:63], v[156:159]\n"
"s_waitcnt vmcnt(12)\n"
"v_and_b32 v239, s82, v239\n"
"v_fmac_f32 v239, v253, v35\n"
"v_mfma_f32_16x16x32_bf16 v[160:163], a[104:107], a[0:3], v[160:163]\n"
"v_cvt_pk_bf16_f32 v3, v252, v239\n"
"v_mfma_f32_16x16x32_bf16 v[160:163], a[108:111], a[4:7], v[160:163]\n"
"buffer_store_dwordx4 v[0:3], v240, s[52:55], s68 offen offset:0 sc1\n"
"v_mfma_f32_16x16x32_bf16 v[164:167], a[104:107], a[8:11], v[164:167]\n"
"v_mfma_f32_16x16x32_bf16 v[164:167], a[108:111], a[12:15], v[164:167]\n"
"v_mfma_f32_16x16x32_bf16 v[168:171], a[104:107], a[16:19], v[168:171]\n"
"v_mfma_f32_16x16x32_bf16 v[168:171], a[108:111], a[20:23], v[168:171]\n"
"v_mfma_f32_16x16x32_bf16 v[172:175], a[104:107], a[24:27], v[172:175]\n"
"buffer_load_dwordx4 a[100:103], v240, s[36:39], s80 offen offset:3072 sc1\n"
"v_mfma_f32_16x16x32_bf16 v[172:175], a[108:111], a[28:31], v[172:175]\n"
"s_add_u32 s80, s80, 0x1000\n"
"v_mfma_f32_16x16x32_bf16 v[176:179], a[104:107], a[32:35], v[176:179]\n"
"v_mfma_f32_16x16x32_bf16 v[176:179], a[108:111], a[36:39], v[176:179]\n"
"v_mfma_f32_16x16x32_bf16 v[180:183], a[104:107], a[40:43], v[180:183]\n"
"s_waitcnt lgkmcnt(0)\n"
"ds_read_b128 v[236:239], v249 offset:4096\n"
"v_accvgpr_read_b32 v253, a152\n"
"v_mfma_f32_16x16x32_bf16 v[180:183], a[108:111], a[44:47], v[180:183]\n"
"v_lshlrev_b32 v251, 16, v253\n"
"v_lshlrev_b32 v252, 16, v232\n"
"v_mfma_f32_16x16x32_bf16 v[184:187], a[104:107], a[48:51], v[184:187]\n"
"v_fmac_f32 v252, v251, v4\n"
"v_and_b32 v253, s82, v253\n"
"v_mfma_f32_16x16x32_bf16 v[184:187], a[108:111], a[52:55], v[184:187]\n"
"v_and_b32 v232, s82, v232\n"
"v_fmac_f32 v232, v253, v5\n"
"v_mfma_f32_16x16x32_bf16 v[188:191], a[104:107], a[56:59], v[188:191]\n"
"buffer_load_dwordx4 a[104:107], v240, s[36:39], s80 offen offset:0 sc1\n"
"v_cvt_pk_bf16_f32 v4, v252, v232\n"
"v_mfma_f32_16x16x32_bf16 v[188:191], a[108:111], a[60:63], v[188:191]\n"
"s_waitcnt vmcnt(13)\n"
"v_accvgpr_read_b32 v253, a153\n"
"v_lshlrev_b32 v251, 16, v253\n"
"v_mfma_f32_16x16x32_bf16 v[192:195], v[224:227], a[0:3], v[192:195]\n"
"v_lshlrev_b32 v252, 16, v233\n"
"v_fmac_f32 v252, v251, v6\n"
"v_mfma_f32_16x16x32_bf16 v[192:195], v[228:231], a[4:7], v[192:195]\n"
"v_and_b32 v253, s82, v253\n"
"v_and_b32 v233, s82, v233\n"
"v_mfma_f32_16x16x32_bf16 v[196:199], v[224:227], a[8:11], v[196:199]\n"
"v_fmac_f32 v233, v253, v7\n"
"v_cvt_pk_bf16_f32 v5, v252, v233\n"
"v_mfma_f32_16x16x32_bf16 v[196:199], v[228:231], a[12:15], v[196:199]\n"
"v_accvgpr_read_b32 v253, a154\n"
"v_lshlrev_b32 v251, 16, v253\n"
"v_mfma_f32_16x16x32_bf16 v[200:203], v[224:227], a[16:19], v[200:203]\n"
"v_lshlrev_b32 v252, 16, v234\n"
"v_fmac_f32 v252, v251, v36\n"
"v_mfma_f32_16x16x32_bf16 v[200:203], v[228:231], a[20:23], v[200:203]\n"
"v_and_b32 v253, s82, v253\n"
"v_and_b32 v234, s82, v234\n"
"v_mfma_f32_16x16x32_bf16 v[204:207], v[224:227], a[24:27], v[204:207]\n"
"buffer_load_dwordx4 a[108:111], v240, s[36:39], s80 offen offset:1024 sc1\n"
"v_fmac_f32 v234, v253, v37\n"
"v_mfma_f32_16x16x32_bf16 v[204:207], v[228:231], a[28:31], v[204:207]\n"
"v_cvt_pk_bf16_f32 v6, v252, v234\n"
"v_accvgpr_read_b32 v253, a155\n"
"v_mfma_f32_16x16x32_bf16 v[208:211], v[224:227], a[32:35], v[208:211]\n"
"v_lshlrev_b32 v251, 16, v253\n"
"v_lshlrev_b32 v252, 16, v235\n"
"v_mfma_f32_16x16x32_bf16 v[208:211], v[228:231], a[36:39], v[208:211]\n"
"v_fmac_f32 v252, v251, v38\n"
"v_and_b32 v253, s82, v253\n"
"v_mfma_f32_16x16x32_bf16 v[212:215], v[224:227], a[40:43], v[212:215]\n"
"v_and_b32 v235, s82, v235\n"
"v_fmac_f32 v235, v253, v39\n"
"v_mfma_f32_16x16x32_bf16 v[212:215], v[228:231], a[44:47], v[212:215]\n"
"v_cvt_pk_bf16_f32 v7, v252, v235\n"
"v_mfma_f32_16x16x32_bf16 v[216:219], v[224:227], a[48:51], v[216:219]\n"
"v_mfma_f32_16x16x32_bf16 v[216:219], v[228:231], a[52:55], v[216:219]\n"
"v_mfma_f32_16x16x32_bf16 v[220:223], v[224:227], a[56:59], v[220:223]\n"
"buffer_load_dwordx4 v[224:227], v240, s[36:39], s80 offen offset:2048 sc1\n"
"v_mfma_f32_16x16x32_bf16 v[220:223], v[228:231], a[60:63], v[220:223]\n"
"s_waitcnt vmcnt(13)\n"
"buffer_store_dwordx4 v[4:7], v240, s[52:55], s69 offen offset:0 sc1\n"
"v_mfma_f32_16x16x32_bf16 a[112:115], a[64:67], a[0:3], a[112:115]\n"
"ds_read_b128 a[0:3], v243 offset:0\n"
"v_mfma_f32_16x16x32_bf16 a[112:115], a[68:71], a[4:7], a[112:115]\n"
"ds_read_b128 a[4:7], v244 offset:0\n"
"v_mfma_f32_16x16x32_bf16 a[116:119], a[64:67], a[8:11], a[116:119]\n"
"ds_read_b128 a[8:11], v243 offset:2048\n"
"v_mfma_f32_16x16x32_bf16 a[116:119], a[68:71], a[12:15], a[116:119]\n"
"ds_read_b128 a[12:15], v244 offset:2048\n"
"v_mfma_f32_16x16x32_bf16 a[120:123], a[64:67], a[16:19], a[120:123]\n"
"ds_read_b128 a[16:19], v243 offset:4096\n"
"v_mfma_f32_16x16x32_bf16 a[120:123], a[68:71], a[20:23], a[120:123]\n"
"s_waitcnt lgkmcnt(5)\n"
"ds_read_b128 a[20:23], v244 offset:4096\n"
"v_accvgpr_read_b32 v253, a156\n"
"v_mfma_f32_16x16x32_bf16 a[124:127], a[64:67], a[24:27], a[124:127]\n"
"ds_read_b128 a[24:27], v243 offset:6144\n"
"buffer_load_dwordx4 v[228:231], v240, s[36:39], s80 offen offset:3072 sc1\n"
"v_mfma_f32_16x16x32_bf16 a[124:127], a[68:71], a[28:31], a[124:127]\n"
"s_add_u32 s80, s80, 0x1000\n"
"ds_read_b128 a[28:31], v244 offset:6144\n"
"v_lshlrev_b32 v251, 16, v253\n"
"v_mfma_f32_16x16x32_bf16 a[128:131], a[64:67], a[32:35], a[128:131]\n"
"ds_read_b128 a[32:35], v243 offset:8192\n"
"v_lshlrev_b32 v252, 16, v236\n"
"v_mfma_f32_16x16x32_bf16 a[128:131], a[68:71], a[36:39], a[128:131]\n"
"ds_read_b128 a[36:39], v244 offset:8192\n"
"v_fmac_f32 v252, v251, v8\n"
"v_mfma_f32_16x16x32_bf16 a[132:135], a[64:67], a[40:43], a[132:135]\n"
"ds_read_b128 a[40:43], v243 offset:10240\n"
"v_and_b32 v253, s82, v253\n"
"v_mfma_f32_16x16x32_bf16 a[132:135], a[68:71], a[44:47], a[132:135]\n"
"ds_read_b128 a[44:47], v244 offset:10240\n"
"v_and_b32 v236, s82, v236\n"
"v_mfma_f32_16x16x32_bf16 a[136:139], a[64:67], a[48:51], a[136:139]\n"
"ds_read_b128 a[48:51], v243 offset:12288\n"
"v_fmac_f32 v236, v253, v9\n"
"v_mfma_f32_16x16x32_bf16 a[136:139], a[68:71], a[52:55], a[136:139]\n"
"ds_read_b128 a[52:55], v244 offset:12288\n"
"v_cvt_pk_bf16_f32 v8, v252, v236\n"
"v_mfma_f32_16x16x32_bf16 a[140:143], a[64:67], a[56:59], a[140:143]\n"
"ds_read_b128 a[56:59], v243 offset:14336\n"
"buffer_load_dwordx4 a[64:67], v240, s[36:39], s80 offen offset:0 sc1\n"
"v_mfma_f32_16x16x32_bf16 a[140:143], a[68:71], a[60:63], a[140:143]\n"
"s_waitcnt vmcnt(14) lgkmcnt(11)\n"
"ds_read_b128 a[60:63], v244 offset:14336\n"
"v_accvgpr_read_b32 v253, a157\n"
"v_mfma_f32_16x16x32_bf16 v[128:131], a[72:75], a[0:3], v[128:131]\n"
"ds_read_b128 v[232:235], v249 offset:6144\n"
"v_lshlrev_b32 v251, 16, v253\n"
"v_mfma_f32_16x16x32_bf16 v[128:131], a[76:79], a[4:7], v[128:131]\n"
"v_lshlrev_b32 v252, 16, v237\n"
"v_fmac_f32 v252, v251, v10\n"
"v_mfma_f32_16x16x32_bf16 v[132:135], a[72:75], a[8:11], v[132:135]\n"
"v_and_b32 v253, s82, v253\n"
"v_and_b32 v237, s82, v237\n"
"v_mfma_f32_16x16x32_bf16 v[132:135], a[76:79], a[12:15], v[132:135]\n"
"s_waitcnt lgkmcnt(9)\n"
"v_fmac_f32 v237, v253, v11\n"
"v_cvt_pk_bf16_f32 v9, v252, v237\n"
"v_mfma_f32_16x16x32_bf16 v[136:139], a[72:75], a[16:19], v[136:139]\n"
"v_accvgpr_read_b32 v253, a158\n"
"v_lshlrev_b32 v251, 16, v253\n"
"v_mfma_f32_16x16x32_bf16 v[136:139], a[76:79], a[20:23], v[136:139]\n"
"v_lshlrev_b32 v252, 16, v238\n"
"v_fmac_f32 v252, v251, v40\n"
"v_mfma_f32_16x16x32_bf16 v[140:143], a[72:75], a[24:27], v[140:143]\n"
"buffer_load_dwordx4 a[68:71], v240, s[36:39], s80 offen offset:1024 sc1\n"
"v_and_b32 v253, s82, v253\n"
"v_mfma_f32_16x16x32_bf16 v[140:143], a[76:79], a[28:31], v[140:143]\n"
"s_waitcnt lgkmcnt(5)\n"
"v_and_b32 v238, s82, v238\n"
"v_fmac_f32 v238, v253, v41\n"
"v_mfma_f32_16x16x32_bf16 v[144:147], a[72:75], a[32:35], v[144:147]\n"
"v_cvt_pk_bf16_f32 v10, v252, v238\n"
"v_accvgpr_read_b32 v253, a159\n"
"v_mfma_f32_16x16x32_bf16 v[144:147], a[76:79], a[36:39], v[144:147]\n"
"v_lshlrev_b32 v251, 16, v253\n"
"v_lshlrev_b32 v252, 16, v239\n"
"v_mfma_f32_16x16x32_bf16 v[148:151], a[72:75], a[40:43], v[148:151]\n"
"v_fmac_f32 v252, v251, v42\n"
"v_and_b32 v253, s82, v253\n"
"v_mfma_f32_16x16x32_bf16 v[148:151], a[76:79], a[44:47], v[148:151]\n"
"s_waitcnt lgkmcnt(1)\n"
"v_and_b32 v239, s82, v239\n"
"v_fmac_f32 v239, v253, v43\n"
"v_mfma_f32_16x16x32_bf16 v[152:155], a[72:75], a[48:51], v[152:155]\n"
"v_cvt_pk_bf16_f32 v11, v252, v239\n"
"v_mfma_f32_16x16x32_bf16 v[152:155], a[76:79], a[52:55], v[152:155]\n"
"v_mfma_f32_16x16x32_bf16 v[156:159], a[72:75], a[56:59], v[156:159]\n"
"buffer_load_dwordx4 a[72:75], v240, s[36:39], s80 offen offset:2048 sc1\n"
"v_mfma_f32_16x16x32_bf16 v[156:159], a[76:79], a[60:63], v[156:159]\n"
"s_waitcnt vmcnt(14)\n"
"buffer_store_dwordx4 v[8:11], v240, s[52:55], s70 offen offset:0 sc1\n"
"v_mfma_f32_16x16x32_bf16 v[160:163], a[80:83], a[0:3], v[160:163]\n"
"s_waitcnt lgkmcnt(0)\n"
"ds_read_b128 v[236:239], v249 offset:8192\n"
"v_accvgpr_read_b32 v253, a160\n"
"v_mfma_f32_16x16x32_bf16 v[160:163], a[84:87], a[4:7], v[160:163]\n"
"v_lshlrev_b32 v251, 16, v253\n"
"v_lshlrev_b32 v252, 16, v232\n"
"v_mfma_f32_16x16x32_bf16 v[164:167], a[80:83], a[8:11], v[164:167]\n"
"v_fmac_f32 v252, v251, v12\n"
"v_and_b32 v253, s82, v253\n"
"v_mfma_f32_16x16x32_bf16 v[164:167], a[84:87], a[12:15], v[164:167]\n"
"v_and_b32 v232, s82, v232\n"
"v_fmac_f32 v232, v253, v13\n"
"v_mfma_f32_16x16x32_bf16 v[168:171], a[80:83], a[16:19], v[168:171]\n"
"v_cvt_pk_bf16_f32 v12, v252, v232\n"
"v_accvgpr_read_b32 v253, a161\n"
"v_mfma_f32_16x16x32_bf16 v[168:171], a[84:87], a[20:23], v[168:171]\n"
"v_lshlrev_b32 v251, 16, v253\n"
"v_lshlrev_b32 v252, 16, v233\n"
"v_mfma_f32_16x16x32_bf16 v[172:175], a[80:83], a[24:27], v[172:175]\n"
"buffer_load_dwordx4 a[76:79], v240, s[36:39], s80 offen offset:3072 sc1\n"
"v_fmac_f32 v252, v251, v14\n"
"v_mfma_f32_16x16x32_bf16 v[172:175], a[84:87], a[28:31], v[172:175]\n"
"s_add_u32 s80, s80, 0x1000\n"
"v_and_b32 v253, s82, v253\n"
"v_and_b32 v233, s82, v233\n"
"v_mfma_f32_16x16x32_bf16 v[176:179], a[80:83], a[32:35], v[176:179]\n"
"v_fmac_f32 v233, v253, v15\n"
"v_cvt_pk_bf16_f32 v13, v252, v233\n"
"v_mfma_f32_16x16x32_bf16 v[176:179], a[84:87], a[36:39], v[176:179]\n"
"v_accvgpr_read_b32 v253, a162\n"
"v_lshlrev_b32 v251, 16, v253\n"
"v_mfma_f32_16x16x32_bf16 v[180:183], a[80:83], a[40:43], v[180:183]\n"
"v_lshlrev_b32 v252, 16, v234\n"
"v_fmac_f32 v252, v251, v44\n"
"v_mfma_f32_16x16x32_bf16 v[180:183], a[84:87], a[44:47], v[180:183]\n"
"v_and_b32 v253, s82, v253\n"
"v_and_b32 v234, s82, v234\n"
"v_mfma_f32_16x16x32_bf16 v[184:187], a[80:83], a[48:51], v[184:187]\n"
"v_fmac_f32 v234, v253, v45\n"
"v_cvt_pk_bf16_f32 v14, v252, v234\n"
"v_mfma_f32_16x16x32_bf16 v[184:187], a[84:87], a[52:55], v[184:187]\n"
"v_accvgpr_read_b32 v253, a163\n"
"v_lshlrev_b32 v251, 16, v253\n"
"v_mfma_f32_16x16x32_bf16 v[188:191], a[80:83], a[56:59], v[188:191]\n"
"buffer_load_dwordx4 a[80:83], v240, s[36:39], s80 offen offset:0 sc1\n"
"v_lshlrev_b32 v252, 16, v235\n"
"v_mfma_f32_16x16x32_bf16 v[188:191], a[84:87], a[60:63], v[188:191]\n"
"s_waitcnt vmcnt(14)\n"
"v_fmac_f32 v252, v251, v46\n"
"v_and_b32 v253, s82, v253\n"
"v_mfma_f32_16x16x32_bf16 v[192:195], a[88:91], a[0:3], v[192:195]\n"
"v_and_b32 v235, s82, v235\n"
"v_fmac_f32 v235, v253, v47\n"
"v_mfma_f32_16x16x32_bf16 v[192:195], a[92:95], a[4:7], v[192:195]\n"
"v_cvt_pk_bf16_f32 v15, v252, v235\n"
"v_mfma_f32_16x16x32_bf16 v[196:199], a[88:91], a[8:11], v[196:199]\n"
"buffer_store_dwordx4 v[12:15], v240, s[52:55], s71 offen offset:0 sc1\n"
"v_mfma_f32_16x16x32_bf16 v[196:199], a[92:95], a[12:15], v[196:199]\n"
"v_mfma_f32_16x16x32_bf16 v[200:203], a[88:91], a[16:19], v[200:203]\n"
"v_mfma_f32_16x16x32_bf16 v[200:203], a[92:95], a[20:23], v[200:203]\n"
"v_mfma_f32_16x16x32_bf16 v[204:207], a[88:91], a[24:27], v[204:207]\n"
"buffer_load_dwordx4 a[84:87], v240, s[36:39], s80 offen offset:1024 sc1\n"
"v_mfma_f32_16x16x32_bf16 v[204:207], a[92:95], a[28:31], v[204:207]\n"
"v_mfma_f32_16x16x32_bf16 v[208:211], a[88:91], a[32:35], v[208:211]\n"
"v_mfma_f32_16x16x32_bf16 v[208:211], a[92:95], a[36:39], v[208:211]\n"
"v_mfma_f32_16x16x32_bf16 v[212:215], a[88:91], a[40:43], v[212:215]\n"
"v_mfma_f32_16x16x32_bf16 v[212:215], a[92:95], a[44:47], v[212:215]\n"
"s_waitcnt lgkmcnt(0)\n"
"ds_read_b128 v[232:235], v249 offset:10240\n"
"v_accvgpr_read_b32 v253, a164\n"
"v_mfma_f32_16x16x32_bf16 v[216:219], a[88:91], a[48:51], v[216:219]\n"
"v_lshlrev_b32 v251, 16, v253\n"
"v_lshlrev_b32 v252, 16, v236\n"
"v_mfma_f32_16x16x32_bf16 v[216:219], a[92:95], a[52:55], v[216:219]\n"
"v_fmac_f32 v252, v251, v16\n"
"v_and_b32 v253, s82, v253\n"
"v_mfma_f32_16x16x32_bf16 v[220:223], a[88:91], a[56:59], v[220:223]\n"
"buffer_load_dwordx4 a[88:91], v240, s[36:39], s80 offen offset:2048 sc1\n"
"v_and_b32 v236, s82, v236\n"
"v_mfma_f32_16x16x32_bf16 v[220:223], a[92:95], a[60:63], v[220:223]\n"
"s_waitcnt vmcnt(14)\n"
"v_fmac_f32 v236, v253, v17\n"
"v_cvt_pk_bf16_f32 v16, v252, v236\n"
"v_mfma_f32_16x16x32_bf16 a[112:115], a[96:99], a[0:3], a[112:115]\n"
"ds_read_b128 a[0:3], v243 offset:16384\n"
"v_accvgpr_read_b32 v253, a165\n"
"v_mfma_f32_16x16x32_bf16 a[112:115], a[100:103], a[4:7], a[112:115]\n"
"ds_read_b128 a[4:7], v244 offset:16384\n"
"v_lshlrev_b32 v251, 16, v253\n"
"v_mfma_f32_16x16x32_bf16 a[116:119], a[96:99], a[8:11], a[116:119]\n"
"ds_read_b128 a[8:11], v243 offset:18432\n"
"v_lshlrev_b32 v252, 16, v237\n"
"v_mfma_f32_16x16x32_bf16 a[116:119], a[100:103], a[12:15], a[116:119]\n"
"ds_read_b128 a[12:15], v244 offset:18432\n"
"v_fmac_f32 v252, v251, v18\n"
"v_mfma_f32_16x16x32_bf16 a[120:123], a[96:99], a[16:19], a[120:123]\n"
"ds_read_b128 a[16:19], v243 offset:20480\n"
"v_and_b32 v253, s82, v253\n"
"v_mfma_f32_16x16x32_bf16 a[120:123], a[100:103], a[20:23], a[120:123]\n"
"ds_read_b128 a[20:23], v244 offset:20480\n"
"v_and_b32 v237, s82, v237\n"
"v_mfma_f32_16x16x32_bf16 a[124:127], a[96:99], a[24:27], a[124:127]\n"
"ds_read_b128 a[24:27], v243 offset:22528\n"
"buffer_load_dwordx4 a[92:95], v240, s[36:39], s80 offen offset:3072 sc1\n"
"v_mfma_f32_16x16x32_bf16 a[124:127], a[100:103], a[28:31], a[124:127]\n"
"s_add_u32 s80, s80, 0x1000\n"
"ds_read_b128 a[28:31], v244 offset:22528\n"
"v_fmac_f32 v237, v253, v19\n"
"v_mfma_f32_16x16x32_bf16 a[128:131], a[96:99], a[32:35], a[128:131]\n"
"ds_read_b128 a[32:35], v243 offset:24576\n"
"v_cvt_pk_bf16_f32 v17, v252, v237\n"
"v_mfma_f32_16x16x32_bf16 a[128:131], a[100:103], a[36:39], a[128:131]\n"
"ds_read_b128 a[36:39], v244 offset:24576\n"
"v_accvgpr_read_b32 v253, a166\n"
"v_mfma_f32_16x16x32_bf16 a[132:135], a[96:99], a[40:43], a[132:135]\n"
"ds_read_b128 a[40:43], v243 offset:26624\n"
"v_lshlrev_b32 v251, 16, v253\n"
"v_mfma_f32_16x16x32_bf16 a[132:135], a[100:103], a[44:47], a[132:135]\n"
"ds_read_b128 a[44:47], v244 offset:26624\n"
"v_lshlrev_b32 v252, 16, v238\n"
"v_mfma_f32_16x16x32_bf16 a[136:139], a[96:99], a[48:51], a[136:139]\n"
"ds_read_b128 a[48:51], v243 offset:28672\n"
"v_fmac_f32 v252, v251, v48\n"
"v_mfma_f32_16x16x32_bf16 a[136:139], a[100:103], a[52:55], a[136:139]\n"
"ds_read_b128 a[52:55], v244 offset:28672\n"
"v_and_b32 v253, s82, v253\n"
"v_mfma_f32_16x16x32_bf16 a[140:143], a[96:99], a[56:59], a[140:143]\n"
"ds_read_b128 a[56:59], v243 offset:30720\n"
"buffer_load_dwordx4 a[96:99], v240, s[36:39], s80 offen offset:0 sc1\n"
"v_mfma_f32_16x16x32_bf16 a[140:143], a[100:103], a[60:63], a[140:143]\n"
"s_waitcnt vmcnt(14) lgkmcnt(11)\n"
"ds_read_b128 a[60:63], v244 offset:30720\n"
"v_and_b32 v238, s82, v238\n"
"v_mfma_f32_16x16x32_bf16 v[128:131], a[104:107], a[0:3], v[128:131]\n"
"v_fmac_f32 v238, v253, v49\n"
"v_cvt_pk_bf16_f32 v18, v252, v238\n"
"v_mfma_f32_16x16x32_bf16 v[128:131], a[108:111], a[4:7], v[128:131]\n"
"v_accvgpr_read_b32 v253, a167\n"
"v_lshlrev_b32 v251, 16, v253\n"
"v_mfma_f32_16x16x32_bf16 v[132:135], a[104:107], a[8:11], v[132:135]\n"
"v_lshlrev_b32 v252, 16, v239\n"
"v_fmac_f32 v252, v251, v50\n"
"v_mfma_f32_16x16x32_bf16 v[132:135], a[108:111], a[12:15], v[132:135]\n"
"s_waitcnt lgkmcnt(8)\n"
"v_and_b32 v253, s82, v253\n"
"v_and_b32 v239, s82, v239\n"
"v_mfma_f32_16x16x32_bf16 v[136:139], a[104:107], a[16:19], v[136:139]\n"
"v_fmac_f32 v239, v253, v51\n"
"v_cvt_pk_bf16_f32 v19, v252, v239\n"
"v_mfma_f32_16x16x32_bf16 v[136:139], a[108:111], a[20:23], v[136:139]\n"
"v_mfma_f32_16x16x32_bf16 v[140:143], a[104:107], a[24:27], v[140:143]\n"
"ds_read_b128 v[236:239], v249 offset:12288\n"
"buffer_load_dwordx4 a[100:103], v240, s[36:39], s80 offen offset:1024 sc1\n"
"v_mfma_f32_16x16x32_bf16 v[140:143], a[108:111], a[28:31], v[140:143]\n"
"s_waitcnt lgkmcnt(5)\n"
"buffer_store_dwordx4 v[16:19], v240, s[52:55], s76 offen offset:0 sc1\n"
"v_mfma_f32_16x16x32_bf16 v[144:147], a[104:107], a[32:35], v[144:147]\n"
"v_accvgpr_read_b32 v253, a168\n"
"v_lshlrev_b32 v251, 16, v253\n"
"v_mfma_f32_16x16x32_bf16 v[144:147], a[108:111], a[36:39], v[144:147]\n"
"s_waitcnt lgkmcnt(1)\n"
"s_barrier\n"
"v_lshlrev_b32 v252, 16, v232\n"
"v_mfma_f32_16x16x32_bf16 v[148:151], a[104:107], a[40:43], v[148:151]\n"
"v_fmac_f32 v252, v251, v20\n"
"v_and_b32 v253, s82, v253\n"
"v_mfma_f32_16x16x32_bf16 v[148:151], a[108:111], a[44:47], v[148:151]\n"
"v_and_b32 v232, s82, v232\n"
"v_fmac_f32 v232, v253, v21\n"
"v_mfma_f32_16x16x32_bf16 v[152:155], a[104:107], a[48:51], v[152:155]\n"
"v_cvt_pk_bf16_f32 v20, v252, v232\n"
"v_accvgpr_read_b32 v253, a169\n"
"v_mfma_f32_16x16x32_bf16 v[152:155], a[108:111], a[52:55], v[152:155]\n"
"v_lshlrev_b32 v251, 16, v253\n"
"v_lshlrev_b32 v252, 16, v233\n"
"v_mfma_f32_16x16x32_bf16 v[156:159], a[104:107], a[56:59], v[156:159]\n"
"buffer_load_dwordx4 a[104:107], v240, s[36:39], s80 offen offset:2048 sc1\n"
"v_fmac_f32 v252, v251, v22\n"
"v_mfma_f32_16x16x32_bf16 v[156:159], a[108:111], a[60:63], v[156:159]\n"
"s_waitcnt vmcnt(14)\n"
"v_and_b32 v253, s82, v253\n"
"v_and_b32 v233, s82, v233\n"
"v_mfma_f32_16x16x32_bf16 v[160:163], v[224:227], a[0:3], v[160:163]\n"
"v_fmac_f32 v233, v253, v23\n"
"v_cvt_pk_bf16_f32 v21, v252, v233\n"
"v_mfma_f32_16x16x32_bf16 v[160:163], v[228:231], a[4:7], v[160:163]\n"
"v_accvgpr_read_b32 v253, a170\n"
"v_lshlrev_b32 v251, 16, v253\n"
"v_mfma_f32_16x16x32_bf16 v[164:167], v[224:227], a[8:11], v[164:167]\n"
"v_lshlrev_b32 v252, 16, v234\n"
"v_fmac_f32 v252, v251, v52\n"
"v_mfma_f32_16x16x32_bf16 v[164:167], v[228:231], a[12:15], v[164:167]\n"
"v_and_b32 v253, s82, v253\n"
"v_and_b32 v234, s82, v234\n"
"v_mfma_f32_16x16x32_bf16 v[168:171], v[224:227], a[16:19], v[168:171]\n"
"v_fmac_f32 v234, v253, v53\n"
"v_cvt_pk_bf16_f32 v22, v252, v234\n"
"v_mfma_f32_16x16x32_bf16 v[168:171], v[228:231], a[20:23], v[168:171]\n"
"v_accvgpr_read_b32 v253, a171\n"
"v_lshlrev_b32 v251, 16, v253\n"
"v_mfma_f32_16x16x32_bf16 v[172:175], v[224:227], a[24:27], v[172:175]\n"
"buffer_load_dwordx4 a[108:111], v240, s[36:39], s80 offen offset:3072 sc1\n"
"v_lshlrev_b32 v252, 16, v235\n"
"v_mfma_f32_16x16x32_bf16 v[172:175], v[228:231], a[28:31], v[172:175]\n"
"s_add_u32 s80, s80, 0x1000\n"
"v_fmac_f32 v252, v251, v54\n"
"v_and_b32 v253, s82, v253\n"
"v_mfma_f32_16x16x32_bf16 v[176:179], v[224:227], a[32:35], v[176:179]\n"
"v_and_b32 v235, s82, v235\n"
"v_fmac_f32 v235, v253, v55\n"
"v_mfma_f32_16x16x32_bf16 v[176:179], v[228:231], a[36:39], v[176:179]\n"
"v_cvt_pk_bf16_f32 v23, v252, v235\n"
"v_mfma_f32_16x16x32_bf16 v[180:183], v[224:227], a[40:43], v[180:183]\n"
"buffer_store_dwordx4 v[20:23], v240, s[52:55], s77 offen offset:0 sc1\n"
"v_mfma_f32_16x16x32_bf16 v[180:183], v[228:231], a[44:47], v[180:183]\n"
"ds_write_b128 v249, v[28:31] offset:14336\n"
"v_mfma_f32_16x16x32_bf16 v[184:187], v[224:227], a[48:51], v[184:187]\n"
"v_mfma_f32_16x16x32_bf16 v[184:187], v[228:231], a[52:55], v[184:187]\n"
"v_mfma_f32_16x16x32_bf16 v[188:191], v[224:227], a[56:59], v[188:191]\n"
"buffer_load_dwordx4 v[224:227], v240, s[36:39], s80 offen offset:0 sc1\n"
"v_mfma_f32_16x16x32_bf16 v[188:191], v[228:231], a[60:63], v[188:191]\n"
"s_waitcnt vmcnt(15)\n"
"v_mfma_f32_16x16x32_bf16 v[192:195], a[64:67], a[0:3], v[192:195]\n"
"ds_write_b128 v249, v[0:3] offset:0\n"
"v_mfma_f32_16x16x32_bf16 v[192:195], a[68:71], a[4:7], v[192:195]\n"
"s_waitcnt lgkmcnt(2)\n"
"ds_read_b128 v[232:235], v249 offset:47104\n"
"v_accvgpr_read_b32 v253, a172\n"
"v_mfma_f32_16x16x32_bf16 v[196:199], a[64:67], a[8:11], v[196:199]\n"
"v_lshlrev_b32 v251, 16, v253\n"
"v_lshlrev_b32 v252, 16, v236\n"
"v_mfma_f32_16x16x32_bf16 v[196:199], a[68:71], a[12:15], v[196:199]\n"
"v_fmac_f32 v252, v251, v24\n"
"v_and_b32 v253, s82, v253\n"
"v_mfma_f32_16x16x32_bf16 v[200:203], a[64:67], a[16:19], v[200:203]\n"
"v_and_b32 v236, s82, v236\n"
"v_fmac_f32 v236, v253, v25\n"
"v_mfma_f32_16x16x32_bf16 v[200:203], a[68:71], a[20:23], v[200:203]\n"
"ds_write_b128 v249, v[4:7] offset:2048\n"
"v_mfma_f32_16x16x32_bf16 v[204:207], a[64:67], a[24:27], v[204:207]\n"
"buffer_load_dwordx4 v[228:231], v240, s[36:39], s80 offen offset:1024 sc1\n"
"v_cvt_pk_bf16_f32 v24, v252, v236\n"
"v_mfma_f32_16x16x32_bf16 v[204:207], a[68:71], a[28:31], v[204:207]\n"
"v_accvgpr_read_b32 v253, a173\n"
"v_lshlrev_b32 v251, 16, v253\n"
"v_mfma_f32_16x16x32_bf16 v[208:211], a[64:67], a[32:35], v[208:211]\n"
"v_lshlrev_b32 v252, 16, v237\n"
"v_fmac_f32 v252, v251, v26\n"
"v_mfma_f32_16x16x32_bf16 v[208:211], a[68:71], a[36:39], v[208:211]\n"
"v_and_b32 v253, s82, v253\n"
"v_and_b32 v237, s82, v237\n"
"v_mfma_f32_16x16x32_bf16 v[212:215], a[64:67], a[40:43], v[212:215]\n"
"ds_write_b128 v249, v[8:11] offset:4096\n"
"v_mfma_f32_16x16x32_bf16 v[212:215], a[68:71], a[44:47], v[212:215]\n"
"v_fmac_f32 v237, v253, v27\n"
"v_cvt_pk_bf16_f32 v25, v252, v237\n"
"v_mfma_f32_16x16x32_bf16 v[216:219], a[64:67], a[48:51], v[216:219]\n"
"v_accvgpr_read_b32 v253, a174\n"
"v_lshlrev_b32 v251, 16, v253\n"
"v_mfma_f32_16x16x32_bf16 v[216:219], a[68:71], a[52:55], v[216:219]\n"
"v_lshlrev_b32 v252, 16, v238\n"
"v_fmac_f32 v252, v251, v56\n"
"v_mfma_f32_16x16x32_bf16 v[220:223], a[64:67], a[56:59], v[220:223]\n"
"buffer_load_dwordx4 a[64:67], v240, s[36:39], s80 offen offset:2048 sc1\n"
"v_and_b32 v253, s82, v253\n"
"v_mfma_f32_16x16x32_bf16 v[220:223], a[68:71], a[60:63], v[220:223]\n"
"s_waitcnt vmcnt(14)\n"
"ds_write_b128 v249, v[12:15] offset:6144\n"
"v_mfma_f32_16x16x32_bf16 a[112:115], a[72:75], a[0:3], a[112:115]\n"
"ds_read_b128 a[0:3], v243 offset:32768\n"
"v_and_b32 v238, s82, v238\n"
"v_mfma_f32_16x16x32_bf16 a[112:115], a[76:79], a[4:7], a[112:115]\n"
"ds_read_b128 a[4:7], v244 offset:32768\n"
"v_fmac_f32 v238, v253, v57\n"
"v_mfma_f32_16x16x32_bf16 a[116:119], a[72:75], a[8:11], a[116:119]\n"
"ds_read_b128 a[8:11], v243 offset:34816\n"
"v_cvt_pk_bf16_f32 v26, v252, v238\n"
"v_mfma_f32_16x16x32_bf16 a[116:119], a[76:79], a[12:15], a[116:119]\n"
"ds_read_b128 a[12:15], v244 offset:34816\n"
"v_accvgpr_read_b32 v253, a175\n"
"v_mfma_f32_16x16x32_bf16 a[120:123], a[72:75], a[16:19], a[120:123]\n"
".Lpad13_%=:\n"
"s_cbranch_execz .Lret1_%=\n"
"ds_read_b128 a[16:19], v243 offset:36864\n"
"v_lshlrev_b32 v251, 16, v253\n"
"v_mfma_f32_16x16x32_bf16 a[120:123], a[76:79], a[20:23], a[120:123]\n"
"ds_read_b128 a[20:23], v244 offset:36864\n"
"v_lshlrev_b32 v252, 16, v239\n"
"v_mfma_f32_16x16x32_bf16 a[124:127], a[72:75], a[24:27], a[124:127]\n"
"ds_read_b128 a[24:27], v243 offset:38912\n"
"buffer_load_dwordx4 a[68:71], v240, s[36:39], s80 offen offset:3072 sc1\n"
"v_mfma_f32_16x16x32_bf16 a[124:127], a[76:79], a[28:31], a[124:127]\n"
"s_add_u32 s80, s80, 0x1000\n"
"ds_read_b128 a[28:31], v244 offset:38912\n"
"v_fmac_f32 v252, v251, v58\n"
"v_mfma_f32_16x16x32_bf16 a[128:131], a[72:75], a[32:35], a[128:131]\n"
"ds_read_b128 a[32:35], v243 offset:40960\n"
"v_and_b32 v253, s82, v253\n"
"v_mfma_f32_16x16x32_bf16 a[128:131], a[76:79], a[36:39], a[128:131]\n"
"ds_read_b128 a[36:39], v244 offset:40960\n"
"v_and_b32 v239, s82, v239\n"
"v_mfma_f32_16x16x32_bf16 a[132:135], a[72:75], a[40:43], a[132:135]\n"
"ds_read_b128 a[40:43], v243 offset:43008\n"
"v_fmac_f32 v239, v253, v59\n"
"v_mfma_f32_16x16x32_bf16 a[132:135], a[76:79], a[44:47], a[132:135]\n"
"ds_read_b128 a[44:47], v244 offset:43008\n"
"v_cvt_pk_bf16_f32 v27, v252, v239\n"
"v_mfma_f32_16x16x32_bf16 a[136:139], a[72:75], a[48:51], a[136:139]\n"
"s_waitcnt lgkmcnt(15)\n"
"ds_read_b128 a[48:51], v243 offset:45056\n"
"v_accvgpr_read_b32 v253, a176\n"
"v_mfma_f32_16x16x32_bf16 a[136:139], a[76:79], a[52:55], a[136:139]\n"
"ds_read_b128 a[52:55], v244 offset:45056\n"
"v_lshlrev_b32 v251, 16, v253\n"
"v_mfma_f32_16x16x32_bf16 a[140:143], a[72:75], a[56:59], a[140:143]\n"
"ds_read_b128 a[56:59], v243 offset:47104\n"
"buffer_load_dwordx4 a[72:75], v240, s[36:39], s80 offen offset:0 sc1\n"
"v_mfma_f32_16x16x32_bf16 a[140:143], a[76:79], a[60:63], a[140:143]\n"
"s_waitcnt vmcnt(13) lgkmcnt(11)\n"
"ds_read_b128 a[60:63], v244 offset:47104\n"
"v_lshlrev_b32 v252, 16, v232\n"
"v_mfma_f32_16x16x32_bf16 v[128:131], a[80:83], a[0:3], v[128:131]\n"
"ds_write_b128 v249, v[16:19] offset:8192\n"
"v_mfma_f32_16x16x32_bf16 v[128:131], a[84:87], a[4:7], v[128:131]\n"
"buffer_store_dwordx4 v[24:27], v240, s[52:55], s78 offen offset:0 sc1\n"
"v_mfma_f32_16x16x32_bf16 v[132:135], a[80:83], a[8:11], v[132:135]\n"
"ds_read_b128 v[236:239], v249 offset:32768\n"
"v_fmac_f32 v252, v251, v92\n"
"v_mfma_f32_16x16x32_bf16 v[132:135], a[84:87], a[12:15], v[132:135]\n"
"s_waitcnt lgkmcnt(10)\n"
"v_and_b32 v253, s82, v253\n"
"v_and_b32 v232, s82, v232\n"
"v_mfma_f32_16x16x32_bf16 v[136:139], a[80:83], a[16:19], v[136:139]\n"
"v_fmac_f32 v232, v253, v93\n"
"v_cvt_pk_bf16_f32 v92, v252, v232\n"
"v_mfma_f32_16x16x32_bf16 v[136:139], a[84:87], a[20:23], v[136:139]\n"
"ds_write_b128 v249, v[20:23] offset:10240\n"
"v_mfma_f32_16x16x32_bf16 v[140:143], a[80:83], a[24:27], v[140:143]\n"
"buffer_load_dwordx4 a[76:79], v240, s[36:39], s80 offen offset:1024 sc1\n"
"v_accvgpr_read_b32 v253, a177\n"
"v_mfma_f32_16x16x32_bf16 v[140:143], a[84:87], a[28:31], v[140:143]\n"
"s_waitcnt lgkmcnt(7)\n"
"v_lshlrev_b32 v251, 16, v253\n"
"v_lshlrev_b32 v252, 16, v233\n"
"v_mfma_f32_16x16x32_bf16 v[144:147], a[80:83], a[32:35], v[144:147]\n"
"v_fmac_f32 v252, v251, v94\n"
"v_and_b32 v253, s82, v253\n"
"v_mfma_f32_16x16x32_bf16 v[144:147], a[84:87], a[36:39], v[144:147]\n"
"v_and_b32 v233, s82, v233\n"
"v_fmac_f32 v233, v253, v95\n"
"v_mfma_f32_16x16x32_bf16 v[148:151], a[80:83], a[40:43], v[148:151]\n"
"ds_write_b128 v249, v[24:27] offset:12288\n"
"v_mfma_f32_16x16x32_bf16 v[148:151], a[84:87], a[44:47], v[148:151]\n"
"s_waitcnt lgkmcnt(4)\n"
"v_cvt_pk_bf16_f32 v93, v252, v233\n"
"v_accvgpr_read_b32 v253, a178\n"
"v_mfma_f32_16x16x32_bf16 v[152:155], a[80:83], a[48:51], v[152:155]\n"
"v_lshlrev_b32 v251, 16, v253\n"
"v_lshlrev_b32 v252, 16, v234\n"
"v_mfma_f32_16x16x32_bf16 v[152:155], a[84:87], a[52:55], v[152:155]\n"
"v_fmac_f32 v252, v251, v124\n"
"v_and_b32 v253, s82, v253\n"
"v_mfma_f32_16x16x32_bf16 v[156:159], a[80:83], a[56:59], v[156:159]\n"
"buffer_load_dwordx4 a[80:83], v240, s[36:39], s80 offen offset:2048 sc1\n"
"v_and_b32 v234, s82, v234\n"
"v_mfma_f32_16x16x32_bf16 v[156:159], a[84:87], a[60:63], v[156:159]\n"
"s_waitcnt vmcnt(14)\n"
"v_fmac_f32 v234, v253, v125\n"
"v_cvt_pk_bf16_f32 v94, v252, v234\n"
"v_mfma_f32_16x16x32_bf16 v[160:163], a[88:91], a[0:3], v[160:163]\n"
"v_accvgpr_read_b32 v253, a179\n"
"v_lshlrev_b32 v251, 16, v253\n"
"v_mfma_f32_16x16x32_bf16 v[160:163], a[92:95], a[4:7], v[160:163]\n"
"v_lshlrev_b32 v252, 16, v235\n"
"v_fmac_f32 v252, v251, v126\n"
"v_mfma_f32_16x16x32_bf16 v[164:167], a[88:91], a[8:11], v[164:167]\n"
"v_and_b32 v253, s82, v253\n"
"v_and_b32 v235, s82, v235\n"
"v_mfma_f32_16x16x32_bf16 v[164:167], a[92:95], a[12:15], v[164:167]\n"
"v_fmac_f32 v235, v253, v127\n"
"v_cvt_pk_bf16_f32 v95, v252, v235\n"
"v_mfma_f32_16x16x32_bf16 v[168:171], a[88:91], a[16:19], v[168:171]\n"
"v_lshlrev_b32 v241, 5, v240\n"
"v_mfma_f32_16x16x32_bf16 v[168:171], a[92:95], a[20:23], v[168:171]\n"
"global_load_dword v241, v241, s[34:35]\n"
"v_mfma_f32_16x16x32_bf16 v[172:175], a[88:91], a[24:27], v[172:175]\n"
"buffer_load_dwordx4 a[84:87], v240, s[36:39], s80 offen offset:3072 sc1\n"
"v_mfma_f32_16x16x32_bf16 v[172:175], a[92:95], a[28:31], v[172:175]\n"
"s_add_u32 s80, s80, 0x1000\n"
"buffer_store_dwordx4 v[92:95], v240, s[52:55], s79 offen offset:1024 sc1\n"
"v_mfma_f32_16x16x32_bf16 v[176:179], a[88:91], a[32:35], v[176:179]\n"
"s_waitcnt lgkmcnt(2)\n"
"ds_read_b128 v[232:235], v249 offset:34816\n"
"v_accvgpr_read_b32 v253, a180\n"
"v_mfma_f32_16x16x32_bf16 v[176:179], a[92:95], a[36:39], v[176:179]\n"
"v_lshlrev_b32 v251, 16, v253\n"
"v_lshlrev_b32 v252, 16, v236\n"
"v_mfma_f32_16x16x32_bf16 v[180:183], a[88:91], a[40:43], v[180:183]\n"
"v_fmac_f32 v252, v251, v64\n"
"v_and_b32 v253, s82, v253\n"
"v_mfma_f32_16x16x32_bf16 v[180:183], a[92:95], a[44:47], v[180:183]\n"
"v_and_b32 v236, s82, v236\n"
"v_fmac_f32 v236, v253, v65\n"
"v_mfma_f32_16x16x32_bf16 v[184:187], a[88:91], a[48:51], v[184:187]\n"
"v_cvt_pk_bf16_f32 v64, v252, v236\n"
"v_accvgpr_read_b32 v253, a181\n"
"v_mfma_f32_16x16x32_bf16 v[184:187], a[92:95], a[52:55], v[184:187]\n"
"v_lshlrev_b32 v251, 16, v253\n"
"v_lshlrev_b32 v252, 16, v237\n"
"v_mfma_f32_16x16x32_bf16 v[188:191], a[88:91], a[56:59], v[188:191]\n"
"buffer_load_dwordx4 a[88:91], v240, s[36:39], s80 offen offset:0 sc1\n"
"v_fmac_f32 v252, v251, v66\n"
"v_mfma_f32_16x16x32_bf16 v[188:191], a[92:95], a[60:63], v[188:191]\n"
"s_waitcnt vmcnt(16)\n"
"v_and_b32 v253, s82, v253\n"
"v_and_b32 v237, s82, v237\n"
"v_mfma_f32_16x16x32_bf16 v[192:195], a[96:99], a[0:3], v[192:195]\n"
"v_fmac_f32 v237, v253, v67\n"
"v_cvt_pk_bf16_f32 v65, v252, v237\n"
"v_mfma_f32_16x16x32_bf16 v[192:195], a[100:103], a[4:7], v[192:195]\n"
"v_accvgpr_read_b32 v253, a182\n"
"v_lshlrev_b32 v251, 16, v253\n"
"v_mfma_f32_16x16x32_bf16 v[196:199], a[96:99], a[8:11], v[196:199]\n"
"v_lshlrev_b32 v252, 16, v238\n"
"v_fmac_f32 v252, v251, v96\n"
"v_mfma_f32_16x16x32_bf16 v[196:199], a[100:103], a[12:15], v[196:199]\n"
"v_and_b32 v253, s82, v253\n"
"v_and_b32 v238, s82, v238\n"
"v_mfma_f32_16x16x32_bf16 v[200:203], a[96:99], a[16:19], v[200:203]\n"
"v_fmac_f32 v238, v253, v97\n"
"v_cvt_pk_bf16_f32 v66, v252, v238\n"
"v_mfma_f32_16x16x32_bf16 v[200:203], a[100:103], a[20:23], v[200:203]\n"
"v_accvgpr_read_b32 v253, a183\n"
"v_lshlrev_b32 v251, 16, v253\n"
"v_mfma_f32_16x16x32_bf16 v[204:207], a[96:99], a[24:27], v[204:207]\n"
"buffer_load_dwordx4 a[92:95], v240, s[36:39], s80 offen offset:1024 sc1\n"
"v_lshlrev_b32 v252, 16, v239\n"
"v_mfma_f32_16x16x32_bf16 v[204:207], a[100:103], a[28:31], v[204:207]\n"
"v_fmac_f32 v252, v251, v98\n"
"v_and_b32 v253, s82, v253\n"
"v_mfma_f32_16x16x32_bf16 v[208:211], a[96:99], a[32:35], v[208:211]\n"
"v_and_b32 v239, s82, v239\n"
"v_fmac_f32 v239, v253, v99\n"
"v_mfma_f32_16x16x32_bf16 v[208:211], a[100:103], a[36:39], v[208:211]\n"
"v_cvt_pk_bf16_f32 v67, v252, v239\n"
"v_mfma_f32_16x16x32_bf16 v[212:215], a[96:99], a[40:43], v[212:215]\n"
"buffer_store_dwordx4 v[64:67], v240, s[52:55], s68 offen offset:1024 sc1\n"
"v_mfma_f32_16x16x32_bf16 v[212:215], a[100:103], a[44:47], v[212:215]\n"
"v_mfma_f32_16x16x32_bf16 v[216:219], a[96:99], a[48:51], v[216:219]\n"
"v_mfma_f32_16x16x32_bf16 v[216:219], a[100:103], a[52:55], v[216:219]\n"
"v_mfma_f32_16x16x32_bf16 v[220:223], a[96:99], a[56:59], v[220:223]\n"
"buffer_load_dwordx4 a[96:99], v240, s[36:39], s80 offen offset:2048 sc1\n"
"v_mfma_f32_16x16x32_bf16 v[220:223], a[100:103], a[60:63], v[220:223]\n"
"s_waitcnt vmcnt(16)\n"
"v_mfma_f32_16x16x32_bf16 a[112:115], a[104:107], a[0:3], a[112:115]\n"
"ds_read_b128 a[0:3], v243 offset:49152\n"
"v_mfma_f32_16x16x32_bf16 a[112:115], a[108:111], a[4:7], a[112:115]\n"
"ds_read_b128 a[4:7], v244 offset:49152\n"
"v_mfma_f32_16x16x32_bf16 a[116:119], a[104:107], a[8:11], a[116:119]\n"
"s_waitcnt lgkmcnt(2)\n"
"ds_read_b128 a[8:11], v243 offset:51200\n"
"v_accvgpr_read_b32 v253, a184\n"
"v_mfma_f32_16x16x32_bf16 a[116:119], a[108:111], a[12:15], a[116:119]\n"
"ds_read_b128 a[12:15], v244 offset:51200\n"
"v_lshlrev_b32 v251, 16, v253\n"
"v_mfma_f32_16x16x32_bf16 a[120:123], a[104:107], a[16:19], a[120:123]\n"
"ds_read_b128 a[16:19], v243 offset:53248\n"
"v_lshlrev_b32 v252, 16, v232\n"
"v_mfma_f32_16x16x32_bf16 a[120:123], a[108:111], a[20:23], a[120:123]\n"
"ds_read_b128 a[20:23], v244 offset:53248\n"
"v_fmac_f32 v252, v251, v68\n"
"v_mfma_f32_16x16x32_bf16 a[124:127], a[104:107], a[24:27], a[124:127]\n"
"ds_read_b128 a[24:27], v243 offset:55296\n"
"buffer_load_dwordx4 a[100:103], v240, s[36:39], s80 offen offset:3072 sc1\n"
"v_mfma_f32_16x16x32_bf16 a[124:127], a[108:111], a[28:31], a[124:127]\n"
"s_add_u32 s80, s80, 0x1000\n"
"ds_read_b128 a[28:31], v244 offset:55296\n"
"v_and_b32 v253, s82, v253\n"
"v_mfma_f32_16x16x32_bf16 a[128:131], a[104:107], a[32:35], a[128:131]\n"
"ds_read_b128 a[32:35], v243 offset:57344\n"
"v_and_b32 v232, s82, v232\n"
"v_mfma_f32_16x16x32_bf16 a[128:131], a[108:111], a[36:39], a[128:131]\n"
"ds_read_b128 a[36:39], v244 offset:57344\n"
"v_fmac_f32 v232, v253, v69\n"
"v_mfma_f32_16x16x32_bf16 a[132:135], a[104:107], a[40:43], a[132:135]\n"
"ds_read_b128 a[40:43], v243 offset:59392\n"
"v_cvt_pk_bf16_f32 v68, v252, v232\n"
"v_mfma_f32_16x16x32_bf16 a[132:135], a[108:111], a[44:47], a[132:135]\n"
"ds_read_b128 a[44:47], v244 offset:59392\n"
"v_accvgpr_read_b32 v253, a185\n"
"v_mfma_f32_16x16x32_bf16 a[136:139], a[104:107], a[48:51], a[136:139]\n"
"ds_read_b128 a[48:51], v243 offset:61440\n"
"v_lshlrev_b32 v251, 16, v253\n"
"v_mfma_f32_16x16x32_bf16 a[136:139], a[108:111], a[52:55], a[136:139]\n"
"ds_read_b128 a[52:55], v244 offset:61440\n"
"v_lshlrev_b32 v252, 16, v233\n"
"v_mfma_f32_16x16x32_bf16 a[140:143], a[104:107], a[56:59], a[140:143]\n"
"ds_read_b128 a[56:59], v243 offset:63488\n"
"buffer_load_dwordx4 a[104:107], v240, s[36:39], s80 offen offset:0 sc1\n"
"v_mfma_f32_16x16x32_bf16 a[140:143], a[108:111], a[60:63], a[140:143]\n"
"s_waitcnt vmcnt(15) lgkmcnt(11)\n"
"ds_read_b128 a[60:63], v244 offset:63488\n"
"v_fmac_f32 v252, v251, v70\n"
"v_mfma_f32_16x16x32_bf16 v[128:131], v[224:227], a[0:3], v[128:131]\n"
"ds_read_b128 v[236:239], v249 offset:36864\n"
"v_and_b32 v253, s82, v253\n"
"v_mfma_f32_16x16x32_bf16 v[128:131], v[228:231], a[4:7], v[128:131]\n"
"v_and_b32 v233, s82, v233\n"
"v_fmac_f32 v233, v253, v71\n"
"v_mfma_f32_16x16x32_bf16 v[132:135], v[224:227], a[8:11], v[132:135]\n"
"v_cvt_pk_bf16_f32 v69, v252, v233\n"
"v_accvgpr_read_b32 v253, a186\n"
"v_mfma_f32_16x16x32_bf16 v[132:135], v[228:231], a[12:15], v[132:135]\n"
"s_waitcnt lgkmcnt(9)\n"
"v_lshlrev_b32 v251, 16, v253\n"
"v_lshlrev_b32 v252, 16, v234\n"
"v_mfma_f32_16x16x32_bf16 v[136:139], v[224:227], a[16:19], v[136:139]\n"
"v_fmac_f32 v252, v251, v100\n"
"v_and_b32 v253, s82, v253\n"
"v_mfma_f32_16x16x32_bf16 v[136:139], v[228:231], a[20:23], v[136:139]\n"
"v_and_b32 v234, s82, v234\n"
"v_fmac_f32 v234, v253, v101\n"
"v_mfma_f32_16x16x32_bf16 v[140:143], v[224:227], a[24:27], v[140:143]\n"
"buffer_load_dwordx4 a[108:111], v240, s[36:39], s80 offen offset:1024 sc1\n"
"v_cvt_pk_bf16_f32 v70, v252, v234\n"
"v_mfma_f32_16x16x32_bf16 v[140:143], v[228:231], a[28:31], v[140:143]\n"
"s_waitcnt lgkmcnt(5)\n"
"v_accvgpr_read_b32 v253, a187\n"
"v_lshlrev_b32 v251, 16, v253\n"
"v_mfma_f32_16x16x32_bf16 v[144:147], v[224:227], a[32:35], v[144:147]\n"
"v_lshlrev_b32 v252, 16, v235\n"
"v_fmac_f32 v252, v251, v102\n"
"v_mfma_f32_16x16x32_bf16 v[144:147], v[228:231], a[36:39], v[144:147]\n"
"s_waitcnt lgkmcnt(1)\n"
"s_barrier\n"
"v_and_b32 v253, s82, v253\n"
"v_mfma_f32_16x16x32_bf16 v[148:151], v[224:227], a[40:43], v[148:151]\n"
"v_and_b32 v235, s82, v235\n"
"v_fmac_f32 v235, v253, v103\n"
"v_mfma_f32_16x16x32_bf16 v[148:151], v[228:231], a[44:47], v[148:151]\n"
"v_cvt_pk_bf16_f32 v71, v252, v235\n"
"v_mfma_f32_16x16x32_bf16 v[152:155], v[224:227], a[48:51], v[152:155]\n"
"ds_write_b128 v249, v[92:95] offset:47104\n"
"v_mfma_f32_16x16x32_bf16 v[152:155], v[228:231], a[52:55], v[152:155]\n"
"s_waitcnt lgkmcnt(1)\n"
"ds_read_b128 v[232:235], v249 offset:38912\n"
"v_accvgpr_read_b32 v253, a188\n"
"v_mfma_f32_16x16x32_bf16 v[156:159], v[224:227], a[56:59], v[156:159]\n"
"buffer_load_dwordx4 v[224:227], v240, s[36:39], s80 offen offset:2048 sc1\n"
"v_lshlrev_b32 v251, 16, v253\n"
"v_mfma_f32_16x16x32_bf16 v[156:159], v[228:231], a[60:63], v[156:159]\n"
"s_waitcnt vmcnt(15)\n"
"buffer_store_dwordx4 v[68:71], v240, s[52:55], s69 offen offset:1024 sc1\n"
"v_mfma_f32_16x16x32_bf16 v[160:163], a[64:67], a[0:3], v[160:163]\n"
"v_lshlrev_b32 v252, 16, v236\n"
"v_fmac_f32 v252, v251, v72\n"
"v_mfma_f32_16x16x32_bf16 v[160:163], a[68:71], a[4:7], v[160:163]\n"
"ds_write_b128 v249, v[64:67] offset:32768\n"
"v_mfma_f32_16x16x32_bf16 v[164:167], a[64:67], a[8:11], v[164:167]\n"
"v_and_b32 v253, s82, v253\n"
"v_and_b32 v236, s82, v236\n"
"v_mfma_f32_16x16x32_bf16 v[164:167], a[68:71], a[12:15], v[164:167]\n"
"v_fmac_f32 v236, v253, v73\n"
"v_cvt_pk_bf16_f32 v72, v252, v236\n"
"v_mfma_f32_16x16x32_bf16 v[168:171], a[64:67], a[16:19], v[168:171]\n"
"v_accvgpr_read_b32 v253, a189\n"
"v_lshlrev_b32 v251, 16, v253\n"
"v_mfma_f32_16x16x32_bf16 v[168:171], a[68:71], a[20:23], v[168:171]\n"
"v_lshlrev_b32 v252, 16, v237\n"
"v_fmac_f32 v252, v251, v74\n"
"v_mfma_f32_16x16x32_bf16 v[172:175], a[64:67], a[24:27], v[172:175]\n"
"buffer_load_dwordx4 v[228:231], v240, s[36:39], s80 offen offset:3072 sc1\n"
"v_and_b32 v253, s82, v253\n"
"v_mfma_f32_16x16x32_bf16 v[172:175], a[68:71], a[28:31], v[172:175]\n"
"s_add_u32 s80, s80, 0x1000\n"
"ds_write_b128 v249, v[68:71] offset:34816\n"
"v_mfma_f32_16x16x32_bf16 v[176:179], a[64:67], a[32:35], v[176:179]\n"
"v_and_b32 v237, s82, v237\n"
"v_fmac_f32 v237, v253, v75\n"
"v_mfma_f32_16x16x32_bf16 v[176:179], a[68:71], a[36:39], v[176:179]\n"
"v_cvt_pk_bf16_f32 v73, v252, v237\n"
"v_accvgpr_read_b32 v253, a190\n"
"v_mfma_f32_16x16x32_bf16 v[180:183], a[64:67], a[40:43], v[180:183]\n"
"v_lshlrev_b32 v251, 16, v253\n"
"v_lshlrev_b32 v252, 16, v238\n"
"v_mfma_f32_16x16x32_bf16 v[180:183], a[68:71], a[44:47], v[180:183]\n"
"v_fmac_f32 v252, v251, v104\n"
"v_and_b32 v253, s82, v253\n"
"v_mfma_f32_16x16x32_bf16 v[184:187], a[64:67], a[48:51], v[184:187]\n"
"v_and_b32 v238, s82, v238\n"
"v_fmac_f32 v238, v253, v105\n"
"v_mfma_f32_16x16x32_bf16 v[184:187], a[68:71], a[52:55], v[184:187]\n"
"v_cvt_pk_bf16_f32 v74, v252, v238\n"
"v_accvgpr_read_b32 v253, a191\n"
"v_mfma_f32_16x16x32_bf16 v[188:191], a[64:67], a[56:59], v[188:191]\n"
"buffer_load_dwordx4 a[64:67], v240, s[36:39], s80 offen offset:0 sc1\n"
"v_lshlrev_b32 v251, 16, v253\n"
"v_mfma_f32_16x16x32_bf16 v[188:191], a[68:71], a[60:63], v[188:191]\n"
"s_waitcnt vmcnt(15)\n"
"v_lshlrev_b32 v252, 16, v239\n"
"v_fmac_f32 v252, v251, v106\n"
"v_mfma_f32_16x16x32_bf16 v[192:195], a[72:75], a[0:3], v[192:195]\n"
"v_and_b32 v253, s82, v253\n"
"v_and_b32 v239, s82, v239\n"
"v_mfma_f32_16x16x32_bf16 v[192:195], a[76:79], a[4:7], v[192:195]\n"
"v_fmac_f32 v239, v253, v107\n"
"v_cvt_pk_bf16_f32 v75, v252, v239\n"
"v_mfma_f32_16x16x32_bf16 v[196:199], a[72:75], a[8:11], v[196:199]\n"
"buffer_store_dwordx4 v[72:75], v240, s[52:55], s70 offen offset:1024 sc1\n"
"v_mfma_f32_16x16x32_bf16 v[196:199], a[76:79], a[12:15], v[196:199]\n"
"ds_write_b128 v249, v[72:75] offset:36864\n"
"v_mfma_f32_16x16x32_bf16 v[200:203], a[72:75], a[16:19], v[200:203]\n"
"v_mfma_f32_16x16x32_bf16 v[200:203], a[76:79], a[20:23], v[200:203]\n"
"v_mfma_f32_16x16x32_bf16 v[204:207], a[72:75], a[24:27], v[204:207]\n"
"buffer_load_dwordx4 a[68:71], v240, s[36:39], s80 offen offset:1024 sc1\n"
"v_mfma_f32_16x16x32_bf16 v[204:207], a[76:79], a[28:31], v[204:207]\n"
"v_mfma_f32_16x16x32_bf16 v[208:211], a[72:75], a[32:35], v[208:211]\n"
"s_waitcnt lgkmcnt(3)\n"
"ds_read_b128 v[236:239], v249 offset:40960\n"
"v_accvgpr_read_b32 v253, a192\n"
"v_mfma_f32_16x16x32_bf16 v[208:211], a[76:79], a[36:39], v[208:211]\n"
"v_lshlrev_b32 v251, 16, v253\n"
"v_lshlrev_b32 v252, 16, v232\n"
"v_mfma_f32_16x16x32_bf16 v[212:215], a[72:75], a[40:43], v[212:215]\n"
"v_fmac_f32 v252, v251, v76\n"
"v_and_b32 v253, s82, v253\n"
"v_mfma_f32_16x16x32_bf16 v[212:215], a[76:79], a[44:47], v[212:215]\n"
"v_and_b32 v232, s82, v232\n"
"v_fmac_f32 v232, v253, v77\n"
"v_mfma_f32_16x16x32_bf16 v[216:219], a[72:75], a[48:51], v[216:219]\n"
"v_cvt_pk_bf16_f32 v76, v252, v232\n"
"v_accvgpr_read_b32 v253, a193\n"
"v_mfma_f32_16x16x32_bf16 v[216:219], a[76:79], a[52:55], v[216:219]\n"
"v_lshlrev_b32 v251, 16, v253\n"
"v_lshlrev_b32 v252, 16, v233\n"
"v_mfma_f32_16x16x32_bf16 v[220:223], a[72:75], a[56:59], v[220:223]\n"
"buffer_load_dwordx4 a[72:75], v240, s[36:39], s80 offen offset:2048 sc1\n"
"v_fmac_f32 v252, v251, v78\n"
"v_mfma_f32_16x16x32_bf16 v[220:223], a[76:79], a[60:63], v[220:223]\n"
"s_waitcnt vmcnt(15)\n"
"v_and_b32 v253, s82, v253\n"
"v_and_b32 v233, s82, v233\n"
"v_mfma_f32_16x16x32_bf16 a[112:115], a[80:83], a[0:3], a[112:115]\n"
"ds_read_b128 a[0:3], v245 offset:0\n"
"v_fmac_f32 v233, v253, v79\n"
"v_mfma_f32_16x16x32_bf16 a[112:115], a[84:87], a[4:7], a[112:115]\n"
"ds_read_b128 a[4:7], v246 offset:0\n"
"v_cvt_pk_bf16_f32 v77, v252, v233\n"
"v_mfma_f32_16x16x32_bf16 a[116:119], a[80:83], a[8:11], a[116:119]\n"
"ds_read_b128 a[8:11], v245 offset:2048\n"
"v_accvgpr_read_b32 v253, a194\n"
"v_mfma_f32_16x16x32_bf16 a[116:119], a[84:87], a[12:15], a[116:119]\n"
"ds_read_b128 a[12:15], v246 offset:2048\n"
"v_lshlrev_b32 v251, 16, v253\n"
"v_mfma_f32_16x16x32_bf16 a[120:123], a[80:83], a[16:19], a[120:123]\n"
"ds_read_b128 a[16:19], v245 offset:4096\n"
"v_lshlrev_b32 v252, 16, v234\n"
"v_mfma_f32_16x16x32_bf16 a[120:123], a[84:87], a[20:23], a[120:123]\n"
"ds_read_b128 a[20:23], v246 offset:4096\n"
"v_fmac_f32 v252, v251, v108\n"
"v_mfma_f32_16x16x32_bf16 a[124:127], a[80:83], a[24:27], a[124:127]\n"
"ds_read_b128 a[24:27], v245 offset:6144\n"
"buffer_load_dwordx4 a[76:79], v240, s[36:39], s80 offen offset:3072 sc1\n"
"v_mfma_f32_16x16x32_bf16 a[124:127], a[84:87], a[28:31], a[124:127]\n"
"s_add_u32 s80, s80, 0x1000\n"
"ds_read_b128 a[28:31], v246 offset:6144\n"
"v_and_b32 v253, s82, v253\n"
"v_mfma_f32_16x16x32_bf16 a[128:131], a[80:83], a[32:35], a[128:131]\n"
"ds_read_b128 a[32:35], v245 offset:8192\n"
"v_and_b32 v234, s82, v234\n"
"v_mfma_f32_16x16x32_bf16 a[128:131], a[84:87], a[36:39], a[128:131]\n"
"ds_read_b128 a[36:39], v246 offset:8192\n"
"v_fmac_f32 v234, v253, v109\n"
"v_mfma_f32_16x16x32_bf16 a[132:135], a[80:83], a[40:43], a[132:135]\n"
"ds_read_b128 a[40:43], v245 offset:10240\n"
"v_cvt_pk_bf16_f32 v78, v252, v234\n"
"v_mfma_f32_16x16x32_bf16 a[132:135], a[84:87], a[44:47], a[132:135]\n"
"ds_read_b128 a[44:47], v246 offset:10240\n"
"v_accvgpr_read_b32 v253, a195\n"
"v_mfma_f32_16x16x32_bf16 a[136:139], a[80:83], a[48:51], a[136:139]\n"
"ds_read_b128 a[48:51], v245 offset:12288\n"
"v_lshlrev_b32 v251, 16, v253\n"
"v_mfma_f32_16x16x32_bf16 a[136:139], a[84:87], a[52:55], a[136:139]\n"
"ds_read_b128 a[52:55], v246 offset:12288\n"
"v_lshlrev_b32 v252, 16, v235\n"
"v_mfma_f32_16x16x32_bf16 a[140:143], a[80:83], a[56:59], a[140:143]\n"
"ds_read_b128 a[56:59], v245 offset:14336\n"
"buffer_load_dwordx4 a[80:83], v240, s[36:39], s80 offen offset:0 sc1\n"
"v_mfma_f32_16x16x32_bf16 a[140:143], a[84:87], a[60:63], a[140:143]\n"
"s_waitcnt vmcnt(14) lgkmcnt(11)\n"
"ds_read_b128 a[60:63], v246 offset:14336\n"
"v_fmac_f32 v252, v251, v110\n"
"v_mfma_f32_16x16x32_bf16 v[128:131], a[88:91], a[0:3], v[128:131]\n"
"v_and_b32 v253, s82, v253\n"
"v_and_b32 v235, s82, v235\n"
"v_mfma_f32_16x16x32_bf16 v[128:131], a[92:95], a[4:7], v[128:131]\n"
"v_fmac_f32 v235, v253, v111\n"
"v_cvt_pk_bf16_f32 v79, v252, v235\n"
"v_mfma_f32_16x16x32_bf16 v[132:135], a[88:91], a[8:11], v[132:135]\n"
"buffer_store_dwordx4 v[76:79], v240, s[52:55], s71 offen offset:1024 sc1\n"
"v_mfma_f32_16x16x32_bf16 v[132:135], a[92:95], a[12:15], v[132:135]\n"
"s_waitcnt lgkmcnt(8)\n"
"ds_write_b128 v249, v[76:79] offset:38912\n"
"v_mfma_f32_16x16x32_bf16 v[136:139], a[88:91], a[16:19], v[136:139]\n"
"ds_read_b128 v[232:235], v249 offset:43008\n"
"v_accvgpr_read_b32 v253, a196\n"
"v_mfma_f32_16x16x32_bf16 v[136:139], a[92:95], a[20:23], v[136:139]\n"
"v_lshlrev_b32 v251, 16, v253\n"
"v_lshlrev_b32 v252, 16, v236\n"
"v_mfma_f32_16x16x32_bf16 v[140:143], a[88:91], a[24:27], v[140:143]\n"
"buffer_load_dwordx4 a[84:87], v240, s[36:39], s80 offen offset:1024 sc1\n"
"v_fmac_f32 v252, v251, v80\n"
"v_mfma_f32_16x16x32_bf16 v[140:143], a[92:95], a[28:31], v[140:143]\n"
"s_waitcnt lgkmcnt(6)\n"
"v_and_b32 v253, s82, v253\n"
"v_and_b32 v236, s82, v236\n"
"v_mfma_f32_16x16x32_bf16 v[144:147], a[88:91], a[32:35], v[144:147]\n"
"v_fmac_f32 v236, v253, v81\n"
"v_cvt_pk_bf16_f32 v80, v252, v236\n"
"v_mfma_f32_16x16x32_bf16 v[144:147], a[92:95], a[36:39], v[144:147]\n"
"v_accvgpr_read_b32 v253, a197\n"
"v_lshlrev_b32 v251, 16, v253\n"
"v_mfma_f32_16x16x32_bf16 v[148:151], a[88:91], a[40:43], v[148:151]\n"
"v_lshlrev_b32 v252, 16, v237\n"
"v_fmac_f32 v252, v251, v82\n"
"v_mfma_f32_16x16x32_bf16 v[148:151], a[92:95], a[44:47], v[148:151]\n"
"s_waitcnt lgkmcnt(2)\n"
"v_and_b32 v253, s82, v253\n"
"v_and_b32 v237, s82, v237\n"
"v_mfma_f32_16x16x32_bf16 v[152:155], a[88:91], a[48:51], v[152:155]\n"
"v_fmac_f32 v237, v253, v83\n"
"v_cvt_pk_bf16_f32 v81, v252, v237\n"
"v_mfma_f32_16x16x32_bf16 v[152:155], a[92:95], a[52:55], v[152:155]\n"
"v_accvgpr_read_b32 v253, a198\n"
"v_lshlrev_b32 v251, 16, v253\n"
"v_mfma_f32_16x16x32_bf16 v[156:159], a[88:91], a[56:59], v[156:159]\n"
"buffer_load_dwordx4 a[88:91], v240, s[36:39], s80 offen offset:2048 sc1\n"
"v_lshlrev_b32 v252, 16, v238\n"
"v_mfma_f32_16x16x32_bf16 v[156:159], a[92:95], a[60:63], v[156:159]\n"
"s_waitcnt vmcnt(14)\n"
"v_fmac_f32 v252, v251, v112\n"
"v_and_b32 v253, s82, v253\n"
"v_mfma_f32_16x16x32_bf16 v[160:163], a[96:99], a[0:3], v[160:163]\n"
"v_and_b32 v238, s82, v238\n"
"v_fmac_f32 v238, v253, v113\n"
"v_mfma_f32_16x16x32_bf16 v[160:163], a[100:103], a[4:7], v[160:163]\n"
"v_cvt_pk_bf16_f32 v82, v252, v238\n"
"v_accvgpr_read_b32 v253, a199\n"
"v_mfma_f32_16x16x32_bf16 v[164:167], a[96:99], a[8:11], v[164:167]\n"
"v_lshlrev_b32 v251, 16, v253\n"
"v_lshlrev_b32 v252, 16, v239\n"
"v_mfma_f32_16x16x32_bf16 v[164:167], a[100:103], a[12:15], v[164:167]\n"
"v_fmac_f32 v252, v251, v114\n"
"v_and_b32 v253, s82, v253\n"
"v_mfma_f32_16x16x32_bf16 v[168:171], a[96:99], a[16:19], v[168:171]\n"
"v_and_b32 v239, s82, v239\n"
"v_fmac_f32 v239, v253, v115\n"
"v_mfma_f32_16x16x32_bf16 v[168:171], a[100:103], a[20:23], v[168:171]\n"
"v_cvt_pk_bf16_f32 v83, v252, v239\n"
"v_mfma_f32_16x16x32_bf16 v[172:175], a[96:99], a[24:27], v[172:175]\n"
"buffer_load_dwordx4 a[92:95], v240, s[36:39], s80 offen offset:3072 sc1\n"
"v_mfma_f32_16x16x32_bf16 v[172:175], a[100:103], a[28:31], v[172:175]\n"
"s_add_u32 s80, s80, 0x1000\n"
"buffer_store_dwordx4 v[80:83], v240, s[52:55], s76 offen offset:1024 sc1\n"
"v_mfma_f32_16x16x32_bf16 v[176:179], a[96:99], a[32:35], v[176:179]\n"
"ds_write_b128 v249, v[80:83] offset:40960\n"
"v_mfma_f32_16x16x32_bf16 v[176:179], a[100:103], a[36:39], v[176:179]\n"
"v_mfma_f32_16x16x32_bf16 v[180:183], a[96:99], a[40:43], v[180:183]\n"
"v_mfma_f32_16x16x32_bf16 v[180:183], a[100:103], a[44:47], v[180:183]\n"
"v_mfma_f32_16x16x32_bf16 v[184:187], a[96:99], a[48:51], v[184:187]\n"
"v_mfma_f32_16x16x32_bf16 v[184:187], a[100:103], a[52:55], v[184:187]\n"
"v_mfma_f32_16x16x32_bf16 v[188:191], a[96:99], a[56:59], v[188:191]\n"
"ds_read_b128 v[236:239], v249 offset:45056\n"
"buffer_load_dwordx4 a[96:99], v240, s[36:39], s80 offen offset:0 sc1\n"
"v_mfma_f32_16x16x32_bf16 v[188:191], a[100:103], a[60:63], v[188:191]\n"
"s_waitcnt vmcnt(15) lgkmcnt(2)\n"
"v_accvgpr_read_b32 v253, a200\n"
"v_lshlrev_b32 v251, 16, v253\n"
"v_mfma_f32_16x16x32_bf16 v[192:195], a[104:107], a[0:3], v[192:195]\n"
"v_lshlrev_b32 v252, 16, v232\n"
"v_fmac_f32 v252, v251, v84\n"
"v_mfma_f32_16x16x32_bf16 v[192:195], a[108:111], a[4:7], v[192:195]\n"
"v_and_b32 v253, s82, v253\n"
"v_and_b32 v232, s82, v232\n"
"v_mfma_f32_16x16x32_bf16 v[196:199], a[104:107], a[8:11], v[196:199]\n"
"v_fmac_f32 v232, v253, v85\n"
"v_cvt_pk_bf16_f32 v84, v252, v232\n"
"v_mfma_f32_16x16x32_bf16 v[196:199], a[108:111], a[12:15], v[196:199]\n"
"v_accvgpr_read_b32 v253, a201\n"
"v_lshlrev_b32 v251, 16, v253\n"
"v_mfma_f32_16x16x32_bf16 v[200:203], a[104:107], a[16:19], v[200:203]\n"
"v_lshlrev_b32 v252, 16, v233\n"
"v_fmac_f32 v252, v251, v86\n"
"v_mfma_f32_16x16x32_bf16 v[200:203], a[108:111], a[20:23], v[200:203]\n"
"v_and_b32 v253, s82, v253\n"
"v_and_b32 v233, s82, v233\n"
"v_mfma_f32_16x16x32_bf16 v[204:207], a[104:107], a[24:27], v[204:207]\n"
"buffer_load_dwordx4 a[100:103], v240, s[36:39], s80 offen offset:1024 sc1\n"
"v_fmac_f32 v233, v253, v87\n"
"v_mfma_f32_16x16x32_bf16 v[204:207], a[108:111], a[28:31], v[204:207]\n"
"v_cvt_pk_bf16_f32 v85, v252, v233\n"
"v_accvgpr_read_b32 v253, a202\n"
"v_mfma_f32_16x16x32_bf16 v[208:211], a[104:107], a[32:35], v[208:211]\n"
"v_lshlrev_b32 v251, 16, v253\n"
"v_lshlrev_b32 v252, 16, v234\n"
"v_mfma_f32_16x16x32_bf16 v[208:211], a[108:111], a[36:39], v[208:211]\n"
"v_fmac_f32 v252, v251, v116\n"
"v_and_b32 v253, s82, v253\n"
"v_mfma_f32_16x16x32_bf16 v[212:215], a[104:107], a[40:43], v[212:215]\n"
"v_and_b32 v234, s82, v234\n"
"v_fmac_f32 v234, v253, v117\n"
"v_mfma_f32_16x16x32_bf16 v[212:215], a[108:111], a[44:47], v[212:215]\n"
"v_cvt_pk_bf16_f32 v86, v252, v234\n"
"v_accvgpr_read_b32 v253, a203\n"
"v_mfma_f32_16x16x32_bf16 v[216:219], a[104:107], a[48:51], v[216:219]\n"
"v_lshlrev_b32 v251, 16, v253\n"
"v_lshlrev_b32 v252, 16, v235\n"
"v_mfma_f32_16x16x32_bf16 v[216:219], a[108:111], a[52:55], v[216:219]\n"
"v_fmac_f32 v252, v251, v118\n"
"v_and_b32 v253, s82, v253\n"
"v_mfma_f32_16x16x32_bf16 v[220:223], a[104:107], a[56:59], v[220:223]\n"
"buffer_load_dwordx4 a[104:107], v240, s[36:39], s80 offen offset:2048 sc1\n"
"v_and_b32 v235, s82, v235\n"
"v_mfma_f32_16x16x32_bf16 v[220:223], a[108:111], a[60:63], v[220:223]\n"
"s_waitcnt vmcnt(14)\n"
"v_fmac_f32 v235, v253, v119\n"
"v_cvt_pk_bf16_f32 v87, v252, v235\n"
"v_mfma_f32_16x16x32_bf16 a[112:115], v[224:227], a[0:3], a[112:115]\n"
"ds_read_b128 a[0:3], v245 offset:16384\n"
"v_mfma_f32_16x16x32_bf16 a[112:115], v[228:231], a[4:7], a[112:115]\n"
"ds_read_b128 a[4:7], v246 offset:16384\n"
"v_mfma_f32_16x16x32_bf16 a[116:119], v[224:227], a[8:11], a[116:119]\n"
"ds_read_b128 a[8:11], v245 offset:18432\n"
"v_mfma_f32_16x16x32_bf16 a[116:119], v[228:231], a[12:15], a[116:119]\n"
"ds_read_b128 a[12:15], v246 offset:18432\n"
"v_mfma_f32_16x16x32_bf16 a[120:123], v[224:227], a[16:19], a[120:123]\n"
"ds_read_b128 a[16:19], v245 offset:20480\n"
"v_mfma_f32_16x16x32_bf16 a[120:123], v[228:231], a[20:23], a[120:123]\n"
"ds_read_b128 a[20:23], v246 offset:20480\n"
"v_mfma_f32_16x16x32_bf16 a[124:127], v[224:227], a[24:27], a[124:127]\n"
"ds_read_b128 a[24:27], v245 offset:22528\n"
"buffer_load_dwordx4 a[108:111], v240, s[36:39], s80 offen offset:3072 sc1\n"
"v_mfma_f32_16x16x32_bf16 a[124:127], v[228:231], a[28:31], a[124:127]\n"
"s_add_u32 s80, s80, 0x1000\n"
"ds_read_b128 a[28:31], v246 offset:22528\n"
"v_mfma_f32_16x16x32_bf16 a[128:131], v[224:227], a[32:35], a[128:131]\n"
"ds_read_b128 a[32:35], v245 offset:24576\n"
"v_mfma_f32_16x16x32_bf16 a[128:131], v[228:231], a[36:39], a[128:131]\n"
"s_waitcnt lgkmcnt(9)\n"
"ds_read_b128 a[36:39], v246 offset:24576\n"
"v_accvgpr_read_b32 v253, a204\n"
"v_mfma_f32_16x16x32_bf16 a[132:135], v[224:227], a[40:43], a[132:135]\n"
"ds_read_b128 a[40:43], v245 offset:26624\n"
"v_lshlrev_b32 v251, 16, v253\n"
"v_mfma_f32_16x16x32_bf16 a[132:135], v[228:231], a[44:47], a[132:135]\n"
"ds_read_b128 a[44:47], v246 offset:26624\n"
"v_lshlrev_b32 v252, 16, v236\n"
"v_mfma_f32_16x16x32_bf16 a[136:139], v[224:227], a[48:51], a[136:139]\n"
"ds_read_b128 a[48:51], v245 offset:28672\n"
"v_fmac_f32 v252, v251, v88\n"
"v_mfma_f32_16x16x32_bf16 a[136:139], v[228:231], a[52:55], a[136:139]\n"
"ds_read_b128 a[52:55], v246 offset:28672\n"
"v_and_b32 v253, s82, v253\n"
"v_mfma_f32_16x16x32_bf16 a[140:143], v[224:227], a[56:59], a[140:143]\n"
"ds_read_b128 a[56:59], v245 offset:30720\n"
"buffer_load_dwordx4 v[224:227], v240, s[36:39], s80 offen offset:0 sc1\n"
"v_mfma_f32_16x16x32_bf16 a[140:143], v[228:231], a[60:63], a[140:143]\n"
"s_waitcnt vmcnt(13) lgkmcnt(11)\n"
"ds_read_b128 a[60:63], v246 offset:30720\n"
"v_and_b32 v236, s82, v236\n"
"v_mfma_f32_16x16x32_bf16 v[128:131], a[64:67], a[0:3], v[128:131]\n"
"buffer_store_dwordx4 v[84:87], v240, s[52:55], s77 offen offset:1024 sc1\n"
"v_mfma_f32_16x16x32_bf16 v[128:131], a[68:71], a[4:7], v[128:131]\n"
"ds_write_b128 v249, v[84:87] offset:43008\n"
"v_mfma_f32_16x16x32_bf16 v[132:135], a[64:67], a[8:11], v[132:135]\n"
"ds_read_b128 v[232:235], v250 offset:14336\n"
"v_fmac_f32 v236, v253, v89\n"
"v_mfma_f32_16x16x32_bf16 v[132:135], a[68:71], a[12:15], v[132:135]\n"
"s_waitcnt lgkmcnt(10)\n"
"v_cvt_pk_bf16_f32 v88, v252, v236\n"
"v_accvgpr_read_b32 v253, a205\n"
"v_mfma_f32_16x16x32_bf16 v[136:139], a[64:67], a[16:19], v[136:139]\n"
"buffer_load_dwordx4 v[28:31], v240, s[36:39], s81 offen offset:0 sc1\n"
"v_lshlrev_b32 v251, 16, v253\n"
"v_mfma_f32_16x16x32_bf16 v[136:139], a[68:71], a[20:23], v[136:139]\n"
"v_lshlrev_b32 v252, 16, v237\n"
"v_fmac_f32 v252, v251, v90\n"
"v_mfma_f32_16x16x32_bf16 v[140:143], a[64:67], a[24:27], v[140:143]\n"
"buffer_load_dwordx4 v[228:231], v240, s[36:39], s80 offen offset:1024 sc1\n"
"v_and_b32 v253, s82, v253\n"
"v_mfma_f32_16x16x32_bf16 v[140:143], a[68:71], a[28:31], v[140:143]\n"
"s_waitcnt lgkmcnt(6)\n"
"v_and_b32 v237, s82, v237\n"
"v_fmac_f32 v237, v253, v91\n"
"v_mfma_f32_16x16x32_bf16 v[144:147], a[64:67], a[32:35], v[144:147]\n"
"v_cvt_pk_bf16_f32 v89, v252, v237\n"
"v_accvgpr_read_b32 v253, a206\n"
"v_mfma_f32_16x16x32_bf16 v[144:147], a[68:71], a[36:39], v[144:147]\n"
"s_waitcnt lgkmcnt(2)\n"
"s_barrier\n"
"v_lshlrev_b32 v251, 16, v253\n"
"v_mfma_f32_16x16x32_bf16 v[148:151], a[64:67], a[40:43], v[148:151]\n"
"v_lshlrev_b32 v252, 16, v238\n"
"v_fmac_f32 v252, v251, v120\n"
"v_mfma_f32_16x16x32_bf16 v[148:151], a[68:71], a[44:47], v[148:151]\n"
".Lpad14_%=:\n"
"s_cbranch_execz .Lret1_%=\n"
"v_and_b32 v253, s82, v253\n"
"v_and_b32 v238, s82, v238\n"
"v_mfma_f32_16x16x32_bf16 v[152:155], a[64:67], a[48:51], v[152:155]\n"
"v_fmac_f32 v238, v253, v121\n"
"v_cvt_pk_bf16_f32 v90, v252, v238\n"
"v_mfma_f32_16x16x32_bf16 v[152:155], a[68:71], a[52:55], v[152:155]\n"
"v_accvgpr_read_b32 v253, a207\n"
"v_lshlrev_b32 v251, 16, v253\n"
"v_mfma_f32_16x16x32_bf16 v[156:159], a[64:67], a[56:59], v[156:159]\n"
"buffer_load_dwordx4 a[64:67], v240, s[36:39], s80 offen offset:2048 sc1\n"
"v_lshlrev_b32 v252, 16, v239\n"
"v_mfma_f32_16x16x32_bf16 v[156:159], a[68:71], a[60:63], v[156:159]\n"
"s_waitcnt vmcnt(15)\n"
"v_fmac_f32 v252, v251, v122\n"
"v_and_b32 v253, s82, v253\n"
"v_mfma_f32_16x16x32_bf16 v[160:163], a[72:75], a[0:3], v[160:163]\n"
"v_and_b32 v239, s82, v239\n"
"v_fmac_f32 v239, v253, v123\n"
"v_mfma_f32_16x16x32_bf16 v[160:163], a[76:79], a[4:7], v[160:163]\n"
"buffer_load_dwordx4 v[60:63], v240, s[36:39], s81 offen offset:1024 sc1\n"
"v_cvt_pk_bf16_f32 v91, v252, v239\n"
"v_mfma_f32_16x16x32_bf16 v[164:167], a[72:75], a[8:11], v[164:167]\n"
"ds_write_b128 v249, v[88:91] offset:45056\n"
"v_mfma_f32_16x16x32_bf16 v[164:167], a[76:79], a[12:15], v[164:167]\n"
"v_mfma_f32_16x16x32_bf16 v[168:171], a[72:75], a[16:19], v[168:171]\n"
"v_mfma_f32_16x16x32_bf16 v[168:171], a[76:79], a[20:23], v[168:171]\n"
"v_mfma_f32_16x16x32_bf16 v[172:175], a[72:75], a[24:27], v[172:175]\n"
"buffer_load_dwordx4 a[68:71], v240, s[36:39], s80 offen offset:3072 sc1\n"
"v_mfma_f32_16x16x32_bf16 v[172:175], a[76:79], a[28:31], v[172:175]\n"
"s_add_u32 s80, s80, 0x1000\n"
"buffer_store_dwordx4 v[88:91], v240, s[52:55], s78 offen offset:1024 sc1\n"
"v_mfma_f32_16x16x32_bf16 v[176:179], a[72:75], a[32:35], v[176:179]\n"
"v_mfma_f32_16x16x32_bf16 v[176:179], a[76:79], a[36:39], v[176:179]\n"
"v_mfma_f32_16x16x32_bf16 v[180:183], a[72:75], a[40:43], v[180:183]\n"
"v_mfma_f32_16x16x32_bf16 v[180:183], a[76:79], a[44:47], v[180:183]\n"
"v_mfma_f32_16x16x32_bf16 v[184:187], a[72:75], a[48:51], v[184:187]\n"
"v_mfma_f32_16x16x32_bf16 v[184:187], a[76:79], a[52:55], v[184:187]\n"
"v_mfma_f32_16x16x32_bf16 v[188:191], a[72:75], a[56:59], v[188:191]\n"
"buffer_load_dwordx4 a[72:75], v240, s[36:39], s80 offen offset:0 sc1\n"
"v_mfma_f32_16x16x32_bf16 v[188:191], a[76:79], a[60:63], v[188:191]\n"
"s_waitcnt vmcnt(16)\n"
"v_mfma_f32_16x16x32_bf16 v[192:195], a[80:83], a[0:3], v[192:195]\n"
"buffer_load_dwordx4 v[124:127], v240, s[36:39], s81 offen offset:3072 sc1\n"
"v_mfma_f32_16x16x32_bf16 v[192:195], a[84:87], a[4:7], v[192:195]\n"
"v_mfma_f32_16x16x32_bf16 v[196:199], a[80:83], a[8:11], v[196:199]\n"
"v_mfma_f32_16x16x32_bf16 v[196:199], a[84:87], a[12:15], v[196:199]\n"
"v_mfma_f32_16x16x32_bf16 v[200:203], a[80:83], a[16:19], v[200:203]\n"
"ds_read_b128 v[236:239], v250 offset:0\n"
"v_mfma_f32_16x16x32_bf16 v[200:203], a[84:87], a[20:23], v[200:203]\n"
"s_waitcnt lgkmcnt(2)\n"
"v_accvgpr_read_b32 v253, a208\n"
"v_lshlrev_b32 v251, 16, v253\n"
"v_mfma_f32_16x16x32_bf16 v[204:207], a[80:83], a[24:27], v[204:207]\n"
"buffer_load_dwordx4 a[76:79], v240, s[36:39], s80 offen offset:1024 sc1\n"
"v_lshlrev_b32 v252, 16, v232\n"
"v_mfma_f32_16x16x32_bf16 v[204:207], a[84:87], a[28:31], v[204:207]\n"
"v_fmac_f32 v252, v251, v156\n"
"v_and_b32 v253, s82, v253\n"
"v_mfma_f32_16x16x32_bf16 v[208:211], a[80:83], a[32:35], v[208:211]\n"
"v_and_b32 v232, s82, v232\n"
"v_fmac_f32 v232, v253, v157\n"
"v_mfma_f32_16x16x32_bf16 v[208:211], a[84:87], a[36:39], v[208:211]\n"
"v_cvt_pk_bf16_f32 v156, v252, v232\n"
"v_accvgpr_read_b32 v253, a209\n"
"v_mfma_f32_16x16x32_bf16 v[212:215], a[80:83], a[40:43], v[212:215]\n"
"v_lshlrev_b32 v251, 16, v253\n"
"v_lshlrev_b32 v252, 16, v233\n"
"v_mfma_f32_16x16x32_bf16 v[212:215], a[84:87], a[44:47], v[212:215]\n"
"v_fmac_f32 v252, v251, v158\n"
"v_and_b32 v253, s82, v253\n"
"v_mfma_f32_16x16x32_bf16 v[216:219], a[80:83], a[48:51], v[216:219]\n"
"v_and_b32 v233, s82, v233\n"
"v_fmac_f32 v233, v253, v159\n"
"v_mfma_f32_16x16x32_bf16 v[216:219], a[84:87], a[52:55], v[216:219]\n"
"v_cvt_pk_bf16_f32 v157, v252, v233\n"
"v_accvgpr_read_b32 v253, a210\n"
"v_mfma_f32_16x16x32_bf16 v[220:223], a[80:83], a[56:59], v[220:223]\n"
"buffer_load_dwordx4 a[80:83], v240, s[36:39], s80 offen offset:2048 sc1\n"
"v_lshlrev_b32 v251, 16, v253\n"
"v_mfma_f32_16x16x32_bf16 v[220:223], a[84:87], a[60:63], v[220:223]\n"
"s_waitcnt vmcnt(17)\n"
"v_lshlrev_b32 v252, 16, v234\n"
"v_fmac_f32 v252, v251, v188\n"
"v_mfma_f32_16x16x32_bf16 a[112:115], a[88:91], a[0:3], a[112:115]\n"
"ds_read_b128 a[0:3], v243 offset:0\n"
"v_and_b32 v253, s82, v253\n"
"v_mfma_f32_16x16x32_bf16 a[112:115], a[92:95], a[4:7], a[112:115]\n"
"ds_read_b128 a[4:7], v244 offset:0\n"
"v_and_b32 v234, s82, v234\n"
"v_mfma_f32_16x16x32_bf16 a[116:119], a[88:91], a[8:11], a[116:119]\n"
"ds_read_b128 a[8:11], v243 offset:2048\n"
"v_fmac_f32 v234, v253, v189\n"
"v_mfma_f32_16x16x32_bf16 a[116:119], a[92:95], a[12:15], a[116:119]\n"
"ds_read_b128 a[12:15], v244 offset:2048\n"
"v_cvt_pk_bf16_f32 v158, v252, v234\n"
"v_mfma_f32_16x16x32_bf16 a[120:123], a[88:91], a[16:19], a[120:123]\n"
"ds_read_b128 a[16:19], v243 offset:4096\n"
"v_accvgpr_read_b32 v253, a211\n"
"v_mfma_f32_16x16x32_bf16 a[120:123], a[92:95], a[20:23], a[120:123]\n"
"ds_read_b128 a[20:23], v244 offset:4096\n"
"v_lshlrev_b32 v251, 16, v253\n"
"v_mfma_f32_16x16x32_bf16 a[124:127], a[88:91], a[24:27], a[124:127]\n"
"ds_read_b128 a[24:27], v243 offset:6144\n"
"buffer_load_dwordx4 a[84:87], v240, s[36:39], s80 offen offset:3072 sc1\n"
"v_mfma_f32_16x16x32_bf16 a[124:127], a[92:95], a[28:31], a[124:127]\n"
"s_add_u32 s80, s80, 0x1000\n"
"ds_read_b128 a[28:31], v244 offset:6144\n"
"v_lshlrev_b32 v252, 16, v235\n"
"v_mfma_f32_16x16x32_bf16 a[128:131], a[88:91], a[32:35], a[128:131]\n"
"ds_read_b128 a[32:35], v243 offset:8192\n"
"v_fmac_f32 v252, v251, v190\n"
"v_mfma_f32_16x16x32_bf16 a[128:131], a[92:95], a[36:39], a[128:131]\n"
"ds_read_b128 a[36:39], v244 offset:8192\n"
"v_and_b32 v253, s82, v253\n"
"v_mfma_f32_16x16x32_bf16 a[132:135], a[88:91], a[40:43], a[132:135]\n"
"ds_read_b128 a[40:43], v243 offset:10240\n"
"v_and_b32 v235, s82, v235\n"
"v_mfma_f32_16x16x32_bf16 a[132:135], a[92:95], a[44:47], a[132:135]\n"
"ds_read_b128 a[44:47], v244 offset:10240\n"
"v_fmac_f32 v235, v253, v191\n"
"v_mfma_f32_16x16x32_bf16 a[136:139], a[88:91], a[48:51], a[136:139]\n"
"ds_read_b128 a[48:51], v243 offset:12288\n"
"v_cvt_pk_bf16_f32 v159, v252, v235\n"
"v_mfma_f32_16x16x32_bf16 a[136:139], a[92:95], a[52:55], a[136:139]\n"
"s_waitcnt lgkmcnt(13)\n"
"ds_read_b128 a[52:55], v244 offset:12288\n"
"v_accvgpr_read_b32 v253, a212\n"
"v_mfma_f32_16x16x32_bf16 a[140:143], a[88:91], a[56:59], a[140:143]\n"
"ds_read_b128 a[56:59], v243 offset:14336\n"
"buffer_load_dwordx4 a[88:91], v240, s[36:39], s80 offen offset:0 sc1\n"
"v_mfma_f32_16x16x32_bf16 a[140:143], a[92:95], a[60:63], a[140:143]\n"
"s_waitcnt vmcnt(11) lgkmcnt(11)\n"
"ds_read_b128 a[60:63], v244 offset:14336\n"
"v_lshlrev_b32 v251, 16, v253\n"
"v_mfma_f32_16x16x32_bf16 v[0:3], a[96:99], a[0:3], v[28:31]\n"
"ds_read_b128 v[232:235], v250 offset:2048\n"
"buffer_load_dwordx4 v[92:95], v240, s[36:39], s81 offen offset:2048 sc1\n"
"v_mfma_f32_16x16x32_bf16 v[0:3], a[100:103], a[4:7], v[0:3]\n"
"s_add_u32 s81, s81, 4096\n"
"buffer_store_dwordx4 v[156:159], v240, s[52:55], s79 offen offset:2048 sc1\n"
"v_mfma_f32_16x16x32_bf16 v[4:7], a[96:99], a[8:11], v[28:31]\n"
"ds_write_b128 v250, v[156:159] offset:14336\n"
"v_mfma_f32_16x16x32_bf16 v[4:7], a[100:103], a[12:15], v[4:7]\n"
"s_waitcnt lgkmcnt(10)\n"
"v_lshlrev_b32 v252, 16, v236\n"
"v_fmac_f32 v252, v251, v128\n"
"v_mfma_f32_16x16x32_bf16 v[8:11], a[96:99], a[16:19], v[28:31]\n"
"v_and_b32 v253, s82, v253\n"
"v_and_b32 v236, s82, v236\n"
"v_mfma_f32_16x16x32_bf16 v[8:11], a[100:103], a[20:23], v[8:11]\n"
"v_fmac_f32 v236, v253, v129\n"
"v_cvt_pk_bf16_f32 v128, v252, v236\n"
"v_mfma_f32_16x16x32_bf16 v[12:15], a[96:99], a[24:27], v[28:31]\n"
"buffer_load_dwordx4 a[92:95], v240, s[36:39], s80 offen offset:1024 sc1\n"
"v_accvgpr_read_b32 v253, a213\n"
"v_mfma_f32_16x16x32_bf16 v[12:15], a[100:103], a[28:31], v[12:15]\n"
"s_waitcnt lgkmcnt(6)\n"
"v_lshlrev_b32 v251, 16, v253\n"
"v_lshlrev_b32 v252, 16, v237\n"
"v_mfma_f32_16x16x32_bf16 v[16:19], a[96:99], a[32:35], v[28:31]\n"
"v_fmac_f32 v252, v251, v130\n"
"v_and_b32 v253, s82, v253\n"
"v_mfma_f32_16x16x32_bf16 v[16:19], a[100:103], a[36:39], v[16:19]\n"
"v_and_b32 v237, s82, v237\n"
"v_fmac_f32 v237, v253, v131\n"
"v_mfma_f32_16x16x32_bf16 v[20:23], a[96:99], a[40:43], v[28:31]\n"
"v_cvt_pk_bf16_f32 v129, v252, v237\n"
"v_accvgpr_read_b32 v253, a214\n"
"v_mfma_f32_16x16x32_bf16 v[20:23], a[100:103], a[44:47], v[20:23]\n"
"s_waitcnt lgkmcnt(2)\n"
"v_lshlrev_b32 v251, 16, v253\n"
"v_lshlrev_b32 v252, 16, v238\n"
"v_mfma_f32_16x16x32_bf16 v[24:27], a[96:99], a[48:51], v[28:31]\n"
"v_fmac_f32 v252, v251, v160\n"
"v_and_b32 v253, s82, v253\n"
"v_mfma_f32_16x16x32_bf16 v[24:27], a[100:103], a[52:55], v[24:27]\n"
"v_and_b32 v238, s82, v238\n"
"v_fmac_f32 v238, v253, v161\n"
"v_mfma_f32_16x16x32_bf16 v[28:31], a[96:99], a[56:59], v[28:31]\n"
"buffer_load_dwordx4 a[96:99], v240, s[36:39], s80 offen offset:2048 sc1\n"
"v_cvt_pk_bf16_f32 v130, v252, v238\n"
"v_mfma_f32_16x16x32_bf16 v[28:31], a[100:103], a[60:63], v[28:31]\n"
"s_waitcnt vmcnt(12)\n"
"v_accvgpr_read_b32 v253, a215\n"
"v_lshlrev_b32 v251, 16, v253\n"
"v_mfma_f32_16x16x32_bf16 v[32:35], a[104:107], a[0:3], v[60:63]\n"
"v_lshlrev_b32 v252, 16, v239\n"
"v_fmac_f32 v252, v251, v162\n"
"v_mfma_f32_16x16x32_bf16 v[32:35], a[108:111], a[4:7], v[32:35]\n"
"v_and_b32 v253, s82, v253\n"
"v_and_b32 v239, s82, v239\n"
"v_mfma_f32_16x16x32_bf16 v[36:39], a[104:107], a[8:11], v[60:63]\n"
"v_fmac_f32 v239, v253, v163\n"
"v_cvt_pk_bf16_f32 v131, v252, v239\n"
"v_mfma_f32_16x16x32_bf16 v[36:39], a[108:111], a[12:15], v[36:39]\n"
"buffer_store_dwordx4 v[128:131], v240, s[52:55], s68 offen offset:2048 sc1\n"
"v_mfma_f32_16x16x32_bf16 v[40:43], a[104:107], a[16:19], v[60:63]\n"
"ds_write_b128 v250, v[128:131] offset:0\n"
"v_mfma_f32_16x16x32_bf16 v[40:43], a[108:111], a[20:23], v[40:43]\n"
"s_waitcnt lgkmcnt(2)\n"
"ds_read_b128 v[236:239], v250 offset:4096\n"
"v_accvgpr_read_b32 v253, a216\n"
"v_mfma_f32_16x16x32_bf16 v[44:47], a[104:107], a[24:27], v[60:63]\n"
"ds_read_b128 v[160:163], v254 offset:0\n"
"buffer_load_dwordx4 a[100:103], v240, s[36:39], s80 offen offset:3072 sc1\n"
"v_mfma_f32_16x16x32_bf16 v[44:47], a[108:111], a[28:31], v[44:47]\n"
"s_add_u32 s80, s80, 0x1000\n"
"v_lshlrev_b32 v251, 16, v253\n"
"v_lshlrev_b32 v252, 16, v232\n"
"v_mfma_f32_16x16x32_bf16 v[48:51], a[104:107], a[32:35], v[60:63]\n"
"v_fmac_f32 v252, v251, v132\n"
"v_and_b32 v253, s82, v253\n"
"v_mfma_f32_16x16x32_bf16 v[48:51], a[108:111], a[36:39], v[48:51]\n"
"v_and_b32 v232, s82, v232\n"
"v_fmac_f32 v232, v253, v133\n"
"v_mfma_f32_16x16x32_bf16 v[52:55], a[104:107], a[40:43], v[60:63]\n"
"v_cvt_pk_bf16_f32 v132, v252, v232\n"
"v_accvgpr_read_b32 v253, a217\n"
"v_mfma_f32_16x16x32_bf16 v[52:55], a[108:111], a[44:47], v[52:55]\n"
"v_lshlrev_b32 v251, 16, v253\n"
"v_lshlrev_b32 v252, 16, v233\n"
"v_mfma_f32_16x16x32_bf16 v[56:59], a[104:107], a[48:51], v[60:63]\n"
"v_fmac_f32 v252, v251, v134\n"
"v_and_b32 v253, s82, v253\n"
"v_mfma_f32_16x16x32_bf16 v[56:59], a[108:111], a[52:55], v[56:59]\n"
"v_and_b32 v233, s82, v233\n"
"v_fmac_f32 v233, v253, v135\n"
"v_mfma_f32_16x16x32_bf16 v[60:63], a[104:107], a[56:59], v[60:63]\n"
"buffer_load_dwordx4 a[104:107], v240, s[36:39], s80 offen offset:0 sc1\n"
"v_cvt_pk_bf16_f32 v133, v252, v233\n"
"v_mfma_f32_16x16x32_bf16 v[60:63], a[108:111], a[60:63], v[60:63]\n"
"s_waitcnt vmcnt(11)\n"
"v_accvgpr_read_b32 v253, a218\n"
"v_lshlrev_b32 v251, 16, v253\n"
"v_mfma_f32_16x16x32_bf16 v[96:99], v[224:227], a[0:3], v[124:127]\n"
"v_lshlrev_b32 v252, 16, v234\n"
"v_fmac_f32 v252, v251, v164\n"
"v_mfma_f32_16x16x32_bf16 v[96:99], v[228:231], a[4:7], v[96:99]\n"
"v_and_b32 v253, s82, v253\n"
"v_and_b32 v234, s82, v234\n"
"v_mfma_f32_16x16x32_bf16 v[100:103], v[224:227], a[8:11], v[124:127]\n"
"v_fmac_f32 v234, v253, v165\n"
"v_cvt_pk_bf16_f32 v134, v252, v234\n"
"v_mfma_f32_16x16x32_bf16 v[100:103], v[228:231], a[12:15], v[100:103]\n"
"v_accvgpr_read_b32 v253, a219\n"
"v_lshlrev_b32 v251, 16, v253\n"
"v_mfma_f32_16x16x32_bf16 v[104:107], v[224:227], a[16:19], v[124:127]\n"
"v_lshlrev_b32 v252, 16, v235\n"
"v_fmac_f32 v252, v251, v166\n"
"v_mfma_f32_16x16x32_bf16 v[104:107], v[228:231], a[20:23], v[104:107]\n"
"v_and_b32 v253, s82, v253\n"
"v_and_b32 v235, s82, v235\n"
"v_mfma_f32_16x16x32_bf16 v[108:111], v[224:227], a[24:27], v[124:127]\n"
"buffer_load_dwordx4 a[108:111], v240, s[36:39], s80 offen offset:1024 sc1\n"
"v_fmac_f32 v235, v253, v167\n"
"v_mfma_f32_16x16x32_bf16 v[108:111], v[228:231], a[28:31], v[108:111]\n"
"v_lshrrev_b32 v241, 2, v240\n"
"v_cvt_pk_bf16_f32 v135, v252, v235\n"
"v_mfma_f32_16x16x32_bf16 v[112:115], v[224:227], a[32:35], v[124:127]\n"
"buffer_store_dword v242, v241, s[60:63], s85 offen offset:2048 sc1\n"
"v_mfma_f32_16x16x32_bf16 v[112:115], v[228:231], a[36:39], v[112:115]\n"
"buffer_store_dwordx4 v[132:135], v240, s[52:55], s69 offen offset:2048 sc1\n"
"v_mfma_f32_16x16x32_bf16 v[116:119], v[224:227], a[40:43], v[124:127]\n"
"ds_write_b128 v250, v[132:135] offset:2048\n"
"v_mfma_f32_16x16x32_bf16 v[116:119], v[228:231], a[44:47], v[116:119]\n"
"s_waitcnt lgkmcnt(1)\n"
"ds_read_b128 v[232:235], v250 offset:6144\n"
"v_lshlrev_b32 v251, 16, v160\n"
"v_mfma_f32_16x16x32_bf16 v[120:123], v[224:227], a[48:51], v[124:127]\n"
"ds_read_b128 v[164:167], v254 offset:1024\n"
"v_lshlrev_b32 v252, 16, v236\n"
"v_mfma_f32_16x16x32_bf16 v[120:123], v[228:231], a[52:55], v[120:123]\n"
"v_fmac_f32 v252, v251, v136\n"
"v_and_b32 v160, s82, v160\n"
"v_mfma_f32_16x16x32_bf16 v[124:127], v[224:227], a[56:59], v[124:127]\n"
"buffer_load_dwordx4 v[224:227], v240, s[36:39], s80 offen offset:2048 sc1\n"
"v_and_b32 v236, s82, v236\n"
"v_mfma_f32_16x16x32_bf16 v[124:127], v[228:231], a[60:63], v[124:127]\n"
"s_waitcnt vmcnt(10)\n"
"v_fmac_f32 v236, v160, v137\n"
"v_cvt_pk_bf16_f32 v136, v252, v236\n"
"v_mfma_f32_16x16x32_bf16 v[64:67], a[64:67], a[0:3], v[92:95]\n"
"ds_read_b128 a[0:3], v243 offset:16384\n"
"v_lshlrev_b32 v251, 16, v161\n"
"v_mfma_f32_16x16x32_bf16 v[64:67], a[68:71], a[4:7], v[64:67]\n"
"ds_read_b128 a[4:7], v244 offset:16384\n"
"v_lshlrev_b32 v252, 16, v237\n"
"v_mfma_f32_16x16x32_bf16 v[68:71], a[64:67], a[8:11], v[92:95]\n"
"ds_read_b128 a[8:11], v243 offset:18432\n"
"v_fmac_f32 v252, v251, v138\n"
"v_mfma_f32_16x16x32_bf16 v[68:71], a[68:71], a[12:15], v[68:71]\n"
"ds_read_b128 a[12:15], v244 offset:18432\n"
"v_and_b32 v161, s82, v161\n"
"v_mfma_f32_16x16x32_bf16 v[72:75], a[64:67], a[16:19], v[92:95]\n"
"ds_read_b128 a[16:19], v243 offset:20480\n"
"v_and_b32 v237, s82, v237\n"
"v_mfma_f32_16x16x32_bf16 v[72:75], a[68:71], a[20:23], v[72:75]\n"
"ds_read_b128 a[20:23], v244 offset:20480\n"
"v_fmac_f32 v237, v161, v139\n"
"v_mfma_f32_16x16x32_bf16 v[76:79], a[64:67], a[24:27], v[92:95]\n"
"ds_read_b128 a[24:27], v243 offset:22528\n"
"buffer_load_dwordx4 v[228:231], v240, s[36:39], s80 offen offset:3072 sc1\n"
"v_mfma_f32_16x16x32_bf16 v[76:79], a[68:71], a[28:31], v[76:79]\n"
"s_add_u32 s80, s80, 0x1000\n"
"ds_read_b128 a[28:31], v244 offset:22528\n"
"v_cvt_pk_bf16_f32 v137, v252, v237\n"
"v_mfma_f32_16x16x32_bf16 v[80:83], a[64:67], a[32:35], v[92:95]\n"
"ds_read_b128 a[32:35], v243 offset:24576\n"
"v_lshlrev_b32 v251, 16, v162\n"
"v_mfma_f32_16x16x32_bf16 v[80:83], a[68:71], a[36:39], v[80:83]\n"
"ds_read_b128 a[36:39], v244 offset:24576\n"
"v_lshlrev_b32 v252, 16, v238\n"
"v_mfma_f32_16x16x32_bf16 v[84:87], a[64:67], a[40:43], v[92:95]\n"
"ds_read_b128 a[40:43], v243 offset:26624\n"
"v_fmac_f32 v252, v251, v168\n"
"v_mfma_f32_16x16x32_bf16 v[84:87], a[68:71], a[44:47], v[84:87]\n"
"ds_read_b128 a[44:47], v244 offset:26624\n"
"v_and_b32 v162, s82, v162\n"
"v_mfma_f32_16x16x32_bf16 v[88:91], a[64:67], a[48:51], v[92:95]\n"
"ds_read_b128 a[48:51], v243 offset:28672\n"
"v_and_b32 v238, s82, v238\n"
"v_mfma_f32_16x16x32_bf16 v[88:91], a[68:71], a[52:55], v[88:91]\n"
"ds_read_b128 a[52:55], v244 offset:28672\n"
"v_fmac_f32 v238, v162, v169\n"
"v_mfma_f32_16x16x32_bf16 v[92:95], a[64:67], a[56:59], v[92:95]\n"
"ds_read_b128 a[56:59], v243 offset:30720\n"
"buffer_load_dwordx4 a[64:67], v240, s[36:39], s80 offen offset:0 sc1\n"
"v_mfma_f32_16x16x32_bf16 v[92:95], a[68:71], a[60:63], v[92:95]\n"
"s_waitcnt lgkmcnt(11)\n"
"ds_read_b128 a[60:63], v244 offset:30720\n"
"v_cvt_pk_bf16_f32 v138, v252, v238\n"
"v_mfma_f32_16x16x32_bf16 v[0:3], a[72:75], a[0:3], v[0:3]\n"
"v_lshlrev_b32 v251, 16, v163\n"
"v_lshlrev_b32 v252, 16, v239\n"
"v_mfma_f32_16x16x32_bf16 v[0:3], a[76:79], a[4:7], v[0:3]\n"
"v_fmac_f32 v252, v251, v170\n"
"v_and_b32 v163, s82, v163\n"
"v_mfma_f32_16x16x32_bf16 v[4:7], a[72:75], a[8:11], v[4:7]\n"
"v_and_b32 v239, s82, v239\n"
"v_fmac_f32 v239, v163, v171\n"
"v_mfma_f32_16x16x32_bf16 v[4:7], a[76:79], a[12:15], v[4:7]\n"
"s_waitcnt lgkmcnt(8)\n"
"ds_read_b128 v[168:171], v254 offset:2048\n"
"v_cvt_pk_bf16_f32 v139, v252, v239\n"
"v_mfma_f32_16x16x32_bf16 v[8:11], a[72:75], a[16:19], v[8:11]\n"
"ds_write_b128 v250, v[136:139] offset:4096\n"
"v_mfma_f32_16x16x32_bf16 v[8:11], a[76:79], a[20:23], v[8:11]\n"
"ds_read_b128 v[236:239], v250 offset:8192\n"
"v_lshlrev_b32 v251, 16, v164\n"
"v_mfma_f32_16x16x32_bf16 v[12:15], a[72:75], a[24:27], v[12:15]\n"
"buffer_load_dwordx4 a[68:71], v240, s[36:39], s80 offen offset:1024 sc1\n"
"v_lshlrev_b32 v252, 16, v232\n"
"v_mfma_f32_16x16x32_bf16 v[12:15], a[76:79], a[28:31], v[12:15]\n"
"s_waitcnt lgkmcnt(7)\n"
"buffer_store_dwordx4 v[136:139], v240, s[52:55], s70 offen offset:2048 sc1\n"
"v_mfma_f32_16x16x32_bf16 v[16:19], a[72:75], a[32:35], v[16:19]\n"
"v_fmac_f32 v252, v251, v140\n"
"v_and_b32 v164, s82, v164\n"
"v_mfma_f32_16x16x32_bf16 v[16:19], a[76:79], a[36:39], v[16:19]\n"
"v_and_b32 v232, s82, v232\n"
"v_fmac_f32 v232, v164, v141\n"
"v_mfma_f32_16x16x32_bf16 v[20:23], a[72:75], a[40:43], v[20:23]\n"
"v_cvt_pk_bf16_f32 v140, v252, v232\n"
"v_lshlrev_b32 v251, 16, v165\n"
"v_mfma_f32_16x16x32_bf16 v[20:23], a[76:79], a[44:47], v[20:23]\n"
"s_waitcnt lgkmcnt(3)\n"
"v_lshlrev_b32 v252, 16, v233\n"
"v_fmac_f32 v252, v251, v142\n"
"v_mfma_f32_16x16x32_bf16 v[24:27], a[72:75], a[48:51], v[24:27]\n"
"v_and_b32 v165, s82, v165\n"
"v_and_b32 v233, s82, v233\n"
"v_mfma_f32_16x16x32_bf16 v[24:27], a[76:79], a[52:55], v[24:27]\n"
"v_fmac_f32 v233, v165, v143\n"
"v_cvt_pk_bf16_f32 v141, v252, v233\n"
"v_mfma_f32_16x16x32_bf16 v[28:31], a[72:75], a[56:59], v[28:31]\n"
"buffer_load_dwordx4 a[72:75], v240, s[36:39], s80 offen offset:2048 sc1\n"
"v_lshlrev_b32 v251, 16, v166\n"
"v_mfma_f32_16x16x32_bf16 v[28:31], a[76:79], a[60:63], v[28:31]\n"
"v_lshlrev_b32 v252, 16, v234\n"
"v_fmac_f32 v252, v251, v172\n"
"v_mfma_f32_16x16x32_bf16 v[32:35], a[80:83], a[0:3], v[32:35]\n"
"v_and_b32 v166, s82, v166\n"
"v_and_b32 v234, s82, v234\n"
"v_mfma_f32_16x16x32_bf16 v[32:35], a[84:87], a[4:7], v[32:35]\n"
"v_fmac_f32 v234, v166, v173\n"
"v_cvt_pk_bf16_f32 v142, v252, v234\n"
"v_mfma_f32_16x16x32_bf16 v[36:39], a[80:83], a[8:11], v[36:39]\n"
"v_lshlrev_b32 v251, 16, v167\n"
"v_lshlrev_b32 v252, 16, v235\n"
"v_mfma_f32_16x16x32_bf16 v[36:39], a[84:87], a[12:15], v[36:39]\n"
"v_fmac_f32 v252, v251, v174\n"
"v_and_b32 v167, s82, v167\n"
"v_mfma_f32_16x16x32_bf16 v[40:43], a[80:83], a[16:19], v[40:43]\n"
"v_and_b32 v235, s82, v235\n"
"v_fmac_f32 v235, v167, v175\n"
"v_mfma_f32_16x16x32_bf16 v[40:43], a[84:87], a[20:23], v[40:43]\n"
"v_cvt_pk_bf16_f32 v143, v252, v235\n"
"v_mfma_f32_16x16x32_bf16 v[44:47], a[80:83], a[24:27], v[44:47]\n"
"buffer_load_dwordx4 a[76:79], v240, s[36:39], s80 offen offset:3072 sc1\n"
"v_mfma_f32_16x16x32_bf16 v[44:47], a[84:87], a[28:31], v[44:47]\n"
"s_add_u32 s80, s80, 0x1000\n"
"buffer_store_dwordx4 v[140:143], v240, s[52:55], s71 offen offset:2048 sc1\n"
"v_mfma_f32_16x16x32_bf16 v[48:51], a[80:83], a[32:35], v[48:51]\n"
"ds_write_b128 v250, v[140:143] offset:6144\n"
"v_mfma_f32_16x16x32_bf16 v[48:51], a[84:87], a[36:39], v[48:51]\n"
"s_waitcnt lgkmcnt(1)\n"
"ds_read_b128 v[232:235], v250 offset:10240\n"
"v_lshlrev_b32 v251, 16, v168\n"
"v_mfma_f32_16x16x32_bf16 v[52:55], a[80:83], a[40:43], v[52:55]\n"
"ds_read_b128 v[172:175], v254 offset:3072\n"
"v_lshlrev_b32 v252, 16, v236\n"
"v_mfma_f32_16x16x32_bf16 v[52:55], a[84:87], a[44:47], v[52:55]\n"
"v_fmac_f32 v252, v251, v144\n"
"v_and_b32 v168, s82, v168\n"
"v_mfma_f32_16x16x32_bf16 v[56:59], a[80:83], a[48:51], v[56:59]\n"
"v_and_b32 v236, s82, v236\n"
"v_fmac_f32 v236, v168, v145\n"
"v_mfma_f32_16x16x32_bf16 v[56:59], a[84:87], a[52:55], v[56:59]\n"
"v_cvt_pk_bf16_f32 v144, v252, v236\n"
"v_lshlrev_b32 v251, 16, v169\n"
"v_mfma_f32_16x16x32_bf16 v[60:63], a[80:83], a[56:59], v[60:63]\n"
"buffer_load_dwordx4 a[80:83], v240, s[36:39], s80 offen offset:0 sc1\n"
"v_lshlrev_b32 v252, 16, v237\n"
"v_mfma_f32_16x16x32_bf16 v[60:63], a[84:87], a[60:63], v[60:63]\n"
"s_waitcnt vmcnt(16)\n"
"v_fmac_f32 v252, v251, v146\n"
"v_and_b32 v169, s82, v169\n"
"v_mfma_f32_16x16x32_bf16 v[64:67], a[88:91], a[0:3], v[64:67]\n"
"v_and_b32 v237, s82, v237\n"
"v_fmac_f32 v237, v169, v147\n"
"v_mfma_f32_16x16x32_bf16 v[64:67], a[92:95], a[4:7], v[64:67]\n"
"v_cvt_pk_bf16_f32 v145, v252, v237\n"
"v_lshlrev_b32 v251, 16, v170\n"
"v_mfma_f32_16x16x32_bf16 v[68:71], a[88:91], a[8:11], v[68:71]\n"
"v_lshlrev_b32 v252, 16, v238\n"
"v_fmac_f32 v252, v251, v176\n"
"v_mfma_f32_16x16x32_bf16 v[68:71], a[92:95], a[12:15], v[68:71]\n"
"v_and_b32 v170, s82, v170\n"
"v_and_b32 v238, s82, v238\n"
"v_mfma_f32_16x16x32_bf16 v[72:75], a[88:91], a[16:19], v[72:75]\n"
"v_fmac_f32 v238, v170, v177\n"
"v_cvt_pk_bf16_f32 v146, v252, v238\n"
"v_mfma_f32_16x16x32_bf16 v[72:75], a[92:95], a[20:23], v[72:75]\n"
"v_lshlrev_b32 v251, 16, v171\n"
"v_lshlrev_b32 v252, 16, v239\n"
"v_mfma_f32_16x16x32_bf16 v[76:79], a[88:91], a[24:27], v[76:79]\n"
"buffer_load_dwordx4 a[84:87], v240, s[36:39], s80 offen offset:1024 sc1\n"
"v_fmac_f32 v252, v251, v178\n"
"v_mfma_f32_16x16x32_bf16 v[76:79], a[92:95], a[28:31], v[76:79]\n"
"v_and_b32 v171, s82, v171\n"
"v_and_b32 v239, s82, v239\n"
"v_mfma_f32_16x16x32_bf16 v[80:83], a[88:91], a[32:35], v[80:83]\n"
"v_fmac_f32 v239, v171, v179\n"
"v_cvt_pk_bf16_f32 v147, v252, v239\n"
"v_mfma_f32_16x16x32_bf16 v[80:83], a[92:95], a[36:39], v[80:83]\n"
"buffer_store_dwordx4 v[144:147], v240, s[52:55], s76 offen offset:2048 sc1\n"
"v_mfma_f32_16x16x32_bf16 v[84:87], a[88:91], a[40:43], v[84:87]\n"
"ds_write_b128 v250, v[144:147] offset:8192\n"
"v_mfma_f32_16x16x32_bf16 v[84:87], a[92:95], a[44:47], v[84:87]\n"
"s_waitcnt lgkmcnt(1)\n"
"ds_read_b128 v[236:239], v250 offset:12288\n"
"v_lshlrev_b32 v251, 16, v172\n"
"v_mfma_f32_16x16x32_bf16 v[88:91], a[88:91], a[48:51], v[88:91]\n"
"v_lshlrev_b32 v252, 16, v232\n"
"v_fmac_f32 v252, v251, v148\n"
"v_mfma_f32_16x16x32_bf16 v[88:91], a[92:95], a[52:55], v[88:91]\n"
"s_barrier\n"
"v_and_b32 v172, s82, v172\n"
"v_mfma_f32_16x16x32_bf16 v[92:95], a[88:91], a[56:59], v[92:95]\n"
"buffer_load_dwordx4 a[88:91], v240, s[36:39], s80 offen offset:2048 sc1\n"
"v_and_b32 v232, s82, v232\n"
"v_mfma_f32_16x16x32_bf16 v[92:95], a[92:95], a[60:63], v[92:95]\n"
"s_waitcnt vmcnt(16)\n"
"v_fmac_f32 v232, v172, v149\n"
"v_cvt_pk_bf16_f32 v148, v252, v232\n"
"v_mfma_f32_16x16x32_bf16 v[96:99], a[96:99], a[0:3], v[96:99]\n"
"ds_read_b128 a[0:3], v243 offset:32768\n"
"v_lshlrev_b32 v251, 16, v173\n"
"v_mfma_f32_16x16x32_bf16 v[96:99], a[100:103], a[4:7], v[96:99]\n"
"ds_read_b128 a[4:7], v244 offset:32768\n"
"v_lshlrev_b32 v252, 16, v233\n"
"v_mfma_f32_16x16x32_bf16 v[100:103], a[96:99], a[8:11], v[100:103]\n"
"ds_read_b128 a[8:11], v243 offset:34816\n"
"v_fmac_f32 v252, v251, v150\n"
"v_mfma_f32_16x16x32_bf16 v[100:103], a[100:103], a[12:15], v[100:103]\n"
"ds_read_b128 a[12:15], v244 offset:34816\n"
"v_and_b32 v173, s82, v173\n"
"v_mfma_f32_16x16x32_bf16 v[104:107], a[96:99], a[16:19], v[104:107]\n"
"ds_read_b128 a[16:19], v243 offset:36864\n"
"v_and_b32 v233, s82, v233\n"
"v_mfma_f32_16x16x32_bf16 v[104:107], a[100:103], a[20:23], v[104:107]\n"
"ds_read_b128 a[20:23], v244 offset:36864\n"
"v_fmac_f32 v233, v173, v151\n"
"v_mfma_f32_16x16x32_bf16 v[108:111], a[96:99], a[24:27], v[108:111]\n"
"ds_read_b128 a[24:27], v243 offset:38912\n"
"buffer_load_dwordx4 a[92:95], v240, s[36:39], s80 offen offset:3072 sc1\n"
"v_mfma_f32_16x16x32_bf16 v[108:111], a[100:103], a[28:31], v[108:111]\n"
"s_add_u32 s80, s80, 0x1000\n"
"ds_read_b128 a[28:31], v244 offset:38912\n"
"v_cvt_pk_bf16_f32 v149, v252, v233\n"
"v_mfma_f32_16x16x32_bf16 v[112:115], a[96:99], a[32:35], v[112:115]\n"
"ds_read_b128 a[32:35], v243 offset:40960\n"
"v_lshlrev_b32 v251, 16, v174\n"
"v_mfma_f32_16x16x32_bf16 v[112:115], a[100:103], a[36:39], v[112:115]\n"
"ds_read_b128 a[36:39], v244 offset:40960\n"
"v_lshlrev_b32 v252, 16, v234\n"
"v_mfma_f32_16x16x32_bf16 v[116:119], a[96:99], a[40:43], v[116:119]\n"
"ds_read_b128 a[40:43], v243 offset:43008\n"
"v_fmac_f32 v252, v251, v180\n"
"v_mfma_f32_16x16x32_bf16 v[116:119], a[100:103], a[44:47], v[116:119]\n"
"ds_read_b128 a[44:47], v244 offset:43008\n"
"v_and_b32 v174, s82, v174\n"
"v_mfma_f32_16x16x32_bf16 v[120:123], a[96:99], a[48:51], v[120:123]\n"
"ds_read_b128 a[48:51], v243 offset:45056\n"
"v_and_b32 v234, s82, v234\n"
"v_mfma_f32_16x16x32_bf16 v[120:123], a[100:103], a[52:55], v[120:123]\n"
"ds_read_b128 a[52:55], v244 offset:45056\n"
"v_fmac_f32 v234, v174, v181\n"
"v_mfma_f32_16x16x32_bf16 v[124:127], a[96:99], a[56:59], v[124:127]\n"
"ds_read_b128 a[56:59], v243 offset:47104\n"
"buffer_load_dwordx4 a[96:99], v240, s[36:39], s80 offen offset:0 sc1\n"
"v_mfma_f32_16x16x32_bf16 v[124:127], a[100:103], a[60:63], v[124:127]\n"
"s_waitcnt vmcnt(16) lgkmcnt(11)\n"
"ds_read_b128 a[60:63], v244 offset:47104\n"
"v_cvt_pk_bf16_f32 v150, v252, v234\n"
"v_mfma_f32_16x16x32_bf16 v[0:3], a[104:107], a[0:3], v[0:3]\n"
"v_lshlrev_b32 v251, 16, v175\n"
"v_lshlrev_b32 v252, 16, v235\n"
"v_mfma_f32_16x16x32_bf16 v[0:3], a[108:111], a[4:7], v[0:3]\n"
"v_fmac_f32 v252, v251, v182\n"
"v_and_b32 v175, s82, v175\n"
"v_mfma_f32_16x16x32_bf16 v[4:7], a[104:107], a[8:11], v[4:7]\n"
"v_and_b32 v235, s82, v235\n"
"v_fmac_f32 v235, v175, v183\n"
"v_mfma_f32_16x16x32_bf16 v[4:7], a[108:111], a[12:15], v[4:7]\n"
"s_waitcnt lgkmcnt(8)\n"
"v_cvt_pk_bf16_f32 v151, v252, v235\n"
"v_accvgpr_read_b32 v253, a220\n"
"v_mfma_f32_16x16x32_bf16 v[8:11], a[104:107], a[16:19], v[8:11]\n"
"ds_write_b128 v250, v[148:151] offset:10240\n"
"v_mfma_f32_16x16x32_bf16 v[8:11], a[108:111], a[20:23], v[8:11]\n"
"ds_read_b128 v[232:235], v250 offset:47104\n"
"v_lshlrev_b32 v251, 16, v253\n"
"v_mfma_f32_16x16x32_bf16 v[12:15], a[104:107], a[24:27], v[12:15]\n"
"buffer_load_dwordx4 a[100:103], v240, s[36:39], s80 offen offset:1024 sc1\n"
"v_lshlrev_b32 v252, 16, v236\n"
"v_mfma_f32_16x16x32_bf16 v[12:15], a[108:111], a[28:31], v[12:15]\n"
"s_waitcnt lgkmcnt(6)\n"
"buffer_store_dwordx4 v[148:151], v240, s[52:55], s77 offen offset:2048 sc1\n"
"v_mfma_f32_16x16x32_bf16 v[16:19], a[104:107], a[32:35], v[16:19]\n"
"v_fmac_f32 v252, v251, v152\n"
"v_and_b32 v253, s82, v253\n"
"v_mfma_f32_16x16x32_bf16 v[16:19], a[108:111], a[36:39], v[16:19]\n"
"v_and_b32 v236, s82, v236\n"
"v_fmac_f32 v236, v253, v153\n"
"v_mfma_f32_16x16x32_bf16 v[20:23], a[104:107], a[40:43], v[20:23]\n"
"v_cvt_pk_bf16_f32 v152, v252, v236\n"
"v_accvgpr_read_b32 v253, a221\n"
"v_mfma_f32_16x16x32_bf16 v[20:23], a[108:111], a[44:47], v[20:23]\n"
"s_waitcnt lgkmcnt(2)\n"
"v_lshlrev_b32 v251, 16, v253\n"
"v_lshlrev_b32 v252, 16, v237\n"
"v_mfma_f32_16x16x32_bf16 v[24:27], a[104:107], a[48:51], v[24:27]\n"
"v_fmac_f32 v252, v251, v154\n"
"v_and_b32 v253, s82, v253\n"
"v_mfma_f32_16x16x32_bf16 v[24:27], a[108:111], a[52:55], v[24:27]\n"
"v_and_b32 v237, s82, v237\n"
"v_fmac_f32 v237, v253, v155\n"
"v_mfma_f32_16x16x32_bf16 v[28:31], a[104:107], a[56:59], v[28:31]\n"
"buffer_load_dwordx4 a[104:107], v240, s[36:39], s80 offen offset:2048 sc1\n"
"v_cvt_pk_bf16_f32 v153, v252, v237\n"
"v_mfma_f32_16x16x32_bf16 v[28:31], a[108:111], a[60:63], v[28:31]\n"
"s_waitcnt vmcnt(15)\n"
"v_accvgpr_read_b32 v253, a222\n"
"v_lshlrev_b32 v251, 16, v253\n"
"v_mfma_f32_16x16x32_bf16 v[32:35], v[224:227], a[0:3], v[32:35]\n"
"v_lshlrev_b32 v252, 16, v238\n"
"v_fmac_f32 v252, v251, v184\n"
"v_mfma_f32_16x16x32_bf16 v[32:35], v[228:231], a[4:7], v[32:35]\n"
"v_and_b32 v253, s82, v253\n"
"v_and_b32 v238, s82, v238\n"
"v_mfma_f32_16x16x32_bf16 v[36:39], v[224:227], a[8:11], v[36:39]\n"
"v_fmac_f32 v238, v253, v185\n"
"v_cvt_pk_bf16_f32 v154, v252, v238\n"
"v_mfma_f32_16x16x32_bf16 v[36:39], v[228:231], a[12:15], v[36:39]\n"
"v_accvgpr_read_b32 v253, a223\n"
"v_lshlrev_b32 v251, 16, v253\n"
"v_mfma_f32_16x16x32_bf16 v[40:43], v[224:227], a[16:19], v[40:43]\n"
"v_lshlrev_b32 v252, 16, v239\n"
"v_fmac_f32 v252, v251, v186\n"
"v_mfma_f32_16x16x32_bf16 v[40:43], v[228:231], a[20:23], v[40:43]\n"
"v_and_b32 v253, s82, v253\n"
"v_and_b32 v239, s82, v239\n"
"v_mfma_f32_16x16x32_bf16 v[44:47], v[224:227], a[24:27], v[44:47]\n"
"buffer_load_dwordx4 a[108:111], v240, s[36:39], s80 offen offset:3072 sc1\n"
"v_fmac_f32 v239, v253, v187\n"
"v_mfma_f32_16x16x32_bf16 v[44:47], v[228:231], a[28:31], v[44:47]\n"
"s_add_u32 s80, s80, 0x1000\n"
"v_cvt_pk_bf16_f32 v155, v252, v239\n"
"v_mfma_f32_16x16x32_bf16 v[48:51], v[224:227], a[32:35], v[48:51]\n"
"buffer_store_dwordx4 v[152:155], v240, s[52:55], s78 offen offset:2048 sc1\n"
"v_mfma_f32_16x16x32_bf16 v[48:51], v[228:231], a[36:39], v[48:51]\n"
"ds_write_b128 v250, v[152:155] offset:12288\n"
"v_mfma_f32_16x16x32_bf16 v[52:55], v[224:227], a[40:43], v[52:55]\n"
"s_waitcnt lgkmcnt(1)\n"
"ds_read_b128 v[236:239], v250 offset:32768\n"
"v_accvgpr_read_b32 v253, a224\n"
"v_mfma_f32_16x16x32_bf16 v[52:55], v[228:231], a[44:47], v[52:55]\n"
"v_lshlrev_b32 v251, 16, v253\n"
"v_lshlrev_b32 v252, 16, v232\n"
"v_mfma_f32_16x16x32_bf16 v[56:59], v[224:227], a[48:51], v[56:59]\n"
"v_fmac_f32 v252, v251, v220\n"
"v_and_b32 v253, s82, v253\n"
"v_mfma_f32_16x16x32_bf16 v[56:59], v[228:231], a[52:55], v[56:59]\n"
"v_and_b32 v232, s82, v232\n"
"v_fmac_f32 v232, v253, v221\n"
"v_mfma_f32_16x16x32_bf16 v[60:63], v[224:227], a[56:59], v[60:63]\n"
"buffer_load_dwordx4 v[224:227], v240, s[36:39], s80 offen offset:0 sc1\n"
"v_cvt_pk_bf16_f32 v220, v252, v232\n"
"v_mfma_f32_16x16x32_bf16 v[60:63], v[228:231], a[60:63], v[60:63]\n"
"s_waitcnt vmcnt(16)\n"
"v_accvgpr_read_b32 v253, a225\n"
"v_lshlrev_b32 v251, 16, v253\n"
"v_mfma_f32_16x16x32_bf16 v[64:67], a[64:67], a[0:3], v[64:67]\n"
"v_lshlrev_b32 v252, 16, v233\n"
"v_fmac_f32 v252, v251, v222\n"
"v_mfma_f32_16x16x32_bf16 v[64:67], a[68:71], a[4:7], v[64:67]\n"
"v_and_b32 v253, s82, v253\n"
"v_and_b32 v233, s82, v233\n"
"v_mfma_f32_16x16x32_bf16 v[68:71], a[64:67], a[8:11], v[68:71]\n"
"v_fmac_f32 v233, v253, v223\n"
"v_cvt_pk_bf16_f32 v221, v252, v233\n"
"v_mfma_f32_16x16x32_bf16 v[68:71], a[68:71], a[12:15], v[68:71]\n"
".Lpad15_%=:\n"
"s_cbranch_execz .Lpad19_%=\n"
"v_accvgpr_read_b32 v253, a226\n"
"v_lshlrev_b32 v251, 16, v253\n"
"v_mfma_f32_16x16x32_bf16 v[72:75], a[64:67], a[16:19], v[72:75]\n"
"v_lshlrev_b32 v252, 16, v234\n"
"v_accvgpr_read_b32 v222, a140\n"
"v_mfma_f32_16x16x32_bf16 v[72:75], a[68:71], a[20:23], v[72:75]\n"
"v_fmac_f32 v252, v251, v222\n"
"v_and_b32 v253, s82, v253\n"
"v_mfma_f32_16x16x32_bf16 v[76:79], a[64:67], a[24:27], v[76:79]\n"
"buffer_load_dwordx4 v[228:231], v240, s[36:39], s80 offen offset:1024 sc1\n"
"v_and_b32 v234, s82, v234\n"
"v_mfma_f32_16x16x32_bf16 v[76:79], a[68:71], a[28:31], v[76:79]\n"
"v_accvgpr_read_b32 v251, a141\n"
"v_fmac_f32 v234, v253, v251\n"
"v_mfma_f32_16x16x32_bf16 v[80:83], a[64:67], a[32:35], v[80:83]\n"
"v_cvt_pk_bf16_f32 v222, v252, v234\n"
"v_accvgpr_read_b32 v253, a227\n"
"v_mfma_f32_16x16x32_bf16 v[80:83], a[68:71], a[36:39], v[80:83]\n"
"v_lshlrev_b32 v251, 16, v253\n"
"v_lshlrev_b32 v252, 16, v235\n"
"v_mfma_f32_16x16x32_bf16 v[84:87], a[64:67], a[40:43], v[84:87]\n"
"v_accvgpr_read_b32 v223, a142\n"
"v_fmac_f32 v252, v251, v223\n"
"v_mfma_f32_16x16x32_bf16 v[84:87], a[68:71], a[44:47], v[84:87]\n"
"v_and_b32 v253, s82, v253\n"
"v_and_b32 v235, s82, v235\n"
"v_mfma_f32_16x16x32_bf16 v[88:91], a[64:67], a[48:51], v[88:91]\n"
"v_accvgpr_read_b32 v251, a143\n"
"v_fmac_f32 v235, v253, v251\n"
"v_mfma_f32_16x16x32_bf16 v[88:91], a[68:71], a[52:55], v[88:91]\n"
"s_waitcnt lgkmcnt(0)\n"
"v_cvt_pk_bf16_f32 v223, v252, v235\n"
"v_accvgpr_read_b32 v253, a228\n"
"v_mfma_f32_16x16x32_bf16 v[92:95], a[64:67], a[56:59], v[92:95]\n"
"ds_read_b128 v[232:235], v250 offset:34816\n"
"buffer_load_dwordx4 a[64:67], v240, s[36:39], s80 offen offset:2048 sc1\n"
"v_mfma_f32_16x16x32_bf16 v[92:95], a[68:71], a[60:63], v[92:95]\n"
"s_waitcnt vmcnt(15)\n"
"buffer_store_dwordx4 v[220:223], v240, s[52:55], s79 offen offset:3072 sc1\n"
"v_mfma_f32_16x16x32_bf16 v[96:99], a[72:75], a[0:3], v[96:99]\n"
"ds_read_b128 a[0:3], v243 offset:49152\n"
"v_lshlrev_b32 v251, 16, v253\n"
"v_mfma_f32_16x16x32_bf16 v[96:99], a[76:79], a[4:7], v[96:99]\n"
"ds_read_b128 a[4:7], v244 offset:49152\n"
"v_lshlrev_b32 v252, 16, v236\n"
"v_mfma_f32_16x16x32_bf16 v[100:103], a[72:75], a[8:11], v[100:103]\n"
"ds_read_b128 a[8:11], v243 offset:51200\n"
"v_fmac_f32 v252, v251, v192\n"
"v_mfma_f32_16x16x32_bf16 v[100:103], a[76:79], a[12:15], v[100:103]\n"
"ds_read_b128 a[12:15], v244 offset:51200\n"
"v_and_b32 v253, s82, v253\n"
"v_mfma_f32_16x16x32_bf16 v[104:107], a[72:75], a[16:19], v[104:107]\n"
"ds_read_b128 a[16:19], v243 offset:53248\n"
"v_and_b32 v236, s82, v236\n"
"v_mfma_f32_16x16x32_bf16 v[104:107], a[76:79], a[20:23], v[104:107]\n"
"ds_read_b128 a[20:23], v244 offset:53248\n"
"v_fmac_f32 v236, v253, v193\n"
"v_mfma_f32_16x16x32_bf16 v[108:111], a[72:75], a[24:27], v[108:111]\n"
"ds_read_b128 a[24:27], v243 offset:55296\n"
"buffer_load_dwordx4 a[68:71], v240, s[36:39], s80 offen offset:3072 sc1\n"
"v_mfma_f32_16x16x32_bf16 v[108:111], a[76:79], a[28:31], v[108:111]\n"
"s_add_u32 s80, s80, 0x1000\n"
"ds_read_b128 a[28:31], v244 offset:55296\n"
"v_cvt_pk_bf16_f32 v192, v252, v236\n"
"v_mfma_f32_16x16x32_bf16 v[112:115], a[72:75], a[32:35], v[112:115]\n"
"ds_read_b128 a[32:35], v243 offset:57344\n"
"v_accvgpr_read_b32 v253, a229\n"
"v_mfma_f32_16x16x32_bf16 v[112:115], a[76:79], a[36:39], v[112:115]\n"
"ds_read_b128 a[36:39], v244 offset:57344\n"
"v_lshlrev_b32 v251, 16, v253\n"
"v_mfma_f32_16x16x32_bf16 v[116:119], a[72:75], a[40:43], v[116:119]\n"
"ds_read_b128 a[40:43], v243 offset:59392\n"
"v_lshlrev_b32 v252, 16, v237\n"
"v_mfma_f32_16x16x32_bf16 v[116:119], a[76:79], a[44:47], v[116:119]\n"
"ds_read_b128 a[44:47], v244 offset:59392\n"
"v_fmac_f32 v252, v251, v194\n"
"v_mfma_f32_16x16x32_bf16 v[120:123], a[72:75], a[48:51], v[120:123]\n"
"ds_read_b128 a[48:51], v243 offset:61440\n"
"v_and_b32 v253, s82, v253\n"
"v_mfma_f32_16x16x32_bf16 v[120:123], a[76:79], a[52:55], v[120:123]\n"
"ds_read_b128 a[52:55], v244 offset:61440\n"
"v_and_b32 v237, s82, v237\n"
"v_mfma_f32_16x16x32_bf16 v[124:127], a[72:75], a[56:59], v[124:127]\n"
"ds_read_b128 a[56:59], v243 offset:63488\n"
"buffer_load_dwordx4 a[72:75], v240, s[36:39], s80 offen offset:0 sc1\n"
"v_mfma_f32_16x16x32_bf16 v[124:127], a[76:79], a[60:63], v[124:127]\n"
"s_waitcnt vmcnt(15) lgkmcnt(11)\n"
"ds_read_b128 a[60:63], v244 offset:63488\n"
"v_fmac_f32 v237, v253, v195\n"
"v_mfma_f32_16x16x32_bf16 v[0:3], a[80:83], a[0:3], v[0:3]\n"
"ds_write_b128 v250, v[220:223] offset:47104\n"
"v_mfma_f32_16x16x32_bf16 v[0:3], a[84:87], a[4:7], v[0:3]\n"
"v_cvt_pk_bf16_f32 v193, v252, v237\n"
"v_accvgpr_read_b32 v253, a230\n"
"v_mfma_f32_16x16x32_bf16 v[4:7], a[80:83], a[8:11], v[4:7]\n"
"v_lshlrev_b32 v251, 16, v253\n"
"v_lshlrev_b32 v252, 16, v238\n"
"v_mfma_f32_16x16x32_bf16 v[4:7], a[84:87], a[12:15], v[4:7]\n"
"s_waitcnt lgkmcnt(9)\n"
"v_accvgpr_read_b32 v194, a112\n"
"v_fmac_f32 v252, v251, v194\n"
"v_mfma_f32_16x16x32_bf16 v[8:11], a[80:83], a[16:19], v[8:11]\n"
"v_and_b32 v253, s82, v253\n"
"v_and_b32 v238, s82, v238\n"
"v_mfma_f32_16x16x32_bf16 v[8:11], a[84:87], a[20:23], v[8:11]\n"
"v_accvgpr_read_b32 v251, a113\n"
"v_fmac_f32 v238, v253, v251\n"
"v_mfma_f32_16x16x32_bf16 v[12:15], a[80:83], a[24:27], v[12:15]\n"
"buffer_load_dwordx4 a[76:79], v240, s[36:39], s80 offen offset:1024 sc1\n"
"v_cvt_pk_bf16_f32 v194, v252, v238\n"
"v_mfma_f32_16x16x32_bf16 v[12:15], a[84:87], a[28:31], v[12:15]\n"
"s_waitcnt lgkmcnt(5)\n"
"v_accvgpr_read_b32 v253, a231\n"
"v_lshlrev_b32 v251, 16, v253\n"
"v_mfma_f32_16x16x32_bf16 v[16:19], a[80:83], a[32:35], v[16:19]\n"
"v_lshlrev_b32 v252, 16, v239\n"
"v_accvgpr_read_b32 v195, a114\n"
"v_mfma_f32_16x16x32_bf16 v[16:19], a[84:87], a[36:39], v[16:19]\n"
"v_fmac_f32 v252, v251, v195\n"
"v_and_b32 v253, s82, v253\n"
"v_mfma_f32_16x16x32_bf16 v[20:23], a[80:83], a[40:43], v[20:23]\n"
"v_and_b32 v239, s82, v239\n"
"v_accvgpr_read_b32 v251, a115\n"
"v_mfma_f32_16x16x32_bf16 v[20:23], a[84:87], a[44:47], v[20:23]\n"
"s_waitcnt lgkmcnt(1)\n"
"v_fmac_f32 v239, v253, v251\n"
"v_cvt_pk_bf16_f32 v195, v252, v239\n"
"v_mfma_f32_16x16x32_bf16 v[24:27], a[80:83], a[48:51], v[24:27]\n"
"ds_write_b128 v250, v[192:195] offset:32768\n"
"v_mfma_f32_16x16x32_bf16 v[24:27], a[84:87], a[52:55], v[24:27]\n"
"ds_read_b128 v[236:239], v250 offset:36864\n"
"v_accvgpr_read_b32 v253, a232\n"
"v_mfma_f32_16x16x32_bf16 v[28:31], a[80:83], a[56:59], v[28:31]\n"
"buffer_load_dwordx4 a[80:83], v240, s[36:39], s80 offen offset:2048 sc1\n"
"v_lshlrev_b32 v251, 16, v253\n"
"v_mfma_f32_16x16x32_bf16 v[28:31], a[84:87], a[60:63], v[28:31]\n"
"s_waitcnt vmcnt(14)\n"
"buffer_store_dwordx4 v[192:195], v240, s[52:55], s68 offen offset:3072 sc1\n"
"v_mfma_f32_16x16x32_bf16 v[32:35], a[88:91], a[0:3], v[32:35]\n"
"v_lshlrev_b32 v252, 16, v232\n"
"v_fmac_f32 v252, v251, v196\n"
"v_mfma_f32_16x16x32_bf16 v[32:35], a[92:95], a[4:7], v[32:35]\n"
"v_and_b32 v253, s82, v253\n"
"v_and_b32 v232, s82, v232\n"
"v_mfma_f32_16x16x32_bf16 v[36:39], a[88:91], a[8:11], v[36:39]\n"
"v_fmac_f32 v232, v253, v197\n"
"v_cvt_pk_bf16_f32 v196, v252, v232\n"
"v_mfma_f32_16x16x32_bf16 v[36:39], a[92:95], a[12:15], v[36:39]\n"
"v_accvgpr_read_b32 v253, a233\n"
"v_lshlrev_b32 v251, 16, v253\n"
"v_mfma_f32_16x16x32_bf16 v[40:43], a[88:91], a[16:19], v[40:43]\n"
"v_lshlrev_b32 v252, 16, v233\n"
"v_fmac_f32 v252, v251, v198\n"
"v_mfma_f32_16x16x32_bf16 v[40:43], a[92:95], a[20:23], v[40:43]\n"
"v_and_b32 v253, s82, v253\n"
"v_and_b32 v233, s82, v233\n"
"v_mfma_f32_16x16x32_bf16 v[44:47], a[88:91], a[24:27], v[44:47]\n"
"buffer_load_dwordx4 a[84:87], v240, s[36:39], s80 offen offset:3072 sc1\n"
"v_fmac_f32 v233, v253, v199\n"
"v_mfma_f32_16x16x32_bf16 v[44:47], a[92:95], a[28:31], v[44:47]\n"
"s_add_u32 s80, s80, 0x1000\n"
"v_cvt_pk_bf16_f32 v197, v252, v233\n"
"v_accvgpr_read_b32 v253, a234\n"
"v_mfma_f32_16x16x32_bf16 v[48:51], a[88:91], a[32:35], v[48:51]\n"
"v_lshlrev_b32 v251, 16, v253\n"
"v_lshlrev_b32 v252, 16, v234\n"
"v_mfma_f32_16x16x32_bf16 v[48:51], a[92:95], a[36:39], v[48:51]\n"
"v_accvgpr_read_b32 v198, a116\n"
"v_fmac_f32 v252, v251, v198\n"
"v_mfma_f32_16x16x32_bf16 v[52:55], a[88:91], a[40:43], v[52:55]\n"
"v_and_b32 v253, s82, v253\n"
"v_and_b32 v234, s82, v234\n"
"v_mfma_f32_16x16x32_bf16 v[52:55], a[92:95], a[44:47], v[52:55]\n"
"v_accvgpr_read_b32 v251, a117\n"
"v_fmac_f32 v234, v253, v251\n"
"v_mfma_f32_16x16x32_bf16 v[56:59], a[88:91], a[48:51], v[56:59]\n"
"v_cvt_pk_bf16_f32 v198, v252, v234\n"
"v_accvgpr_read_b32 v253, a235\n"
"v_mfma_f32_16x16x32_bf16 v[56:59], a[92:95], a[52:55], v[56:59]\n"
"v_lshlrev_b32 v251, 16, v253\n"
"v_lshlrev_b32 v252, 16, v235\n"
"v_mfma_f32_16x16x32_bf16 v[60:63], a[88:91], a[56:59], v[60:63]\n"
"buffer_load_dwordx4 a[88:91], v240, s[36:39], s80 offen offset:0 sc1\n"
"v_accvgpr_read_b32 v199, a118\n"
"v_mfma_f32_16x16x32_bf16 v[60:63], a[92:95], a[60:63], v[60:63]\n"
"s_waitcnt vmcnt(15)\n"
"v_fmac_f32 v252, v251, v199\n"
"v_and_b32 v253, s82, v253\n"
"v_mfma_f32_16x16x32_bf16 v[64:67], a[96:99], a[0:3], v[64:67]\n"
"v_and_b32 v235, s82, v235\n"
"v_accvgpr_read_b32 v251, a119\n"
"v_mfma_f32_16x16x32_bf16 v[64:67], a[100:103], a[4:7], v[64:67]\n"
"v_fmac_f32 v235, v253, v251\n"
"v_cvt_pk_bf16_f32 v199, v252, v235\n"
"v_mfma_f32_16x16x32_bf16 v[68:71], a[96:99], a[8:11], v[68:71]\n"
"buffer_store_dwordx4 v[196:199], v240, s[52:55], s69 offen offset:3072 sc1\n"
"v_mfma_f32_16x16x32_bf16 v[68:71], a[100:103], a[12:15], v[68:71]\n"
"ds_write_b128 v250, v[196:199] offset:34816\n"
"v_mfma_f32_16x16x32_bf16 v[72:75], a[96:99], a[16:19], v[72:75]\n"
"s_waitcnt lgkmcnt(1)\n"
"ds_read_b128 v[232:235], v250 offset:38912\n"
"v_accvgpr_read_b32 v253, a236\n"
"v_mfma_f32_16x16x32_bf16 v[72:75], a[100:103], a[20:23], v[72:75]\n"
"v_lshlrev_b32 v251, 16, v253\n"
"v_lshlrev_b32 v252, 16, v236\n"
"v_mfma_f32_16x16x32_bf16 v[76:79], a[96:99], a[24:27], v[76:79]\n"
"buffer_load_dwordx4 a[92:95], v240, s[36:39], s80 offen offset:1024 sc1\n"
"v_fmac_f32 v252, v251, v200\n"
"v_mfma_f32_16x16x32_bf16 v[76:79], a[100:103], a[28:31], v[76:79]\n"
"v_and_b32 v253, s82, v253\n"
"v_and_b32 v236, s82, v236\n"
"v_mfma_f32_16x16x32_bf16 v[80:83], a[96:99], a[32:35], v[80:83]\n"
"v_fmac_f32 v236, v253, v201\n"
"v_cvt_pk_bf16_f32 v200, v252, v236\n"
"v_mfma_f32_16x16x32_bf16 v[80:83], a[100:103], a[36:39], v[80:83]\n"
"v_accvgpr_read_b32 v253, a237\n"
"v_lshlrev_b32 v251, 16, v253\n"
"v_mfma_f32_16x16x32_bf16 v[84:87], a[96:99], a[40:43], v[84:87]\n"
"v_lshlrev_b32 v252, 16, v237\n"
"v_fmac_f32 v252, v251, v202\n"
"v_mfma_f32_16x16x32_bf16 v[84:87], a[100:103], a[44:47], v[84:87]\n"
"v_and_b32 v253, s82, v253\n"
"v_and_b32 v237, s82, v237\n"
"v_mfma_f32_16x16x32_bf16 v[88:91], a[96:99], a[48:51], v[88:91]\n"
"v_fmac_f32 v237, v253, v203\n"
"v_cvt_pk_bf16_f32 v201, v252, v237\n"
"v_mfma_f32_16x16x32_bf16 v[88:91], a[100:103], a[52:55], v[88:91]\n"
"s_barrier\n"
"v_accvgpr_read_b32 v253, a238\n"
"v_mfma_f32_16x16x32_bf16 v[92:95], a[96:99], a[56:59], v[92:95]\n"
"buffer_load_dwordx4 a[96:99], v240, s[36:39], s80 offen offset:2048 sc1\n"
"v_lshlrev_b32 v251, 16, v253\n"
"v_mfma_f32_16x16x32_bf16 v[92:95], a[100:103], a[60:63], v[92:95]\n"
"s_waitcnt vmcnt(15)\n"
"v_lshlrev_b32 v252, 16, v238\n"
"v_accvgpr_read_b32 v202, a120\n"
"v_mfma_f32_16x16x32_bf16 v[96:99], a[104:107], a[0:3], v[96:99]\n"
"ds_read_b128 a[0:3], v245 offset:0\n"
"v_fmac_f32 v252, v251, v202\n"
"v_mfma_f32_16x16x32_bf16 v[96:99], a[108:111], a[4:7], v[96:99]\n"
"ds_read_b128 a[4:7], v246 offset:0\n"
"v_and_b32 v253, s82, v253\n"
"v_mfma_f32_16x16x32_bf16 v[100:103], a[104:107], a[8:11], v[100:103]\n"
"ds_read_b128 a[8:11], v245 offset:2048\n"
"v_and_b32 v238, s82, v238\n"
"v_mfma_f32_16x16x32_bf16 v[100:103], a[108:111], a[12:15], v[100:103]\n"
"ds_read_b128 a[12:15], v246 offset:2048\n"
"v_accvgpr_read_b32 v251, a121\n"
"v_mfma_f32_16x16x32_bf16 v[104:107], a[104:107], a[16:19], v[104:107]\n"
"ds_read_b128 a[16:19], v245 offset:4096\n"
"v_fmac_f32 v238, v253, v251\n"
"v_mfma_f32_16x16x32_bf16 v[104:107], a[108:111], a[20:23], v[104:107]\n"
"ds_read_b128 a[20:23], v246 offset:4096\n"
"v_cvt_pk_bf16_f32 v202, v252, v238\n"
"v_mfma_f32_16x16x32_bf16 v[108:111], a[104:107], a[24:27], v[108:111]\n"
"ds_read_b128 a[24:27], v245 offset:6144\n"
"buffer_load_dwordx4 a[100:103], v240, s[36:39], s80 offen offset:3072 sc1\n"
"v_mfma_f32_16x16x32_bf16 v[108:111], a[108:111], a[28:31], v[108:111]\n"
"s_add_u32 s80, s80, 0x1000\n"
"ds_read_b128 a[28:31], v246 offset:6144\n"
"v_accvgpr_read_b32 v253, a239\n"
"v_mfma_f32_16x16x32_bf16 v[112:115], a[104:107], a[32:35], v[112:115]\n"
"ds_read_b128 a[32:35], v245 offset:8192\n"
"v_lshlrev_b32 v251, 16, v253\n"
"v_mfma_f32_16x16x32_bf16 v[112:115], a[108:111], a[36:39], v[112:115]\n"
"ds_read_b128 a[36:39], v246 offset:8192\n"
"v_lshlrev_b32 v252, 16, v239\n"
"v_mfma_f32_16x16x32_bf16 v[116:119], a[104:107], a[40:43], v[116:119]\n"
"ds_read_b128 a[40:43], v245 offset:10240\n"
"v_accvgpr_read_b32 v203, a122\n"
"v_mfma_f32_16x16x32_bf16 v[116:119], a[108:111], a[44:47], v[116:119]\n"
"ds_read_b128 a[44:47], v246 offset:10240\n"
"v_fmac_f32 v252, v251, v203\n"
"v_mfma_f32_16x16x32_bf16 v[120:123], a[104:107], a[48:51], v[120:123]\n"
"ds_read_b128 a[48:51], v245 offset:12288\n"
"v_and_b32 v253, s82, v253\n"
"v_mfma_f32_16x16x32_bf16 v[120:123], a[108:111], a[52:55], v[120:123]\n"
"ds_read_b128 a[52:55], v246 offset:12288\n"
"v_and_b32 v239, s82, v239\n"
"v_mfma_f32_16x16x32_bf16 v[124:127], a[104:107], a[56:59], v[124:127]\n"
"ds_read_b128 a[56:59], v245 offset:14336\n"
"buffer_load_dwordx4 a[104:107], v240, s[36:39], s80 offen offset:0 sc1\n"
"v_mfma_f32_16x16x32_bf16 v[124:127], a[108:111], a[60:63], v[124:127]\n"
"s_waitcnt vmcnt(14) lgkmcnt(11)\n"
"ds_read_b128 a[60:63], v246 offset:14336\n"
"v_accvgpr_read_b32 v251, a123\n"
"v_mfma_f32_16x16x32_bf16 v[0:3], v[224:227], a[0:3], v[0:3]\n"
"v_fmac_f32 v239, v253, v251\n"
"v_cvt_pk_bf16_f32 v203, v252, v239\n"
"v_mfma_f32_16x16x32_bf16 v[0:3], v[228:231], a[4:7], v[0:3]\n"
"buffer_store_dwordx4 v[200:203], v240, s[52:55], s70 offen offset:3072 sc1\n"
"v_mfma_f32_16x16x32_bf16 v[4:7], v[224:227], a[8:11], v[4:7]\n"
"ds_write_b128 v250, v[200:203] offset:36864\n"
"v_mfma_f32_16x16x32_bf16 v[4:7], v[228:231], a[12:15], v[4:7]\n"
"s_waitcnt lgkmcnt(9)\n"
"ds_read_b128 v[236:239], v250 offset:40960\n"
"v_accvgpr_read_b32 v253, a240\n"
"v_mfma_f32_16x16x32_bf16 v[8:11], v[224:227], a[16:19], v[8:11]\n"
"v_lshlrev_b32 v251, 16, v253\n"
"v_lshlrev_b32 v252, 16, v232\n"
"v_mfma_f32_16x16x32_bf16 v[8:11], v[228:231], a[20:23], v[8:11]\n"
"v_fmac_f32 v252, v251, v204\n"
"v_and_b32 v253, s82, v253\n"
"v_mfma_f32_16x16x32_bf16 v[12:15], v[224:227], a[24:27], v[12:15]\n"
"buffer_load_dwordx4 a[108:111], v240, s[36:39], s80 offen offset:1024 sc1\n"
"v_and_b32 v232, s82, v232\n"
"v_mfma_f32_16x16x32_bf16 v[12:15], v[228:231], a[28:31], v[12:15]\n"
"s_waitcnt lgkmcnt(6)\n"
"v_fmac_f32 v232, v253, v205\n"
"v_cvt_pk_bf16_f32 v204, v252, v232\n"
"v_mfma_f32_16x16x32_bf16 v[16:19], v[224:227], a[32:35], v[16:19]\n"
"v_accvgpr_read_b32 v253, a241\n"
"v_lshlrev_b32 v251, 16, v253\n"
"v_mfma_f32_16x16x32_bf16 v[16:19], v[228:231], a[36:39], v[16:19]\n"
"v_lshlrev_b32 v252, 16, v233\n"
"v_fmac_f32 v252, v251, v206\n"
"v_mfma_f32_16x16x32_bf16 v[20:23], v[224:227], a[40:43], v[20:23]\n"
"v_and_b32 v253, s82, v253\n"
"v_and_b32 v233, s82, v233\n"
"v_mfma_f32_16x16x32_bf16 v[20:23], v[228:231], a[44:47], v[20:23]\n"
"s_waitcnt lgkmcnt(2)\n"
"v_fmac_f32 v233, v253, v207\n"
"v_cvt_pk_bf16_f32 v205, v252, v233\n"
"v_mfma_f32_16x16x32_bf16 v[24:27], v[224:227], a[48:51], v[24:27]\n"
"v_accvgpr_read_b32 v253, a242\n"
"v_lshlrev_b32 v251, 16, v253\n"
"v_mfma_f32_16x16x32_bf16 v[24:27], v[228:231], a[52:55], v[24:27]\n"
"v_lshlrev_b32 v252, 16, v234\n"
"v_accvgpr_read_b32 v206, a124\n"
"v_mfma_f32_16x16x32_bf16 v[28:31], v[224:227], a[56:59], v[28:31]\n"
"buffer_load_dwordx4 v[224:227], v240, s[36:39], s80 offen offset:2048 sc1\n"
"v_fmac_f32 v252, v251, v206\n"
"v_mfma_f32_16x16x32_bf16 v[28:31], v[228:231], a[60:63], v[28:31]\n"
"s_waitcnt vmcnt(14)\n"
"v_and_b32 v253, s82, v253\n"
"v_and_b32 v234, s82, v234\n"
"v_mfma_f32_16x16x32_bf16 v[32:35], a[64:67], a[0:3], v[32:35]\n"
"v_accvgpr_read_b32 v251, a125\n"
"v_fmac_f32 v234, v253, v251\n"
"v_mfma_f32_16x16x32_bf16 v[32:35], a[68:71], a[4:7], v[32:35]\n"
"v_cvt_pk_bf16_f32 v206, v252, v234\n"
"v_accvgpr_read_b32 v253, a243\n"
"v_mfma_f32_16x16x32_bf16 v[36:39], a[64:67], a[8:11], v[36:39]\n"
"v_lshlrev_b32 v251, 16, v253\n"
"v_lshlrev_b32 v252, 16, v235\n"
"v_mfma_f32_16x16x32_bf16 v[36:39], a[68:71], a[12:15], v[36:39]\n"
"v_accvgpr_read_b32 v207, a126\n"
"v_fmac_f32 v252, v251, v207\n"
"v_mfma_f32_16x16x32_bf16 v[40:43], a[64:67], a[16:19], v[40:43]\n"
"v_and_b32 v253, s82, v253\n"
"v_and_b32 v235, s82, v235\n"
"v_mfma_f32_16x16x32_bf16 v[40:43], a[68:71], a[20:23], v[40:43]\n"
"v_accvgpr_read_b32 v251, a127\n"
"v_fmac_f32 v235, v253, v251\n"
"v_mfma_f32_16x16x32_bf16 v[44:47], a[64:67], a[24:27], v[44:47]\n"
"buffer_load_dwordx4 v[228:231], v240, s[36:39], s80 offen offset:3072 sc1\n"
"v_cvt_pk_bf16_f32 v207, v252, v235\n"
"v_mfma_f32_16x16x32_bf16 v[44:47], a[68:71], a[28:31], v[44:47]\n"
"s_add_u32 s80, s80, 0x1000\n"
"buffer_store_dwordx4 v[204:207], v240, s[52:55], s71 offen offset:3072 sc1\n"
"v_mfma_f32_16x16x32_bf16 v[48:51], a[64:67], a[32:35], v[48:51]\n"
"ds_write_b128 v250, v[204:207] offset:38912\n"
"v_mfma_f32_16x16x32_bf16 v[48:51], a[68:71], a[36:39], v[48:51]\n"
"s_waitcnt lgkmcnt(1)\n"
"ds_read_b128 v[232:235], v250 offset:43008\n"
"v_accvgpr_read_b32 v253, a244\n"
"v_mfma_f32_16x16x32_bf16 v[52:55], a[64:67], a[40:43], v[52:55]\n"
"v_lshlrev_b32 v251, 16, v253\n"
"v_lshlrev_b32 v252, 16, v236\n"
"v_mfma_f32_16x16x32_bf16 v[52:55], a[68:71], a[44:47], v[52:55]\n"
"v_fmac_f32 v252, v251, v208\n"
"v_and_b32 v253, s82, v253\n"
"v_mfma_f32_16x16x32_bf16 v[56:59], a[64:67], a[48:51], v[56:59]\n"
"v_and_b32 v236, s82, v236\n"
"v_fmac_f32 v236, v253, v209\n"
"v_mfma_f32_16x16x32_bf16 v[56:59], a[68:71], a[52:55], v[56:59]\n"
"v_cvt_pk_bf16_f32 v208, v252, v236\n"
"v_accvgpr_read_b32 v253, a245\n"
"v_mfma_f32_16x16x32_bf16 v[60:63], a[64:67], a[56:59], v[60:63]\n"
"buffer_load_dwordx4 a[64:67], v240, s[36:39], s80 offen offset:0 sc1\n"
"v_lshlrev_b32 v251, 16, v253\n"
"v_mfma_f32_16x16x32_bf16 v[60:63], a[68:71], a[60:63], v[60:63]\n"
"s_waitcnt vmcnt(15)\n"
"v_lshlrev_b32 v252, 16, v237\n"
"v_fmac_f32 v252, v251, v210\n"
"v_mfma_f32_16x16x32_bf16 v[64:67], a[72:75], a[0:3], v[64:67]\n"
"v_and_b32 v253, s82, v253\n"
"v_and_b32 v237, s82, v237\n"
"v_mfma_f32_16x16x32_bf16 v[64:67], a[76:79], a[4:7], v[64:67]\n"
"v_fmac_f32 v237, v253, v211\n"
"v_cvt_pk_bf16_f32 v209, v252, v237\n"
"v_mfma_f32_16x16x32_bf16 v[68:71], a[72:75], a[8:11], v[68:71]\n"
"v_accvgpr_read_b32 v253, a246\n"
"v_lshlrev_b32 v251, 16, v253\n"
"v_mfma_f32_16x16x32_bf16 v[68:71], a[76:79], a[12:15], v[68:71]\n"
"v_lshlrev_b32 v252, 16, v238\n"
"v_accvgpr_read_b32 v210, a128\n"
"v_mfma_f32_16x16x32_bf16 v[72:75], a[72:75], a[16:19], v[72:75]\n"
"v_fmac_f32 v252, v251, v210\n"
"v_and_b32 v253, s82, v253\n"
"v_mfma_f32_16x16x32_bf16 v[72:75], a[76:79], a[20:23], v[72:75]\n"
"v_and_b32 v238, s82, v238\n"
"v_accvgpr_read_b32 v251, a129\n"
"v_mfma_f32_16x16x32_bf16 v[76:79], a[72:75], a[24:27], v[76:79]\n"
"buffer_load_dwordx4 a[68:71], v240, s[36:39], s80 offen offset:1024 sc1\n"
"v_fmac_f32 v238, v253, v251\n"
"v_mfma_f32_16x16x32_bf16 v[76:79], a[76:79], a[28:31], v[76:79]\n"
"v_cvt_pk_bf16_f32 v210, v252, v238\n"
"v_accvgpr_read_b32 v253, a247\n"
"v_mfma_f32_16x16x32_bf16 v[80:83], a[72:75], a[32:35], v[80:83]\n"
"v_lshlrev_b32 v251, 16, v253\n"
"v_lshlrev_b32 v252, 16, v239\n"
"v_mfma_f32_16x16x32_bf16 v[80:83], a[76:79], a[36:39], v[80:83]\n"
"v_accvgpr_read_b32 v211, a130\n"
"v_fmac_f32 v252, v251, v211\n"
"v_mfma_f32_16x16x32_bf16 v[84:87], a[72:75], a[40:43], v[84:87]\n"
"v_and_b32 v253, s82, v253\n"
"v_and_b32 v239, s82, v239\n"
"v_mfma_f32_16x16x32_bf16 v[84:87], a[76:79], a[44:47], v[84:87]\n"
"v_accvgpr_read_b32 v251, a131\n"
"v_fmac_f32 v239, v253, v251\n"
"v_mfma_f32_16x16x32_bf16 v[88:91], a[72:75], a[48:51], v[88:91]\n"
"s_waitcnt lgkmcnt(0)\n"
"v_cvt_pk_bf16_f32 v211, v252, v239\n"
"v_accvgpr_read_b32 v253, a248\n"
"v_mfma_f32_16x16x32_bf16 v[88:91], a[76:79], a[52:55], v[88:91]\n"
"ds_write_b128 v250, v[208:211] offset:40960\n"
"v_mfma_f32_16x16x32_bf16 v[92:95], a[72:75], a[56:59], v[92:95]\n"
"ds_read_b128 v[236:239], v250 offset:45056\n"
"buffer_load_dwordx4 a[72:75], v240, s[36:39], s80 offen offset:2048 sc1\n"
"v_mfma_f32_16x16x32_bf16 v[92:95], a[76:79], a[60:63], v[92:95]\n"
"s_waitcnt vmcnt(14)\n"
"buffer_load_dword v241, v240, s[64:67], 0 offen offset:2048 sc1\n"
"v_lshlrev_b32 v251, 16, v253\n"
"v_mfma_f32_16x16x32_bf16 v[96:99], a[80:83], a[0:3], v[96:99]\n"
"ds_read_b128 a[0:3], v245 offset:16384\n"
"v_lshlrev_b32 v252, 16, v232\n"
"v_mfma_f32_16x16x32_bf16 v[96:99], a[84:87], a[4:7], v[96:99]\n"
"ds_read_b128 a[4:7], v246 offset:16384\n"
"v_fmac_f32 v252, v251, v212\n"
"v_mfma_f32_16x16x32_bf16 v[100:103], a[80:83], a[8:11], v[100:103]\n"
"ds_read_b128 a[8:11], v245 offset:18432\n"
"v_and_b32 v253, s82, v253\n"
"v_mfma_f32_16x16x32_bf16 v[100:103], a[84:87], a[12:15], v[100:103]\n"
"ds_read_b128 a[12:15], v246 offset:18432\n"
"v_and_b32 v232, s82, v232\n"
"v_mfma_f32_16x16x32_bf16 v[104:107], a[80:83], a[16:19], v[104:107]\n"
"ds_read_b128 a[16:19], v245 offset:20480\n"
"v_fmac_f32 v232, v253, v213\n"
"v_mfma_f32_16x16x32_bf16 v[104:107], a[84:87], a[20:23], v[104:107]\n"
"ds_read_b128 a[20:23], v246 offset:20480\n"
"v_cvt_pk_bf16_f32 v212, v252, v232\n"
"v_mfma_f32_16x16x32_bf16 v[108:111], a[80:83], a[24:27], v[108:111]\n"
"ds_read_b128 a[24:27], v245 offset:22528\n"
"buffer_load_dwordx4 a[76:79], v240, s[36:39], s80 offen offset:3072 sc1\n"
"v_mfma_f32_16x16x32_bf16 v[108:111], a[84:87], a[28:31], v[108:111]\n"
"s_add_u32 s80, s80, 0x1000\n"
"ds_read_b128 a[28:31], v246 offset:22528\n"
"v_accvgpr_read_b32 v253, a249\n"
"v_mfma_f32_16x16x32_bf16 v[112:115], a[80:83], a[32:35], v[112:115]\n"
"ds_read_b128 a[32:35], v245 offset:24576\n"
"v_lshlrev_b32 v251, 16, v253\n"
"v_mfma_f32_16x16x32_bf16 v[112:115], a[84:87], a[36:39], v[112:115]\n"
"ds_read_b128 a[36:39], v246 offset:24576\n"
"v_lshlrev_b32 v252, 16, v233\n"
"v_mfma_f32_16x16x32_bf16 v[116:119], a[80:83], a[40:43], v[116:119]\n"
"ds_read_b128 a[40:43], v245 offset:26624\n"
"v_fmac_f32 v252, v251, v214\n"
"v_mfma_f32_16x16x32_bf16 v[116:119], a[84:87], a[44:47], v[116:119]\n"
"ds_read_b128 a[44:47], v246 offset:26624\n"
"v_and_b32 v253, s82, v253\n"
"v_mfma_f32_16x16x32_bf16 v[120:123], a[80:83], a[48:51], v[120:123]\n"
"ds_read_b128 a[48:51], v245 offset:28672\n"
"v_and_b32 v233, s82, v233\n"
"v_mfma_f32_16x16x32_bf16 v[120:123], a[84:87], a[52:55], v[120:123]\n"
"ds_read_b128 a[52:55], v246 offset:28672\n"
"v_fmac_f32 v233, v253, v215\n"
"v_mfma_f32_16x16x32_bf16 v[124:127], a[80:83], a[56:59], v[124:127]\n"
"ds_read_b128 a[56:59], v245 offset:30720\n"
"buffer_load_dwordx4 a[80:83], v240, s[36:39], s80 offen offset:0 sc1\n"
"v_mfma_f32_16x16x32_bf16 v[124:127], a[84:87], a[60:63], v[124:127]\n"
"s_waitcnt vmcnt(14) lgkmcnt(11)\n"
"ds_read_b128 a[60:63], v246 offset:30720\n"
"v_cvt_pk_bf16_f32 v213, v252, v233\n"
"v_mfma_f32_16x16x32_bf16 v[0:3], a[88:91], a[0:3], v[0:3]\n"
"buffer_store_dwordx4 v[208:211], v240, s[52:55], s76 offen offset:3072 sc1\n"
"v_mfma_f32_16x16x32_bf16 v[0:3], a[92:95], a[4:7], v[0:3]\n"
"v_accvgpr_read_b32 v253, a250\n"
"v_lshlrev_b32 v251, 16, v253\n"
"v_mfma_f32_16x16x32_bf16 v[4:7], a[88:91], a[8:11], v[4:7]\n"
"v_lshlrev_b32 v252, 16, v234\n"
"v_accvgpr_read_b32 v214, a132\n"
"v_mfma_f32_16x16x32_bf16 v[4:7], a[92:95], a[12:15], v[4:7]\n"
"s_waitcnt lgkmcnt(8)\n"
"v_fmac_f32 v252, v251, v214\n"
"v_and_b32 v253, s82, v253\n"
"v_mfma_f32_16x16x32_bf16 v[8:11], a[88:91], a[16:19], v[8:11]\n"
"v_and_b32 v234, s82, v234\n"
"v_accvgpr_read_b32 v251, a133\n"
"v_mfma_f32_16x16x32_bf16 v[8:11], a[92:95], a[20:23], v[8:11]\n"
"v_fmac_f32 v234, v253, v251\n"
"v_cvt_pk_bf16_f32 v214, v252, v234\n"
"v_mfma_f32_16x16x32_bf16 v[12:15], a[88:91], a[24:27], v[12:15]\n"
"buffer_load_dwordx4 a[84:87], v240, s[36:39], s80 offen offset:1024 sc1\n"
"v_accvgpr_read_b32 v253, a251\n"
"v_mfma_f32_16x16x32_bf16 v[12:15], a[92:95], a[28:31], v[12:15]\n"
"s_waitcnt lgkmcnt(4)\n"
"v_lshlrev_b32 v251, 16, v253\n"
"v_lshlrev_b32 v252, 16, v235\n"
"v_mfma_f32_16x16x32_bf16 v[16:19], a[88:91], a[32:35], v[16:19]\n"
"v_accvgpr_read_b32 v215, a134\n"
"v_fmac_f32 v252, v251, v215\n"
"v_mfma_f32_16x16x32_bf16 v[16:19], a[92:95], a[36:39], v[16:19]\n"
"v_and_b32 v253, s82, v253\n"
"v_and_b32 v235, s82, v235\n"
"v_mfma_f32_16x16x32_bf16 v[20:23], a[88:91], a[40:43], v[20:23]\n"
"v_accvgpr_read_b32 v251, a135\n"
"v_fmac_f32 v235, v253, v251\n"
"v_mfma_f32_16x16x32_bf16 v[20:23], a[92:95], a[44:47], v[20:23]\n"
"s_waitcnt lgkmcnt(0)\n"
"v_cvt_pk_bf16_f32 v215, v252, v235\n"
"v_accvgpr_read_b32 v253, a252\n"
"v_mfma_f32_16x16x32_bf16 v[24:27], a[88:91], a[48:51], v[24:27]\n"
"ds_write_b128 v250, v[212:215] offset:43008\n"
"v_mfma_f32_16x16x32_bf16 v[24:27], a[92:95], a[52:55], v[24:27]\n"
"v_lshlrev_b32 v251, 16, v253\n"
"v_lshlrev_b32 v252, 16, v236\n"
"v_mfma_f32_16x16x32_bf16 v[28:31], a[88:91], a[56:59], v[28:31]\n"
"buffer_load_dwordx4 a[88:91], v240, s[36:39], s80 offen offset:2048 sc1\n"
"v_fmac_f32 v252, v251, v216\n"
"v_mfma_f32_16x16x32_bf16 v[28:31], a[92:95], a[60:63], v[28:31]\n"
"s_waitcnt vmcnt(15)\n"
"buffer_store_dwordx4 v[212:215], v240, s[52:55], s77 offen offset:3072 sc1\n"
"v_mfma_f32_16x16x32_bf16 v[32:35], a[96:99], a[0:3], v[32:35]\n"
"v_and_b32 v253, s82, v253\n"
"v_and_b32 v236, s82, v236\n"
"v_mfma_f32_16x16x32_bf16 v[32:35], a[100:103], a[4:7], v[32:35]\n"
"v_fmac_f32 v236, v253, v217\n"
"v_cvt_pk_bf16_f32 v216, v252, v236\n"
"v_mfma_f32_16x16x32_bf16 v[36:39], a[96:99], a[8:11], v[36:39]\n"
"v_accvgpr_read_b32 v253, a253\n"
"v_lshlrev_b32 v251, 16, v253\n"
"v_mfma_f32_16x16x32_bf16 v[36:39], a[100:103], a[12:15], v[36:39]\n"
"v_lshlrev_b32 v252, 16, v237\n"
"v_fmac_f32 v252, v251, v218\n"
"v_mfma_f32_16x16x32_bf16 v[40:43], a[96:99], a[16:19], v[40:43]\n"
"v_and_b32 v253, s82, v253\n"
"v_and_b32 v237, s82, v237\n"
"v_mfma_f32_16x16x32_bf16 v[40:43], a[100:103], a[20:23], v[40:43]\n"
"v_fmac_f32 v237, v253, v219\n"
"v_cvt_pk_bf16_f32 v217, v252, v237\n"
"v_mfma_f32_16x16x32_bf16 v[44:47], a[96:99], a[24:27], v[44:47]\n"
"buffer_load_dwordx4 a[92:95], v240, s[36:39], s80 offen offset:3072 sc1\n"
"v_accvgpr_read_b32 v253, a254\n"
"v_mfma_f32_16x16x32_bf16 v[44:47], a[100:103], a[28:31], v[44:47]\n"
"s_add_u32 s80, s80, 0x1000\n"
"v_lshlrev_b32 v251, 16, v253\n"
"v_lshlrev_b32 v252, 16, v238\n"
"v_mfma_f32_16x16x32_bf16 v[48:51], a[96:99], a[32:35], v[48:51]\n"
"v_accvgpr_read_b32 v218, a136\n"
"v_fmac_f32 v252, v251, v218\n"
"v_mfma_f32_16x16x32_bf16 v[48:51], a[100:103], a[36:39], v[48:51]\n"
"v_and_b32 v253, s82, v253\n"
"v_and_b32 v238, s82, v238\n"
"v_mfma_f32_16x16x32_bf16 v[52:55], a[96:99], a[40:43], v[52:55]\n"
"v_accvgpr_read_b32 v251, a137\n"
"v_fmac_f32 v238, v253, v251\n"
"v_mfma_f32_16x16x32_bf16 v[52:55], a[100:103], a[44:47], v[52:55]\n"
"v_cvt_pk_bf16_f32 v218, v252, v238\n"
"v_accvgpr_read_b32 v253, a255\n"
"v_mfma_f32_16x16x32_bf16 v[56:59], a[96:99], a[48:51], v[56:59]\n"
"v_lshlrev_b32 v251, 16, v253\n"
"v_lshlrev_b32 v252, 16, v239\n"
"v_mfma_f32_16x16x32_bf16 v[56:59], a[100:103], a[52:55], v[56:59]\n"
"v_accvgpr_read_b32 v219, a138\n"
"v_fmac_f32 v252, v251, v219\n"
"v_mfma_f32_16x16x32_bf16 v[60:63], a[96:99], a[56:59], v[60:63]\n"
"buffer_load_dwordx4 a[96:99], v240, s[36:39], s80 offen offset:0 sc1\n"
"v_and_b32 v253, s82, v253\n"
"v_mfma_f32_16x16x32_bf16 v[60:63], a[100:103], a[60:63], v[60:63]\n"
"s_waitcnt vmcnt(15)\n"
"v_and_b32 v239, s82, v239\n"
"v_accvgpr_read_b32 v251, a139\n"
"v_mfma_f32_16x16x32_bf16 v[64:67], a[104:107], a[0:3], v[64:67]\n"
"v_fmac_f32 v239, v253, v251\n"
"v_cvt_pk_bf16_f32 v219, v252, v239\n"
"v_mfma_f32_16x16x32_bf16 v[64:67], a[108:111], a[4:7], v[64:67]\n"
"buffer_store_dwordx4 v[216:219], v240, s[52:55], s78 offen offset:3072 sc1\n"
"v_mfma_f32_16x16x32_bf16 v[68:71], a[104:107], a[8:11], v[68:71]\n"
"ds_write_b128 v250, v[216:219] offset:45056\n"
"v_mfma_f32_16x16x32_bf16 v[68:71], a[108:111], a[12:15], v[68:71]\n"
"s_mov_b64 exec, 0\n"
"s_cmp_eq_u32 s97, 0\n"
"s_cbranch_scc1 .Lpad15_%=\n"
"s_cmp_eq_u32 s97, 1\n"
"s_cbranch_scc1 .Lpad16_%=\n"
"s_cmp_eq_u32 s97, 2\n"
"s_cbranch_scc1 .Lpad17_%=\n"
"s_branch .Lpad18_%=\n"
".Lret2_%=:\n"
"s_mov_b64 exec, -1\n"
"v_mfma_f32_16x16x32_bf16 v[72:75], a[104:107], a[16:19], v[72:75]\n"
"v_mfma_f32_16x16x32_bf16 v[72:75], a[108:111], a[20:23], v[72:75]\n"
"v_mfma_f32_16x16x32_bf16 v[76:79], a[104:107], a[24:27], v[76:79]\n"
"buffer_load_dwordx4 a[100:103], v240, s[36:39], s80 offen offset:1024 sc1\n"
"v_mfma_f32_16x16x32_bf16 v[76:79], a[108:111], a[28:31], v[76:79]\n"
"v_mfma_f32_16x16x32_bf16 v[80:83], a[104:107], a[32:35], v[80:83]\n"
"v_mfma_f32_16x16x32_bf16 v[80:83], a[108:111], a[36:39], v[80:83]\n"
"v_mfma_f32_16x16x32_bf16 v[84:87], a[104:107], a[40:43], v[84:87]\n"
"v_mfma_f32_16x16x32_bf16 v[84:87], a[108:111], a[44:47], v[84:87]\n"
"s_waitcnt vmcnt(10)\n"
"v_cmp_ne_u32 vcc, s33, v241\n"
"v_mfma_f32_16x16x32_bf16 v[88:91], a[104:107], a[48:51], v[88:91]\n"
"v_mfma_f32_16x16x32_bf16 v[88:91], a[108:111], a[52:55], v[88:91]\n"
"s_waitcnt lgkmcnt(0)\n"
"s_barrier\n"
"v_mfma_f32_16x16x32_bf16 v[92:95], a[104:107], a[56:59], v[92:95]\n"
"s_cmp_eq_u64 vcc, 0\n"
"s_cbranch_scc0 .Lslow2_%=\n"
".Lback2_%=:\n"
"buffer_load_dwordx4 a[104:107], v240, s[36:39], s80 offen offset:2048 sc1\n"
"v_mfma_f32_16x16x32_bf16 v[92:95], a[108:111], a[60:63], v[92:95]\n"
"v_mfma_f32_16x16x32_bf16 v[96:99], v[224:227], a[0:3], v[96:99]\n"
".Lpad16_%=:\n"
"s_cbranch_execz .Lpad20_%=\n"
"ds_read_b128 a[0:3], v245 offset:32768\n"
"v_mfma_f32_16x16x32_bf16 v[96:99], v[228:231], a[4:7], v[96:99]\n"
"ds_read_b128 a[4:7], v246 offset:32768\n"
"v_mfma_f32_16x16x32_bf16 v[100:103], v[224:227], a[8:11], v[100:103]\n"
"ds_read_b128 a[8:11], v245 offset:34816\n"
"v_mfma_f32_16x16x32_bf16 v[100:103], v[228:231], a[12:15], v[100:103]\n"
"ds_read_b128 a[12:15], v246 offset:34816\n"
"v_mfma_f32_16x16x32_bf16 v[104:107], v[224:227], a[16:19], v[104:107]\n"
"ds_read_b128 a[16:19], v245 offset:36864\n"
"v_mfma_f32_16x16x32_bf16 v[104:107], v[228:231], a[20:23], v[104:107]\n"
"ds_read_b128 a[20:23], v246 offset:36864\n"
"v_mfma_f32_16x16x32_bf16 v[108:111], v[224:227], a[24:27], v[108:111]\n"
"ds_read_b128 a[24:27], v245 offset:38912\n"
"buffer_load_dwordx4 a[108:111], v240, s[36:39], s80 offen offset:3072 sc1\n"
"v_mfma_f32_16x16x32_bf16 v[108:111], v[228:231], a[28:31], v[108:111]\n"
"s_add_u32 s80, s80, 0x1000\n"
"ds_read_b128 a[28:31], v246 offset:38912\n"
"v_mfma_f32_16x16x32_bf16 v[112:115], v[224:227], a[32:35], v[112:115]\n"
"ds_read_b128 a[32:35], v245 offset:40960\n"
"v_mfma_f32_16x16x32_bf16 v[112:115], v[228:231], a[36:39], v[112:115]\n"
"ds_read_b128 a[36:39], v246 offset:40960\n"
"v_mfma_f32_16x16x32_bf16 v[116:119], v[224:227], a[40:43], v[116:119]\n"
"ds_read_b128 a[40:43], v245 offset:43008\n"
"v_mfma_f32_16x16x32_bf16 v[116:119], v[228:231], a[44:47], v[116:119]\n"
"ds_read_b128 a[44:47], v246 offset:43008\n"
"v_mfma_f32_16x16x32_bf16 v[120:123], v[224:227], a[48:51], v[120:123]\n"
"ds_read_b128 a[48:51], v245 offset:45056\n"
"v_mfma_f32_16x16x32_bf16 v[120:123], v[228:231], a[52:55], v[120:123]\n"
"ds_read_b128 a[52:55], v246 offset:45056\n"
"v_mfma_f32_16x16x32_bf16 v[124:127], v[224:227], a[56:59], v[124:127]\n"
"ds_read_b128 a[56:59], v245 offset:47104\n"
"buffer_load_dwordx4 v[224:227], v240, s[36:39], s80 offen offset:0 sc1\n"
"v_mfma_f32_16x16x32_bf16 v[124:127], v[228:231], a[60:63], v[124:127]\n"
"s_waitcnt lgkmcnt(11)\n"
"ds_read_b128 a[60:63], v246 offset:47104\n"
"v_mfma_f32_16x16x32_bf16 v[0:3], a[64:67], a[0:3], v[0:3]\n"
"s_add_u32 m0, s83, 131072\n"
"v_mfma_f32_16x16x32_bf16 v[0:3], a[68:71], a[4:7], v[0:3]\n"
"buffer_load_dwordx4 v240, s[56:59], s68 offen offset:0 sc1 lds\n"
"v_mfma_f32_16x16x32_bf16 v[4:7], a[64:67], a[8:11], v[4:7]\n"
"v_mfma_f32_16x16x32_bf16 v[4:7], a[68:71], a[12:15], v[4:7]\n"
"s_waitcnt lgkmcnt(8)\n"
"v_mfma_f32_16x16x32_bf16 v[8:11], a[64:67], a[16:19], v[8:11]\n"
"v_mfma_f32_16x16x32_bf16 v[8:11], a[68:71], a[20:23], v[8:11]\n"
"v_mfma_f32_16x16x32_bf16 v[12:15], a[64:67], a[24:27], v[12:15]\n"
"s_add_u32 m0, s83, 133120\n"
"buffer_load_dwordx4 v[228:231], v240, s[36:39], s80 offen offset:1024 sc1\n"
"v_mfma_f32_16x16x32_bf16 v[12:15], a[68:71], a[28:31], v[12:15]\n"
"s_waitcnt lgkmcnt(4)\n"
"buffer_load_dwordx4 v240, s[56:59], s69 offen offset:0 sc1 lds\n"
"v_mfma_f32_16x16x32_bf16 v[16:19], a[64:67], a[32:35], v[16:19]\n"
"v_mfma_f32_16x16x32_bf16 v[16:19], a[68:71], a[36:39], v[16:19]\n"
"v_mfma_f32_16x16x32_bf16 v[20:23], a[64:67], a[40:43], v[20:23]\n"
"v_mfma_f32_16x16x32_bf16 v[20:23], a[68:71], a[44:47], v[20:23]\n"
"s_waitcnt lgkmcnt(0)\n"
"v_mfma_f32_16x16x32_bf16 v[24:27], a[64:67], a[48:51], v[24:27]\n"
"s_add_u32 m0, s83, 135168\n"
"v_mfma_f32_16x16x32_bf16 v[24:27], a[68:71], a[52:55], v[24:27]\n"
"buffer_load_dwordx4 v240, s[56:59], s70 offen offset:0 sc1 lds\n"
"v_mfma_f32_16x16x32_bf16 v[28:31], a[64:67], a[56:59], v[28:31]\n"
"buffer_load_dwordx4 a[64:67], v240, s[36:39], s80 offen offset:2048 sc1\n"
"v_mfma_f32_16x16x32_bf16 v[28:31], a[68:71], a[60:63], v[28:31]\n"
"s_waitcnt vmcnt(17)\n"
"v_mfma_f32_16x16x32_bf16 v[32:35], a[72:75], a[0:3], v[32:35]\n"
"v_mfma_f32_16x16x32_bf16 v[32:35], a[76:79], a[4:7], v[32:35]\n"
"v_mfma_f32_16x16x32_bf16 v[36:39], a[72:75], a[8:11], v[36:39]\n"
"s_add_u32 m0, s83, 137216\n"
"v_mfma_f32_16x16x32_bf16 v[36:39], a[76:79], a[12:15], v[36:39]\n"
"buffer_load_dwordx4 v240, s[56:59], s71 offen offset:0 sc1 lds\n"
"v_mfma_f32_16x16x32_bf16 v[40:43], a[72:75], a[16:19], v[40:43]\n"
"v_mfma_f32_16x16x32_bf16 v[40:43], a[76:79], a[20:23], v[40:43]\n"
"v_mfma_f32_16x16x32_bf16 v[44:47], a[72:75], a[24:27], v[44:47]\n"
"buffer_load_dwordx4 a[68:71], v240, s[36:39], s80 offen offset:3072 sc1\n"
"v_mfma_f32_16x16x32_bf16 v[44:47], a[76:79], a[28:31], v[44:47]\n"
"s_add_u32 s80, s80, 0x1000\n"
"v_mfma_f32_16x16x32_bf16 v[48:51], a[72:75], a[32:35], v[48:51]\n"
"v_mfma_f32_16x16x32_bf16 v[48:51], a[76:79], a[36:39], v[48:51]\n"
"v_mfma_f32_16x16x32_bf16 v[52:55], a[72:75], a[40:43], v[52:55]\n"
"v_mfma_f32_16x16x32_bf16 v[52:55], a[76:79], a[44:47], v[52:55]\n"
"v_mfma_f32_16x16x32_bf16 v[56:59], a[72:75], a[48:51], v[56:59]\n"
"v_mfma_f32_16x16x32_bf16 v[56:59], a[76:79], a[52:55], v[56:59]\n"
"v_mfma_f32_16x16x32_bf16 v[60:63], a[72:75], a[56:59], v[60:63]\n"
"buffer_load_dwordx4 a[72:75], v240, s[36:39], s80 offen offset:0 sc1\n"
"v_mfma_f32_16x16x32_bf16 v[60:63], a[76:79], a[60:63], v[60:63]\n"
"s_waitcnt vmcnt(17)\n"
"v_mfma_f32_16x16x32_bf16 v[64:67], a[80:83], a[0:3], v[64:67]\n"
"v_mfma_f32_16x16x32_bf16 v[64:67], a[84:87], a[4:7], v[64:67]\n"
"v_mfma_f32_16x16x32_bf16 v[68:71], a[80:83], a[8:11], v[68:71]\n"
"v_mfma_f32_16x16x32_bf16 v[68:71], a[84:87], a[12:15], v[68:71]\n"
"v_mfma_f32_16x16x32_bf16 v[72:75], a[80:83], a[16:19], v[72:75]\n"
"v_mfma_f32_16x16x32_bf16 v[72:75], a[84:87], a[20:23], v[72:75]\n"
"v_mfma_f32_16x16x32_bf16 v[76:79], a[80:83], a[24:27], v[76:79]\n"
"buffer_load_dwordx4 a[76:79], v240, s[36:39], s80 offen offset:1024 sc1\n"
"v_mfma_f32_16x16x32_bf16 v[76:79], a[84:87], a[28:31], v[76:79]\n"
"v_mfma_f32_16x16x32_bf16 v[80:83], a[80:83], a[32:35], v[80:83]\n"
"v_mfma_f32_16x16x32_bf16 v[80:83], a[84:87], a[36:39], v[80:83]\n"
"v_mfma_f32_16x16x32_bf16 v[84:87], a[80:83], a[40:43], v[84:87]\n"
"v_mfma_f32_16x16x32_bf16 v[84:87], a[84:87], a[44:47], v[84:87]\n"
"v_mfma_f32_16x16x32_bf16 v[88:91], a[80:83], a[48:51], v[88:91]\n"
"v_mfma_f32_16x16x32_bf16 v[88:91], a[84:87], a[52:55], v[88:91]\n"
"v_mfma_f32_16x16x32_bf16 v[92:95], a[80:83], a[56:59], v[92:95]\n"
"buffer_load_dwordx4 a[80:83], v240, s[36:39], s80 offen offset:2048 sc1\n"
"v_mfma_f32_16x16x32_bf16 v[92:95], a[84:87], a[60:63], v[92:95]\n"
"s_waitcnt vmcnt(16)\n"
"v_mfma_f32_16x16x32_bf16 v[96:99], a[88:91], a[0:3], v[96:99]\n"
"ds_read_b128 a[0:3], v245 offset:49152\n"
"v_mfma_f32_16x16x32_bf16 v[96:99], a[92:95], a[4:7], v[96:99]\n"
"ds_read_b128 a[4:7], v246 offset:49152\n"
"v_lshrrev_b32 v241, 2, v240\n"
"v_mfma_f32_16x16x32_bf16 v[100:103], a[88:91], a[8:11], v[100:103]\n"
"ds_read_b128 a[8:11], v245 offset:51200\n"
"v_mfma_f32_16x16x32_bf16 v[100:103], a[92:95], a[12:15], v[100:103]\n"
"ds_read_b128 a[12:15], v246 offset:51200\n"
"v_mfma_f32_16x16x32_bf16 v[104:107], a[88:91], a[16:19], v[104:107]\n"
"ds_read_b128 a[16:19], v245 offset:53248\n"
"v_mfma_f32_16x16x32_bf16 v[104:107], a[92:95], a[20:23], v[104:107]\n"
"ds_read_b128 a[20:23], v246 offset:53248\n"
"v_mfma_f32_16x16x32_bf16 v[108:111], a[88:91], a[24:27], v[108:111]\n"
"ds_read_b128 a[24:27], v245 offset:55296\n"
"buffer_load_dwordx4 a[84:87], v240, s[36:39], s80 offen offset:3072 sc1\n"
"v_mfma_f32_16x16x32_bf16 v[108:111], a[92:95], a[28:31], v[108:111]\n"
"s_add_u32 s80, s80, 0x1000\n"
"ds_read_b128 a[28:31], v246 offset:55296\n"
"v_mfma_f32_16x16x32_bf16 v[112:115], a[88:91], a[32:35], v[112:115]\n"
"ds_read_b128 a[32:35], v245 offset:57344\n"
"v_mfma_f32_16x16x32_bf16 v[112:115], a[92:95], a[36:39], v[112:115]\n"
"ds_read_b128 a[36:39], v246 offset:57344\n"
"v_mfma_f32_16x16x32_bf16 v[116:119], a[88:91], a[40:43], v[116:119]\n"
"ds_read_b128 a[40:43], v245 offset:59392\n"
"v_mfma_f32_16x16x32_bf16 v[116:119], a[92:95], a[44:47], v[116:119]\n"
"ds_read_b128 a[44:47], v246 offset:59392\n"
"v_mfma_f32_16x16x32_bf16 v[120:123], a[88:91], a[48:51], v[120:123]\n"
"ds_read_b128 a[48:51], v245 offset:61440\n"
"v_mfma_f32_16x16x32_bf16 v[120:123], a[92:95], a[52:55], v[120:123]\n"
"ds_read_b128 a[52:55], v246 offset:61440\n"
"v_mfma_f32_16x16x32_bf16 v[124:127], a[88:91], a[56:59], v[124:127]\n"
"ds_read_b128 a[56:59], v245 offset:63488\n"
"buffer_load_dwordx4 a[88:91], v240, s[36:39], s80 offen offset:0 sc1\n"
"v_mfma_f32_16x16x32_bf16 v[124:127], a[92:95], a[60:63], v[124:127]\n"
"s_waitcnt vmcnt(15) lgkmcnt(11)\n"
"ds_read_b128 a[60:63], v246 offset:63488\n"
"v_mfma_f32_16x16x32_bf16 v[0:3], a[96:99], a[0:3], v[0:3]\n"
"buffer_store_dword v242, v241, s[60:63], s85 offen offset:3072 sc1\n"
"v_mfma_f32_16x16x32_bf16 v[0:3], a[100:103], a[4:7], v[0:3]\n"
"v_mfma_f32_16x16x32_bf16 v[4:7], a[96:99], a[8:11], v[4:7]\n"
"v_mfma_f32_16x16x32_bf16 v[4:7], a[100:103], a[12:15], v[4:7]\n"
"s_waitcnt lgkmcnt(8)\n"
"v_mfma_f32_16x16x32_bf16 v[8:11], a[96:99], a[16:19], v[8:11]\n"
"v_mfma_f32_16x16x32_bf16 v[8:11], a[100:103], a[20:23], v[8:11]\n"
"v_mfma_f32_16x16x32_bf16 v[12:15], a[96:99], a[24:27], v[12:15]\n"
"buffer_load_dwordx4 a[92:95], v240, s[36:39], s80 offen offset:1024 sc1\n"
"v_mfma_f32_16x16x32_bf16 v[12:15], a[100:103], a[28:31], v[12:15]\n"
"s_waitcnt lgkmcnt(4)\n"
"v_mfma_f32_16x16x32_bf16 v[16:19], a[96:99], a[32:35], v[16:19]\n"
"v_mfma_f32_16x16x32_bf16 v[16:19], a[100:103], a[36:39], v[16:19]\n"
"v_mfma_f32_16x16x32_bf16 v[20:23], a[96:99], a[40:43], v[20:23]\n"
"v_mfma_f32_16x16x32_bf16 v[20:23], a[100:103], a[44:47], v[20:23]\n"
"s_waitcnt lgkmcnt(0)\n"
"v_mfma_f32_16x16x32_bf16 v[24:27], a[96:99], a[48:51], v[24:27]\n"
"v_mfma_f32_16x16x32_bf16 v[24:27], a[100:103], a[52:55], v[24:27]\n"
"v_mfma_f32_16x16x32_bf16 v[28:31], a[96:99], a[56:59], v[28:31]\n"
"buffer_load_dwordx4 a[96:99], v240, s[36:39], s80 offen offset:2048 sc1\n"
"v_mfma_f32_16x16x32_bf16 v[28:31], a[100:103], a[60:63], v[28:31]\n"
"s_waitcnt vmcnt(16)\n"
"v_mfma_f32_16x16x32_bf16 v[32:35], a[104:107], a[0:3], v[32:35]\n"
"v_mfma_f32_16x16x32_bf16 v[32:35], a[108:111], a[4:7], v[32:35]\n"
"v_mfma_f32_16x16x32_bf16 v[36:39], a[104:107], a[8:11], v[36:39]\n"
"v_mfma_f32_16x16x32_bf16 v[36:39], a[108:111], a[12:15], v[36:39]\n"
"v_mfma_f32_16x16x32_bf16 v[40:43], a[104:107], a[16:19], v[40:43]\n"
"v_mfma_f32_16x16x32_bf16 v[40:43], a[108:111], a[20:23], v[40:43]\n"
"v_mfma_f32_16x16x32_bf16 v[44:47], a[104:107], a[24:27], v[44:47]\n"
"buffer_load_dwordx4 a[100:103], v240, s[36:39], s80 offen offset:3072 sc1\n"
"v_mfma_f32_16x16x32_bf16 v[44:47], a[108:111], a[28:31], v[44:47]\n"
"s_add_u32 s80, s80, 0x1000\n"
"v_mfma_f32_16x16x32_bf16 v[48:51], a[104:107], a[32:35], v[48:51]\n"
"v_mfma_f32_16x16x32_bf16 v[48:51], a[108:111], a[36:39], v[48:51]\n"
"v_mfma_f32_16x16x32_bf16 v[52:55], a[104:107], a[40:43], v[52:55]\n"
"v_mfma_f32_16x16x32_bf16 v[52:55], a[108:111], a[44:47], v[52:55]\n"
"v_mfma_f32_16x16x32_bf16 v[56:59], a[104:107], a[48:51], v[56:59]\n"
"v_mfma_f32_16x16x32_bf16 v[56:59], a[108:111], a[52:55], v[56:59]\n"
"v_mfma_f32_16x16x32_bf16 v[60:63], a[104:107], a[56:59], v[60:63]\n"
"buffer_load_dwordx4 a[104:107], v240, s[36:39], s80 offen offset:0 sc1\n"
"v_mfma_f32_16x16x32_bf16 v[60:63], a[108:111], a[60:63], v[60:63]\n"
"s_waitcnt vmcnt(15)\n"
"v_mfma_f32_16x16x32_bf16 v[64:67], v[224:227], a[0:3], v[64:67]\n"
"v_mfma_f32_16x16x32_bf16 v[64:67], v[228:231], a[4:7], v[64:67]\n"
"v_mfma_f32_16x16x32_bf16 v[68:71], v[224:227], a[8:11], v[68:71]\n"
"v_mfma_f32_16x16x32_bf16 v[68:71], v[228:231], a[12:15], v[68:71]\n"
"v_mfma_f32_16x16x32_bf16 v[72:75], v[224:227], a[16:19], v[72:75]\n"
"v_mfma_f32_16x16x32_bf16 v[72:75], v[228:231], a[20:23], v[72:75]\n"
"v_mfma_f32_16x16x32_bf16 v[76:79], v[224:227], a[24:27], v[76:79]\n"
"buffer_load_dwordx4 a[108:111], v240, s[36:39], s80 offen offset:1024 sc1\n"
"v_mfma_f32_16x16x32_bf16 v[76:79], v[228:231], a[28:31], v[76:79]\n"
"v_mfma_f32_16x16x32_bf16 v[80:83], v[224:227], a[32:35], v[80:83]\n"
"v_mfma_f32_16x16x32_bf16 v[80:83], v[228:231], a[36:39], v[80:83]\n"
"v_mfma_f32_16x16x32_bf16 v[84:87], v[224:227], a[40:43], v[84:87]\n"
"v_mfma_f32_16x16x32_bf16 v[84:87], v[228:231], a[44:47], v[84:87]\n"
"v_mfma_f32_16x16x32_bf16 v[88:91], v[224:227], a[48:51], v[88:91]\n"
"v_mfma_f32_16x16x32_bf16 v[88:91], v[228:231], a[52:55], v[88:91]\n"
"s_waitcnt vmcnt(12)\n"
"s_barrier\n"
"v_mfma_f32_16x16x32_bf16 v[92:95], v[224:227], a[56:59], v[92:95]\n"
"buffer_load_dwordx4 v[224:227], v240, s[36:39], s80 offen offset:2048 sc1\n"
"v_mfma_f32_16x16x32_bf16 v[92:95], v[228:231], a[60:63], v[92:95]\n"
"s_waitcnt vmcnt(12)\n"
"v_mfma_f32_16x16x32_bf16 v[96:99], a[64:67], a[0:3], v[96:99]\n"
"ds_read_b128 a[0:3], v247 offset:0\n"
"v_mfma_f32_16x16x32_bf16 v[96:99], a[68:71], a[4:7], v[96:99]\n"
"ds_read_b128 a[4:7], v248 offset:0\n"
"v_mfma_f32_16x16x32_bf16 v[100:103], a[64:67], a[8:11], v[100:103]\n"
"ds_read_b128 a[8:11], v247 offset:2048\n"
"v_mfma_f32_16x16x32_bf16 v[100:103], a[68:71], a[12:15], v[100:103]\n"
"ds_read_b128 a[12:15], v248 offset:2048\n"
"v_mfma_f32_16x16x32_bf16 v[104:107], a[64:67], a[16:19], v[104:107]\n"
"ds_read_b128 a[16:19], v247 offset:4096\n"
"v_mfma_f32_16x16x32_bf16 v[104:107], a[68:71], a[20:23], v[104:107]\n"
"ds_read_b128 a[20:23], v248 offset:4096\n"
"v_mfma_f32_16x16x32_bf16 v[108:111], a[64:67], a[24:27], v[108:111]\n"
"ds_read_b128 a[24:27], v247 offset:6144\n"
"buffer_load_dwordx4 v[228:231], v240, s[36:39], s80 offen offset:3072 sc1\n"
"v_mfma_f32_16x16x32_bf16 v[108:111], a[68:71], a[28:31], v[108:111]\n"
"s_add_u32 s80, s80, 0x1000\n"
"ds_read_b128 a[28:31], v248 offset:6144\n"
"v_mfma_f32_16x16x32_bf16 v[112:115], a[64:67], a[32:35], v[112:115]\n"
"ds_read_b128 a[32:35], v247 offset:8192\n"
"v_mfma_f32_16x16x32_bf16 v[112:115], a[68:71], a[36:39], v[112:115]\n"
"ds_read_b128 a[36:39], v248 offset:8192\n"
"v_mfma_f32_16x16x32_bf16 v[116:119], a[64:67], a[40:43], v[116:119]\n"
"ds_read_b128 a[40:43], v247 offset:10240\n"
"v_mfma_f32_16x16x32_bf16 v[116:119], a[68:71], a[44:47], v[116:119]\n"
"ds_read_b128 a[44:47], v248 offset:10240\n"
"v_mfma_f32_16x16x32_bf16 v[120:123], a[64:67], a[48:51], v[120:123]\n"
"ds_read_b128 a[48:51], v247 offset:12288\n"
"v_mfma_f32_16x16x32_bf16 v[120:123], a[68:71], a[52:55], v[120:123]\n"
"ds_read_b128 a[52:55], v248 offset:12288\n"
"v_mfma_f32_16x16x32_bf16 v[124:127], a[64:67], a[56:59], v[124:127]\n"
"ds_read_b128 a[56:59], v247 offset:14336\n"
"buffer_load_dwordx4 a[64:67], v240, s[36:39], s80 offen offset:0 sc1\n"
"v_mfma_f32_16x16x32_bf16 v[124:127], a[68:71], a[60:63], v[124:127]\n"
"s_waitcnt vmcnt(12) lgkmcnt(11)\n"
"ds_read_b128 a[60:63], v248 offset:14336\n"
"v_mfma_f32_16x16x32_bf16 v[0:3], a[72:75], a[0:3], v[0:3]\n"
"buffer_load_dwordx4 v[156:159], v240, s[36:39], s81 offen offset:0 sc1\n"
"v_mfma_f32_16x16x32_bf16 v[0:3], a[76:79], a[4:7], v[0:3]\n"
"v_mfma_f32_16x16x32_bf16 v[4:7], a[72:75], a[8:11], v[4:7]\n"
"v_mfma_f32_16x16x32_bf16 v[4:7], a[76:79], a[12:15], v[4:7]\n"
"s_waitcnt lgkmcnt(8)\n"
"v_mfma_f32_16x16x32_bf16 v[8:11], a[72:75], a[16:19], v[8:11]\n"
"v_mfma_f32_16x16x32_bf16 v[8:11], a[76:79], a[20:23], v[8:11]\n"
"v_mfma_f32_16x16x32_bf16 v[12:15], a[72:75], a[24:27], v[12:15]\n"
"buffer_load_dwordx4 a[68:71], v240, s[36:39], s80 offen offset:1024 sc1\n"
"v_mfma_f32_16x16x32_bf16 v[12:15], a[76:79], a[28:31], v[12:15]\n"
"s_waitcnt lgkmcnt(4)\n"
"v_mfma_f32_16x16x32_bf16 v[16:19], a[72:75], a[32:35], v[16:19]\n"
"v_mfma_f32_16x16x32_bf16 v[16:19], a[76:79], a[36:39], v[16:19]\n"
"s_waitcnt lgkmcnt(0)\n"
"s_barrier\n"
"v_mfma_f32_16x16x32_bf16 v[20:23], a[72:75], a[40:43], v[20:23]\n"
"s_add_u32 m0, s83, 131072\n"
"v_mfma_f32_16x16x32_bf16 v[20:23], a[76:79], a[44:47], v[20:23]\n"
"buffer_load_dwordx4 v240, s[56:59], s72 offen offset:0 sc1 lds\n"
"v_mfma_f32_16x16x32_bf16 v[24:27], a[72:75], a[48:51], v[24:27]\n"
"v_mfma_f32_16x16x32_bf16 v[24:27], a[76:79], a[52:55], v[24:27]\n"
"v_mfma_f32_16x16x32_bf16 v[28:31], a[72:75], a[56:59], v[28:31]\n"
"buffer_load_dwordx4 a[72:75], v240, s[36:39], s80 offen offset:2048 sc1\n"
"v_mfma_f32_16x16x32_bf16 v[28:31], a[76:79], a[60:63], v[28:31]\n"
"s_waitcnt vmcnt(14)\n"
"v_mfma_f32_16x16x32_bf16 v[32:35], a[80:83], a[0:3], v[32:35]\n"
"s_add_u32 m0, s83, 133120\n"
"buffer_load_dwordx4 v[188:191], v240, s[36:39], s81 offen offset:1024 sc1\n"
"v_mfma_f32_16x16x32_bf16 v[32:35], a[84:87], a[4:7], v[32:35]\n"
"buffer_load_dwordx4 v240, s[56:59], s73 offen offset:0 sc1 lds\n"
"v_mfma_f32_16x16x32_bf16 v[36:39], a[80:83], a[8:11], v[36:39]\n"
"v_mfma_f32_16x16x32_bf16 v[36:39], a[84:87], a[12:15], v[36:39]\n"
"v_mfma_f32_16x16x32_bf16 v[40:43], a[80:83], a[16:19], v[40:43]\n"
"v_mfma_f32_16x16x32_bf16 v[40:43], a[84:87], a[20:23], v[40:43]\n"
"v_mfma_f32_16x16x32_bf16 v[44:47], a[80:83], a[24:27], v[44:47]\n"
"s_add_u32 m0, s83, 135168\n"
"buffer_load_dwordx4 a[76:79], v240, s[36:39], s80 offen offset:3072 sc1\n"
"v_mfma_f32_16x16x32_bf16 v[44:47], a[84:87], a[28:31], v[44:47]\n"
"s_add_u32 s80, s80, 0x1000\n"
"buffer_load_dwordx4 v240, s[56:59], s74 offen offset:0 sc1 lds\n"
"v_mfma_f32_16x16x32_bf16 v[48:51], a[80:83], a[32:35], v[48:51]\n"
"v_mfma_f32_16x16x32_bf16 v[48:51], a[84:87], a[36:39], v[48:51]\n"
"v_mfma_f32_16x16x32_bf16 v[52:55], a[80:83], a[40:43], v[52:55]\n"
"v_mfma_f32_16x16x32_bf16 v[52:55], a[84:87], a[44:47], v[52:55]\n"
"v_mfma_f32_16x16x32_bf16 v[56:59], a[80:83], a[48:51], v[56:59]\n"
"s_add_u32 m0, s83, 137216\n"
"v_mfma_f32_16x16x32_bf16 v[56:59], a[84:87], a[52:55], v[56:59]\n"
"buffer_load_dwordx4 v240, s[56:59], s75 offen offset:0 sc1 lds\n"
"v_mfma_f32_16x16x32_bf16 v[60:63], a[80:83], a[56:59], v[60:63]\n"
"buffer_load_dwordx4 a[80:83], v240, s[36:39], s80 offen offset:0 sc1\n"
"v_mfma_f32_16x16x32_bf16 v[60:63], a[84:87], a[60:63], v[60:63]\n"
"s_waitcnt vmcnt(17)\n"
"v_mfma_f32_16x16x32_bf16 v[96:99], a[88:91], a[0:3], v[96:99]\n"
"buffer_load_dwordx4 a[140:143], v240, s[36:39], s81 offen offset:3072 sc1\n"
"v_mfma_f32_16x16x32_bf16 v[96:99], a[92:95], a[4:7], v[96:99]\n"
"v_mfma_f32_16x16x32_bf16 v[100:103], a[88:91], a[8:11], v[100:103]\n"
"v_mfma_f32_16x16x32_bf16 v[100:103], a[92:95], a[12:15], v[100:103]\n"
"v_mfma_f32_16x16x32_bf16 v[104:107], a[88:91], a[16:19], v[104:107]\n"
"v_mfma_f32_16x16x32_bf16 v[104:107], a[92:95], a[20:23], v[104:107]\n"
"v_mfma_f32_16x16x32_bf16 v[108:111], a[88:91], a[24:27], v[108:111]\n"
"buffer_load_dwordx4 a[84:87], v240, s[36:39], s80 offen offset:1024 sc1\n"
"v_mfma_f32_16x16x32_bf16 v[108:111], a[92:95], a[28:31], v[108:111]\n"
"v_mfma_f32_16x16x32_bf16 v[112:115], a[88:91], a[32:35], v[112:115]\n"
"v_mfma_f32_16x16x32_bf16 v[112:115], a[92:95], a[36:39], v[112:115]\n"
"v_mfma_f32_16x16x32_bf16 v[116:119], a[88:91], a[40:43], v[116:119]\n"
"v_mfma_f32_16x16x32_bf16 v[116:119], a[92:95], a[44:47], v[116:119]\n"
"v_mfma_f32_16x16x32_bf16 v[120:123], a[88:91], a[48:51], v[120:123]\n"
"v_mfma_f32_16x16x32_bf16 v[120:123], a[92:95], a[52:55], v[120:123]\n"
"v_mfma_f32_16x16x32_bf16 v[124:127], a[88:91], a[56:59], v[124:127]\n"
"buffer_load_dwordx4 a[88:91], v240, s[36:39], s80 offen offset:2048 sc1\n"
"v_mfma_f32_16x16x32_bf16 v[124:127], a[92:95], a[60:63], v[124:127]\n"
"s_waitcnt vmcnt(18)\n"
"v_mfma_f32_16x16x32_bf16 v[64:67], a[96:99], a[0:3], v[64:67]\n"
"buffer_load_dwordx4 v[220:223], v240, s[36:39], s81 offen offset:2048 sc1\n"
"v_mfma_f32_16x16x32_bf16 v[64:67], a[100:103], a[4:7], v[64:67]\n"
"s_add_u32 s81, s81, 61440\n"
"v_mfma_f32_16x16x32_bf16 v[68:71], a[96:99], a[8:11], v[68:71]\n"
"v_mfma_f32_16x16x32_bf16 v[68:71], a[100:103], a[12:15], v[68:71]\n"
"v_mfma_f32_16x16x32_bf16 v[72:75], a[96:99], a[16:19], v[72:75]\n"
"v_mfma_f32_16x16x32_bf16 v[72:75], a[100:103], a[20:23], v[72:75]\n"
"v_mfma_f32_16x16x32_bf16 v[76:79], a[96:99], a[24:27], v[76:79]\n"
"buffer_load_dwordx4 a[92:95], v240, s[36:39], s80 offen offset:3072 sc1\n"
"v_mfma_f32_16x16x32_bf16 v[76:79], a[100:103], a[28:31], v[76:79]\n"
"s_add_u32 s80, s80, 0x1000\n"
"v_mfma_f32_16x16x32_bf16 v[80:83], a[96:99], a[32:35], v[80:83]\n"
"v_mfma_f32_16x16x32_bf16 v[80:83], a[100:103], a[36:39], v[80:83]\n"
"v_mfma_f32_16x16x32_bf16 v[84:87], a[96:99], a[40:43], v[84:87]\n"
"v_mfma_f32_16x16x32_bf16 v[84:87], a[100:103], a[44:47], v[84:87]\n"
"v_mfma_f32_16x16x32_bf16 v[88:91], a[96:99], a[48:51], v[88:91]\n"
"v_mfma_f32_16x16x32_bf16 v[88:91], a[100:103], a[52:55], v[88:91]\n"
"v_mfma_f32_16x16x32_bf16 v[92:95], a[96:99], a[56:59], v[92:95]\n"
"buffer_load_dwordx4 a[96:99], v240, s[36:39], s80 offen offset:0 sc1\n"
"v_mfma_f32_16x16x32_bf16 v[92:95], a[100:103], a[60:63], v[92:95]\n"
"s_waitcnt vmcnt(15)\n"
"v_mfma_f32_16x16x32_bf16 v[128:131], a[104:107], a[0:3], v[156:159]\n"
"v_mfma_f32_16x16x32_bf16 v[128:131], a[108:111], a[4:7], v[128:131]\n"
"v_mfma_f32_16x16x32_bf16 v[132:135], a[104:107], a[8:11], v[156:159]\n"
"v_mfma_f32_16x16x32_bf16 v[132:135], a[108:111], a[12:15], v[132:135]\n"
"v_mfma_f32_16x16x32_bf16 v[136:139], a[104:107], a[16:19], v[156:159]\n"
"v_mfma_f32_16x16x32_bf16 v[136:139], a[108:111], a[20:23], v[136:139]\n"
"v_mfma_f32_16x16x32_bf16 v[140:143], a[104:107], a[24:27], v[156:159]\n"
"buffer_load_dwordx4 a[100:103], v240, s[36:39], s80 offen offset:1024 sc1\n"
"v_mfma_f32_16x16x32_bf16 v[140:143], a[108:111], a[28:31], v[140:143]\n"
"v_mfma_f32_16x16x32_bf16 v[144:147], a[104:107], a[32:35], v[156:159]\n"
"v_mfma_f32_16x16x32_bf16 v[144:147], a[108:111], a[36:39], v[144:147]\n"
"v_mfma_f32_16x16x32_bf16 v[148:151], a[104:107], a[40:43], v[156:159]\n"
"v_mfma_f32_16x16x32_bf16 v[148:151], a[108:111], a[44:47], v[148:151]\n"
"v_mfma_f32_16x16x32_bf16 v[152:155], a[104:107], a[48:51], v[156:159]\n"
"v_mfma_f32_16x16x32_bf16 v[152:155], a[108:111], a[52:55], v[152:155]\n"
"v_mfma_f32_16x16x32_bf16 v[156:159], a[104:107], a[56:59], v[156:159]\n"
"buffer_load_dwordx4 a[104:107], v240, s[36:39], s80 offen offset:2048 sc1\n"
"v_mfma_f32_16x16x32_bf16 v[156:159], a[108:111], a[60:63], v[156:159]\n"
"s_waitcnt vmcnt(13)\n"
"v_mfma_f32_16x16x32_bf16 v[160:163], v[224:227], a[0:3], v[188:191]\n"
"v_mfma_f32_16x16x32_bf16 v[160:163], v[228:231], a[4:7], v[160:163]\n"
"v_mfma_f32_16x16x32_bf16 v[164:167], v[224:227], a[8:11], v[188:191]\n"
"v_mfma_f32_16x16x32_bf16 v[164:167], v[228:231], a[12:15], v[164:167]\n"
"v_mfma_f32_16x16x32_bf16 v[168:171], v[224:227], a[16:19], v[188:191]\n"
"v_mfma_f32_16x16x32_bf16 v[168:171], v[228:231], a[20:23], v[168:171]\n"
"v_mfma_f32_16x16x32_bf16 v[172:175], v[224:227], a[24:27], v[188:191]\n"
"buffer_load_dwordx4 a[108:111], v240, s[36:39], s80 offen offset:3072 sc1\n"
"v_mfma_f32_16x16x32_bf16 v[172:175], v[228:231], a[28:31], v[172:175]\n"
"s_add_u32 s80, s80, 0x1000\n"
"v_mfma_f32_16x16x32_bf16 v[176:179], v[224:227], a[32:35], v[188:191]\n"
"v_mfma_f32_16x16x32_bf16 v[176:179], v[228:231], a[36:39], v[176:179]\n"
"v_mfma_f32_16x16x32_bf16 v[180:183], v[224:227], a[40:43], v[188:191]\n"
"v_mfma_f32_16x16x32_bf16 v[180:183], v[228:231], a[44:47], v[180:183]\n"
"v_mfma_f32_16x16x32_bf16 v[184:187], v[224:227], a[48:51], v[188:191]\n"
"v_mfma_f32_16x16x32_bf16 v[184:187], v[228:231], a[52:55], v[184:187]\n"
"v_mfma_f32_16x16x32_bf16 v[188:191], v[224:227], a[56:59], v[188:191]\n"
"buffer_load_dwordx4 v[224:227], v240, s[36:39], s80 offen offset:0 sc1\n"
"v_mfma_f32_16x16x32_bf16 v[188:191], v[228:231], a[60:63], v[188:191]\n"
"s_waitcnt vmcnt(9)\n"
"v_mfma_f32_16x16x32_bf16 a[112:115], a[64:67], a[0:3], a[140:143]\n"
"v_mfma_f32_16x16x32_bf16 a[112:115], a[68:71], a[4:7], a[112:115]\n"
"v_mfma_f32_16x16x32_bf16 a[116:119], a[64:67], a[8:11], a[140:143]\n"
"v_mfma_f32_16x16x32_bf16 a[116:119], a[68:71], a[12:15], a[116:119]\n"
"v_mfma_f32_16x16x32_bf16 a[120:123], a[64:67], a[16:19], a[140:143]\n"
"v_mfma_f32_16x16x32_bf16 a[120:123], a[68:71], a[20:23], a[120:123]\n"
"v_mfma_f32_16x16x32_bf16 a[124:127], a[64:67], a[24:27], a[140:143]\n"
"buffer_load_dwordx4 v[228:231], v240, s[36:39], s80 offen offset:1024 sc1\n"
"v_mfma_f32_16x16x32_bf16 a[124:127], a[68:71], a[28:31], a[124:127]\n"
"v_mfma_f32_16x16x32_bf16 a[128:131], a[64:67], a[32:35], a[140:143]\n"
"v_mfma_f32_16x16x32_bf16 a[128:131], a[68:71], a[36:39], a[128:131]\n"
"v_mfma_f32_16x16x32_bf16 a[132:135], a[64:67], a[40:43], a[140:143]\n"
"v_mfma_f32_16x16x32_bf16 a[132:135], a[68:71], a[44:47], a[132:135]\n"
"v_mfma_f32_16x16x32_bf16 a[136:139], a[64:67], a[48:51], a[140:143]\n"
"v_mfma_f32_16x16x32_bf16 a[136:139], a[68:71], a[52:55], a[136:139]\n"
"s_barrier\n"
"v_mfma_f32_16x16x32_bf16 a[140:143], a[64:67], a[56:59], a[140:143]\n"
"buffer_load_dwordx4 a[64:67], v240, s[36:39], s80 offen offset:2048 sc1\n"
"v_mfma_f32_16x16x32_bf16 a[140:143], a[68:71], a[60:63], a[140:143]\n"
"s_waitcnt vmcnt(8)\n"
"v_mfma_f32_16x16x32_bf16 v[192:195], a[72:75], a[0:3], v[220:223]\n"
"ds_read_b128 a[0:3], v247 offset:0\n"
"v_mfma_f32_16x16x32_bf16 v[192:195], a[76:79], a[4:7], v[192:195]\n"
"ds_read_b128 a[4:7], v248 offset:0\n"
"v_mfma_f32_16x16x32_bf16 v[196:199], a[72:75], a[8:11], v[220:223]\n"
"ds_read_b128 a[8:11], v247 offset:2048\n"
"v_mfma_f32_16x16x32_bf16 v[196:199], a[76:79], a[12:15], v[196:199]\n"
"ds_read_b128 a[12:15], v248 offset:2048\n"
"v_mfma_f32_16x16x32_bf16 v[200:203], a[72:75], a[16:19], v[220:223]\n"
"ds_read_b128 a[16:19], v247 offset:4096\n"
"v_mfma_f32_16x16x32_bf16 v[200:203], a[76:79], a[20:23], v[200:203]\n"
"ds_read_b128 a[20:23], v248 offset:4096\n"
"v_mfma_f32_16x16x32_bf16 v[204:207], a[72:75], a[24:27], v[220:223]\n"
"ds_read_b128 a[24:27], v247 offset:6144\n"
"buffer_load_dwordx4 a[68:71], v240, s[36:39], s80 offen offset:3072 sc1\n"
"v_mfma_f32_16x16x32_bf16 v[204:207], a[76:79], a[28:31], v[204:207]\n"
"s_add_u32 s80, s80, 0x1000\n"
"ds_read_b128 a[28:31], v248 offset:6144\n"
"v_mfma_f32_16x16x32_bf16 v[208:211], a[72:75], a[32:35], v[220:223]\n"
"ds_read_b128 a[32:35], v247 offset:8192\n"
"v_mfma_f32_16x16x32_bf16 v[208:211], a[76:79], a[36:39], v[208:211]\n"
"ds_read_b128 a[36:39], v248 offset:8192\n"
"v_mfma_f32_16x16x32_bf16 v[212:215], a[72:75], a[40:43], v[220:223]\n"
"ds_read_b128 a[40:43], v247 offset:10240\n"
"v_mfma_f32_16x16x32_bf16 v[212:215], a[76:79], a[44:47], v[212:215]\n"
"ds_read_b128 a[44:47], v248 offset:10240\n"
"v_mfma_f32_16x16x32_bf16 v[216:219], a[72:75], a[48:51], v[220:223]\n"
"ds_read_b128 a[48:51], v247 offset:12288\n"
"v_mfma_f32_16x16x32_bf16 v[216:219], a[76:79], a[52:55], v[216:219]\n"
"ds_read_b128 a[52:55], v248 offset:12288\n"
"v_mfma_f32_16x16x32_bf16 v[220:223], a[72:75], a[56:59], v[220:223]\n"
"ds_read_b128 a[56:59], v247 offset:14336\n"
"buffer_load_dwordx4 a[72:75], v240, s[36:39], s80 offen offset:0 sc1\n"
"v_mfma_f32_16x16x32_bf16 v[220:223], a[76:79], a[60:63], v[220:223]\n"
"s_waitcnt lgkmcnt(11)\n"
"ds_read_b128 a[60:63], v248 offset:14336\n"
"v_mfma_f32_16x16x32_bf16 v[0:3], a[80:83], a[0:3], v[0:3]\n"
"v_and_b32 v251, 0xf0, v240\n"
"v_lshrrev_b32 v252, 8, v240\n"
"v_mfma_f32_16x16x32_bf16 v[0:3], a[84:87], a[4:7], v[0:3]\n"
"v_lshlrev_b32 v252, 4, v252\n"
"v_lshl_add_u32 v242, v251, 7, v252\n"
"v_mfma_f32_16x16x32_bf16 v[4:7], a[80:83], a[8:11], v[4:7]\n"
"v_add_u32 v242, s94, v242\n"
"v_mfma_f32_16x16x32_bf16 v[4:7], a[84:87], a[12:15], v[4:7]\n"
"s_waitcnt lgkmcnt(8)\n"
"v_mfma_f32_16x16x32_bf16 v[8:11], a[80:83], a[16:19], v[8:11]\n"
"v_mfma_f32_16x16x32_bf16 v[8:11], a[84:87], a[20:23], v[8:11]\n"
"v_mfma_f32_16x16x32_bf16 v[12:15], a[80:83], a[24:27], v[12:15]\n"
"buffer_load_dwordx4 a[76:79], v240, s[36:39], s80 offen offset:1024 sc1\n"
"v_mfma_f32_16x16x32_bf16 v[12:15], a[84:87], a[28:31], v[12:15]\n"
"s_waitcnt lgkmcnt(4)\n"
"v_mfma_f32_16x16x32_bf16 v[16:19], a[80:83], a[32:35], v[16:19]\n"
"v_mfma_f32_16x16x32_bf16 v[16:19], a[84:87], a[36:39], v[16:19]\n"
"s_waitcnt lgkmcnt(0)\n"
"s_barrier\n"
"v_mfma_f32_16x16x32_bf16 v[20:23], a[80:83], a[40:43], v[20:23]\n"
"s_add_u32 m0, s83, 130048\n"
"v_mfma_f32_16x16x32_bf16 v[20:23], a[84:87], a[44:47], v[20:23]\n"
"buffer_load_dwordx4 v240, s[56:59], s68 offen offset:1024 sc1 lds\n"
"v_mfma_f32_16x16x32_bf16 v[24:27], a[80:83], a[48:51], v[24:27]\n"
"v_mfma_f32_16x16x32_bf16 v[24:27], a[84:87], a[52:55], v[24:27]\n"
"v_mfma_f32_16x16x32_bf16 v[28:31], a[80:83], a[56:59], v[28:31]\n"
"buffer_load_dwordx4 a[80:83], v240, s[36:39], s80 offen offset:2048 sc1\n"
"v_mfma_f32_16x16x32_bf16 v[28:31], a[84:87], a[60:63], v[28:31]\n"
"s_waitcnt vmcnt(12)\n"
"v_mfma_f32_16x16x32_bf16 v[32:35], a[88:91], a[0:3], v[32:35]\n"
"s_add_u32 m0, s83, 132096\n"
"v_mfma_f32_16x16x32_bf16 v[32:35], a[92:95], a[4:7], v[32:35]\n"
"buffer_load_dwordx4 v240, s[56:59], s69 offen offset:1024 sc1 lds\n"
"v_mfma_f32_16x16x32_bf16 v[36:39], a[88:91], a[8:11], v[36:39]\n"
"v_mfma_f32_16x16x32_bf16 v[36:39], a[92:95], a[12:15], v[36:39]\n"
"v_mfma_f32_16x16x32_bf16 v[40:43], a[88:91], a[16:19], v[40:43]\n"
"v_mfma_f32_16x16x32_bf16 v[40:43], a[92:95], a[20:23], v[40:43]\n"
"v_mfma_f32_16x16x32_bf16 v[44:47], a[88:91], a[24:27], v[44:47]\n"
"s_add_u32 m0, s83, 134144\n"
"buffer_load_dwordx4 a[84:87], v240, s[36:39], s80 offen offset:3072 sc1\n"
"v_mfma_f32_16x16x32_bf16 v[44:47], a[92:95], a[28:31], v[44:47]\n"
"s_add_u32 s80, s80, 0x1000\n"
"buffer_load_dwordx4 v240, s[56:59], s70 offen offset:1024 sc1 lds\n"
"v_mfma_f32_16x16x32_bf16 v[48:51], a[88:91], a[32:35], v[48:51]\n"
"v_mfma_f32_16x16x32_bf16 v[48:51], a[92:95], a[36:39], v[48:51]\n"
"v_mfma_f32_16x16x32_bf16 v[52:55], a[88:91], a[40:43], v[52:55]\n"
"v_mfma_f32_16x16x32_bf16 v[52:55], a[92:95], a[44:47], v[52:55]\n"
"v_mfma_f32_16x16x32_bf16 v[56:59], a[88:91], a[48:51], v[56:59]\n"
"s_add_u32 m0, s83, 136192\n"
"v_mfma_f32_16x16x32_bf16 v[56:59], a[92:95], a[52:55], v[56:59]\n"
"buffer_load_dwordx4 v240, s[56:59], s71 offen offset:1024 sc1 lds\n"
"v_mfma_f32_16x16x32_bf16 v[60:63], a[88:91], a[56:59], v[60:63]\n"
"buffer_load_dwordx4 a[88:91], v240, s[36:39], s80 offen offset:0 sc1\n"
"v_mfma_f32_16x16x32_bf16 v[60:63], a[92:95], a[60:63], v[60:63]\n"
"s_waitcnt vmcnt(15)\n"
"v_mfma_f32_16x16x32_bf16 v[64:67], a[96:99], a[0:3], v[64:67]\n"
"v_mfma_f32_16x16x32_bf16 v[64:67], a[100:103], a[4:7], v[64:67]\n"
"v_mfma_f32_16x16x32_bf16 v[68:71], a[96:99], a[8:11], v[68:71]\n"
"v_mfma_f32_16x16x32_bf16 v[68:71], a[100:103], a[12:15], v[68:71]\n"
"v_mfma_f32_16x16x32_bf16 v[72:75], a[96:99], a[16:19], v[72:75]\n"
"v_mfma_f32_16x16x32_bf16 v[72:75], a[100:103], a[20:23], v[72:75]\n"
"v_mfma_f32_16x16x32_bf16 v[76:79], a[96:99], a[24:27], v[76:79]\n"
"buffer_load_dwordx4 a[92:95], v240, s[36:39], s80 offen offset:1024 sc1\n"
"v_mfma_f32_16x16x32_bf16 v[76:79], a[100:103], a[28:31], v[76:79]\n"
"v_mfma_f32_16x16x32_bf16 v[80:83], a[96:99], a[32:35], v[80:83]\n"
"v_mfma_f32_16x16x32_bf16 v[80:83], a[100:103], a[36:39], v[80:83]\n"
"v_mfma_f32_16x16x32_bf16 v[84:87], a[96:99], a[40:43], v[84:87]\n"
"v_mfma_f32_16x16x32_bf16 v[84:87], a[100:103], a[44:47], v[84:87]\n"
"v_mfma_f32_16x16x32_bf16 v[88:91], a[96:99], a[48:51], v[88:91]\n"
"v_mfma_f32_16x16x32_bf16 v[88:91], a[100:103], a[52:55], v[88:91]\n"
"v_mfma_f32_16x16x32_bf16 v[92:95], a[96:99], a[56:59], v[92:95]\n"
"buffer_load_dwordx4 a[96:99], v240, s[36:39], s80 offen offset:2048 sc1\n"
"v_mfma_f32_16x16x32_bf16 v[92:95], a[100:103], a[60:63], v[92:95]\n"
"s_waitcnt vmcnt(15)\n"
"v_mfma_f32_16x16x32_bf16 v[96:99], a[104:107], a[0:3], v[96:99]\n"
"v_mfma_f32_16x16x32_bf16 v[96:99], a[108:111], a[4:7], v[96:99]\n"
"v_mfma_f32_16x16x32_bf16 v[100:103], a[104:107], a[8:11], v[100:103]\n"
"v_mfma_f32_16x16x32_bf16 v[100:103], a[108:111], a[12:15], v[100:103]\n"
"v_mfma_f32_16x16x32_bf16 v[104:107], a[104:107], a[16:19], v[104:107]\n"
"v_mfma_f32_16x16x32_bf16 v[104:107], a[108:111], a[20:23], v[104:107]\n"
"v_mfma_f32_16x16x32_bf16 v[108:111], a[104:107], a[24:27], v[108:111]\n"
"buffer_load_dwordx4 a[100:103], v240, s[36:39], s80 offen offset:3072 sc1\n"
"v_mfma_f32_16x16x32_bf16 v[108:111], a[108:111], a[28:31], v[108:111]\n"
"s_add_u32 s80, s80, 0x1000\n"
"v_mfma_f32_16x16x32_bf16 v[112:115], a[104:107], a[32:35], v[112:115]\n"
"v_mfma_f32_16x16x32_bf16 v[112:115], a[108:111], a[36:39], v[112:115]\n"
"v_mfma_f32_16x16x32_bf16 v[116:119], a[104:107], a[40:43], v[116:119]\n"
"v_mfma_f32_16x16x32_bf16 v[116:119], a[108:111], a[44:47], v[116:119]\n"
"v_mfma_f32_16x16x32_bf16 v[120:123], a[104:107], a[48:51], v[120:123]\n"
"v_mfma_f32_16x16x32_bf16 v[120:123], a[108:111], a[52:55], v[120:123]\n"
"v_mfma_f32_16x16x32_bf16 v[124:127], a[104:107], a[56:59], v[124:127]\n"
"buffer_load_dwordx4 a[104:107], v240, s[36:39], s80 offen offset:0 sc1\n"
"v_mfma_f32_16x16x32_bf16 v[124:127], a[108:111], a[60:63], v[124:127]\n"
"s_waitcnt vmcnt(15)\n"
"v_mfma_f32_16x16x32_bf16 v[128:131], v[224:227], a[0:3], v[128:131]\n"
"v_mfma_f32_16x16x32_bf16 v[128:131], v[228:231], a[4:7], v[128:131]\n"
"v_mfma_f32_16x16x32_bf16 v[132:135], v[224:227], a[8:11], v[132:135]\n"
"v_mfma_f32_16x16x32_bf16 v[132:135], v[228:231], a[12:15], v[132:135]\n"
"v_mfma_f32_16x16x32_bf16 v[136:139], v[224:227], a[16:19], v[136:139]\n"
"v_mfma_f32_16x16x32_bf16 v[136:139], v[228:231], a[20:23], v[136:139]\n"
"v_mfma_f32_16x16x32_bf16 v[140:143], v[224:227], a[24:27], v[140:143]\n"
".Lpad17_%=:\n"
"s_cbranch_execz .Lpad21_%=\n"
"buffer_load_dwordx4 a[108:111], v240, s[36:39], s80 offen offset:1024 sc1\n"
"v_mfma_f32_16x16x32_bf16 v[140:143], v[228:231], a[28:31], v[140:143]\n"
"v_mfma_f32_16x16x32_bf16 v[144:147], v[224:227], a[32:35], v[144:147]\n"
"v_mfma_f32_16x16x32_bf16 v[144:147], v[228:231], a[36:39], v[144:147]\n"
"v_mfma_f32_16x16x32_bf16 v[148:151], v[224:227], a[40:43], v[148:151]\n"
"v_mfma_f32_16x16x32_bf16 v[148:151], v[228:231], a[44:47], v[148:151]\n"
"v_mfma_f32_16x16x32_bf16 v[152:155], v[224:227], a[48:51], v[152:155]\n"
"v_mfma_f32_16x16x32_bf16 v[152:155], v[228:231], a[52:55], v[152:155]\n"
"v_mfma_f32_16x16x32_bf16 v[156:159], v[224:227], a[56:59], v[156:159]\n"
"buffer_load_dwordx4 v[224:227], v240, s[36:39], s80 offen offset:2048 sc1\n"
"v_mfma_f32_16x16x32_bf16 v[156:159], v[228:231], a[60:63], v[156:159]\n"
"s_waitcnt vmcnt(15)\n"
"v_mfma_f32_16x16x32_bf16 v[160:163], a[64:67], a[0:3], v[160:163]\n"
"v_mfma_f32_16x16x32_bf16 v[160:163], a[68:71], a[4:7], v[160:163]\n"
"v_mfma_f32_16x16x32_bf16 v[164:167], a[64:67], a[8:11], v[164:167]\n"
"v_mfma_f32_16x16x32_bf16 v[164:167], a[68:71], a[12:15], v[164:167]\n"
"v_mfma_f32_16x16x32_bf16 v[168:171], a[64:67], a[16:19], v[168:171]\n"
"v_mfma_f32_16x16x32_bf16 v[168:171], a[68:71], a[20:23], v[168:171]\n"
"v_mfma_f32_16x16x32_bf16 v[172:175], a[64:67], a[24:27], v[172:175]\n"
"buffer_load_dwordx4 v[228:231], v240, s[36:39], s80 offen offset:3072 sc1\n"
"v_mfma_f32_16x16x32_bf16 v[172:175], a[68:71], a[28:31], v[172:175]\n"
"s_add_u32 s80, s80, 0x1000\n"
"v_mfma_f32_16x16x32_bf16 v[176:179], a[64:67], a[32:35], v[176:179]\n"
"v_mfma_f32_16x16x32_bf16 v[176:179], a[68:71], a[36:39], v[176:179]\n"
"v_mfma_f32_16x16x32_bf16 v[180:183], a[64:67], a[40:43], v[180:183]\n"
"v_mfma_f32_16x16x32_bf16 v[180:183], a[68:71], a[44:47], v[180:183]\n"
"v_mfma_f32_16x16x32_bf16 v[184:187], a[64:67], a[48:51], v[184:187]\n"
"v_mfma_f32_16x16x32_bf16 v[184:187], a[68:71], a[52:55], v[184:187]\n"
"v_mfma_f32_16x16x32_bf16 v[188:191], a[64:67], a[56:59], v[188:191]\n"
"buffer_load_dwordx4 a[64:67], v240, s[36:39], s80 offen offset:0 sc1\n"
"v_mfma_f32_16x16x32_bf16 v[188:191], a[68:71], a[60:63], v[188:191]\n"
"s_waitcnt vmcnt(15)\n"
"v_mfma_f32_16x16x32_bf16 v[192:195], a[72:75], a[0:3], v[192:195]\n"
"v_mfma_f32_16x16x32_bf16 v[192:195], a[76:79], a[4:7], v[192:195]\n"
"v_mfma_f32_16x16x32_bf16 v[196:199], a[72:75], a[8:11], v[196:199]\n"
"v_mfma_f32_16x16x32_bf16 v[196:199], a[76:79], a[12:15], v[196:199]\n"
"v_mfma_f32_16x16x32_bf16 v[200:203], a[72:75], a[16:19], v[200:203]\n"
"v_mfma_f32_16x16x32_bf16 v[200:203], a[76:79], a[20:23], v[200:203]\n"
"v_mfma_f32_16x16x32_bf16 v[204:207], a[72:75], a[24:27], v[204:207]\n"
"buffer_load_dwordx4 a[68:71], v240, s[36:39], s80 offen offset:1024 sc1\n"
"v_mfma_f32_16x16x32_bf16 v[204:207], a[76:79], a[28:31], v[204:207]\n"
"v_mfma_f32_16x16x32_bf16 v[208:211], a[72:75], a[32:35], v[208:211]\n"
"v_mfma_f32_16x16x32_bf16 v[208:211], a[76:79], a[36:39], v[208:211]\n"
"v_mfma_f32_16x16x32_bf16 v[212:215], a[72:75], a[40:43], v[212:215]\n"
"v_mfma_f32_16x16x32_bf16 v[212:215], a[76:79], a[44:47], v[212:215]\n"
"v_mfma_f32_16x16x32_bf16 v[216:219], a[72:75], a[48:51], v[216:219]\n"
"v_mfma_f32_16x16x32_bf16 v[216:219], a[76:79], a[52:55], v[216:219]\n"
"s_waitcnt vmcnt(10)\n"
"s_barrier\n"
"v_mfma_f32_16x16x32_bf16 v[220:223], a[72:75], a[56:59], v[220:223]\n"
"buffer_load_dwordx4 a[72:75], v240, s[36:39], s80 offen offset:2048 sc1\n"
"v_mfma_f32_16x16x32_bf16 v[220:223], a[76:79], a[60:63], v[220:223]\n"
"v_mfma_f32_16x16x32_bf16 a[112:115], a[80:83], a[0:3], a[112:115]\n"
"ds_read_b128 a[0:3], v247 offset:0\n"
"v_mfma_f32_16x16x32_bf16 a[112:115], a[84:87], a[4:7], a[112:115]\n"
"ds_read_b128 a[4:7], v248 offset:0\n"
"v_mfma_f32_16x16x32_bf16 a[116:119], a[80:83], a[8:11], a[116:119]\n"
"ds_read_b128 a[8:11], v247 offset:2048\n"
"v_mfma_f32_16x16x32_bf16 a[116:119], a[84:87], a[12:15], a[116:119]\n"
"ds_read_b128 a[12:15], v248 offset:2048\n"
"v_mfma_f32_16x16x32_bf16 a[120:123], a[80:83], a[16:19], a[120:123]\n"
"ds_read_b128 a[16:19], v247 offset:4096\n"
"v_mfma_f32_16x16x32_bf16 a[120:123], a[84:87], a[20:23], a[120:123]\n"
"ds_read_b128 a[20:23], v248 offset:4096\n"
"v_mfma_f32_16x16x32_bf16 a[124:127], a[80:83], a[24:27], a[124:127]\n"
"ds_read_b128 a[24:27], v247 offset:6144\n"
"buffer_load_dwordx4 a[76:79], v240, s[36:39], s80 offen offset:3072 sc1\n"
"v_mfma_f32_16x16x32_bf16 a[124:127], a[84:87], a[28:31], a[124:127]\n"
"s_add_u32 s80, s80, 0x1000\n"
"ds_read_b128 a[28:31], v248 offset:6144\n"
"v_mfma_f32_16x16x32_bf16 a[128:131], a[80:83], a[32:35], a[128:131]\n"
"ds_read_b128 a[32:35], v247 offset:8192\n"
"v_mfma_f32_16x16x32_bf16 a[128:131], a[84:87], a[36:39], a[128:131]\n"
"ds_read_b128 a[36:39], v248 offset:8192\n"
"v_mfma_f32_16x16x32_bf16 a[132:135], a[80:83], a[40:43], a[132:135]\n"
"ds_read_b128 a[40:43], v247 offset:10240\n"
"v_mfma_f32_16x16x32_bf16 a[132:135], a[84:87], a[44:47], a[132:135]\n"
"ds_read_b128 a[44:47], v248 offset:10240\n"
"v_mfma_f32_16x16x32_bf16 a[136:139], a[80:83], a[48:51], a[136:139]\n"
"ds_read_b128 a[48:51], v247 offset:12288\n"
"v_mfma_f32_16x16x32_bf16 a[136:139], a[84:87], a[52:55], a[136:139]\n"
"ds_read_b128 a[52:55], v248 offset:12288\n"
"v_mfma_f32_16x16x32_bf16 a[140:143], a[80:83], a[56:59], a[140:143]\n"
"ds_read_b128 a[56:59], v247 offset:14336\n"
"buffer_load_dwordx4 a[80:83], v240, s[36:39], s80 offen offset:0 sc1\n"
"v_mfma_f32_16x16x32_bf16 a[140:143], a[84:87], a[60:63], a[140:143]\n"
"s_waitcnt vmcnt(11) lgkmcnt(11)\n"
"ds_read_b128 a[60:63], v248 offset:14336\n"
"v_mfma_f32_16x16x32_bf16 v[0:3], a[88:91], a[0:3], v[0:3]\n"
"v_mfma_f32_16x16x32_bf16 v[0:3], a[92:95], a[4:7], v[0:3]\n"
"v_mfma_f32_16x16x32_bf16 v[4:7], a[88:91], a[8:11], v[4:7]\n"
"v_mfma_f32_16x16x32_bf16 v[4:7], a[92:95], a[12:15], v[4:7]\n"
"s_waitcnt lgkmcnt(8)\n"
"v_mfma_f32_16x16x32_bf16 v[8:11], a[88:91], a[16:19], v[8:11]\n"
"v_mfma_f32_16x16x32_bf16 v[8:11], a[92:95], a[20:23], v[8:11]\n"
"v_mfma_f32_16x16x32_bf16 v[12:15], a[88:91], a[24:27], v[12:15]\n"
"buffer_load_dwordx4 a[84:87], v240, s[36:39], s80 offen offset:1024 sc1\n"
"v_mfma_f32_16x16x32_bf16 v[12:15], a[92:95], a[28:31], v[12:15]\n"
"s_waitcnt lgkmcnt(4)\n"
"v_mfma_f32_16x16x32_bf16 v[16:19], a[88:91], a[32:35], v[16:19]\n"
"v_mfma_f32_16x16x32_bf16 v[16:19], a[92:95], a[36:39], v[16:19]\n"
"s_waitcnt lgkmcnt(0)\n"
"s_barrier\n"
"v_mfma_f32_16x16x32_bf16 v[20:23], a[88:91], a[40:43], v[20:23]\n"
"s_add_u32 m0, s83, 130048\n"
"v_mfma_f32_16x16x32_bf16 v[20:23], a[92:95], a[44:47], v[20:23]\n"
"buffer_load_dwordx4 v240, s[56:59], s72 offen offset:1024 sc1 lds\n"
"v_mfma_f32_16x16x32_bf16 v[24:27], a[88:91], a[48:51], v[24:27]\n"
"v_mfma_f32_16x16x32_bf16 v[24:27], a[92:95], a[52:55], v[24:27]\n"
"v_mfma_f32_16x16x32_bf16 v[28:31], a[88:91], a[56:59], v[28:31]\n"
"buffer_load_dwordx4 a[88:91], v240, s[36:39], s80 offen offset:2048 sc1\n"
"v_mfma_f32_16x16x32_bf16 v[28:31], a[92:95], a[60:63], v[28:31]\n"
"s_waitcnt vmcnt(12)\n"
"v_mfma_f32_16x16x32_bf16 v[32:35], a[96:99], a[0:3], v[32:35]\n"
"s_add_u32 m0, s83, 132096\n"
"v_mfma_f32_16x16x32_bf16 v[32:35], a[100:103], a[4:7], v[32:35]\n"
"buffer_load_dwordx4 v240, s[56:59], s73 offen offset:1024 sc1 lds\n"
"v_mfma_f32_16x16x32_bf16 v[36:39], a[96:99], a[8:11], v[36:39]\n"
"v_mfma_f32_16x16x32_bf16 v[36:39], a[100:103], a[12:15], v[36:39]\n"
"v_mfma_f32_16x16x32_bf16 v[40:43], a[96:99], a[16:19], v[40:43]\n"
"v_mfma_f32_16x16x32_bf16 v[40:43], a[100:103], a[20:23], v[40:43]\n"
"v_mfma_f32_16x16x32_bf16 v[44:47], a[96:99], a[24:27], v[44:47]\n"
"s_add_u32 m0, s83, 134144\n"
"buffer_load_dwordx4 a[92:95], v240, s[36:39], s80 offen offset:3072 sc1\n"
"v_mfma_f32_16x16x32_bf16 v[44:47], a[100:103], a[28:31], v[44:47]\n"
"s_add_u32 s80, s80, 0x1000\n"
"buffer_load_dwordx4 v240, s[56:59], s74 offen offset:1024 sc1 lds\n"
"v_mfma_f32_16x16x32_bf16 v[48:51], a[96:99], a[32:35], v[48:51]\n"
"v_mfma_f32_16x16x32_bf16 v[48:51], a[100:103], a[36:39], v[48:51]\n"
"v_mfma_f32_16x16x32_bf16 v[52:55], a[96:99], a[40:43], v[52:55]\n"
"v_mfma_f32_16x16x32_bf16 v[52:55], a[100:103], a[44:47], v[52:55]\n"
"v_mfma_f32_16x16x32_bf16 v[56:59], a[96:99], a[48:51], v[56:59]\n"
"s_add_u32 m0, s83, 136192\n"
"v_mfma_f32_16x16x32_bf16 v[56:59], a[100:103], a[52:55], v[56:59]\n"
"buffer_load_dwordx4 v240, s[56:59], s75 offen offset:1024 sc1 lds\n"
"v_mfma_f32_16x16x32_bf16 v[60:63], a[96:99], a[56:59], v[60:63]\n"
"buffer_load_dwordx4 a[96:99], v240, s[36:39], s80 offen offset:0 sc1\n"
"v_mfma_f32_16x16x32_bf16 v[60:63], a[100:103], a[60:63], v[60:63]\n"
"s_waitcnt vmcnt(15)\n"
"v_mfma_f32_16x16x32_bf16 v[64:67], a[104:107], a[0:3], v[64:67]\n"
"v_mfma_f32_16x16x32_bf16 v[64:67], a[108:111], a[4:7], v[64:67]\n"
"v_mfma_f32_16x16x32_bf16 v[68:71], a[104:107], a[8:11], v[68:71]\n"
"v_mfma_f32_16x16x32_bf16 v[68:71], a[108:111], a[12:15], v[68:71]\n"
"v_mfma_f32_16x16x32_bf16 v[72:75], a[104:107], a[16:19], v[72:75]\n"
"v_mfma_f32_16x16x32_bf16 v[72:75], a[108:111], a[20:23], v[72:75]\n"
"v_mfma_f32_16x16x32_bf16 v[76:79], a[104:107], a[24:27], v[76:79]\n"
"buffer_load_dwordx4 a[100:103], v240, s[36:39], s80 offen offset:1024 sc1\n"
"v_mfma_f32_16x16x32_bf16 v[76:79], a[108:111], a[28:31], v[76:79]\n"
"v_mfma_f32_16x16x32_bf16 v[80:83], a[104:107], a[32:35], v[80:83]\n"
"v_mfma_f32_16x16x32_bf16 v[80:83], a[108:111], a[36:39], v[80:83]\n"
"v_mfma_f32_16x16x32_bf16 v[84:87], a[104:107], a[40:43], v[84:87]\n"
"v_mfma_f32_16x16x32_bf16 v[84:87], a[108:111], a[44:47], v[84:87]\n"
"v_mfma_f32_16x16x32_bf16 v[88:91], a[104:107], a[48:51], v[88:91]\n"
"v_mfma_f32_16x16x32_bf16 v[88:91], a[108:111], a[52:55], v[88:91]\n"
"v_mfma_f32_16x16x32_bf16 v[92:95], a[104:107], a[56:59], v[92:95]\n"
"buffer_load_dwordx4 a[104:107], v240, s[36:39], s80 offen offset:2048 sc1\n"
"v_mfma_f32_16x16x32_bf16 v[92:95], a[108:111], a[60:63], v[92:95]\n"
"s_waitcnt vmcnt(15)\n"
"v_mfma_f32_16x16x32_bf16 v[96:99], v[224:227], a[0:3], v[96:99]\n"
"v_mfma_f32_16x16x32_bf16 v[96:99], v[228:231], a[4:7], v[96:99]\n"
"v_mfma_f32_16x16x32_bf16 v[100:103], v[224:227], a[8:11], v[100:103]\n"
"v_mfma_f32_16x16x32_bf16 v[100:103], v[228:231], a[12:15], v[100:103]\n"
"v_mfma_f32_16x16x32_bf16 v[104:107], v[224:227], a[16:19], v[104:107]\n"
"v_mfma_f32_16x16x32_bf16 v[104:107], v[228:231], a[20:23], v[104:107]\n"
"v_mfma_f32_16x16x32_bf16 v[108:111], v[224:227], a[24:27], v[108:111]\n"
"buffer_load_dwordx4 a[108:111], v240, s[36:39], s80 offen offset:3072 sc1\n"
"v_mfma_f32_16x16x32_bf16 v[108:111], v[228:231], a[28:31], v[108:111]\n"
"s_add_u32 s80, s80, 0x1000\n"
"v_mfma_f32_16x16x32_bf16 v[112:115], v[224:227], a[32:35], v[112:115]\n"
"v_mfma_f32_16x16x32_bf16 v[112:115], v[228:231], a[36:39], v[112:115]\n"
"v_mfma_f32_16x16x32_bf16 v[116:119], v[224:227], a[40:43], v[116:119]\n"
"v_mfma_f32_16x16x32_bf16 v[116:119], v[228:231], a[44:47], v[116:119]\n"
"v_mfma_f32_16x16x32_bf16 v[120:123], v[224:227], a[48:51], v[120:123]\n"
"v_mfma_f32_16x16x32_bf16 v[120:123], v[228:231], a[52:55], v[120:123]\n"
"v_mfma_f32_16x16x32_bf16 v[124:127], v[224:227], a[56:59], v[124:127]\n"
"buffer_load_dwordx4 v[224:227], v240, s[36:39], s80 offen offset:0 sc1\n"
"v_mfma_f32_16x16x32_bf16 v[124:127], v[228:231], a[60:63], v[124:127]\n"
"s_waitcnt vmcnt(15)\n"
"v_mfma_f32_16x16x32_bf16 v[128:131], a[64:67], a[0:3], v[128:131]\n"
"v_mfma_f32_16x16x32_bf16 v[128:131], a[68:71], a[4:7], v[128:131]\n"
"v_mfma_f32_16x16x32_bf16 v[132:135], a[64:67], a[8:11], v[132:135]\n"
"v_mfma_f32_16x16x32_bf16 v[132:135], a[68:71], a[12:15], v[132:135]\n"
"buffer_load_dword v241, v240, s[64:67], 0 offen offset:3072 sc1\n"
"v_mfma_f32_16x16x32_bf16 v[136:139], a[64:67], a[16:19], v[136:139]\n"
"v_mfma_f32_16x16x32_bf16 v[136:139], a[68:71], a[20:23], v[136:139]\n"
"v_mfma_f32_16x16x32_bf16 v[140:143], a[64:67], a[24:27], v[140:143]\n"
"buffer_load_dwordx4 v[228:231], v240, s[36:39], s80 offen offset:1024 sc1\n"
"v_mfma_f32_16x16x32_bf16 v[140:143], a[68:71], a[28:31], v[140:143]\n"
"v_mfma_f32_16x16x32_bf16 v[144:147], a[64:67], a[32:35], v[144:147]\n"
"v_mfma_f32_16x16x32_bf16 v[144:147], a[68:71], a[36:39], v[144:147]\n"
"v_mfma_f32_16x16x32_bf16 v[148:151], a[64:67], a[40:43], v[148:151]\n"
"v_mfma_f32_16x16x32_bf16 v[148:151], a[68:71], a[44:47], v[148:151]\n"
"v_mfma_f32_16x16x32_bf16 v[152:155], a[64:67], a[48:51], v[152:155]\n"
"v_mfma_f32_16x16x32_bf16 v[152:155], a[68:71], a[52:55], v[152:155]\n"
"v_mfma_f32_16x16x32_bf16 v[156:159], a[64:67], a[56:59], v[156:159]\n"
"buffer_load_dwordx4 a[64:67], v240, s[36:39], s80 offen offset:2048 sc1\n"
"v_mfma_f32_16x16x32_bf16 v[156:159], a[68:71], a[60:63], v[156:159]\n"
"s_waitcnt vmcnt(16)\n"
"v_mfma_f32_16x16x32_bf16 v[160:163], a[72:75], a[0:3], v[160:163]\n"
"v_mfma_f32_16x16x32_bf16 v[160:163], a[76:79], a[4:7], v[160:163]\n"
"v_mfma_f32_16x16x32_bf16 v[164:167], a[72:75], a[8:11], v[164:167]\n"
"v_mfma_f32_16x16x32_bf16 v[164:167], a[76:79], a[12:15], v[164:167]\n"
"v_mfma_f32_16x16x32_bf16 v[168:171], a[72:75], a[16:19], v[168:171]\n"
"v_mfma_f32_16x16x32_bf16 v[168:171], a[76:79], a[20:23], v[168:171]\n"
"v_mfma_f32_16x16x32_bf16 v[172:175], a[72:75], a[24:27], v[172:175]\n"
"buffer_load_dwordx4 a[68:71], v240, s[36:39], s80 offen offset:3072 sc1\n"
"v_mfma_f32_16x16x32_bf16 v[172:175], a[76:79], a[28:31], v[172:175]\n"
"s_add_u32 s80, s80, 0x1000\n"
"v_mfma_f32_16x16x32_bf16 v[176:179], a[72:75], a[32:35], v[176:179]\n"
"v_mfma_f32_16x16x32_bf16 v[176:179], a[76:79], a[36:39], v[176:179]\n"
"v_mfma_f32_16x16x32_bf16 v[180:183], a[72:75], a[40:43], v[180:183]\n"
"v_mfma_f32_16x16x32_bf16 v[180:183], a[76:79], a[44:47], v[180:183]\n"
"v_mfma_f32_16x16x32_bf16 v[184:187], a[72:75], a[48:51], v[184:187]\n"
"v_mfma_f32_16x16x32_bf16 v[184:187], a[76:79], a[52:55], v[184:187]\n"
"v_mfma_f32_16x16x32_bf16 v[188:191], a[72:75], a[56:59], v[188:191]\n"
"buffer_load_dwordx4 a[72:75], v240, s[36:39], s80 offen offset:0 sc1\n"
"v_mfma_f32_16x16x32_bf16 v[188:191], a[76:79], a[60:63], v[188:191]\n"
"s_waitcnt vmcnt(16)\n"
"v_mfma_f32_16x16x32_bf16 v[192:195], a[80:83], a[0:3], v[192:195]\n"
"v_mfma_f32_16x16x32_bf16 v[192:195], a[84:87], a[4:7], v[192:195]\n"
"v_mfma_f32_16x16x32_bf16 v[196:199], a[80:83], a[8:11], v[196:199]\n"
"v_mfma_f32_16x16x32_bf16 v[196:199], a[84:87], a[12:15], v[196:199]\n"
"v_mfma_f32_16x16x32_bf16 v[200:203], a[80:83], a[16:19], v[200:203]\n"
"v_mfma_f32_16x16x32_bf16 v[200:203], a[84:87], a[20:23], v[200:203]\n"
"v_mfma_f32_16x16x32_bf16 v[204:207], a[80:83], a[24:27], v[204:207]\n"
"buffer_load_dwordx4 a[76:79], v240, s[36:39], s80 offen offset:1024 sc1\n"
"v_mfma_f32_16x16x32_bf16 v[204:207], a[84:87], a[28:31], v[204:207]\n"
"v_mfma_f32_16x16x32_bf16 v[208:211], a[80:83], a[32:35], v[208:211]\n"
"v_mfma_f32_16x16x32_bf16 v[208:211], a[84:87], a[36:39], v[208:211]\n"
"v_mfma_f32_16x16x32_bf16 v[212:215], a[80:83], a[40:43], v[212:215]\n"
"v_mfma_f32_16x16x32_bf16 v[212:215], a[84:87], a[44:47], v[212:215]\n"
"v_mfma_f32_16x16x32_bf16 v[216:219], a[80:83], a[48:51], v[216:219]\n"
"v_mfma_f32_16x16x32_bf16 v[216:219], a[84:87], a[52:55], v[216:219]\n"
"s_waitcnt vmcnt(11)\n"
"s_barrier\n"
"v_mfma_f32_16x16x32_bf16 v[220:223], a[80:83], a[56:59], v[220:223]\n"
"buffer_load_dwordx4 a[80:83], v240, s[36:39], s80 offen offset:2048 sc1\n"
"v_mfma_f32_16x16x32_bf16 v[220:223], a[84:87], a[60:63], v[220:223]\n"
"v_mfma_f32_16x16x32_bf16 a[112:115], a[88:91], a[0:3], a[112:115]\n"
"ds_read_b128 a[0:3], v247 offset:0\n"
"v_mfma_f32_16x16x32_bf16 a[112:115], a[92:95], a[4:7], a[112:115]\n"
"ds_read_b128 a[4:7], v248 offset:0\n"
"v_mfma_f32_16x16x32_bf16 a[116:119], a[88:91], a[8:11], a[116:119]\n"
"ds_read_b128 a[8:11], v247 offset:2048\n"
"v_mfma_f32_16x16x32_bf16 a[116:119], a[92:95], a[12:15], a[116:119]\n"
"ds_read_b128 a[12:15], v248 offset:2048\n"
"v_mfma_f32_16x16x32_bf16 a[120:123], a[88:91], a[16:19], a[120:123]\n"
"ds_read_b128 a[16:19], v247 offset:4096\n"
"v_mfma_f32_16x16x32_bf16 a[120:123], a[92:95], a[20:23], a[120:123]\n"
"ds_read_b128 a[20:23], v248 offset:4096\n"
"v_mfma_f32_16x16x32_bf16 a[124:127], a[88:91], a[24:27], a[124:127]\n"
"ds_read_b128 a[24:27], v247 offset:6144\n"
"buffer_load_dwordx4 a[84:87], v240, s[36:39], s80 offen offset:3072 sc1\n"
"v_mfma_f32_16x16x32_bf16 a[124:127], a[92:95], a[28:31], a[124:127]\n"
"s_add_u32 s80, s80, 0x1000\n"
"ds_read_b128 a[28:31], v248 offset:6144\n"
"v_mfma_f32_16x16x32_bf16 a[128:131], a[88:91], a[32:35], a[128:131]\n"
"ds_read_b128 a[32:35], v247 offset:8192\n"
"v_mfma_f32_16x16x32_bf16 a[128:131], a[92:95], a[36:39], a[128:131]\n"
"ds_read_b128 a[36:39], v248 offset:8192\n"
"v_mfma_f32_16x16x32_bf16 a[132:135], a[88:91], a[40:43], a[132:135]\n"
"ds_read_b128 a[40:43], v247 offset:10240\n"
"v_mfma_f32_16x16x32_bf16 a[132:135], a[92:95], a[44:47], a[132:135]\n"
"ds_read_b128 a[44:47], v248 offset:10240\n"
"v_mfma_f32_16x16x32_bf16 a[136:139], a[88:91], a[48:51], a[136:139]\n"
"ds_read_b128 a[48:51], v247 offset:12288\n"
"v_mfma_f32_16x16x32_bf16 a[136:139], a[92:95], a[52:55], a[136:139]\n"
"ds_read_b128 a[52:55], v248 offset:12288\n"
"v_mfma_f32_16x16x32_bf16 a[140:143], a[88:91], a[56:59], a[140:143]\n"
"ds_read_b128 a[56:59], v247 offset:14336\n"
"buffer_load_dwordx4 a[88:91], v240, s[36:39], s80 offen offset:0 sc1\n"
"v_mfma_f32_16x16x32_bf16 a[140:143], a[92:95], a[60:63], a[140:143]\n"
"s_waitcnt vmcnt(12) lgkmcnt(11)\n"
"ds_read_b128 a[60:63], v248 offset:14336\n"
"v_mfma_f32_16x16x32_bf16 v[0:3], a[96:99], a[0:3], v[0:3]\n"
"v_mfma_f32_16x16x32_bf16 v[0:3], a[100:103], a[4:7], v[0:3]\n"
"s_waitcnt vmcnt(8)\n"
"v_cmp_ne_u32 vcc, s33, v241\n"
"v_mfma_f32_16x16x32_bf16 v[4:7], a[96:99], a[8:11], v[4:7]\n"
"v_mfma_f32_16x16x32_bf16 v[4:7], a[100:103], a[12:15], v[4:7]\n"
"s_waitcnt lgkmcnt(8)\n"
"v_mfma_f32_16x16x32_bf16 v[8:11], a[96:99], a[16:19], v[8:11]\n"
"s_cmp_eq_u64 vcc, 0\n"
"s_cbranch_scc0 .Lslow3_%=\n"
".Lback3_%=:\n"
"v_mfma_f32_16x16x32_bf16 v[8:11], a[100:103], a[20:23], v[8:11]\n"
"v_mfma_f32_16x16x32_bf16 v[12:15], a[96:99], a[24:27], v[12:15]\n"
"buffer_load_dwordx4 a[92:95], v240, s[36:39], s80 offen offset:1024 sc1\n"
"v_mfma_f32_16x16x32_bf16 v[12:15], a[100:103], a[28:31], v[12:15]\n"
"s_waitcnt lgkmcnt(4)\n"
"v_mfma_f32_16x16x32_bf16 v[16:19], a[96:99], a[32:35], v[16:19]\n"
"v_mfma_f32_16x16x32_bf16 v[16:19], a[100:103], a[36:39], v[16:19]\n"
"s_waitcnt lgkmcnt(0)\n"
"s_barrier\n"
"v_mfma_f32_16x16x32_bf16 v[20:23], a[96:99], a[40:43], v[20:23]\n"
"s_add_u32 m0, s83, 129024\n"
"v_mfma_f32_16x16x32_bf16 v[20:23], a[100:103], a[44:47], v[20:23]\n"
"buffer_load_dwordx4 v240, s[56:59], s68 offen offset:2048 sc1 lds\n"
"v_mfma_f32_16x16x32_bf16 v[24:27], a[96:99], a[48:51], v[24:27]\n"
"v_mfma_f32_16x16x32_bf16 v[24:27], a[100:103], a[52:55], v[24:27]\n"
"v_mfma_f32_16x16x32_bf16 v[28:31], a[96:99], a[56:59], v[28:31]\n"
"buffer_load_dwordx4 a[96:99], v240, s[36:39], s80 offen offset:2048 sc1\n"
"v_mfma_f32_16x16x32_bf16 v[28:31], a[100:103], a[60:63], v[28:31]\n"
"v_mfma_f32_16x16x32_bf16 v[32:35], a[104:107], a[0:3], v[32:35]\n"
"s_add_u32 m0, s83, 131072\n"
"v_mfma_f32_16x16x32_bf16 v[32:35], a[108:111], a[4:7], v[32:35]\n"
"buffer_load_dwordx4 v240, s[56:59], s69 offen offset:2048 sc1 lds\n"
"v_mfma_f32_16x16x32_bf16 v[36:39], a[104:107], a[8:11], v[36:39]\n"
"v_mfma_f32_16x16x32_bf16 v[36:39], a[108:111], a[12:15], v[36:39]\n"
"v_mfma_f32_16x16x32_bf16 v[40:43], a[104:107], a[16:19], v[40:43]\n"
"v_mfma_f32_16x16x32_bf16 v[40:43], a[108:111], a[20:23], v[40:43]\n"
"v_mfma_f32_16x16x32_bf16 v[44:47], a[104:107], a[24:27], v[44:47]\n"
"s_add_u32 m0, s83, 133120\n"
"buffer_load_dwordx4 a[100:103], v240, s[36:39], s80 offen offset:3072 sc1\n"
"v_mfma_f32_16x16x32_bf16 v[44:47], a[108:111], a[28:31], v[44:47]\n"
"s_add_u32 s80, s80, 0x1000\n"
"buffer_load_dwordx4 v240, s[56:59], s70 offen offset:2048 sc1 lds\n"
"v_mfma_f32_16x16x32_bf16 v[48:51], a[104:107], a[32:35], v[48:51]\n"
"v_mfma_f32_16x16x32_bf16 v[48:51], a[108:111], a[36:39], v[48:51]\n"
"v_mfma_f32_16x16x32_bf16 v[52:55], a[104:107], a[40:43], v[52:55]\n"
"v_mfma_f32_16x16x32_bf16 v[52:55], a[108:111], a[44:47], v[52:55]\n"
"v_mfma_f32_16x16x32_bf16 v[56:59], a[104:107], a[48:51], v[56:59]\n"
"s_add_u32 m0, s83, 135168\n"
"v_mfma_f32_16x16x32_bf16 v[56:59], a[108:111], a[52:55], v[56:59]\n"
"buffer_load_dwordx4 v240, s[56:59], s71 offen offset:2048 sc1 lds\n"
"v_mfma_f32_16x16x32_bf16 v[60:63], a[104:107], a[56:59], v[60:63]\n"
"buffer_load_dwordx4 a[104:107], v240, s[36:39], s80 offen offset:0 sc1\n"
"v_mfma_f32_16x16x32_bf16 v[60:63], a[108:111], a[60:63], v[60:63]\n"
"s_waitcnt vmcnt(15)\n"
"v_mfma_f32_16x16x32_bf16 v[64:67], v[224:227], a[0:3], v[64:67]\n"
"v_mfma_f32_16x16x32_bf16 v[64:67], v[228:231], a[4:7], v[64:67]\n"
"v_mfma_f32_16x16x32_bf16 v[68:71], v[224:227], a[8:11], v[68:71]\n"
"v_mfma_f32_16x16x32_bf16 v[68:71], v[228:231], a[12:15], v[68:71]\n"
"v_mfma_f32_16x16x32_bf16 v[72:75], v[224:227], a[16:19], v[72:75]\n"
"v_mfma_f32_16x16x32_bf16 v[72:75], v[228:231], a[20:23], v[72:75]\n"
"v_mfma_f32_16x16x32_bf16 v[76:79], v[224:227], a[24:27], v[76:79]\n"
"buffer_load_dwordx4 a[108:111], v240, s[36:39], s80 offen offset:1024 sc1\n"
"v_mfma_f32_16x16x32_bf16 v[76:79], v[228:231], a[28:31], v[76:79]\n"
"v_mfma_f32_16x16x32_bf16 v[80:83], v[224:227], a[32:35], v[80:83]\n"
"v_mfma_f32_16x16x32_bf16 v[80:83], v[228:231], a[36:39], v[80:83]\n"
"v_mfma_f32_16x16x32_bf16 v[84:87], v[224:227], a[40:43], v[84:87]\n"
"v_mfma_f32_16x16x32_bf16 v[84:87], v[228:231], a[44:47], v[84:87]\n"
"v_mfma_f32_16x16x32_bf16 v[88:91], v[224:227], a[48:51], v[88:91]\n"
"v_mfma_f32_16x16x32_bf16 v[88:91], v[228:231], a[52:55], v[88:91]\n"
"v_mfma_f32_16x16x32_bf16 v[92:95], v[224:227], a[56:59], v[92:95]\n"
"buffer_load_dwordx4 v[224:227], v240, s[36:39], s80 offen offset:2048 sc1\n"
"v_mfma_f32_16x16x32_bf16 v[92:95], v[228:231], a[60:63], v[92:95]\n"
"s_waitcnt vmcnt(15)\n"
"v_mfma_f32_16x16x32_bf16 v[96:99], a[64:67], a[0:3], v[96:99]\n"
"v_mfma_f32_16x16x32_bf16 v[96:99], a[68:71], a[4:7], v[96:99]\n"
"v_mfma_f32_16x16x32_bf16 v[100:103], a[64:67], a[8:11], v[100:103]\n"
"v_mfma_f32_16x16x32_bf16 v[100:103], a[68:71], a[12:15], v[100:103]\n"
"v_mfma_f32_16x16x32_bf16 v[104:107], a[64:67], a[16:19], v[104:107]\n"
"v_mfma_f32_16x16x32_bf16 v[104:107], a[68:71], a[20:23], v[104:107]\n"
"v_mfma_f32_16x16x32_bf16 v[108:111], a[64:67], a[24:27], v[108:111]\n"
"buffer_load_dwordx4 v[228:231], v240, s[36:39], s80 offen offset:3072 sc1\n"
"v_mfma_f32_16x16x32_bf16 v[108:111], a[68:71], a[28:31], v[108:111]\n"
"s_add_u32 s80, s80, 0x1000\n"
"v_mfma_f32_16x16x32_bf16 v[112:115], a[64:67], a[32:35], v[112:115]\n"
"v_mfma_f32_16x16x32_bf16 v[112:115], a[68:71], a[36:39], v[112:115]\n"
"v_mfma_f32_16x16x32_bf16 v[116:119], a[64:67], a[40:43], v[116:119]\n"
"v_mfma_f32_16x16x32_bf16 v[116:119], a[68:71], a[44:47], v[116:119]\n"
"v_mfma_f32_16x16x32_bf16 v[120:123], a[64:67], a[48:51], v[120:123]\n"
"v_mfma_f32_16x16x32_bf16 v[120:123], a[68:71], a[52:55], v[120:123]\n"
"v_mfma_f32_16x16x32_bf16 v[124:127], a[64:67], a[56:59], v[124:127]\n"
"buffer_load_dwordx4 a[64:67], v240, s[36:39], s80 offen offset:0 sc1\n"
"v_mfma_f32_16x16x32_bf16 v[124:127], a[68:71], a[60:63], v[124:127]\n"
"s_waitcnt vmcnt(15)\n"
"v_mfma_f32_16x16x32_bf16 v[128:131], a[72:75], a[0:3], v[128:131]\n"
"v_mfma_f32_16x16x32_bf16 v[128:131], a[76:79], a[4:7], v[128:131]\n"
"v_mfma_f32_16x16x32_bf16 v[132:135], a[72:75], a[8:11], v[132:135]\n"
"v_mfma_f32_16x16x32_bf16 v[132:135], a[76:79], a[12:15], v[132:135]\n"
"v_mfma_f32_16x16x32_bf16 v[136:139], a[72:75], a[16:19], v[136:139]\n"
"v_mfma_f32_16x16x32_bf16 v[136:139], a[76:79], a[20:23], v[136:139]\n"
"v_mfma_f32_16x16x32_bf16 v[140:143], a[72:75], a[24:27], v[140:143]\n"
"buffer_load_dwordx4 a[68:71], v240, s[36:39], s80 offen offset:1024 sc1\n"
"v_mfma_f32_16x16x32_bf16 v[140:143], a[76:79], a[28:31], v[140:143]\n"
"v_mfma_f32_16x16x32_bf16 v[144:147], a[72:75], a[32:35], v[144:147]\n"
"v_mfma_f32_16x16x32_bf16 v[144:147], a[76:79], a[36:39], v[144:147]\n"
"v_mfma_f32_16x16x32_bf16 v[148:151], a[72:75], a[40:43], v[148:151]\n"
"v_mfma_f32_16x16x32_bf16 v[148:151], a[76:79], a[44:47], v[148:151]\n"
"v_mfma_f32_16x16x32_bf16 v[152:155], a[72:75], a[48:51], v[152:155]\n"
"v_mfma_f32_16x16x32_bf16 v[152:155], a[76:79], a[52:55], v[152:155]\n"
"v_mfma_f32_16x16x32_bf16 v[156:159], a[72:75], a[56:59], v[156:159]\n"
"buffer_load_dwordx4 a[72:75], v240, s[36:39], s80 offen offset:2048 sc1\n"
"v_mfma_f32_16x16x32_bf16 v[156:159], a[76:79], a[60:63], v[156:159]\n"
"s_waitcnt vmcnt(15)\n"
"v_mfma_f32_16x16x32_bf16 v[160:163], a[80:83], a[0:3], v[160:163]\n"
"v_mfma_f32_16x16x32_bf16 v[160:163], a[84:87], a[4:7], v[160:163]\n"
"v_mfma_f32_16x16x32_bf16 v[164:167], a[80:83], a[8:11], v[164:167]\n"
"v_mfma_f32_16x16x32_bf16 v[164:167], a[84:87], a[12:15], v[164:167]\n"
"v_mfma_f32_16x16x32_bf16 v[168:171], a[80:83], a[16:19], v[168:171]\n"
"v_mfma_f32_16x16x32_bf16 v[168:171], a[84:87], a[20:23], v[168:171]\n"
"v_mfma_f32_16x16x32_bf16 v[172:175], a[80:83], a[24:27], v[172:175]\n"
"buffer_load_dwordx4 a[76:79], v240, s[36:39], s80 offen offset:3072 sc1\n"
"v_mfma_f32_16x16x32_bf16 v[172:175], a[84:87], a[28:31], v[172:175]\n"
"s_add_u32 s80, s80, 0x1000\n"
"v_mfma_f32_16x16x32_bf16 v[176:179], a[80:83], a[32:35], v[176:179]\n"
"v_mfma_f32_16x16x32_bf16 v[176:179], a[84:87], a[36:39], v[176:179]\n"
"v_mfma_f32_16x16x32_bf16 v[180:183], a[80:83], a[40:43], v[180:183]\n"
"v_mfma_f32_16x16x32_bf16 v[180:183], a[84:87], a[44:47], v[180:183]\n"
"v_mfma_f32_16x16x32_bf16 v[184:187], a[80:83], a[48:51], v[184:187]\n"
"v_mfma_f32_16x16x32_bf16 v[184:187], a[84:87], a[52:55], v[184:187]\n"
"v_mfma_f32_16x16x32_bf16 v[188:191], a[80:83], a[56:59], v[188:191]\n"
"buffer_load_dwordx4 a[80:83], v240, s[36:39], s80 offen offset:0 sc1\n"
"v_mfma_f32_16x16x32_bf16 v[188:191], a[84:87], a[60:63], v[188:191]\n"
"s_waitcnt vmcnt(15)\n"
"v_mfma_f32_16x16x32_bf16 v[192:195], a[88:91], a[0:3], v[192:195]\n"
"v_mfma_f32_16x16x32_bf16 v[192:195], a[92:95], a[4:7], v[192:195]\n"
"v_mfma_f32_16x16x32_bf16 v[196:199], a[88:91], a[8:11], v[196:199]\n"
"v_mfma_f32_16x16x32_bf16 v[196:199], a[92:95], a[12:15], v[196:199]\n"
"v_mfma_f32_16x16x32_bf16 v[200:203], a[88:91], a[16:19], v[200:203]\n"
"v_mfma_f32_16x16x32_bf16 v[200:203], a[92:95], a[20:23], v[200:203]\n"
"v_mfma_f32_16x16x32_bf16 v[204:207], a[88:91], a[24:27], v[204:207]\n"
"buffer_load_dwordx4 a[84:87], v240, s[36:39], s80 offen offset:1024 sc1\n"
"v_mfma_f32_16x16x32_bf16 v[204:207], a[92:95], a[28:31], v[204:207]\n"
"v_mfma_f32_16x16x32_bf16 v[208:211], a[88:91], a[32:35], v[208:211]\n"
"v_mfma_f32_16x16x32_bf16 v[208:211], a[92:95], a[36:39], v[208:211]\n"
"v_mfma_f32_16x16x32_bf16 v[212:215], a[88:91], a[40:43], v[212:215]\n"
"v_mfma_f32_16x16x32_bf16 v[212:215], a[92:95], a[44:47], v[212:215]\n"
"v_mfma_f32_16x16x32_bf16 v[216:219], a[88:91], a[48:51], v[216:219]\n"
"v_mfma_f32_16x16x32_bf16 v[216:219], a[92:95], a[52:55], v[216:219]\n"
"s_waitcnt vmcnt(10)\n"
"s_barrier\n"
"v_mfma_f32_16x16x32_bf16 v[220:223], a[88:91], a[56:59], v[220:223]\n"
"buffer_load_dwordx4 a[88:91], v240, s[36:39], s80 offen offset:2048 sc1\n"
"v_mfma_f32_16x16x32_bf16 v[220:223], a[92:95], a[60:63], v[220:223]\n"
"v_mfma_f32_16x16x32_bf16 a[112:115], a[96:99], a[0:3], a[112:115]\n"
"ds_read_b128 a[0:3], v247 offset:0\n"
"v_mfma_f32_16x16x32_bf16 a[112:115], a[100:103], a[4:7], a[112:115]\n"
"ds_read_b128 a[4:7], v248 offset:0\n"
"v_mfma_f32_16x16x32_bf16 a[116:119], a[96:99], a[8:11], a[116:119]\n"
"ds_read_b128 a[8:11], v247 offset:2048\n"
"v_mfma_f32_16x16x32_bf16 a[116:119], a[100:103], a[12:15], a[116:119]\n"
"ds_read_b128 a[12:15], v248 offset:2048\n"
"v_mfma_f32_16x16x32_bf16 a[120:123], a[96:99], a[16:19], a[120:123]\n"
"ds_read_b128 a[16:19], v247 offset:4096\n"
"v_mfma_f32_16x16x32_bf16 a[120:123], a[100:103], a[20:23], a[120:123]\n"
"ds_read_b128 a[20:23], v248 offset:4096\n"
"v_mfma_f32_16x16x32_bf16 a[124:127], a[96:99], a[24:27], a[124:127]\n"
"ds_read_b128 a[24:27], v247 offset:6144\n"
"buffer_load_dwordx4 a[92:95], v240, s[36:39], s80 offen offset:3072 sc1\n"
"v_mfma_f32_16x16x32_bf16 a[124:127], a[100:103], a[28:31], a[124:127]\n"
"s_add_u32 s80, s80, 0x1000\n"
"ds_read_b128 a[28:31], v248 offset:6144\n"
"v_mfma_f32_16x16x32_bf16 a[128:131], a[96:99], a[32:35], a[128:131]\n"
"ds_read_b128 a[32:35], v247 offset:8192\n"
"v_mfma_f32_16x16x32_bf16 a[128:131], a[100:103], a[36:39], a[128:131]\n"
"ds_read_b128 a[36:39], v248 offset:8192\n"
"v_mfma_f32_16x16x32_bf16 a[132:135], a[96:99], a[40:43], a[132:135]\n"
"ds_read_b128 a[40:43], v247 offset:10240\n"
"v_mfma_f32_16x16x32_bf16 a[132:135], a[100:103], a[44:47], a[132:135]\n"
"ds_read_b128 a[44:47], v248 offset:10240\n"
"v_mfma_f32_16x16x32_bf16 a[136:139], a[96:99], a[48:51], a[136:139]\n"
"ds_read_b128 a[48:51], v247 offset:12288\n"
"v_mfma_f32_16x16x32_bf16 a[136:139], a[100:103], a[52:55], a[136:139]\n"
"ds_read_b128 a[52:55], v248 offset:12288\n"
"v_mfma_f32_16x16x32_bf16 a[140:143], a[96:99], a[56:59], a[140:143]\n"
"ds_read_b128 a[56:59], v247 offset:14336\n"
"buffer_load_dwordx4 a[96:99], v240, s[36:39], s80 offen offset:0 sc1\n"
"v_mfma_f32_16x16x32_bf16 a[140:143], a[100:103], a[60:63], a[140:143]\n"
"s_waitcnt vmcnt(11) lgkmcnt(11)\n"
"ds_read_b128 a[60:63], v248 offset:14336\n"
"v_mfma_f32_16x16x32_bf16 v[0:3], a[104:107], a[0:3], v[0:3]\n"
"v_mfma_f32_16x16x32_bf16 v[0:3], a[108:111], a[4:7], v[0:3]\n"
"v_mfma_f32_16x16x32_bf16 v[4:7], a[104:107], a[8:11], v[4:7]\n"
"v_mfma_f32_16x16x32_bf16 v[4:7], a[108:111], a[12:15], v[4:7]\n"
"s_waitcnt lgkmcnt(8)\n"
"v_mfma_f32_16x16x32_bf16 v[8:11], a[104:107], a[16:19], v[8:11]\n"
"v_mfma_f32_16x16x32_bf16 v[8:11], a[108:111], a[20:23], v[8:11]\n"
"v_mfma_f32_16x16x32_bf16 v[12:15], a[104:107], a[24:27], v[12:15]\n"
"buffer_load_dwordx4 a[100:103], v240, s[36:39], s80 offen offset:1024 sc1\n"
"v_mfma_f32_16x16x32_bf16 v[12:15], a[108:111], a[28:31], v[12:15]\n"
"s_waitcnt lgkmcnt(4)\n"
"v_mfma_f32_16x16x32_bf16 v[16:19], a[104:107], a[32:35], v[16:19]\n"
"v_mfma_f32_16x16x32_bf16 v[16:19], a[108:111], a[36:39], v[16:19]\n"
"s_waitcnt lgkmcnt(0)\n"
"s_barrier\n"
"v_mfma_f32_16x16x32_bf16 v[20:23], a[104:107], a[40:43], v[20:23]\n"
"s_add_u32 m0, s83, 129024\n"
"v_mfma_f32_16x16x32_bf16 v[20:23], a[108:111], a[44:47], v[20:23]\n"
"buffer_load_dwordx4 v240, s[56:59], s72 offen offset:2048 sc1 lds\n"
"v_mfma_f32_16x16x32_bf16 v[24:27], a[104:107], a[48:51], v[24:27]\n"
"v_mfma_f32_16x16x32_bf16 v[24:27], a[108:111], a[52:55], v[24:27]\n"
"v_mfma_f32_16x16x32_bf16 v[28:31], a[104:107], a[56:59], v[28:31]\n"
"buffer_load_dwordx4 a[104:107], v240, s[36:39], s80 offen offset:2048 sc1\n"
"v_mfma_f32_16x16x32_bf16 v[28:31], a[108:111], a[60:63], v[28:31]\n"
"s_waitcnt vmcnt(12)\n"
"v_mfma_f32_16x16x32_bf16 v[32:35], v[224:227], a[0:3], v[32:35]\n"
"s_add_u32 m0, s83, 131072\n"
"v_mfma_f32_16x16x32_bf16 v[32:35], v[228:231], a[4:7], v[32:35]\n"
"buffer_load_dwordx4 v240, s[56:59], s73 offen offset:2048 sc1 lds\n"
"v_mfma_f32_16x16x32_bf16 v[36:39], v[224:227], a[8:11], v[36:39]\n"
"v_mfma_f32_16x16x32_bf16 v[36:39], v[228:231], a[12:15], v[36:39]\n"
"v_mfma_f32_16x16x32_bf16 v[40:43], v[224:227], a[16:19], v[40:43]\n"
"v_mfma_f32_16x16x32_bf16 v[40:43], v[228:231], a[20:23], v[40:43]\n"
"v_mfma_f32_16x16x32_bf16 v[44:47], v[224:227], a[24:27], v[44:47]\n"
"s_add_u32 m0, s83, 133120\n"
"buffer_load_dwordx4 a[108:111], v240, s[36:39], s80 offen offset:3072 sc1\n"
"v_mfma_f32_16x16x32_bf16 v[44:47], v[228:231], a[28:31], v[44:47]\n"
"s_add_u32 s80, s80, 0x1000\n"
"buffer_load_dwordx4 v240, s[56:59], s74 offen offset:2048 sc1 lds\n"
"v_mfma_f32_16x16x32_bf16 v[48:51], v[224:227], a[32:35], v[48:51]\n"
"v_mfma_f32_16x16x32_bf16 v[48:51], v[228:231], a[36:39], v[48:51]\n"
"v_mfma_f32_16x16x32_bf16 v[52:55], v[224:227], a[40:43], v[52:55]\n"
"v_mfma_f32_16x16x32_bf16 v[52:55], v[228:231], a[44:47], v[52:55]\n"
"v_mfma_f32_16x16x32_bf16 v[56:59], v[224:227], a[48:51], v[56:59]\n"
"s_add_u32 m0, s83, 135168\n"
"v_mfma_f32_16x16x32_bf16 v[56:59], v[228:231], a[52:55], v[56:59]\n"
"buffer_load_dwordx4 v240, s[56:59], s75 offen offset:2048 sc1 lds\n"
"v_mfma_f32_16x16x32_bf16 v[60:63], v[224:227], a[56:59], v[60:63]\n"
"buffer_load_dwordx4 v[224:227], v240, s[36:39], s80 offen offset:0 sc1\n"
"v_mfma_f32_16x16x32_bf16 v[60:63], v[228:231], a[60:63], v[60:63]\n"
"s_waitcnt vmcnt(15)\n"
"v_mfma_f32_16x16x32_bf16 v[64:67], a[64:67], a[0:3], v[64:67]\n"
"v_mfma_f32_16x16x32_bf16 v[64:67], a[68:71], a[4:7], v[64:67]\n"
"v_mfma_f32_16x16x32_bf16 v[68:71], a[64:67], a[8:11], v[68:71]\n"
"v_mfma_f32_16x16x32_bf16 v[68:71], a[68:71], a[12:15], v[68:71]\n"
"v_mfma_f32_16x16x32_bf16 v[72:75], a[64:67], a[16:19], v[72:75]\n"
"v_mfma_f32_16x16x32_bf16 v[72:75], a[68:71], a[20:23], v[72:75]\n"
"v_mfma_f32_16x16x32_bf16 v[76:79], a[64:67], a[24:27], v[76:79]\n"
"buffer_load_dwordx4 v[228:231], v240, s[36:39], s80 offen offset:1024 sc1\n"
"v_mfma_f32_16x16x32_bf16 v[76:79], a[68:71], a[28:31], v[76:79]\n"
"v_mfma_f32_16x16x32_bf16 v[80:83], a[64:67], a[32:35], v[80:83]\n"
"v_mfma_f32_16x16x32_bf16 v[80:83], a[68:71], a[36:39], v[80:83]\n"
"v_mfma_f32_16x16x32_bf16 v[84:87], a[64:67], a[40:43], v[84:87]\n"
"v_mfma_f32_16x16x32_bf16 v[84:87], a[68:71], a[44:47], v[84:87]\n"
"v_mfma_f32_16x16x32_bf16 v[88:91], a[64:67], a[48:51], v[88:91]\n"
"v_mfma_f32_16x16x32_bf16 v[88:91], a[68:71], a[52:55], v[88:91]\n"
"v_mfma_f32_16x16x32_bf16 v[92:95], a[64:67], a[56:59], v[92:95]\n"
"buffer_load_dwordx4 a[64:67], v240, s[36:39], s80 offen offset:2048 sc1\n"
"v_mfma_f32_16x16x32_bf16 v[92:95], a[68:71], a[60:63], v[92:95]\n"
".Lpad18_%=:\n"
"s_cbranch_execz .Lpad22_%=\n"
"s_waitcnt vmcnt(15)\n"
"v_mfma_f32_16x16x32_bf16 v[96:99], a[72:75], a[0:3], v[96:99]\n"
"v_mfma_f32_16x16x32_bf16 v[96:99], a[76:79], a[4:7], v[96:99]\n"
"v_mfma_f32_16x16x32_bf16 v[100:103], a[72:75], a[8:11], v[100:103]\n"
"v_mfma_f32_16x16x32_bf16 v[100:103], a[76:79], a[12:15], v[100:103]\n"
"v_mfma_f32_16x16x32_bf16 v[104:107], a[72:75], a[16:19], v[104:107]\n"
"v_mfma_f32_16x16x32_bf16 v[104:107], a[76:79], a[20:23], v[104:107]\n"
"v_mfma_f32_16x16x32_bf16 v[108:111], a[72:75], a[24:27], v[108:111]\n"
"buffer_load_dwordx4 a[68:71], v240, s[36:39], s80 offen offset:3072 sc1\n"
"v_mfma_f32_16x16x32_bf16 v[108:111], a[76:79], a[28:31], v[108:111]\n"
"s_add_u32 s80, s80, 0x1000\n"
"v_mfma_f32_16x16x32_bf16 v[112:115], a[72:75], a[32:35], v[112:115]\n"
"v_mfma_f32_16x16x32_bf16 v[112:115], a[76:79], a[36:39], v[112:115]\n"
"v_mfma_f32_16x16x32_bf16 v[116:119], a[72:75], a[40:43], v[116:119]\n"
"v_mfma_f32_16x16x32_bf16 v[116:119], a[76:79], a[44:47], v[116:119]\n"
"v_mfma_f32_16x16x32_bf16 v[120:123], a[72:75], a[48:51], v[120:123]\n"
"v_mfma_f32_16x16x32_bf16 v[120:123], a[76:79], a[52:55], v[120:123]\n"
"v_mfma_f32_16x16x32_bf16 v[124:127], a[72:75], a[56:59], v[124:127]\n"
"buffer_load_dwordx4 a[72:75], v240, s[36:39], s80 offen offset:0 sc1\n"
"v_mfma_f32_16x16x32_bf16 v[124:127], a[76:79], a[60:63], v[124:127]\n"
"s_waitcnt vmcnt(15)\n"
"v_mfma_f32_16x16x32_bf16 v[128:131], a[80:83], a[0:3], v[128:131]\n"
"v_mfma_f32_16x16x32_bf16 v[128:131], a[84:87], a[4:7], v[128:131]\n"
"v_mfma_f32_16x16x32_bf16 v[132:135], a[80:83], a[8:11], v[132:135]\n"
"v_mfma_f32_16x16x32_bf16 v[132:135], a[84:87], a[12:15], v[132:135]\n"
"v_mfma_f32_16x16x32_bf16 v[136:139], a[80:83], a[16:19], v[136:139]\n"
"v_mfma_f32_16x16x32_bf16 v[136:139], a[84:87], a[20:23], v[136:139]\n"
"v_mfma_f32_16x16x32_bf16 v[140:143], a[80:83], a[24:27], v[140:143]\n"
"buffer_load_dwordx4 a[76:79], v240, s[36:39], s80 offen offset:1024 sc1\n"
"v_mfma_f32_16x16x32_bf16 v[140:143], a[84:87], a[28:31], v[140:143]\n"
"v_mfma_f32_16x16x32_bf16 v[144:147], a[80:83], a[32:35], v[144:147]\n"
"v_mfma_f32_16x16x32_bf16 v[144:147], a[84:87], a[36:39], v[144:147]\n"
"v_mfma_f32_16x16x32_bf16 v[148:151], a[80:83], a[40:43], v[148:151]\n"
"v_mfma_f32_16x16x32_bf16 v[148:151], a[84:87], a[44:47], v[148:151]\n"
"v_mfma_f32_16x16x32_bf16 v[152:155], a[80:83], a[48:51], v[152:155]\n"
"v_mfma_f32_16x16x32_bf16 v[152:155], a[84:87], a[52:55], v[152:155]\n"
"v_mfma_f32_16x16x32_bf16 v[156:159], a[80:83], a[56:59], v[156:159]\n"
"buffer_load_dwordx4 a[80:83], v240, s[36:39], s80 offen offset:2048 sc1\n"
"v_mfma_f32_16x16x32_bf16 v[156:159], a[84:87], a[60:63], v[156:159]\n"
"s_waitcnt vmcnt(15)\n"
"v_mfma_f32_16x16x32_bf16 v[160:163], a[88:91], a[0:3], v[160:163]\n"
"v_mfma_f32_16x16x32_bf16 v[160:163], a[92:95], a[4:7], v[160:163]\n"
"v_mfma_f32_16x16x32_bf16 v[164:167], a[88:91], a[8:11], v[164:167]\n"
"v_mfma_f32_16x16x32_bf16 v[164:167], a[92:95], a[12:15], v[164:167]\n"
"v_mfma_f32_16x16x32_bf16 v[168:171], a[88:91], a[16:19], v[168:171]\n"
"v_mfma_f32_16x16x32_bf16 v[168:171], a[92:95], a[20:23], v[168:171]\n"
"v_mfma_f32_16x16x32_bf16 v[172:175], a[88:91], a[24:27], v[172:175]\n"
"buffer_load_dwordx4 a[84:87], v240, s[36:39], s80 offen offset:3072 sc1\n"
"v_mfma_f32_16x16x32_bf16 v[172:175], a[92:95], a[28:31], v[172:175]\n"
"s_add_u32 s80, s80, 0x1000\n"
"v_mfma_f32_16x16x32_bf16 v[176:179], a[88:91], a[32:35], v[176:179]\n"
"v_mfma_f32_16x16x32_bf16 v[176:179], a[92:95], a[36:39], v[176:179]\n"
"v_mfma_f32_16x16x32_bf16 v[180:183], a[88:91], a[40:43], v[180:183]\n"
"v_mfma_f32_16x16x32_bf16 v[180:183], a[92:95], a[44:47], v[180:183]\n"
"v_mfma_f32_16x16x32_bf16 v[184:187], a[88:91], a[48:51], v[184:187]\n"
"v_mfma_f32_16x16x32_bf16 v[184:187], a[92:95], a[52:55], v[184:187]\n"
"v_mfma_f32_16x16x32_bf16 v[188:191], a[88:91], a[56:59], v[188:191]\n"
"buffer_load_dwordx4 a[88:91], v240, s[36:39], s80 offen offset:0 sc1\n"
"v_mfma_f32_16x16x32_bf16 v[188:191], a[92:95], a[60:63], v[188:191]\n"
"s_waitcnt vmcnt(15)\n"
"v_mfma_f32_16x16x32_bf16 v[192:195], a[96:99], a[0:3], v[192:195]\n"
"v_mfma_f32_16x16x32_bf16 v[192:195], a[100:103], a[4:7], v[192:195]\n"
"v_mfma_f32_16x16x32_bf16 v[196:199], a[96:99], a[8:11], v[196:199]\n"
"v_mfma_f32_16x16x32_bf16 v[196:199], a[100:103], a[12:15], v[196:199]\n"
"v_mfma_f32_16x16x32_bf16 v[200:203], a[96:99], a[16:19], v[200:203]\n"
"v_mfma_f32_16x16x32_bf16 v[200:203], a[100:103], a[20:23], v[200:203]\n"
"v_mfma_f32_16x16x32_bf16 v[204:207], a[96:99], a[24:27], v[204:207]\n"
"buffer_load_dwordx4 a[92:95], v240, s[36:39], s80 offen offset:1024 sc1\n"
"v_mfma_f32_16x16x32_bf16 v[204:207], a[100:103], a[28:31], v[204:207]\n"
"v_mfma_f32_16x16x32_bf16 v[208:211], a[96:99], a[32:35], v[208:211]\n"
"v_mfma_f32_16x16x32_bf16 v[208:211], a[100:103], a[36:39], v[208:211]\n"
"v_mfma_f32_16x16x32_bf16 v[212:215], a[96:99], a[40:43], v[212:215]\n"
"v_mfma_f32_16x16x32_bf16 v[212:215], a[100:103], a[44:47], v[212:215]\n"
"v_mfma_f32_16x16x32_bf16 v[216:219], a[96:99], a[48:51], v[216:219]\n"
"v_mfma_f32_16x16x32_bf16 v[216:219], a[100:103], a[52:55], v[216:219]\n"
"s_waitcnt vmcnt(10)\n"
"s_barrier\n"
"v_mfma_f32_16x16x32_bf16 v[220:223], a[96:99], a[56:59], v[220:223]\n"
"buffer_load_dwordx4 a[96:99], v240, s[36:39], s80 offen offset:2048 sc1\n"
"v_mfma_f32_16x16x32_bf16 v[220:223], a[100:103], a[60:63], v[220:223]\n"
"v_mfma_f32_16x16x32_bf16 a[112:115], a[104:107], a[0:3], a[112:115]\n"
"ds_read_b128 a[0:3], v247 offset:0\n"
"v_mfma_f32_16x16x32_bf16 a[112:115], a[108:111], a[4:7], a[112:115]\n"
"ds_read_b128 a[4:7], v248 offset:0\n"
"v_mfma_f32_16x16x32_bf16 a[116:119], a[104:107], a[8:11], a[116:119]\n"
"ds_read_b128 a[8:11], v247 offset:2048\n"
"v_mfma_f32_16x16x32_bf16 a[116:119], a[108:111], a[12:15], a[116:119]\n"
"ds_read_b128 a[12:15], v248 offset:2048\n"
"v_mfma_f32_16x16x32_bf16 a[120:123], a[104:107], a[16:19], a[120:123]\n"
"ds_read_b128 a[16:19], v247 offset:4096\n"
"v_mfma_f32_16x16x32_bf16 a[120:123], a[108:111], a[20:23], a[120:123]\n"
"ds_read_b128 a[20:23], v248 offset:4096\n"
"v_mfma_f32_16x16x32_bf16 a[124:127], a[104:107], a[24:27], a[124:127]\n"
"ds_read_b128 a[24:27], v247 offset:6144\n"
"buffer_load_dwordx4 a[100:103], v240, s[36:39], s80 offen offset:3072 sc1\n"
"v_mfma_f32_16x16x32_bf16 a[124:127], a[108:111], a[28:31], a[124:127]\n"
"s_add_u32 s80, s80, 0x1000\n"
"ds_read_b128 a[28:31], v248 offset:6144\n"
"v_mfma_f32_16x16x32_bf16 a[128:131], a[104:107], a[32:35], a[128:131]\n"
"ds_read_b128 a[32:35], v247 offset:8192\n"
"v_mfma_f32_16x16x32_bf16 a[128:131], a[108:111], a[36:39], a[128:131]\n"
"ds_read_b128 a[36:39], v248 offset:8192\n"
"v_mfma_f32_16x16x32_bf16 a[132:135], a[104:107], a[40:43], a[132:135]\n"
"ds_read_b128 a[40:43], v247 offset:10240\n"
"v_mfma_f32_16x16x32_bf16 a[132:135], a[108:111], a[44:47], a[132:135]\n"
"ds_read_b128 a[44:47], v248 offset:10240\n"
"v_mfma_f32_16x16x32_bf16 a[136:139], a[104:107], a[48:51], a[136:139]\n"
"ds_read_b128 a[48:51], v247 offset:12288\n"
"v_mfma_f32_16x16x32_bf16 a[136:139], a[108:111], a[52:55], a[136:139]\n"
"ds_read_b128 a[52:55], v248 offset:12288\n"
"v_mfma_f32_16x16x32_bf16 a[140:143], a[104:107], a[56:59], a[140:143]\n"
"ds_read_b128 a[56:59], v247 offset:14336\n"
"buffer_load_dwordx4 a[104:107], v240, s[36:39], s80 offen offset:0 sc1\n"
"v_mfma_f32_16x16x32_bf16 a[140:143], a[108:111], a[60:63], a[140:143]\n"
"s_waitcnt vmcnt(11) lgkmcnt(11)\n"
"ds_read_b128 a[60:63], v248 offset:14336\n"
"v_mfma_f32_16x16x32_bf16 v[0:3], v[224:227], a[0:3], v[0:3]\n"
"v_mfma_f32_16x16x32_bf16 v[0:3], v[228:231], a[4:7], v[0:3]\n"
"v_mfma_f32_16x16x32_bf16 v[4:7], v[224:227], a[8:11], v[4:7]\n"
"v_mfma_f32_16x16x32_bf16 v[4:7], v[228:231], a[12:15], v[4:7]\n"
"s_waitcnt lgkmcnt(8)\n"
"v_mfma_f32_16x16x32_bf16 v[8:11], v[224:227], a[16:19], v[8:11]\n"
"v_mfma_f32_16x16x32_bf16 v[8:11], v[228:231], a[20:23], v[8:11]\n"
"v_mfma_f32_16x16x32_bf16 v[12:15], v[224:227], a[24:27], v[12:15]\n"
"buffer_load_dwordx4 a[108:111], v240, s[36:39], s80 offen offset:1024 sc1\n"
"v_mfma_f32_16x16x32_bf16 v[12:15], v[228:231], a[28:31], v[12:15]\n"
"s_waitcnt lgkmcnt(4)\n"
"v_mfma_f32_16x16x32_bf16 v[16:19], v[224:227], a[32:35], v[16:19]\n"
"v_mfma_f32_16x16x32_bf16 v[16:19], v[228:231], a[36:39], v[16:19]\n"
"s_waitcnt lgkmcnt(0)\n"
"s_barrier\n"
"v_mfma_f32_16x16x32_bf16 v[20:23], v[224:227], a[40:43], v[20:23]\n"
"s_add_u32 m0, s83, 128000\n"
"v_mfma_f32_16x16x32_bf16 v[20:23], v[228:231], a[44:47], v[20:23]\n"
"buffer_load_dwordx4 v240, s[56:59], s68 offen offset:3072 sc1 lds\n"
"v_mfma_f32_16x16x32_bf16 v[24:27], v[224:227], a[48:51], v[24:27]\n"
"v_mfma_f32_16x16x32_bf16 v[24:27], v[228:231], a[52:55], v[24:27]\n"
"v_mfma_f32_16x16x32_bf16 v[28:31], v[224:227], a[56:59], v[28:31]\n"
"buffer_load_dwordx4 v[224:227], v240, s[36:39], s80 offen offset:2048 sc1\n"
"v_mfma_f32_16x16x32_bf16 v[28:31], v[228:231], a[60:63], v[28:31]\n"
"s_waitcnt vmcnt(12)\n"
"v_mfma_f32_16x16x32_bf16 v[32:35], a[64:67], a[0:3], v[32:35]\n"
"s_add_u32 m0, s83, 130048\n"
"v_mfma_f32_16x16x32_bf16 v[32:35], a[68:71], a[4:7], v[32:35]\n"
"buffer_load_dwordx4 v240, s[56:59], s69 offen offset:3072 sc1 lds\n"
"v_mfma_f32_16x16x32_bf16 v[36:39], a[64:67], a[8:11], v[36:39]\n"
"v_mfma_f32_16x16x32_bf16 v[36:39], a[68:71], a[12:15], v[36:39]\n"
"v_mfma_f32_16x16x32_bf16 v[40:43], a[64:67], a[16:19], v[40:43]\n"
"v_mfma_f32_16x16x32_bf16 v[40:43], a[68:71], a[20:23], v[40:43]\n"
"v_mfma_f32_16x16x32_bf16 v[44:47], a[64:67], a[24:27], v[44:47]\n"
"s_add_u32 m0, s83, 132096\n"
"buffer_load_dwordx4 v[228:231], v240, s[36:39], s80 offen offset:3072 sc1\n"
"v_mfma_f32_16x16x32_bf16 v[44:47], a[68:71], a[28:31], v[44:47]\n"
"s_add_u32 s80, s80, 0x1000\n"
"buffer_load_dwordx4 v240, s[56:59], s70 offen offset:3072 sc1 lds\n"
"v_mfma_f32_16x16x32_bf16 v[48:51], a[64:67], a[32:35], v[48:51]\n"
"v_mfma_f32_16x16x32_bf16 v[48:51], a[68:71], a[36:39], v[48:51]\n"
"v_mfma_f32_16x16x32_bf16 v[52:55], a[64:67], a[40:43], v[52:55]\n"
"v_mfma_f32_16x16x32_bf16 v[52:55], a[68:71], a[44:47], v[52:55]\n"
"v_mfma_f32_16x16x32_bf16 v[56:59], a[64:67], a[48:51], v[56:59]\n"
"s_add_u32 m0, s83, 134144\n"
"v_mfma_f32_16x16x32_bf16 v[56:59], a[68:71], a[52:55], v[56:59]\n"
"buffer_load_dwordx4 v240, s[56:59], s71 offen offset:3072 sc1 lds\n"
"v_mfma_f32_16x16x32_bf16 v[60:63], a[64:67], a[56:59], v[60:63]\n"
"buffer_load_dwordx4 a[64:67], v240, s[36:39], s80 offen offset:0 sc1\n"
"v_mfma_f32_16x16x32_bf16 v[60:63], a[68:71], a[60:63], v[60:63]\n"
"s_waitcnt vmcnt(15)\n"
"v_mfma_f32_16x16x32_bf16 v[64:67], a[72:75], a[0:3], v[64:67]\n"
"v_mfma_f32_16x16x32_bf16 v[64:67], a[76:79], a[4:7], v[64:67]\n"
"v_mfma_f32_16x16x32_bf16 v[68:71], a[72:75], a[8:11], v[68:71]\n"
"v_mfma_f32_16x16x32_bf16 v[68:71], a[76:79], a[12:15], v[68:71]\n"
"v_mfma_f32_16x16x32_bf16 v[72:75], a[72:75], a[16:19], v[72:75]\n"
"v_mfma_f32_16x16x32_bf16 v[72:75], a[76:79], a[20:23], v[72:75]\n"
"v_mfma_f32_16x16x32_bf16 v[76:79], a[72:75], a[24:27], v[76:79]\n"
"buffer_load_dwordx4 a[68:71], v240, s[36:39], s80 offen offset:1024 sc1\n"
"v_mfma_f32_16x16x32_bf16 v[76:79], a[76:79], a[28:31], v[76:79]\n"
"v_mfma_f32_16x16x32_bf16 v[80:83], a[72:75], a[32:35], v[80:83]\n"
"v_mfma_f32_16x16x32_bf16 v[80:83], a[76:79], a[36:39], v[80:83]\n"
"v_mfma_f32_16x16x32_bf16 v[84:87], a[72:75], a[40:43], v[84:87]\n"
"v_mfma_f32_16x16x32_bf16 v[84:87], a[76:79], a[44:47], v[84:87]\n"
"v_mfma_f32_16x16x32_bf16 v[88:91], a[72:75], a[48:51], v[88:91]\n"
"v_mfma_f32_16x16x32_bf16 v[88:91], a[76:79], a[52:55], v[88:91]\n"
"v_mfma_f32_16x16x32_bf16 v[92:95], a[72:75], a[56:59], v[92:95]\n"
"buffer_load_dwordx4 a[72:75], v240, s[36:39], s80 offen offset:2048 sc1\n"
"v_mfma_f32_16x16x32_bf16 v[92:95], a[76:79], a[60:63], v[92:95]\n"
"s_waitcnt vmcnt(15)\n"
"v_mfma_f32_16x16x32_bf16 v[96:99], a[80:83], a[0:3], v[96:99]\n"
"v_mfma_f32_16x16x32_bf16 v[96:99], a[84:87], a[4:7], v[96:99]\n"
"v_mfma_f32_16x16x32_bf16 v[100:103], a[80:83], a[8:11], v[100:103]\n"
"v_mfma_f32_16x16x32_bf16 v[100:103], a[84:87], a[12:15], v[100:103]\n"
"v_mfma_f32_16x16x32_bf16 v[104:107], a[80:83], a[16:19], v[104:107]\n"
"v_mfma_f32_16x16x32_bf16 v[104:107], a[84:87], a[20:23], v[104:107]\n"
"v_mfma_f32_16x16x32_bf16 v[108:111], a[80:83], a[24:27], v[108:111]\n"
"buffer_load_dwordx4 a[76:79], v240, s[36:39], s80 offen offset:3072 sc1\n"
"v_mfma_f32_16x16x32_bf16 v[108:111], a[84:87], a[28:31], v[108:111]\n"
"s_add_u32 s80, s80, 0x1000\n"
"v_mfma_f32_16x16x32_bf16 v[112:115], a[80:83], a[32:35], v[112:115]\n"
"v_mfma_f32_16x16x32_bf16 v[112:115], a[84:87], a[36:39], v[112:115]\n"
"v_mfma_f32_16x16x32_bf16 v[116:119], a[80:83], a[40:43], v[116:119]\n"
"v_mfma_f32_16x16x32_bf16 v[116:119], a[84:87], a[44:47], v[116:119]\n"
"v_mfma_f32_16x16x32_bf16 v[120:123], a[80:83], a[48:51], v[120:123]\n"
"v_mfma_f32_16x16x32_bf16 v[120:123], a[84:87], a[52:55], v[120:123]\n"
"v_mfma_f32_16x16x32_bf16 v[124:127], a[80:83], a[56:59], v[124:127]\n"
"buffer_load_dwordx4 a[80:83], v240, s[36:39], s80 offen offset:0 sc1\n"
"v_mfma_f32_16x16x32_bf16 v[124:127], a[84:87], a[60:63], v[124:127]\n"
"s_waitcnt vmcnt(15)\n"
"v_mfma_f32_16x16x32_bf16 v[128:131], a[88:91], a[0:3], v[128:131]\n"
"v_mfma_f32_16x16x32_bf16 v[128:131], a[92:95], a[4:7], v[128:131]\n"
"v_mfma_f32_16x16x32_bf16 v[132:135], a[88:91], a[8:11], v[132:135]\n"
"v_mfma_f32_16x16x32_bf16 v[132:135], a[92:95], a[12:15], v[132:135]\n"
"v_mfma_f32_16x16x32_bf16 v[136:139], a[88:91], a[16:19], v[136:139]\n"
"v_mfma_f32_16x16x32_bf16 v[136:139], a[92:95], a[20:23], v[136:139]\n"
"v_mfma_f32_16x16x32_bf16 v[140:143], a[88:91], a[24:27], v[140:143]\n"
"buffer_load_dwordx4 a[84:87], v240, s[36:39], s80 offen offset:1024 sc1\n"
"v_mfma_f32_16x16x32_bf16 v[140:143], a[92:95], a[28:31], v[140:143]\n"
"v_mfma_f32_16x16x32_bf16 v[144:147], a[88:91], a[32:35], v[144:147]\n"
"v_mfma_f32_16x16x32_bf16 v[144:147], a[92:95], a[36:39], v[144:147]\n"
"v_mfma_f32_16x16x32_bf16 v[148:151], a[88:91], a[40:43], v[148:151]\n"
"v_mfma_f32_16x16x32_bf16 v[148:151], a[92:95], a[44:47], v[148:151]\n"
"v_mfma_f32_16x16x32_bf16 v[152:155], a[88:91], a[48:51], v[152:155]\n"
"v_mfma_f32_16x16x32_bf16 v[152:155], a[92:95], a[52:55], v[152:155]\n"
"v_mfma_f32_16x16x32_bf16 v[156:159], a[88:91], a[56:59], v[156:159]\n"
"buffer_load_dwordx4 a[88:91], v240, s[36:39], s80 offen offset:2048 sc1\n"
"v_mfma_f32_16x16x32_bf16 v[156:159], a[92:95], a[60:63], v[156:159]\n"
"s_waitcnt vmcnt(15)\n"
"v_mfma_f32_16x16x32_bf16 v[160:163], a[96:99], a[0:3], v[160:163]\n"
"v_mfma_f32_16x16x32_bf16 v[160:163], a[100:103], a[4:7], v[160:163]\n"
"v_mfma_f32_16x16x32_bf16 v[164:167], a[96:99], a[8:11], v[164:167]\n"
"v_mfma_f32_16x16x32_bf16 v[164:167], a[100:103], a[12:15], v[164:167]\n"
"v_mfma_f32_16x16x32_bf16 v[168:171], a[96:99], a[16:19], v[168:171]\n"
"v_mfma_f32_16x16x32_bf16 v[168:171], a[100:103], a[20:23], v[168:171]\n"
"v_mfma_f32_16x16x32_bf16 v[172:175], a[96:99], a[24:27], v[172:175]\n"
"buffer_load_dwordx4 a[92:95], v240, s[36:39], s80 offen offset:3072 sc1\n"
"v_mfma_f32_16x16x32_bf16 v[172:175], a[100:103], a[28:31], v[172:175]\n"
"s_add_u32 s80, s80, 0x1000\n"
"v_mfma_f32_16x16x32_bf16 v[176:179], a[96:99], a[32:35], v[176:179]\n"
"v_mfma_f32_16x16x32_bf16 v[176:179], a[100:103], a[36:39], v[176:179]\n"
"v_mfma_f32_16x16x32_bf16 v[180:183], a[96:99], a[40:43], v[180:183]\n"
"v_mfma_f32_16x16x32_bf16 v[180:183], a[100:103], a[44:47], v[180:183]\n"
"v_mfma_f32_16x16x32_bf16 v[184:187], a[96:99], a[48:51], v[184:187]\n"
"v_mfma_f32_16x16x32_bf16 v[184:187], a[100:103], a[52:55], v[184:187]\n"
"v_mfma_f32_16x16x32_bf16 v[188:191], a[96:99], a[56:59], v[188:191]\n"
"buffer_load_dwordx4 a[96:99], v240, s[36:39], s80 offen offset:0 sc1\n"
"v_mfma_f32_16x16x32_bf16 v[188:191], a[100:103], a[60:63], v[188:191]\n"
"s_waitcnt vmcnt(15)\n"
"v_mfma_f32_16x16x32_bf16 v[192:195], a[104:107], a[0:3], v[192:195]\n"
"v_mfma_f32_16x16x32_bf16 v[192:195], a[108:111], a[4:7], v[192:195]\n"
"v_mfma_f32_16x16x32_bf16 v[196:199], a[104:107], a[8:11], v[196:199]\n"
"v_mfma_f32_16x16x32_bf16 v[196:199], a[108:111], a[12:15], v[196:199]\n"
"v_mfma_f32_16x16x32_bf16 v[200:203], a[104:107], a[16:19], v[200:203]\n"
"v_mfma_f32_16x16x32_bf16 v[200:203], a[108:111], a[20:23], v[200:203]\n"
"v_mfma_f32_16x16x32_bf16 v[204:207], a[104:107], a[24:27], v[204:207]\n"
"buffer_load_dwordx4 a[100:103], v240, s[36:39], s80 offen offset:1024 sc1\n"
"v_mfma_f32_16x16x32_bf16 v[204:207], a[108:111], a[28:31], v[204:207]\n"
"v_mfma_f32_16x16x32_bf16 v[208:211], a[104:107], a[32:35], v[208:211]\n"
"v_mfma_f32_16x16x32_bf16 v[208:211], a[108:111], a[36:39], v[208:211]\n"
"v_mfma_f32_16x16x32_bf16 v[212:215], a[104:107], a[40:43], v[212:215]\n"
"v_mfma_f32_16x16x32_bf16 v[212:215], a[108:111], a[44:47], v[212:215]\n"
"v_mfma_f32_16x16x32_bf16 v[216:219], a[104:107], a[48:51], v[216:219]\n"
"v_mfma_f32_16x16x32_bf16 v[216:219], a[108:111], a[52:55], v[216:219]\n"
"s_waitcnt vmcnt(10)\n"
"s_barrier\n"
"v_mfma_f32_16x16x32_bf16 v[220:223], a[104:107], a[56:59], v[220:223]\n"
"buffer_load_dwordx4 a[104:107], v240, s[36:39], s80 offen offset:2048 sc1\n"
"v_mfma_f32_16x16x32_bf16 v[220:223], a[108:111], a[60:63], v[220:223]\n"
"v_mfma_f32_16x16x32_bf16 a[112:115], v[224:227], a[0:3], a[112:115]\n"
"ds_read_b128 a[0:3], v247 offset:0\n"
"v_mfma_f32_16x16x32_bf16 a[112:115], v[228:231], a[4:7], a[112:115]\n"
"ds_read_b128 a[4:7], v248 offset:0\n"
"v_mfma_f32_16x16x32_bf16 a[116:119], v[224:227], a[8:11], a[116:119]\n"
"ds_read_b128 a[8:11], v247 offset:2048\n"
"v_mfma_f32_16x16x32_bf16 a[116:119], v[228:231], a[12:15], a[116:119]\n"
"ds_read_b128 a[12:15], v248 offset:2048\n"
"v_mfma_f32_16x16x32_bf16 a[120:123], v[224:227], a[16:19], a[120:123]\n"
"ds_read_b128 a[16:19], v247 offset:4096\n"
"v_mfma_f32_16x16x32_bf16 a[120:123], v[228:231], a[20:23], a[120:123]\n"
"ds_read_b128 a[20:23], v248 offset:4096\n"
"v_mfma_f32_16x16x32_bf16 a[124:127], v[224:227], a[24:27], a[124:127]\n"
"ds_read_b128 a[24:27], v247 offset:6144\n"
"buffer_load_dwordx4 a[108:111], v240, s[36:39], s80 offen offset:3072 sc1\n"
"v_mfma_f32_16x16x32_bf16 a[124:127], v[228:231], a[28:31], a[124:127]\n"
"s_add_u32 s80, s80, 0x1000\n"
"ds_read_b128 a[28:31], v248 offset:6144\n"
"v_mfma_f32_16x16x32_bf16 a[128:131], v[224:227], a[32:35], a[128:131]\n"
"ds_read_b128 a[32:35], v247 offset:8192\n"
"v_mfma_f32_16x16x32_bf16 a[128:131], v[228:231], a[36:39], a[128:131]\n"
"ds_read_b128 a[36:39], v248 offset:8192\n"
"v_mfma_f32_16x16x32_bf16 a[132:135], v[224:227], a[40:43], a[132:135]\n"
"ds_read_b128 a[40:43], v247 offset:10240\n"
"v_mfma_f32_16x16x32_bf16 a[132:135], v[228:231], a[44:47], a[132:135]\n"
"ds_read_b128 a[44:47], v248 offset:10240\n"
"v_mfma_f32_16x16x32_bf16 a[136:139], v[224:227], a[48:51], a[136:139]\n"
"ds_read_b128 a[48:51], v247 offset:12288\n"
"v_mfma_f32_16x16x32_bf16 a[136:139], v[228:231], a[52:55], a[136:139]\n"
"ds_read_b128 a[52:55], v248 offset:12288\n"
"v_mfma_f32_16x16x32_bf16 a[140:143], v[224:227], a[56:59], a[140:143]\n"
"ds_read_b128 a[56:59], v247 offset:14336\n"
"buffer_load_dwordx4 v[224:227], v240, s[36:39], s80 offen offset:0 sc1\n"
"v_mfma_f32_16x16x32_bf16 a[140:143], v[228:231], a[60:63], a[140:143]\n"
"s_waitcnt vmcnt(11) lgkmcnt(11)\n"
"ds_read_b128 a[60:63], v248 offset:14336\n"
"v_mfma_f32_16x16x32_bf16 v[0:3], a[64:67], a[0:3], v[0:3]\n"
"v_mfma_f32_16x16x32_bf16 v[0:3], a[68:71], a[4:7], v[0:3]\n"
"v_mfma_f32_16x16x32_bf16 v[4:7], a[64:67], a[8:11], v[4:7]\n"
"v_mfma_f32_16x16x32_bf16 v[4:7], a[68:71], a[12:15], v[4:7]\n"
"s_waitcnt lgkmcnt(8)\n"
"v_mfma_f32_16x16x32_bf16 v[8:11], a[64:67], a[16:19], v[8:11]\n"
"v_mfma_f32_16x16x32_bf16 v[8:11], a[68:71], a[20:23], v[8:11]\n"
"v_mfma_f32_16x16x32_bf16 v[12:15], a[64:67], a[24:27], v[12:15]\n"
"buffer_load_dwordx4 v[228:231], v240, s[36:39], s80 offen offset:1024 sc1\n"
"v_mfma_f32_16x16x32_bf16 v[12:15], a[68:71], a[28:31], v[12:15]\n"
"s_waitcnt lgkmcnt(4)\n"
"v_mfma_f32_16x16x32_bf16 v[16:19], a[64:67], a[32:35], v[16:19]\n"
"v_mfma_f32_16x16x32_bf16 v[16:19], a[68:71], a[36:39], v[16:19]\n"
"s_waitcnt lgkmcnt(0)\n"
"s_barrier\n"
"v_mfma_f32_16x16x32_bf16 v[20:23], a[64:67], a[40:43], v[20:23]\n"
"s_add_u32 m0, s83, 128000\n"
"v_mfma_f32_16x16x32_bf16 v[20:23], a[68:71], a[44:47], v[20:23]\n"
"buffer_load_dwordx4 v240, s[56:59], s72 offen offset:3072 sc1 lds\n"
"v_mfma_f32_16x16x32_bf16 v[24:27], a[64:67], a[48:51], v[24:27]\n"
"v_mfma_f32_16x16x32_bf16 v[24:27], a[68:71], a[52:55], v[24:27]\n"
"v_mfma_f32_16x16x32_bf16 v[28:31], a[64:67], a[56:59], v[28:31]\n"
"buffer_load_dwordx4 a[64:67], v240, s[36:39], s80 offen offset:2048 sc1\n"
"v_mfma_f32_16x16x32_bf16 v[28:31], a[68:71], a[60:63], v[28:31]\n"
"s_waitcnt vmcnt(12)\n"
"v_mfma_f32_16x16x32_bf16 v[32:35], a[72:75], a[0:3], v[32:35]\n"
"s_add_u32 m0, s83, 130048\n"
"v_mfma_f32_16x16x32_bf16 v[32:35], a[76:79], a[4:7], v[32:35]\n"
"buffer_load_dwordx4 v240, s[56:59], s73 offen offset:3072 sc1 lds\n"
"v_mfma_f32_16x16x32_bf16 v[36:39], a[72:75], a[8:11], v[36:39]\n"
"v_mfma_f32_16x16x32_bf16 v[36:39], a[76:79], a[12:15], v[36:39]\n"
"v_mfma_f32_16x16x32_bf16 v[40:43], a[72:75], a[16:19], v[40:43]\n"
"v_mfma_f32_16x16x32_bf16 v[40:43], a[76:79], a[20:23], v[40:43]\n"
"v_mfma_f32_16x16x32_bf16 v[44:47], a[72:75], a[24:27], v[44:47]\n"
"s_add_u32 m0, s83, 132096\n"
"buffer_load_dwordx4 a[68:71], v240, s[36:39], s80 offen offset:3072 sc1\n"
"v_mfma_f32_16x16x32_bf16 v[44:47], a[76:79], a[28:31], v[44:47]\n"
"s_add_u32 s80, s80, 0x1000\n"
"buffer_load_dwordx4 v240, s[56:59], s74 offen offset:3072 sc1 lds\n"
"v_mfma_f32_16x16x32_bf16 v[48:51], a[72:75], a[32:35], v[48:51]\n"
"v_mfma_f32_16x16x32_bf16 v[48:51], a[76:79], a[36:39], v[48:51]\n"
"v_mfma_f32_16x16x32_bf16 v[52:55], a[72:75], a[40:43], v[52:55]\n"
"v_mfma_f32_16x16x32_bf16 v[52:55], a[76:79], a[44:47], v[52:55]\n"
"v_mfma_f32_16x16x32_bf16 v[56:59], a[72:75], a[48:51], v[56:59]\n"
"s_add_u32 m0, s83, 134144\n"
"v_mfma_f32_16x16x32_bf16 v[56:59], a[76:79], a[52:55], v[56:59]\n"
"buffer_load_dwordx4 v240, s[56:59], s75 offen offset:3072 sc1 lds\n"
"v_mfma_f32_16x16x32_bf16 v[60:63], a[72:75], a[56:59], v[60:63]\n"
"buffer_load_dwordx4 a[72:75], v240, s[36:39], s80 offen offset:0 sc1\n"
"v_mfma_f32_16x16x32_bf16 v[60:63], a[76:79], a[60:63], v[60:63]\n"
"s_waitcnt vmcnt(15)\n"
"v_mfma_f32_16x16x32_bf16 v[64:67], a[80:83], a[0:3], v[64:67]\n"
"v_mfma_f32_16x16x32_bf16 v[64:67], a[84:87], a[4:7], v[64:67]\n"
"v_mfma_f32_16x16x32_bf16 v[68:71], a[80:83], a[8:11], v[68:71]\n"
"v_mfma_f32_16x16x32_bf16 v[68:71], a[84:87], a[12:15], v[68:71]\n"
"v_mfma_f32_16x16x32_bf16 v[72:75], a[80:83], a[16:19], v[72:75]\n"
"v_mfma_f32_16x16x32_bf16 v[72:75], a[84:87], a[20:23], v[72:75]\n"
"v_mfma_f32_16x16x32_bf16 v[76:79], a[80:83], a[24:27], v[76:79]\n"
"buffer_load_dwordx4 a[76:79], v240, s[36:39], s80 offen offset:1024 sc1\n"
"v_mfma_f32_16x16x32_bf16 v[76:79], a[84:87], a[28:31], v[76:79]\n"
"v_mfma_f32_16x16x32_bf16 v[80:83], a[80:83], a[32:35], v[80:83]\n"
"v_mfma_f32_16x16x32_bf16 v[80:83], a[84:87], a[36:39], v[80:83]\n"
"v_mfma_f32_16x16x32_bf16 v[84:87], a[80:83], a[40:43], v[84:87]\n"
"v_mfma_f32_16x16x32_bf16 v[84:87], a[84:87], a[44:47], v[84:87]\n"
"v_mfma_f32_16x16x32_bf16 v[88:91], a[80:83], a[48:51], v[88:91]\n"
"v_mfma_f32_16x16x32_bf16 v[88:91], a[84:87], a[52:55], v[88:91]\n"
"v_mfma_f32_16x16x32_bf16 v[92:95], a[80:83], a[56:59], v[92:95]\n"
"buffer_load_dwordx4 a[80:83], v240, s[36:39], s80 offen offset:2048 sc1\n"
"v_mfma_f32_16x16x32_bf16 v[92:95], a[84:87], a[60:63], v[92:95]\n"
"s_waitcnt vmcnt(15)\n"
"v_mfma_f32_16x16x32_bf16 v[96:99], a[88:91], a[0:3], v[96:99]\n"
"v_mfma_f32_16x16x32_bf16 v[96:99], a[92:95], a[4:7], v[96:99]\n"
"v_mfma_f32_16x16x32_bf16 v[100:103], a[88:91], a[8:11], v[100:103]\n"
"v_mfma_f32_16x16x32_bf16 v[100:103], a[92:95], a[12:15], v[100:103]\n"
"v_mfma_f32_16x16x32_bf16 v[104:107], a[88:91], a[16:19], v[104:107]\n"
"v_mfma_f32_16x16x32_bf16 v[104:107], a[92:95], a[20:23], v[104:107]\n"
"v_mfma_f32_16x16x32_bf16 v[108:111], a[88:91], a[24:27], v[108:111]\n"
"buffer_load_dwordx4 a[84:87], v240, s[36:39], s80 offen offset:3072 sc1\n"
"v_mfma_f32_16x16x32_bf16 v[108:111], a[92:95], a[28:31], v[108:111]\n"
"s_add_u32 s80, s80, 0x1000\n"
"v_mfma_f32_16x16x32_bf16 v[112:115], a[88:91], a[32:35], v[112:115]\n"
"v_mfma_f32_16x16x32_bf16 v[112:115], a[92:95], a[36:39], v[112:115]\n"
"v_mfma_f32_16x16x32_bf16 v[116:119], a[88:91], a[40:43], v[116:119]\n"
"v_mfma_f32_16x16x32_bf16 v[116:119], a[92:95], a[44:47], v[116:119]\n"
"v_mfma_f32_16x16x32_bf16 v[120:123], a[88:91], a[48:51], v[120:123]\n"
"v_mfma_f32_16x16x32_bf16 v[120:123], a[92:95], a[52:55], v[120:123]\n"
"v_mfma_f32_16x16x32_bf16 v[124:127], a[88:91], a[56:59], v[124:127]\n"
"buffer_load_dwordx4 a[88:91], v240, s[36:39], s80 offen offset:0 sc1\n"
"v_mfma_f32_16x16x32_bf16 v[124:127], a[92:95], a[60:63], v[124:127]\n"
"s_waitcnt vmcnt(15)\n"
"v_mfma_f32_16x16x32_bf16 v[128:131], a[96:99], a[0:3], v[128:131]\n"
"v_mfma_f32_16x16x32_bf16 v[128:131], a[100:103], a[4:7], v[128:131]\n"
"v_mfma_f32_16x16x32_bf16 v[132:135], a[96:99], a[8:11], v[132:135]\n"
"v_mfma_f32_16x16x32_bf16 v[132:135], a[100:103], a[12:15], v[132:135]\n"
"v_mfma_f32_16x16x32_bf16 v[136:139], a[96:99], a[16:19], v[136:139]\n"
"v_mfma_f32_16x16x32_bf16 v[136:139], a[100:103], a[20:23], v[136:139]\n"
"v_mfma_f32_16x16x32_bf16 v[140:143], a[96:99], a[24:27], v[140:143]\n"
"buffer_load_dwordx4 a[92:95], v240, s[36:39], s80 offen offset:1024 sc1\n"
"v_mfma_f32_16x16x32_bf16 v[140:143], a[100:103], a[28:31], v[140:143]\n"
"v_mfma_f32_16x16x32_bf16 v[144:147], a[96:99], a[32:35], v[144:147]\n"
"v_mfma_f32_16x16x32_bf16 v[144:147], a[100:103], a[36:39], v[144:147]\n"
"v_mfma_f32_16x16x32_bf16 v[148:151], a[96:99], a[40:43], v[148:151]\n"
"v_mfma_f32_16x16x32_bf16 v[148:151], a[100:103], a[44:47], v[148:151]\n"
"v_mfma_f32_16x16x32_bf16 v[152:155], a[96:99], a[48:51], v[152:155]\n"
"v_mfma_f32_16x16x32_bf16 v[152:155], a[100:103], a[52:55], v[152:155]\n"
"v_mfma_f32_16x16x32_bf16 v[156:159], a[96:99], a[56:59], v[156:159]\n"
"buffer_load_dwordx4 a[96:99], v240, s[36:39], s80 offen offset:2048 sc1\n"
"v_mfma_f32_16x16x32_bf16 v[156:159], a[100:103], a[60:63], v[156:159]\n"
"s_waitcnt vmcnt(15)\n"
"v_mfma_f32_16x16x32_bf16 v[160:163], a[104:107], a[0:3], v[160:163]\n"
"v_mfma_f32_16x16x32_bf16 v[160:163], a[108:111], a[4:7], v[160:163]\n"
"v_mfma_f32_16x16x32_bf16 v[164:167], a[104:107], a[8:11], v[164:167]\n"
"v_mfma_f32_16x16x32_bf16 v[164:167], a[108:111], a[12:15], v[164:167]\n"
"v_mfma_f32_16x16x32_bf16 v[168:171], a[104:107], a[16:19], v[168:171]\n"
"v_mfma_f32_16x16x32_bf16 v[168:171], a[108:111], a[20:23], v[168:171]\n"
"v_mfma_f32_16x16x32_bf16 v[172:175], a[104:107], a[24:27], v[172:175]\n"
"buffer_load_dwordx4 a[100:103], v240, s[36:39], s80 offen offset:3072 sc1\n"
"v_mfma_f32_16x16x32_bf16 v[172:175], a[108:111], a[28:31], v[172:175]\n"
"s_add_u32 s80, s80, 0x1000\n"
"v_mfma_f32_16x16x32_bf16 v[176:179], a[104:107], a[32:35], v[176:179]\n"
"v_mfma_f32_16x16x32_bf16 v[176:179], a[108:111], a[36:39], v[176:179]\n"
"v_mfma_f32_16x16x32_bf16 v[180:183], a[104:107], a[40:43], v[180:183]\n"
"v_mfma_f32_16x16x32_bf16 v[180:183], a[108:111], a[44:47], v[180:183]\n"
"v_mfma_f32_16x16x32_bf16 v[184:187], a[104:107], a[48:51], v[184:187]\n"
"v_mfma_f32_16x16x32_bf16 v[184:187], a[108:111], a[52:55], v[184:187]\n"
"v_mfma_f32_16x16x32_bf16 v[188:191], a[104:107], a[56:59], v[188:191]\n"
"buffer_load_dwordx4 a[104:107], v240, s[36:39], s80 offen offset:0 sc1\n"
"v_mfma_f32_16x16x32_bf16 v[188:191], a[108:111], a[60:63], v[188:191]\n"
"s_waitcnt vmcnt(15)\n"
"v_mfma_f32_16x16x32_bf16 v[192:195], v[224:227], a[0:3], v[192:195]\n"
"v_mfma_f32_16x16x32_bf16 v[192:195], v[228:231], a[4:7], v[192:195]\n"
"v_mfma_f32_16x16x32_bf16 v[196:199], v[224:227], a[8:11], v[196:199]\n"
"v_mfma_f32_16x16x32_bf16 v[196:199], v[228:231], a[12:15], v[196:199]\n"
"v_mfma_f32_16x16x32_bf16 v[200:203], v[224:227], a[16:19], v[200:203]\n"
"v_mfma_f32_16x16x32_bf16 v[200:203], v[228:231], a[20:23], v[200:203]\n"
"v_mfma_f32_16x16x32_bf16 v[204:207], v[224:227], a[24:27], v[204:207]\n"
"buffer_load_dwordx4 a[108:111], v240, s[36:39], s80 offen offset:1024 sc1\n"
"v_mfma_f32_16x16x32_bf16 v[204:207], v[228:231], a[28:31], v[204:207]\n"
"v_mfma_f32_16x16x32_bf16 v[208:211], v[224:227], a[32:35], v[208:211]\n"
"v_mfma_f32_16x16x32_bf16 v[208:211], v[228:231], a[36:39], v[208:211]\n"
"v_mfma_f32_16x16x32_bf16 v[212:215], v[224:227], a[40:43], v[212:215]\n"
"v_mfma_f32_16x16x32_bf16 v[212:215], v[228:231], a[44:47], v[212:215]\n"
"v_mfma_f32_16x16x32_bf16 v[216:219], v[224:227], a[48:51], v[216:219]\n"
"v_mfma_f32_16x16x32_bf16 v[216:219], v[228:231], a[52:55], v[216:219]\n"
"s_waitcnt vmcnt(10)\n"
"s_barrier\n"
"v_mfma_f32_16x16x32_bf16 v[220:223], v[224:227], a[56:59], v[220:223]\n"
"buffer_load_dwordx4 v[224:227], v240, s[36:39], s80 offen offset:2048 sc1\n"
"v_mfma_f32_16x16x32_bf16 v[220:223], v[228:231], a[60:63], v[220:223]\n"
"v_mfma_f32_16x16x32_bf16 a[112:115], a[64:67], a[0:3], a[112:115]\n"
"ds_read_b128 a[0:3], v247 offset:0\n"
"v_mfma_f32_16x16x32_bf16 a[112:115], a[68:71], a[4:7], a[112:115]\n"
"ds_read_b128 a[4:7], v248 offset:0\n"
"v_mfma_f32_16x16x32_bf16 a[116:119], a[64:67], a[8:11], a[116:119]\n"
"ds_read_b128 a[8:11], v247 offset:2048\n"
"v_mfma_f32_16x16x32_bf16 a[116:119], a[68:71], a[12:15], a[116:119]\n"
"ds_read_b128 a[12:15], v248 offset:2048\n"
"v_mfma_f32_16x16x32_bf16 a[120:123], a[64:67], a[16:19], a[120:123]\n"
"ds_read_b128 a[16:19], v247 offset:4096\n"
"v_mfma_f32_16x16x32_bf16 a[120:123], a[68:71], a[20:23], a[120:123]\n"
"ds_read_b128 a[20:23], v248 offset:4096\n"
"v_mfma_f32_16x16x32_bf16 a[124:127], a[64:67], a[24:27], a[124:127]\n"
"ds_read_b128 a[24:27], v247 offset:6144\n"
"buffer_load_dwordx4 v[228:231], v240, s[36:39], s80 offen offset:3072 sc1\n"
"v_mfma_f32_16x16x32_bf16 a[124:127], a[68:71], a[28:31], a[124:127]\n"
"s_add_u32 s80, s80, 0x1000\n"
"ds_read_b128 a[28:31], v248 offset:6144\n"
"v_mfma_f32_16x16x32_bf16 a[128:131], a[64:67], a[32:35], a[128:131]\n"
"ds_read_b128 a[32:35], v247 offset:8192\n"
"v_mfma_f32_16x16x32_bf16 a[128:131], a[68:71], a[36:39], a[128:131]\n"
"ds_read_b128 a[36:39], v248 offset:8192\n"
"v_mfma_f32_16x16x32_bf16 a[132:135], a[64:67], a[40:43], a[132:135]\n"
"ds_read_b128 a[40:43], v247 offset:10240\n"
"v_mfma_f32_16x16x32_bf16 a[132:135], a[68:71], a[44:47], a[132:135]\n"
"ds_read_b128 a[44:47], v248 offset:10240\n"
"v_mfma_f32_16x16x32_bf16 a[136:139], a[64:67], a[48:51], a[136:139]\n"
"ds_read_b128 a[48:51], v247 offset:12288\n"
"v_mfma_f32_16x16x32_bf16 a[136:139], a[68:71], a[52:55], a[136:139]\n"
"ds_read_b128 a[52:55], v248 offset:12288\n"
"v_mfma_f32_16x16x32_bf16 a[140:143], a[64:67], a[56:59], a[140:143]\n"
"ds_read_b128 a[56:59], v247 offset:14336\n"
"buffer_load_dwordx4 a[64:67], v240, s[36:39], s80 offen offset:0 sc1\n"
"v_mfma_f32_16x16x32_bf16 a[140:143], a[68:71], a[60:63], a[140:143]\n"
"s_waitcnt vmcnt(11) lgkmcnt(11)\n"
"ds_read_b128 a[60:63], v248 offset:14336\n"
"v_mfma_f32_16x16x32_bf16 v[0:3], a[72:75], a[0:3], v[0:3]\n"
"v_mfma_f32_16x16x32_bf16 v[0:3], a[76:79], a[4:7], v[0:3]\n"
"v_mfma_f32_16x16x32_bf16 v[4:7], a[72:75], a[8:11], v[4:7]\n"
"v_mfma_f32_16x16x32_bf16 v[4:7], a[76:79], a[12:15], v[4:7]\n"
"s_waitcnt lgkmcnt(8)\n"
"v_mfma_f32_16x16x32_bf16 v[8:11], a[72:75], a[16:19], v[8:11]\n"
"v_mfma_f32_16x16x32_bf16 v[8:11], a[76:79], a[20:23], v[8:11]\n"
"v_mfma_f32_16x16x32_bf16 v[12:15], a[72:75], a[24:27], v[12:15]\n"
"buffer_load_dwordx4 a[68:71], v240, s[36:39], s80 offen offset:1024 sc1\n"
"v_mfma_f32_16x16x32_bf16 v[12:15], a[76:79], a[28:31], v[12:15]\n"
"s_waitcnt lgkmcnt(4)\n"
"v_mfma_f32_16x16x32_bf16 v[16:19], a[72:75], a[32:35], v[16:19]\n"
"v_mfma_f32_16x16x32_bf16 v[16:19], a[76:79], a[36:39], v[16:19]\n"
"v_mfma_f32_16x16x32_bf16 v[20:23], a[72:75], a[40:43], v[20:23]\n"
"v_mfma_f32_16x16x32_bf16 v[20:23], a[76:79], a[44:47], v[20:23]\n"
"s_waitcnt lgkmcnt(0)\n"
"v_mfma_f32_16x16x32_bf16 v[24:27], a[72:75], a[48:51], v[24:27]\n"
"v_mfma_f32_16x16x32_bf16 v[24:27], a[76:79], a[52:55], v[24:27]\n"
"v_mfma_f32_16x16x32_bf16 v[28:31], a[72:75], a[56:59], v[28:31]\n"
"buffer_load_dwordx4 a[72:75], v240, s[36:39], s80 offen offset:2048 sc1\n"
"v_mfma_f32_16x16x32_bf16 v[28:31], a[76:79], a[60:63], v[28:31]\n"
"s_waitcnt vmcnt(11)\n"
"v_mfma_f32_16x16x32_bf16 v[32:35], a[80:83], a[0:3], v[32:35]\n"
"v_mfma_f32_16x16x32_bf16 v[32:35], a[84:87], a[4:7], v[32:35]\n"
"v_mfma_f32_16x16x32_bf16 v[36:39], a[80:83], a[8:11], v[36:39]\n"
"v_mfma_f32_16x16x32_bf16 v[36:39], a[84:87], a[12:15], v[36:39]\n"
"v_mfma_f32_16x16x32_bf16 v[40:43], a[80:83], a[16:19], v[40:43]\n"
"v_mfma_f32_16x16x32_bf16 v[40:43], a[84:87], a[20:23], v[40:43]\n"
"v_mfma_f32_16x16x32_bf16 v[44:47], a[80:83], a[24:27], v[44:47]\n"
"buffer_load_dwordx4 a[76:79], v240, s[36:39], s80 offen offset:3072 sc1\n"
"v_mfma_f32_16x16x32_bf16 v[44:47], a[84:87], a[28:31], v[44:47]\n"
"s_add_u32 s80, s80, 0x1000\n"
"v_mfma_f32_16x16x32_bf16 v[48:51], a[80:83], a[32:35], v[48:51]\n"
"ds_read_b128 v[232:235], v249 offset:14336\n"
"v_mfma_f32_16x16x32_bf16 v[48:51], a[84:87], a[36:39], v[48:51]\n"
"v_mfma_f32_16x16x32_bf16 v[52:55], a[80:83], a[40:43], v[52:55]\n"
"v_mfma_f32_16x16x32_bf16 v[52:55], a[84:87], a[44:47], v[52:55]\n"
"v_mfma_f32_16x16x32_bf16 v[56:59], a[80:83], a[48:51], v[56:59]\n"
"v_mfma_f32_16x16x32_bf16 v[56:59], a[84:87], a[52:55], v[56:59]\n"
"v_mfma_f32_16x16x32_bf16 v[60:63], a[80:83], a[56:59], v[60:63]\n"
"buffer_load_dwordx4 a[80:83], v240, s[36:39], s80 offen offset:0 sc1\n"
"v_mfma_f32_16x16x32_bf16 v[60:63], a[84:87], a[60:63], v[60:63]\n"
"s_waitcnt vmcnt(11)\n"
"v_mfma_f32_16x16x32_bf16 v[64:67], a[88:91], a[0:3], v[64:67]\n"
"v_mfma_f32_16x16x32_bf16 v[64:67], a[92:95], a[4:7], v[64:67]\n"
".Lpad19_%=:\n"
"s_cbranch_execz .Lret2_%=\n"
"v_mfma_f32_16x16x32_bf16 v[68:71], a[88:91], a[8:11], v[68:71]\n"
"v_mfma_f32_16x16x32_bf16 v[68:71], a[92:95], a[12:15], v[68:71]\n"
"v_mfma_f32_16x16x32_bf16 v[72:75], a[88:91], a[16:19], v[72:75]\n"
"s_waitcnt lgkmcnt(0)\n"
"ds_read_b128 v[236:239], v249 offset:0\n"
"v_accvgpr_read_b32 v253, a144\n"
"v_mfma_f32_16x16x32_bf16 v[72:75], a[92:95], a[20:23], v[72:75]\n"
"v_lshlrev_b32 v251, 16, v253\n"
"v_lshlrev_b32 v252, 16, v232\n"
"v_mfma_f32_16x16x32_bf16 v[76:79], a[88:91], a[24:27], v[76:79]\n"
"buffer_load_dwordx4 a[84:87], v240, s[36:39], s80 offen offset:1024 sc1\n"
"v_fmac_f32 v252, v251, v28\n"
"v_mfma_f32_16x16x32_bf16 v[76:79], a[92:95], a[28:31], v[76:79]\n"
"v_and_b32 v253, s82, v253\n"
"v_and_b32 v232, s82, v232\n"
"v_mfma_f32_16x16x32_bf16 v[80:83], a[88:91], a[32:35], v[80:83]\n"
"v_fmac_f32 v232, v253, v29\n"
"v_cvt_pk_bf16_f32 v28, v252, v232\n"
"v_mfma_f32_16x16x32_bf16 v[80:83], a[92:95], a[36:39], v[80:83]\n"
"v_accvgpr_read_b32 v253, a145\n"
"v_lshlrev_b32 v251, 16, v253\n"
"v_mfma_f32_16x16x32_bf16 v[84:87], a[88:91], a[40:43], v[84:87]\n"
"v_lshlrev_b32 v252, 16, v233\n"
"v_fmac_f32 v252, v251, v30\n"
"v_mfma_f32_16x16x32_bf16 v[84:87], a[92:95], a[44:47], v[84:87]\n"
"v_and_b32 v253, s82, v253\n"
"v_and_b32 v233, s82, v233\n"
"v_mfma_f32_16x16x32_bf16 v[88:91], a[88:91], a[48:51], v[88:91]\n"
"v_fmac_f32 v233, v253, v31\n"
"v_cvt_pk_bf16_f32 v29, v252, v233\n"
"v_mfma_f32_16x16x32_bf16 v[88:91], a[92:95], a[52:55], v[88:91]\n"
"v_accvgpr_read_b32 v253, a146\n"
"v_lshlrev_b32 v251, 16, v253\n"
"v_mfma_f32_16x16x32_bf16 v[92:95], a[88:91], a[56:59], v[92:95]\n"
"buffer_load_dwordx4 a[88:91], v240, s[36:39], s80 offen offset:2048 sc1\n"
"v_lshlrev_b32 v252, 16, v234\n"
"v_mfma_f32_16x16x32_bf16 v[92:95], a[92:95], a[60:63], v[92:95]\n"
"s_waitcnt vmcnt(11)\n"
"v_fmac_f32 v252, v251, v60\n"
"v_and_b32 v253, s82, v253\n"
"v_mfma_f32_16x16x32_bf16 v[96:99], a[96:99], a[0:3], v[96:99]\n"
"v_and_b32 v234, s82, v234\n"
"v_fmac_f32 v234, v253, v61\n"
"v_mfma_f32_16x16x32_bf16 v[96:99], a[100:103], a[4:7], v[96:99]\n"
"v_cvt_pk_bf16_f32 v30, v252, v234\n"
"v_accvgpr_read_b32 v253, a147\n"
"v_mfma_f32_16x16x32_bf16 v[100:103], a[96:99], a[8:11], v[100:103]\n"
"v_lshlrev_b32 v251, 16, v253\n"
"v_lshlrev_b32 v252, 16, v235\n"
"v_mfma_f32_16x16x32_bf16 v[100:103], a[100:103], a[12:15], v[100:103]\n"
"v_fmac_f32 v252, v251, v62\n"
"v_and_b32 v253, s82, v253\n"
"v_mfma_f32_16x16x32_bf16 v[104:107], a[96:99], a[16:19], v[104:107]\n"
"v_and_b32 v235, s82, v235\n"
"v_fmac_f32 v235, v253, v63\n"
"v_mfma_f32_16x16x32_bf16 v[104:107], a[100:103], a[20:23], v[104:107]\n"
"v_cvt_pk_bf16_f32 v31, v252, v235\n"
"v_mfma_f32_16x16x32_bf16 v[108:111], a[96:99], a[24:27], v[108:111]\n"
"buffer_load_dwordx4 a[92:95], v240, s[36:39], s80 offen offset:3072 sc1\n"
"v_mfma_f32_16x16x32_bf16 v[108:111], a[100:103], a[28:31], v[108:111]\n"
"s_add_u32 s80, s80, 0x1000\n"
"buffer_store_dwordx4 v[28:31], v242, s[48:51], s75 offen offset:0 sc1\n"
"v_mfma_f32_16x16x32_bf16 v[112:115], a[96:99], a[32:35], v[112:115]\n"
"s_waitcnt lgkmcnt(0)\n"
"ds_read_b128 v[232:235], v249 offset:2048\n"
"v_accvgpr_read_b32 v253, a148\n"
"v_mfma_f32_16x16x32_bf16 v[112:115], a[100:103], a[36:39], v[112:115]\n"
"v_lshlrev_b32 v251, 16, v253\n"
"v_lshlrev_b32 v252, 16, v236\n"
"v_mfma_f32_16x16x32_bf16 v[116:119], a[96:99], a[40:43], v[116:119]\n"
"v_fmac_f32 v252, v251, v0\n"
"v_and_b32 v253, s82, v253\n"
"v_mfma_f32_16x16x32_bf16 v[116:119], a[100:103], a[44:47], v[116:119]\n"
"v_and_b32 v236, s82, v236\n"
"v_fmac_f32 v236, v253, v1\n"
"v_mfma_f32_16x16x32_bf16 v[120:123], a[96:99], a[48:51], v[120:123]\n"
"v_cvt_pk_bf16_f32 v0, v252, v236\n"
"v_accvgpr_read_b32 v253, a149\n"
"v_mfma_f32_16x16x32_bf16 v[120:123], a[100:103], a[52:55], v[120:123]\n"
"v_lshlrev_b32 v251, 16, v253\n"
"v_lshlrev_b32 v252, 16, v237\n"
"v_mfma_f32_16x16x32_bf16 v[124:127], a[96:99], a[56:59], v[124:127]\n"
"buffer_load_dwordx4 a[96:99], v240, s[36:39], s80 offen offset:0 sc1\n"
"v_fmac_f32 v252, v251, v2\n"
"v_mfma_f32_16x16x32_bf16 v[124:127], a[100:103], a[60:63], v[124:127]\n"
"s_waitcnt vmcnt(12)\n"
"v_and_b32 v253, s82, v253\n"
"v_and_b32 v237, s82, v237\n"
"v_mfma_f32_16x16x32_bf16 v[128:131], a[104:107], a[0:3], v[128:131]\n"
"v_fmac_f32 v237, v253, v3\n"
"v_cvt_pk_bf16_f32 v1, v252, v237\n"
"v_mfma_f32_16x16x32_bf16 v[128:131], a[108:111], a[4:7], v[128:131]\n"
"v_accvgpr_read_b32 v253, a150\n"
"v_lshlrev_b32 v251, 16, v253\n"
"v_mfma_f32_16x16x32_bf16 v[132:135], a[104:107], a[8:11], v[132:135]\n"
"v_lshlrev_b32 v252, 16, v238\n"
"v_fmac_f32 v252, v251, v32\n"
"v_mfma_f32_16x16x32_bf16 v[132:135], a[108:111], a[12:15], v[132:135]\n"
"v_and_b32 v253, s82, v253\n"
"v_and_b32 v238, s82, v238\n"
"v_mfma_f32_16x16x32_bf16 v[136:139], a[104:107], a[16:19], v[136:139]\n"
"v_fmac_f32 v238, v253, v33\n"
"v_cvt_pk_bf16_f32 v2, v252, v238\n"
"v_mfma_f32_16x16x32_bf16 v[136:139], a[108:111], a[20:23], v[136:139]\n"
"v_accvgpr_read_b32 v253, a151\n"
"v_lshlrev_b32 v251, 16, v253\n"
"v_mfma_f32_16x16x32_bf16 v[140:143], a[104:107], a[24:27], v[140:143]\n"
"buffer_load_dwordx4 a[100:103], v240, s[36:39], s80 offen offset:1024 sc1\n"
"v_lshlrev_b32 v252, 16, v239\n"
"v_mfma_f32_16x16x32_bf16 v[140:143], a[108:111], a[28:31], v[140:143]\n"
"v_fmac_f32 v252, v251, v34\n"
"v_and_b32 v253, s82, v253\n"
"v_mfma_f32_16x16x32_bf16 v[144:147], a[104:107], a[32:35], v[144:147]\n"
"v_and_b32 v239, s82, v239\n"
"v_fmac_f32 v239, v253, v35\n"
"v_mfma_f32_16x16x32_bf16 v[144:147], a[108:111], a[36:39], v[144:147]\n"
"v_cvt_pk_bf16_f32 v3, v252, v239\n"
"v_mfma_f32_16x16x32_bf16 v[148:151], a[104:107], a[40:43], v[148:151]\n"
"buffer_store_dwordx4 v[0:3], v242, s[48:51], s68 offen offset:0 sc1\n"
"v_mfma_f32_16x16x32_bf16 v[148:151], a[108:111], a[44:47], v[148:151]\n"
"v_mfma_f32_16x16x32_bf16 v[152:155], a[104:107], a[48:51], v[152:155]\n"
"s_waitcnt lgkmcnt(0)\n"
"ds_read_b128 v[236:239], v249 offset:4096\n"
"v_accvgpr_read_b32 v253, a152\n"
"v_mfma_f32_16x16x32_bf16 v[152:155], a[108:111], a[52:55], v[152:155]\n"
"v_lshlrev_b32 v251, 16, v253\n"
"v_lshlrev_b32 v252, 16, v232\n"
"v_mfma_f32_16x16x32_bf16 v[156:159], a[104:107], a[56:59], v[156:159]\n"
"buffer_load_dwordx4 a[104:107], v240, s[36:39], s80 offen offset:2048 sc1\n"
"v_fmac_f32 v252, v251, v4\n"
"v_mfma_f32_16x16x32_bf16 v[156:159], a[108:111], a[60:63], v[156:159]\n"
"s_waitcnt vmcnt(13)\n"
"v_and_b32 v253, s82, v253\n"
"v_and_b32 v232, s82, v232\n"
"v_mfma_f32_16x16x32_bf16 v[160:163], v[224:227], a[0:3], v[160:163]\n"
"v_fmac_f32 v232, v253, v5\n"
"v_cvt_pk_bf16_f32 v4, v252, v232\n"
"v_mfma_f32_16x16x32_bf16 v[160:163], v[228:231], a[4:7], v[160:163]\n"
"v_accvgpr_read_b32 v253, a153\n"
"v_lshlrev_b32 v251, 16, v253\n"
"v_mfma_f32_16x16x32_bf16 v[164:167], v[224:227], a[8:11], v[164:167]\n"
"v_lshlrev_b32 v252, 16, v233\n"
"v_fmac_f32 v252, v251, v6\n"
"v_mfma_f32_16x16x32_bf16 v[164:167], v[228:231], a[12:15], v[164:167]\n"
"v_and_b32 v253, s82, v253\n"
"v_and_b32 v233, s82, v233\n"
"v_mfma_f32_16x16x32_bf16 v[168:171], v[224:227], a[16:19], v[168:171]\n"
"v_fmac_f32 v233, v253, v7\n"
"v_cvt_pk_bf16_f32 v5, v252, v233\n"
"v_mfma_f32_16x16x32_bf16 v[168:171], v[228:231], a[20:23], v[168:171]\n"
"v_accvgpr_read_b32 v253, a154\n"
"v_lshlrev_b32 v251, 16, v253\n"
"v_mfma_f32_16x16x32_bf16 v[172:175], v[224:227], a[24:27], v[172:175]\n"
"buffer_load_dwordx4 a[108:111], v240, s[36:39], s80 offen offset:3072 sc1\n"
"v_lshlrev_b32 v252, 16, v234\n"
"v_mfma_f32_16x16x32_bf16 v[172:175], v[228:231], a[28:31], v[172:175]\n"
"s_add_u32 s80, s80, 0x1000\n"
"v_fmac_f32 v252, v251, v36\n"
"v_and_b32 v253, s82, v253\n"
"v_mfma_f32_16x16x32_bf16 v[176:179], v[224:227], a[32:35], v[176:179]\n"
"v_and_b32 v234, s82, v234\n"
"v_fmac_f32 v234, v253, v37\n"
"v_mfma_f32_16x16x32_bf16 v[176:179], v[228:231], a[36:39], v[176:179]\n"
"v_cvt_pk_bf16_f32 v6, v252, v234\n"
"v_accvgpr_read_b32 v253, a155\n"
"v_mfma_f32_16x16x32_bf16 v[180:183], v[224:227], a[40:43], v[180:183]\n"
"v_lshlrev_b32 v251, 16, v253\n"
"v_lshlrev_b32 v252, 16, v235\n"
"v_mfma_f32_16x16x32_bf16 v[180:183], v[228:231], a[44:47], v[180:183]\n"
"v_fmac_f32 v252, v251, v38\n"
"v_and_b32 v253, s82, v253\n"
"v_mfma_f32_16x16x32_bf16 v[184:187], v[224:227], a[48:51], v[184:187]\n"
"v_and_b32 v235, s82, v235\n"
"v_fmac_f32 v235, v253, v39\n"
"v_mfma_f32_16x16x32_bf16 v[184:187], v[228:231], a[52:55], v[184:187]\n"
"v_cvt_pk_bf16_f32 v7, v252, v235\n"
"v_mfma_f32_16x16x32_bf16 v[188:191], v[224:227], a[56:59], v[188:191]\n"
"buffer_load_dwordx4 v[224:227], v240, s[36:39], s80 offen offset:0 sc1\n"
"v_mfma_f32_16x16x32_bf16 v[188:191], v[228:231], a[60:63], v[188:191]\n"
"s_waitcnt vmcnt(13)\n"
"buffer_store_dwordx4 v[4:7], v242, s[48:51], s69 offen offset:0 sc1\n"
"v_mfma_f32_16x16x32_bf16 v[192:195], a[64:67], a[0:3], v[192:195]\n"
"s_waitcnt lgkmcnt(0)\n"
"ds_read_b128 v[232:235], v249 offset:6144\n"
"v_accvgpr_read_b32 v253, a156\n"
"v_mfma_f32_16x16x32_bf16 v[192:195], a[68:71], a[4:7], v[192:195]\n"
"v_lshlrev_b32 v251, 16, v253\n"
"v_lshlrev_b32 v252, 16, v236\n"
"v_mfma_f32_16x16x32_bf16 v[196:199], a[64:67], a[8:11], v[196:199]\n"
"v_fmac_f32 v252, v251, v8\n"
"v_and_b32 v253, s82, v253\n"
"v_mfma_f32_16x16x32_bf16 v[196:199], a[68:71], a[12:15], v[196:199]\n"
"v_and_b32 v236, s82, v236\n"
"v_fmac_f32 v236, v253, v9\n"
"v_mfma_f32_16x16x32_bf16 v[200:203], a[64:67], a[16:19], v[200:203]\n"
"v_cvt_pk_bf16_f32 v8, v252, v236\n"
"v_accvgpr_read_b32 v253, a157\n"
"v_mfma_f32_16x16x32_bf16 v[200:203], a[68:71], a[20:23], v[200:203]\n"
"v_lshlrev_b32 v251, 16, v253\n"
"v_lshlrev_b32 v252, 16, v237\n"
"v_mfma_f32_16x16x32_bf16 v[204:207], a[64:67], a[24:27], v[204:207]\n"
"buffer_load_dwordx4 v[228:231], v240, s[36:39], s80 offen offset:1024 sc1\n"
"v_fmac_f32 v252, v251, v10\n"
"v_mfma_f32_16x16x32_bf16 v[204:207], a[68:71], a[28:31], v[204:207]\n"
"v_and_b32 v253, s82, v253\n"
"v_and_b32 v237, s82, v237\n"
"v_mfma_f32_16x16x32_bf16 v[208:211], a[64:67], a[32:35], v[208:211]\n"
"v_fmac_f32 v237, v253, v11\n"
"v_cvt_pk_bf16_f32 v9, v252, v237\n"
"v_mfma_f32_16x16x32_bf16 v[208:211], a[68:71], a[36:39], v[208:211]\n"
"v_accvgpr_read_b32 v253, a158\n"
"v_lshlrev_b32 v251, 16, v253\n"
"v_mfma_f32_16x16x32_bf16 v[212:215], a[64:67], a[40:43], v[212:215]\n"
"v_lshlrev_b32 v252, 16, v238\n"
"v_fmac_f32 v252, v251, v40\n"
"v_mfma_f32_16x16x32_bf16 v[212:215], a[68:71], a[44:47], v[212:215]\n"
"v_and_b32 v253, s82, v253\n"
"v_and_b32 v238, s82, v238\n"
"v_mfma_f32_16x16x32_bf16 v[216:219], a[64:67], a[48:51], v[216:219]\n"
"v_fmac_f32 v238, v253, v41\n"
"v_cvt_pk_bf16_f32 v10, v252, v238\n"
"v_mfma_f32_16x16x32_bf16 v[216:219], a[68:71], a[52:55], v[216:219]\n"
"v_accvgpr_read_b32 v253, a159\n"
"v_lshlrev_b32 v251, 16, v253\n"
"v_mfma_f32_16x16x32_bf16 v[220:223], a[64:67], a[56:59], v[220:223]\n"
"buffer_load_dwordx4 a[64:67], v240, s[36:39], s80 offen offset:2048 sc1\n"
"v_lshlrev_b32 v252, 16, v239\n"
"v_mfma_f32_16x16x32_bf16 v[220:223], a[68:71], a[60:63], v[220:223]\n"
"s_waitcnt vmcnt(14)\n"
"v_fmac_f32 v252, v251, v42\n"
"v_and_b32 v253, s82, v253\n"
"v_mfma_f32_16x16x32_bf16 a[112:115], a[72:75], a[0:3], a[112:115]\n"
"ds_read_b128 a[0:3], v243 offset:0\n"
"v_and_b32 v239, s82, v239\n"
"v_mfma_f32_16x16x32_bf16 a[112:115], a[76:79], a[4:7], a[112:115]\n"
"ds_read_b128 a[4:7], v244 offset:0\n"
"v_fmac_f32 v239, v253, v43\n"
"v_mfma_f32_16x16x32_bf16 a[116:119], a[72:75], a[8:11], a[116:119]\n"
"ds_read_b128 a[8:11], v243 offset:2048\n"
"v_cvt_pk_bf16_f32 v11, v252, v239\n"
"v_mfma_f32_16x16x32_bf16 a[116:119], a[76:79], a[12:15], a[116:119]\n"
"ds_read_b128 a[12:15], v244 offset:2048\n"
"v_mfma_f32_16x16x32_bf16 a[120:123], a[72:75], a[16:19], a[120:123]\n"
"s_waitcnt lgkmcnt(4)\n"
"ds_read_b128 a[16:19], v243 offset:4096\n"
"v_accvgpr_read_b32 v253, a160\n"
"v_mfma_f32_16x16x32_bf16 a[120:123], a[76:79], a[20:23], a[120:123]\n"
"ds_read_b128 a[20:23], v244 offset:4096\n"
"v_lshlrev_b32 v251, 16, v253\n"
"v_mfma_f32_16x16x32_bf16 a[124:127], a[72:75], a[24:27], a[124:127]\n"
"ds_read_b128 a[24:27], v243 offset:6144\n"
"buffer_load_dwordx4 a[68:71], v240, s[36:39], s80 offen offset:3072 sc1\n"
"v_mfma_f32_16x16x32_bf16 a[124:127], a[76:79], a[28:31], a[124:127]\n"
"s_add_u32 s80, s80, 0x1000\n"
"ds_read_b128 a[28:31], v244 offset:6144\n"
"v_lshlrev_b32 v252, 16, v232\n"
"v_mfma_f32_16x16x32_bf16 a[128:131], a[72:75], a[32:35], a[128:131]\n"
"ds_read_b128 a[32:35], v243 offset:8192\n"
"v_fmac_f32 v252, v251, v12\n"
"v_mfma_f32_16x16x32_bf16 a[128:131], a[76:79], a[36:39], a[128:131]\n"
"ds_read_b128 a[36:39], v244 offset:8192\n"
"v_and_b32 v253, s82, v253\n"
"v_mfma_f32_16x16x32_bf16 a[132:135], a[72:75], a[40:43], a[132:135]\n"
"ds_read_b128 a[40:43], v243 offset:10240\n"
"v_and_b32 v232, s82, v232\n"
"v_mfma_f32_16x16x32_bf16 a[132:135], a[76:79], a[44:47], a[132:135]\n"
"ds_read_b128 a[44:47], v244 offset:10240\n"
"v_fmac_f32 v232, v253, v13\n"
"v_mfma_f32_16x16x32_bf16 a[136:139], a[72:75], a[48:51], a[136:139]\n"
"ds_read_b128 a[48:51], v243 offset:12288\n"
"v_cvt_pk_bf16_f32 v12, v252, v232\n"
"v_mfma_f32_16x16x32_bf16 a[136:139], a[76:79], a[52:55], a[136:139]\n"
"ds_read_b128 a[52:55], v244 offset:12288\n"
"v_accvgpr_read_b32 v253, a161\n"
"v_mfma_f32_16x16x32_bf16 a[140:143], a[72:75], a[56:59], a[140:143]\n"
"ds_read_b128 a[56:59], v243 offset:14336\n"
"buffer_load_dwordx4 a[72:75], v240, s[36:39], s80 offen offset:0 sc1\n"
"v_mfma_f32_16x16x32_bf16 a[140:143], a[76:79], a[60:63], a[140:143]\n"
"s_waitcnt vmcnt(14) lgkmcnt(11)\n"
"ds_read_b128 a[60:63], v244 offset:14336\n"
"v_lshlrev_b32 v251, 16, v253\n"
"v_mfma_f32_16x16x32_bf16 v[128:131], a[80:83], a[0:3], v[128:131]\n"
"buffer_store_dwordx4 v[8:11], v242, s[48:51], s70 offen offset:0 sc1\n"
"v_mfma_f32_16x16x32_bf16 v[128:131], a[84:87], a[4:7], v[128:131]\n"
"ds_read_b128 v[236:239], v249 offset:8192\n"
"v_lshlrev_b32 v252, 16, v233\n"
"v_mfma_f32_16x16x32_bf16 v[132:135], a[80:83], a[8:11], v[132:135]\n"
"v_fmac_f32 v252, v251, v14\n"
"v_and_b32 v253, s82, v253\n"
"v_mfma_f32_16x16x32_bf16 v[132:135], a[84:87], a[12:15], v[132:135]\n"
"s_waitcnt lgkmcnt(9)\n"
"v_and_b32 v233, s82, v233\n"
"v_fmac_f32 v233, v253, v15\n"
"v_mfma_f32_16x16x32_bf16 v[136:139], a[80:83], a[16:19], v[136:139]\n"
"v_cvt_pk_bf16_f32 v13, v252, v233\n"
"v_accvgpr_read_b32 v253, a162\n"
"v_mfma_f32_16x16x32_bf16 v[136:139], a[84:87], a[20:23], v[136:139]\n"
"v_lshlrev_b32 v251, 16, v253\n"
"v_lshlrev_b32 v252, 16, v234\n"
"v_mfma_f32_16x16x32_bf16 v[140:143], a[80:83], a[24:27], v[140:143]\n"
"buffer_load_dwordx4 a[76:79], v240, s[36:39], s80 offen offset:1024 sc1\n"
"v_fmac_f32 v252, v251, v44\n"
"v_mfma_f32_16x16x32_bf16 v[140:143], a[84:87], a[28:31], v[140:143]\n"
"s_waitcnt lgkmcnt(5)\n"
"v_and_b32 v253, s82, v253\n"
"v_and_b32 v234, s82, v234\n"
"v_mfma_f32_16x16x32_bf16 v[144:147], a[80:83], a[32:35], v[144:147]\n"
"v_fmac_f32 v234, v253, v45\n"
"v_cvt_pk_bf16_f32 v14, v252, v234\n"
"v_mfma_f32_16x16x32_bf16 v[144:147], a[84:87], a[36:39], v[144:147]\n"
"v_accvgpr_read_b32 v253, a163\n"
"v_lshlrev_b32 v251, 16, v253\n"
"v_mfma_f32_16x16x32_bf16 v[148:151], a[80:83], a[40:43], v[148:151]\n"
"v_lshlrev_b32 v252, 16, v235\n"
"v_fmac_f32 v252, v251, v46\n"
"v_mfma_f32_16x16x32_bf16 v[148:151], a[84:87], a[44:47], v[148:151]\n"
"s_waitcnt lgkmcnt(1)\n"
"v_and_b32 v253, s82, v253\n"
"v_and_b32 v235, s82, v235\n"
"v_mfma_f32_16x16x32_bf16 v[152:155], a[80:83], a[48:51], v[152:155]\n"
"v_fmac_f32 v235, v253, v47\n"
"v_cvt_pk_bf16_f32 v15, v252, v235\n"
"v_mfma_f32_16x16x32_bf16 v[152:155], a[84:87], a[52:55], v[152:155]\n"
"s_waitcnt lgkmcnt(0)\n"
"ds_read_b128 v[232:235], v249 offset:10240\n"
"v_accvgpr_read_b32 v253, a164\n"
"v_mfma_f32_16x16x32_bf16 v[156:159], a[80:83], a[56:59], v[156:159]\n"
"buffer_load_dwordx4 a[80:83], v240, s[36:39], s80 offen offset:2048 sc1\n"
"v_lshlrev_b32 v251, 16, v253\n"
"v_mfma_f32_16x16x32_bf16 v[156:159], a[84:87], a[60:63], v[156:159]\n"
"s_waitcnt vmcnt(15)\n"
"buffer_store_dwordx4 v[12:15], v242, s[48:51], s71 offen offset:0 sc1\n"
"v_mfma_f32_16x16x32_bf16 v[160:163], a[88:91], a[0:3], v[160:163]\n"
"v_lshlrev_b32 v252, 16, v236\n"
"v_fmac_f32 v252, v251, v16\n"
"v_mfma_f32_16x16x32_bf16 v[160:163], a[92:95], a[4:7], v[160:163]\n"
"v_and_b32 v253, s82, v253\n"
"v_and_b32 v236, s82, v236\n"
"v_mfma_f32_16x16x32_bf16 v[164:167], a[88:91], a[8:11], v[164:167]\n"
"v_fmac_f32 v236, v253, v17\n"
"v_cvt_pk_bf16_f32 v16, v252, v236\n"
"v_mfma_f32_16x16x32_bf16 v[164:167], a[92:95], a[12:15], v[164:167]\n"
"v_accvgpr_read_b32 v253, a165\n"
"v_lshlrev_b32 v251, 16, v253\n"
"v_mfma_f32_16x16x32_bf16 v[168:171], a[88:91], a[16:19], v[168:171]\n"
"v_lshlrev_b32 v252, 16, v237\n"
"v_fmac_f32 v252, v251, v18\n"
"v_mfma_f32_16x16x32_bf16 v[168:171], a[92:95], a[20:23], v[168:171]\n"
"v_and_b32 v253, s82, v253\n"
"v_and_b32 v237, s82, v237\n"
"v_mfma_f32_16x16x32_bf16 v[172:175], a[88:91], a[24:27], v[172:175]\n"
"buffer_load_dwordx4 a[84:87], v240, s[36:39], s80 offen offset:3072 sc1\n"
"v_fmac_f32 v237, v253, v19\n"
"v_mfma_f32_16x16x32_bf16 v[172:175], a[92:95], a[28:31], v[172:175]\n"
"s_add_u32 s80, s80, 0x1000\n"
"v_cvt_pk_bf16_f32 v17, v252, v237\n"
"v_accvgpr_read_b32 v253, a166\n"
"v_mfma_f32_16x16x32_bf16 v[176:179], a[88:91], a[32:35], v[176:179]\n"
"v_lshlrev_b32 v251, 16, v253\n"
"v_lshlrev_b32 v252, 16, v238\n"
"v_mfma_f32_16x16x32_bf16 v[176:179], a[92:95], a[36:39], v[176:179]\n"
"v_fmac_f32 v252, v251, v48\n"
"v_and_b32 v253, s82, v253\n"
"v_mfma_f32_16x16x32_bf16 v[180:183], a[88:91], a[40:43], v[180:183]\n"
"v_and_b32 v238, s82, v238\n"
"v_fmac_f32 v238, v253, v49\n"
"v_mfma_f32_16x16x32_bf16 v[180:183], a[92:95], a[44:47], v[180:183]\n"
"v_cvt_pk_bf16_f32 v18, v252, v238\n"
"v_accvgpr_read_b32 v253, a167\n"
"v_mfma_f32_16x16x32_bf16 v[184:187], a[88:91], a[48:51], v[184:187]\n"
"v_lshlrev_b32 v251, 16, v253\n"
"v_lshlrev_b32 v252, 16, v239\n"
"v_mfma_f32_16x16x32_bf16 v[184:187], a[92:95], a[52:55], v[184:187]\n"
"v_fmac_f32 v252, v251, v50\n"
"v_and_b32 v253, s82, v253\n"
"v_mfma_f32_16x16x32_bf16 v[188:191], a[88:91], a[56:59], v[188:191]\n"
"buffer_load_dwordx4 a[88:91], v240, s[36:39], s80 offen offset:0 sc1\n"
"v_and_b32 v239, s82, v239\n"
"v_mfma_f32_16x16x32_bf16 v[188:191], a[92:95], a[60:63], v[188:191]\n"
"s_waitcnt vmcnt(15)\n"
"v_fmac_f32 v239, v253, v51\n"
"v_cvt_pk_bf16_f32 v19, v252, v239\n"
"v_mfma_f32_16x16x32_bf16 v[192:195], a[96:99], a[0:3], v[192:195]\n"
"buffer_store_dwordx4 v[16:19], v242, s[48:51], s72 offen offset:0 sc1\n"
"v_mfma_f32_16x16x32_bf16 v[192:195], a[100:103], a[4:7], v[192:195]\n"
"s_waitcnt lgkmcnt(0)\n"
"ds_read_b128 v[236:239], v249 offset:12288\n"
"v_accvgpr_read_b32 v253, a168\n"
"v_mfma_f32_16x16x32_bf16 v[196:199], a[96:99], a[8:11], v[196:199]\n"
"v_lshlrev_b32 v251, 16, v253\n"
"v_lshlrev_b32 v252, 16, v232\n"
"v_mfma_f32_16x16x32_bf16 v[196:199], a[100:103], a[12:15], v[196:199]\n"
"v_fmac_f32 v252, v251, v20\n"
"v_and_b32 v253, s82, v253\n"
"v_mfma_f32_16x16x32_bf16 v[200:203], a[96:99], a[16:19], v[200:203]\n"
"v_and_b32 v232, s82, v232\n"
"v_fmac_f32 v232, v253, v21\n"
"v_mfma_f32_16x16x32_bf16 v[200:203], a[100:103], a[20:23], v[200:203]\n"
"v_cvt_pk_bf16_f32 v20, v252, v232\n"
"v_accvgpr_read_b32 v253, a169\n"
"v_mfma_f32_16x16x32_bf16 v[204:207], a[96:99], a[24:27], v[204:207]\n"
"buffer_load_dwordx4 a[92:95], v240, s[36:39], s80 offen offset:1024 sc1\n"
"v_lshlrev_b32 v251, 16, v253\n"
"v_mfma_f32_16x16x32_bf16 v[204:207], a[100:103], a[28:31], v[204:207]\n"
"v_lshlrev_b32 v252, 16, v233\n"
"v_fmac_f32 v252, v251, v22\n"
"v_mfma_f32_16x16x32_bf16 v[208:211], a[96:99], a[32:35], v[208:211]\n"
"v_and_b32 v253, s82, v253\n"
"v_and_b32 v233, s82, v233\n"
"v_mfma_f32_16x16x32_bf16 v[208:211], a[100:103], a[36:39], v[208:211]\n"
"v_fmac_f32 v233, v253, v23\n"
"v_cvt_pk_bf16_f32 v21, v252, v233\n"
"v_mfma_f32_16x16x32_bf16 v[212:215], a[96:99], a[40:43], v[212:215]\n"
"v_accvgpr_read_b32 v253, a170\n"
"v_lshlrev_b32 v251, 16, v253\n"
"v_mfma_f32_16x16x32_bf16 v[212:215], a[100:103], a[44:47], v[212:215]\n"
"v_lshlrev_b32 v252, 16, v234\n"
"v_fmac_f32 v252, v251, v52\n"
"v_mfma_f32_16x16x32_bf16 v[216:219], a[96:99], a[48:51], v[216:219]\n"
"v_and_b32 v253, s82, v253\n"
"v_and_b32 v234, s82, v234\n"
"v_mfma_f32_16x16x32_bf16 v[216:219], a[100:103], a[52:55], v[216:219]\n"
"v_fmac_f32 v234, v253, v53\n"
"v_cvt_pk_bf16_f32 v22, v252, v234\n"
"v_mfma_f32_16x16x32_bf16 v[220:223], a[96:99], a[56:59], v[220:223]\n"
"buffer_load_dwordx4 a[96:99], v240, s[36:39], s80 offen offset:2048 sc1\n"
"v_accvgpr_read_b32 v253, a171\n"
"v_mfma_f32_16x16x32_bf16 v[220:223], a[100:103], a[60:63], v[220:223]\n"
"s_waitcnt vmcnt(15)\n"
"v_lshlrev_b32 v251, 16, v253\n"
"v_lshlrev_b32 v252, 16, v235\n"
"v_mfma_f32_16x16x32_bf16 a[112:115], a[104:107], a[0:3], a[112:115]\n"
"ds_read_b128 a[0:3], v243 offset:16384\n"
"v_fmac_f32 v252, v251, v54\n"
"v_mfma_f32_16x16x32_bf16 a[112:115], a[108:111], a[4:7], a[112:115]\n"
"ds_read_b128 a[4:7], v244 offset:16384\n"
"v_and_b32 v253, s82, v253\n"
"v_mfma_f32_16x16x32_bf16 a[116:119], a[104:107], a[8:11], a[116:119]\n"
"ds_read_b128 a[8:11], v243 offset:18432\n"
"v_and_b32 v235, s82, v235\n"
"v_mfma_f32_16x16x32_bf16 a[116:119], a[108:111], a[12:15], a[116:119]\n"
"ds_read_b128 a[12:15], v244 offset:18432\n"
"v_fmac_f32 v235, v253, v55\n"
"v_mfma_f32_16x16x32_bf16 a[120:123], a[104:107], a[16:19], a[120:123]\n"
"ds_read_b128 a[16:19], v243 offset:20480\n"
"v_cvt_pk_bf16_f32 v23, v252, v235\n"
"v_mfma_f32_16x16x32_bf16 a[120:123], a[108:111], a[20:23], a[120:123]\n"
"s_waitcnt lgkmcnt(5)\n"
"ds_read_b128 a[20:23], v244 offset:20480\n"
"v_accvgpr_read_b32 v253, a172\n"
"v_mfma_f32_16x16x32_bf16 a[124:127], a[104:107], a[24:27], a[124:127]\n"
"ds_read_b128 a[24:27], v243 offset:22528\n"
"buffer_load_dwordx4 a[100:103], v240, s[36:39], s80 offen offset:3072 sc1\n"
"v_mfma_f32_16x16x32_bf16 a[124:127], a[108:111], a[28:31], a[124:127]\n"
"s_add_u32 s80, s80, 0x1000\n"
"ds_read_b128 a[28:31], v244 offset:22528\n"
"v_lshlrev_b32 v251, 16, v253\n"
"v_mfma_f32_16x16x32_bf16 a[128:131], a[104:107], a[32:35], a[128:131]\n"
"ds_read_b128 a[32:35], v243 offset:24576\n"
"v_lshlrev_b32 v252, 16, v236\n"
"v_mfma_f32_16x16x32_bf16 a[128:131], a[108:111], a[36:39], a[128:131]\n"
"ds_read_b128 a[36:39], v244 offset:24576\n"
"v_fmac_f32 v252, v251, v24\n"
"v_mfma_f32_16x16x32_bf16 a[132:135], a[104:107], a[40:43], a[132:135]\n"
"ds_read_b128 a[40:43], v243 offset:26624\n"
"v_and_b32 v253, s82, v253\n"
"v_mfma_f32_16x16x32_bf16 a[132:135], a[108:111], a[44:47], a[132:135]\n"
"ds_read_b128 a[44:47], v244 offset:26624\n"
"v_and_b32 v236, s82, v236\n"
"v_mfma_f32_16x16x32_bf16 a[136:139], a[104:107], a[48:51], a[136:139]\n"
"ds_read_b128 a[48:51], v243 offset:28672\n"
"v_fmac_f32 v236, v253, v25\n"
"v_mfma_f32_16x16x32_bf16 a[136:139], a[108:111], a[52:55], a[136:139]\n"
"ds_read_b128 a[52:55], v244 offset:28672\n"
"v_cvt_pk_bf16_f32 v24, v252, v236\n"
"v_mfma_f32_16x16x32_bf16 a[140:143], a[104:107], a[56:59], a[140:143]\n"
"ds_read_b128 a[56:59], v243 offset:30720\n"
"buffer_load_dwordx4 a[104:107], v240, s[36:39], s80 offen offset:0 sc1\n"
"v_mfma_f32_16x16x32_bf16 a[140:143], a[108:111], a[60:63], a[140:143]\n"
"s_waitcnt vmcnt(14) lgkmcnt(11)\n"
"ds_read_b128 a[60:63], v244 offset:30720\n"
"v_accvgpr_read_b32 v253, a173\n"
"v_mfma_f32_16x16x32_bf16 v[128:131], v[224:227], a[0:3], v[128:131]\n"
"buffer_store_dwordx4 v[20:23], v242, s[48:51], s73 offen offset:0 sc1\n"
"v_mfma_f32_16x16x32_bf16 v[128:131], v[228:231], a[4:7], v[128:131]\n"
"ds_read_b128 v[232:235], v249 offset:47104\n"
"v_lshlrev_b32 v251, 16, v253\n"
"v_mfma_f32_16x16x32_bf16 v[132:135], v[224:227], a[8:11], v[132:135]\n"
"v_lshlrev_b32 v252, 16, v237\n"
"v_fmac_f32 v252, v251, v26\n"
"v_mfma_f32_16x16x32_bf16 v[132:135], v[228:231], a[12:15], v[132:135]\n"
"s_waitcnt lgkmcnt(9)\n"
"v_and_b32 v253, s82, v253\n"
"v_and_b32 v237, s82, v237\n"
"v_mfma_f32_16x16x32_bf16 v[136:139], v[224:227], a[16:19], v[136:139]\n"
"v_fmac_f32 v237, v253, v27\n"
"v_cvt_pk_bf16_f32 v25, v252, v237\n"
"v_mfma_f32_16x16x32_bf16 v[136:139], v[228:231], a[20:23], v[136:139]\n"
"v_accvgpr_read_b32 v253, a174\n"
"v_lshlrev_b32 v251, 16, v253\n"
"v_mfma_f32_16x16x32_bf16 v[140:143], v[224:227], a[24:27], v[140:143]\n"
"buffer_load_dwordx4 a[108:111], v240, s[36:39], s80 offen offset:1024 sc1\n"
"v_lshlrev_b32 v252, 16, v238\n"
"v_mfma_f32_16x16x32_bf16 v[140:143], v[228:231], a[28:31], v[140:143]\n"
"s_waitcnt lgkmcnt(5)\n"
"v_fmac_f32 v252, v251, v56\n"
"v_and_b32 v253, s82, v253\n"
"v_mfma_f32_16x16x32_bf16 v[144:147], v[224:227], a[32:35], v[144:147]\n"
"v_and_b32 v238, s82, v238\n"
"v_fmac_f32 v238, v253, v57\n"
"v_mfma_f32_16x16x32_bf16 v[144:147], v[228:231], a[36:39], v[144:147]\n"
"v_cvt_pk_bf16_f32 v26, v252, v238\n"
"v_accvgpr_read_b32 v253, a175\n"
"v_mfma_f32_16x16x32_bf16 v[148:151], v[224:227], a[40:43], v[148:151]\n"
"v_lshlrev_b32 v251, 16, v253\n"
"v_lshlrev_b32 v252, 16, v239\n"
"v_mfma_f32_16x16x32_bf16 v[148:151], v[228:231], a[44:47], v[148:151]\n"
"s_waitcnt lgkmcnt(1)\n"
"v_fmac_f32 v252, v251, v58\n"
"v_and_b32 v253, s82, v253\n"
"v_mfma_f32_16x16x32_bf16 v[152:155], v[224:227], a[48:51], v[152:155]\n"
"v_and_b32 v239, s82, v239\n"
"v_fmac_f32 v239, v253, v59\n"
"v_mfma_f32_16x16x32_bf16 v[152:155], v[228:231], a[52:55], v[152:155]\n"
"s_waitcnt lgkmcnt(0)\n"
"v_cvt_pk_bf16_f32 v27, v252, v239\n"
"v_accvgpr_read_b32 v253, a176\n"
"v_mfma_f32_16x16x32_bf16 v[156:159], v[224:227], a[56:59], v[156:159]\n"
"ds_read_b128 v[236:239], v249 offset:32768\n"
"buffer_load_dwordx4 v[224:227], v240, s[36:39], s80 offen offset:2048 sc1\n"
"v_mfma_f32_16x16x32_bf16 v[156:159], v[228:231], a[60:63], v[156:159]\n"
"s_waitcnt vmcnt(15)\n"
"buffer_store_dwordx4 v[24:27], v242, s[48:51], s74 offen offset:0 sc1\n"
"v_mfma_f32_16x16x32_bf16 v[160:163], a[64:67], a[0:3], v[160:163]\n"
"v_lshlrev_b32 v251, 16, v253\n"
"v_lshlrev_b32 v252, 16, v232\n"
"v_mfma_f32_16x16x32_bf16 v[160:163], a[68:71], a[4:7], v[160:163]\n"
"v_fmac_f32 v252, v251, v92\n"
"v_and_b32 v253, s82, v253\n"
"v_mfma_f32_16x16x32_bf16 v[164:167], a[64:67], a[8:11], v[164:167]\n"
"v_and_b32 v232, s82, v232\n"
"v_fmac_f32 v232, v253, v93\n"
"v_mfma_f32_16x16x32_bf16 v[164:167], a[68:71], a[12:15], v[164:167]\n"
"v_cvt_pk_bf16_f32 v92, v252, v232\n"
"v_accvgpr_read_b32 v253, a177\n"
"v_mfma_f32_16x16x32_bf16 v[168:171], a[64:67], a[16:19], v[168:171]\n"
"v_lshlrev_b32 v251, 16, v253\n"
"v_lshlrev_b32 v252, 16, v233\n"
"v_mfma_f32_16x16x32_bf16 v[168:171], a[68:71], a[20:23], v[168:171]\n"
"v_fmac_f32 v252, v251, v94\n"
"v_and_b32 v253, s82, v253\n"
"v_mfma_f32_16x16x32_bf16 v[172:175], a[64:67], a[24:27], v[172:175]\n"
"buffer_load_dwordx4 v[228:231], v240, s[36:39], s80 offen offset:3072 sc1\n"
"v_and_b32 v233, s82, v233\n"
"v_mfma_f32_16x16x32_bf16 v[172:175], a[68:71], a[28:31], v[172:175]\n"
"s_add_u32 s80, s80, 0x1000\n"
"v_fmac_f32 v233, v253, v95\n"
"v_cvt_pk_bf16_f32 v93, v252, v233\n"
"v_mfma_f32_16x16x32_bf16 v[176:179], a[64:67], a[32:35], v[176:179]\n"
"v_accvgpr_read_b32 v253, a178\n"
"v_lshlrev_b32 v251, 16, v253\n"
"v_mfma_f32_16x16x32_bf16 v[176:179], a[68:71], a[36:39], v[176:179]\n"
"v_lshlrev_b32 v252, 16, v234\n"
"v_fmac_f32 v252, v251, v124\n"
"v_mfma_f32_16x16x32_bf16 v[180:183], a[64:67], a[40:43], v[180:183]\n"
"v_and_b32 v253, s82, v253\n"
"v_and_b32 v234, s82, v234\n"
"v_mfma_f32_16x16x32_bf16 v[180:183], a[68:71], a[44:47], v[180:183]\n"
"v_fmac_f32 v234, v253, v125\n"
"v_cvt_pk_bf16_f32 v94, v252, v234\n"
"v_mfma_f32_16x16x32_bf16 v[184:187], a[64:67], a[48:51], v[184:187]\n"
"v_accvgpr_read_b32 v253, a179\n"
"v_lshlrev_b32 v251, 16, v253\n"
"v_mfma_f32_16x16x32_bf16 v[184:187], a[68:71], a[52:55], v[184:187]\n"
"v_lshlrev_b32 v252, 16, v235\n"
"v_fmac_f32 v252, v251, v126\n"
"v_mfma_f32_16x16x32_bf16 v[188:191], a[64:67], a[56:59], v[188:191]\n"
"buffer_load_dwordx4 a[64:67], v240, s[36:39], s80 offen offset:0 sc1\n"
"v_and_b32 v253, s82, v253\n"
"v_mfma_f32_16x16x32_bf16 v[188:191], a[68:71], a[60:63], v[188:191]\n"
"s_waitcnt vmcnt(15)\n"
"v_and_b32 v235, s82, v235\n"
"v_fmac_f32 v235, v253, v127\n"
"v_mfma_f32_16x16x32_bf16 v[192:195], a[72:75], a[0:3], v[192:195]\n"
"v_cvt_pk_bf16_f32 v95, v252, v235\n"
"v_mfma_f32_16x16x32_bf16 v[192:195], a[76:79], a[4:7], v[192:195]\n"
"buffer_store_dwordx4 v[92:95], v242, s[48:51], s75 offen offset:256 sc1\n"
"v_mfma_f32_16x16x32_bf16 v[196:199], a[72:75], a[8:11], v[196:199]\n"
"s_waitcnt lgkmcnt(0)\n"
"ds_read_b128 v[232:235], v249 offset:34816\n"
"v_accvgpr_read_b32 v253, a180\n"
"v_mfma_f32_16x16x32_bf16 v[196:199], a[76:79], a[12:15], v[196:199]\n"
"v_lshlrev_b32 v251, 16, v253\n"
"v_lshlrev_b32 v252, 16, v236\n"
"v_mfma_f32_16x16x32_bf16 v[200:203], a[72:75], a[16:19], v[200:203]\n"
"v_fmac_f32 v252, v251, v64\n"
"v_and_b32 v253, s82, v253\n"
"v_mfma_f32_16x16x32_bf16 v[200:203], a[76:79], a[20:23], v[200:203]\n"
"v_and_b32 v236, s82, v236\n"
"v_fmac_f32 v236, v253, v65\n"
"v_mfma_f32_16x16x32_bf16 v[204:207], a[72:75], a[24:27], v[204:207]\n"
"buffer_load_dwordx4 a[68:71], v240, s[36:39], s80 offen offset:1024 sc1\n"
"v_cvt_pk_bf16_f32 v64, v252, v236\n"
"v_mfma_f32_16x16x32_bf16 v[204:207], a[76:79], a[28:31], v[204:207]\n"
"v_accvgpr_read_b32 v253, a181\n"
"v_lshlrev_b32 v251, 16, v253\n"
"v_mfma_f32_16x16x32_bf16 v[208:211], a[72:75], a[32:35], v[208:211]\n"
"v_lshlrev_b32 v252, 16, v237\n"
"v_fmac_f32 v252, v251, v66\n"
"v_mfma_f32_16x16x32_bf16 v[208:211], a[76:79], a[36:39], v[208:211]\n"
"v_and_b32 v253, s82, v253\n"
"v_and_b32 v237, s82, v237\n"
"v_mfma_f32_16x16x32_bf16 v[212:215], a[72:75], a[40:43], v[212:215]\n"
"v_fmac_f32 v237, v253, v67\n"
"v_cvt_pk_bf16_f32 v65, v252, v237\n"
"v_mfma_f32_16x16x32_bf16 v[212:215], a[76:79], a[44:47], v[212:215]\n"
"v_accvgpr_read_b32 v253, a182\n"
"v_lshlrev_b32 v251, 16, v253\n"
"v_mfma_f32_16x16x32_bf16 v[216:219], a[72:75], a[48:51], v[216:219]\n"
"v_lshlrev_b32 v252, 16, v238\n"
"v_fmac_f32 v252, v251, v96\n"
"v_mfma_f32_16x16x32_bf16 v[216:219], a[76:79], a[52:55], v[216:219]\n"
"v_and_b32 v253, s82, v253\n"
"v_and_b32 v238, s82, v238\n"
"v_mfma_f32_16x16x32_bf16 v[220:223], a[72:75], a[56:59], v[220:223]\n"
"buffer_load_dwordx4 a[72:75], v240, s[36:39], s80 offen offset:2048 sc1\n"
"v_fmac_f32 v238, v253, v97\n"
"v_mfma_f32_16x16x32_bf16 v[220:223], a[76:79], a[60:63], v[220:223]\n"
"s_waitcnt vmcnt(15)\n"
"v_cvt_pk_bf16_f32 v66, v252, v238\n"
"v_accvgpr_read_b32 v253, a183\n"
"v_mfma_f32_16x16x32_bf16 a[112:115], a[80:83], a[0:3], a[112:115]\n"
"ds_read_b128 a[0:3], v243 offset:32768\n"
"v_lshlrev_b32 v251, 16, v253\n"
"v_mfma_f32_16x16x32_bf16 a[112:115], a[84:87], a[4:7], a[112:115]\n"
"ds_read_b128 a[4:7], v244 offset:32768\n"
"v_lshlrev_b32 v252, 16, v239\n"
"v_mfma_f32_16x16x32_bf16 a[116:119], a[80:83], a[8:11], a[116:119]\n"
"ds_read_b128 a[8:11], v243 offset:34816\n"
"v_fmac_f32 v252, v251, v98\n"
"v_mfma_f32_16x16x32_bf16 a[116:119], a[84:87], a[12:15], a[116:119]\n"
"ds_read_b128 a[12:15], v244 offset:34816\n"
"v_and_b32 v253, s82, v253\n"
"v_mfma_f32_16x16x32_bf16 a[120:123], a[80:83], a[16:19], a[120:123]\n"
"ds_read_b128 a[16:19], v243 offset:36864\n"
"v_and_b32 v239, s82, v239\n"
"v_mfma_f32_16x16x32_bf16 a[120:123], a[84:87], a[20:23], a[120:123]\n"
".Lpad20_%=:\n"
"s_cbranch_execz .Lret2_%=\n"
"ds_read_b128 a[20:23], v244 offset:36864\n"
"v_fmac_f32 v239, v253, v99\n"
"v_mfma_f32_16x16x32_bf16 a[124:127], a[80:83], a[24:27], a[124:127]\n"
"ds_read_b128 a[24:27], v243 offset:38912\n"
"buffer_load_dwordx4 a[76:79], v240, s[36:39], s80 offen offset:3072 sc1\n"
"v_mfma_f32_16x16x32_bf16 a[124:127], a[84:87], a[28:31], a[124:127]\n"
"s_add_u32 s80, s80, 0x1000\n"
"ds_read_b128 a[28:31], v244 offset:38912\n"
"v_cvt_pk_bf16_f32 v67, v252, v239\n"
"v_mfma_f32_16x16x32_bf16 a[128:131], a[80:83], a[32:35], a[128:131]\n"
"s_waitcnt lgkmcnt(8)\n"
"ds_read_b128 a[32:35], v243 offset:40960\n"
"v_accvgpr_read_b32 v253, a184\n"
"v_mfma_f32_16x16x32_bf16 a[128:131], a[84:87], a[36:39], a[128:131]\n"
"ds_read_b128 a[36:39], v244 offset:40960\n"
"v_lshlrev_b32 v251, 16, v253\n"
"v_mfma_f32_16x16x32_bf16 a[132:135], a[80:83], a[40:43], a[132:135]\n"
"ds_read_b128 a[40:43], v243 offset:43008\n"
"v_lshlrev_b32 v252, 16, v232\n"
"v_mfma_f32_16x16x32_bf16 a[132:135], a[84:87], a[44:47], a[132:135]\n"
"ds_read_b128 a[44:47], v244 offset:43008\n"
"v_fmac_f32 v252, v251, v68\n"
"v_mfma_f32_16x16x32_bf16 a[136:139], a[80:83], a[48:51], a[136:139]\n"
"ds_read_b128 a[48:51], v243 offset:45056\n"
"v_and_b32 v253, s82, v253\n"
"v_mfma_f32_16x16x32_bf16 a[136:139], a[84:87], a[52:55], a[136:139]\n"
"ds_read_b128 a[52:55], v244 offset:45056\n"
"v_and_b32 v232, s82, v232\n"
"v_mfma_f32_16x16x32_bf16 a[140:143], a[80:83], a[56:59], a[140:143]\n"
"ds_read_b128 a[56:59], v243 offset:47104\n"
"buffer_load_dwordx4 a[80:83], v240, s[36:39], s80 offen offset:0 sc1\n"
"v_mfma_f32_16x16x32_bf16 a[140:143], a[84:87], a[60:63], a[140:143]\n"
"s_waitcnt vmcnt(14) lgkmcnt(11)\n"
"ds_read_b128 a[60:63], v244 offset:47104\n"
"v_fmac_f32 v232, v253, v69\n"
"v_mfma_f32_16x16x32_bf16 v[128:131], a[88:91], a[0:3], v[128:131]\n"
"buffer_store_dwordx4 v[64:67], v242, s[48:51], s68 offen offset:256 sc1\n"
"v_mfma_f32_16x16x32_bf16 v[128:131], a[92:95], a[4:7], v[128:131]\n"
"ds_read_b128 v[236:239], v249 offset:36864\n"
"v_cvt_pk_bf16_f32 v68, v252, v232\n"
"v_mfma_f32_16x16x32_bf16 v[132:135], a[88:91], a[8:11], v[132:135]\n"
"v_accvgpr_read_b32 v253, a185\n"
"v_lshlrev_b32 v251, 16, v253\n"
"v_mfma_f32_16x16x32_bf16 v[132:135], a[92:95], a[12:15], v[132:135]\n"
"s_waitcnt lgkmcnt(9)\n"
"v_lshlrev_b32 v252, 16, v233\n"
"v_fmac_f32 v252, v251, v70\n"
"v_mfma_f32_16x16x32_bf16 v[136:139], a[88:91], a[16:19], v[136:139]\n"
"v_and_b32 v253, s82, v253\n"
"v_and_b32 v233, s82, v233\n"
"v_mfma_f32_16x16x32_bf16 v[136:139], a[92:95], a[20:23], v[136:139]\n"
"v_fmac_f32 v233, v253, v71\n"
"v_cvt_pk_bf16_f32 v69, v252, v233\n"
"v_mfma_f32_16x16x32_bf16 v[140:143], a[88:91], a[24:27], v[140:143]\n"
"buffer_load_dwordx4 a[84:87], v240, s[36:39], s80 offen offset:1024 sc1\n"
"v_accvgpr_read_b32 v253, a186\n"
"v_mfma_f32_16x16x32_bf16 v[140:143], a[92:95], a[28:31], v[140:143]\n"
"s_waitcnt lgkmcnt(5)\n"
"v_lshlrev_b32 v251, 16, v253\n"
"v_lshlrev_b32 v252, 16, v234\n"
"v_mfma_f32_16x16x32_bf16 v[144:147], a[88:91], a[32:35], v[144:147]\n"
"v_fmac_f32 v252, v251, v100\n"
"v_and_b32 v253, s82, v253\n"
"v_mfma_f32_16x16x32_bf16 v[144:147], a[92:95], a[36:39], v[144:147]\n"
"v_and_b32 v234, s82, v234\n"
"v_fmac_f32 v234, v253, v101\n"
"v_mfma_f32_16x16x32_bf16 v[148:151], a[88:91], a[40:43], v[148:151]\n"
"v_cvt_pk_bf16_f32 v70, v252, v234\n"
"v_accvgpr_read_b32 v253, a187\n"
"v_mfma_f32_16x16x32_bf16 v[148:151], a[92:95], a[44:47], v[148:151]\n"
"s_waitcnt lgkmcnt(1)\n"
"v_lshlrev_b32 v251, 16, v253\n"
"v_lshlrev_b32 v252, 16, v235\n"
"v_mfma_f32_16x16x32_bf16 v[152:155], a[88:91], a[48:51], v[152:155]\n"
"v_fmac_f32 v252, v251, v102\n"
"v_and_b32 v253, s82, v253\n"
"v_mfma_f32_16x16x32_bf16 v[152:155], a[92:95], a[52:55], v[152:155]\n"
"v_and_b32 v235, s82, v235\n"
"v_fmac_f32 v235, v253, v103\n"
"v_mfma_f32_16x16x32_bf16 v[156:159], a[88:91], a[56:59], v[156:159]\n"
"buffer_load_dwordx4 a[88:91], v240, s[36:39], s80 offen offset:2048 sc1\n"
"v_cvt_pk_bf16_f32 v71, v252, v235\n"
"v_mfma_f32_16x16x32_bf16 v[156:159], a[92:95], a[60:63], v[156:159]\n"
"s_waitcnt vmcnt(15)\n"
"buffer_store_dwordx4 v[68:71], v242, s[48:51], s69 offen offset:256 sc1\n"
"v_mfma_f32_16x16x32_bf16 v[160:163], a[96:99], a[0:3], v[160:163]\n"
"s_waitcnt lgkmcnt(0)\n"
"ds_read_b128 a[0:3], v243 offset:49152\n"
"v_accvgpr_read_b32 v253, a188\n"
"v_mfma_f32_16x16x32_bf16 v[160:163], a[100:103], a[4:7], v[160:163]\n"
"ds_read_b128 a[4:7], v244 offset:49152\n"
"v_lshlrev_b32 v251, 16, v253\n"
"v_mfma_f32_16x16x32_bf16 v[164:167], a[96:99], a[8:11], v[164:167]\n"
"ds_read_b128 a[8:11], v243 offset:51200\n"
"v_lshlrev_b32 v252, 16, v236\n"
"v_mfma_f32_16x16x32_bf16 v[164:167], a[100:103], a[12:15], v[164:167]\n"
"ds_read_b128 a[12:15], v244 offset:51200\n"
"v_fmac_f32 v252, v251, v72\n"
"v_mfma_f32_16x16x32_bf16 v[168:171], a[96:99], a[16:19], v[168:171]\n"
"ds_read_b128 a[16:19], v243 offset:53248\n"
"v_and_b32 v253, s82, v253\n"
"v_mfma_f32_16x16x32_bf16 v[168:171], a[100:103], a[20:23], v[168:171]\n"
"ds_read_b128 a[20:23], v244 offset:53248\n"
"v_and_b32 v236, s82, v236\n"
"v_mfma_f32_16x16x32_bf16 v[172:175], a[96:99], a[24:27], v[172:175]\n"
"ds_read_b128 a[24:27], v243 offset:55296\n"
"buffer_load_dwordx4 a[92:95], v240, s[36:39], s80 offen offset:3072 sc1\n"
"v_mfma_f32_16x16x32_bf16 v[172:175], a[100:103], a[28:31], v[172:175]\n"
"s_add_u32 s80, s80, 0x1000\n"
"ds_read_b128 a[28:31], v244 offset:55296\n"
"v_fmac_f32 v236, v253, v73\n"
"v_mfma_f32_16x16x32_bf16 v[176:179], a[96:99], a[32:35], v[176:179]\n"
"ds_read_b128 a[32:35], v243 offset:57344\n"
"v_cvt_pk_bf16_f32 v72, v252, v236\n"
"v_mfma_f32_16x16x32_bf16 v[176:179], a[100:103], a[36:39], v[176:179]\n"
"ds_read_b128 a[36:39], v244 offset:57344\n"
"v_accvgpr_read_b32 v253, a189\n"
"v_mfma_f32_16x16x32_bf16 v[180:183], a[96:99], a[40:43], v[180:183]\n"
"ds_read_b128 a[40:43], v243 offset:59392\n"
"v_lshlrev_b32 v251, 16, v253\n"
"v_mfma_f32_16x16x32_bf16 v[180:183], a[100:103], a[44:47], v[180:183]\n"
"ds_read_b128 a[44:47], v244 offset:59392\n"
"v_lshlrev_b32 v252, 16, v237\n"
"v_mfma_f32_16x16x32_bf16 v[184:187], a[96:99], a[48:51], v[184:187]\n"
"ds_read_b128 a[48:51], v243 offset:61440\n"
"v_fmac_f32 v252, v251, v74\n"
"v_mfma_f32_16x16x32_bf16 v[184:187], a[100:103], a[52:55], v[184:187]\n"
"ds_read_b128 a[52:55], v244 offset:61440\n"
"v_and_b32 v253, s82, v253\n"
"v_mfma_f32_16x16x32_bf16 v[188:191], a[96:99], a[56:59], v[188:191]\n"
"ds_read_b128 a[56:59], v243 offset:63488\n"
"buffer_load_dwordx4 a[96:99], v240, s[36:39], s80 offen offset:0 sc1\n"
"v_mfma_f32_16x16x32_bf16 v[188:191], a[100:103], a[60:63], v[188:191]\n"
"s_waitcnt vmcnt(15) lgkmcnt(11)\n"
"ds_read_b128 a[60:63], v244 offset:63488\n"
"v_and_b32 v237, s82, v237\n"
"v_mfma_f32_16x16x32_bf16 v[128:131], a[104:107], a[0:3], v[128:131]\n"
"ds_read_b128 v[232:235], v249 offset:38912\n"
"v_fmac_f32 v237, v253, v75\n"
"v_mfma_f32_16x16x32_bf16 v[128:131], a[108:111], a[4:7], v[128:131]\n"
"v_cvt_pk_bf16_f32 v73, v252, v237\n"
"v_accvgpr_read_b32 v253, a190\n"
"v_mfma_f32_16x16x32_bf16 v[132:135], a[104:107], a[8:11], v[132:135]\n"
"v_lshlrev_b32 v251, 16, v253\n"
"v_lshlrev_b32 v252, 16, v238\n"
"v_mfma_f32_16x16x32_bf16 v[132:135], a[108:111], a[12:15], v[132:135]\n"
"s_waitcnt lgkmcnt(9)\n"
"v_fmac_f32 v252, v251, v104\n"
"v_and_b32 v253, s82, v253\n"
"v_mfma_f32_16x16x32_bf16 v[136:139], a[104:107], a[16:19], v[136:139]\n"
"v_and_b32 v238, s82, v238\n"
"v_fmac_f32 v238, v253, v105\n"
"v_mfma_f32_16x16x32_bf16 v[136:139], a[108:111], a[20:23], v[136:139]\n"
"v_cvt_pk_bf16_f32 v74, v252, v238\n"
"v_accvgpr_read_b32 v253, a191\n"
"v_mfma_f32_16x16x32_bf16 v[140:143], a[104:107], a[24:27], v[140:143]\n"
"buffer_load_dwordx4 a[100:103], v240, s[36:39], s80 offen offset:1024 sc1\n"
"v_lshlrev_b32 v251, 16, v253\n"
"v_mfma_f32_16x16x32_bf16 v[140:143], a[108:111], a[28:31], v[140:143]\n"
"s_waitcnt lgkmcnt(5)\n"
"v_lshlrev_b32 v252, 16, v239\n"
"v_fmac_f32 v252, v251, v106\n"
"v_mfma_f32_16x16x32_bf16 v[144:147], a[104:107], a[32:35], v[144:147]\n"
"v_and_b32 v253, s82, v253\n"
"v_and_b32 v239, s82, v239\n"
"v_mfma_f32_16x16x32_bf16 v[144:147], a[108:111], a[36:39], v[144:147]\n"
"v_fmac_f32 v239, v253, v107\n"
"v_cvt_pk_bf16_f32 v75, v252, v239\n"
"v_mfma_f32_16x16x32_bf16 v[148:151], a[104:107], a[40:43], v[148:151]\n"
"buffer_store_dwordx4 v[72:75], v242, s[48:51], s70 offen offset:256 sc1\n"
"v_mfma_f32_16x16x32_bf16 v[148:151], a[108:111], a[44:47], v[148:151]\n"
"s_waitcnt lgkmcnt(0)\n"
"ds_read_b128 v[236:239], v249 offset:40960\n"
"v_accvgpr_read_b32 v253, a192\n"
"v_mfma_f32_16x16x32_bf16 v[152:155], a[104:107], a[48:51], v[152:155]\n"
"v_lshlrev_b32 v251, 16, v253\n"
"v_lshlrev_b32 v252, 16, v232\n"
"v_mfma_f32_16x16x32_bf16 v[152:155], a[108:111], a[52:55], v[152:155]\n"
"v_fmac_f32 v252, v251, v76\n"
"v_and_b32 v253, s82, v253\n"
"v_mfma_f32_16x16x32_bf16 v[156:159], a[104:107], a[56:59], v[156:159]\n"
"buffer_load_dwordx4 a[104:107], v240, s[36:39], s80 offen offset:2048 sc1\n"
"v_and_b32 v232, s82, v232\n"
"v_mfma_f32_16x16x32_bf16 v[156:159], a[108:111], a[60:63], v[156:159]\n"
"s_waitcnt vmcnt(15)\n"
"v_fmac_f32 v232, v253, v77\n"
"v_cvt_pk_bf16_f32 v76, v252, v232\n"
"v_mfma_f32_16x16x32_bf16 v[160:163], v[224:227], a[0:3], v[160:163]\n"
"ds_read_b128 a[0:3], v245 offset:0\n"
"v_accvgpr_read_b32 v253, a193\n"
"v_mfma_f32_16x16x32_bf16 v[160:163], v[228:231], a[4:7], v[160:163]\n"
"ds_read_b128 a[4:7], v246 offset:0\n"
"v_lshlrev_b32 v251, 16, v253\n"
"v_mfma_f32_16x16x32_bf16 v[164:167], v[224:227], a[8:11], v[164:167]\n"
"ds_read_b128 a[8:11], v245 offset:2048\n"
"v_lshlrev_b32 v252, 16, v233\n"
"v_mfma_f32_16x16x32_bf16 v[164:167], v[228:231], a[12:15], v[164:167]\n"
"ds_read_b128 a[12:15], v246 offset:2048\n"
"v_fmac_f32 v252, v251, v78\n"
"v_mfma_f32_16x16x32_bf16 v[168:171], v[224:227], a[16:19], v[168:171]\n"
"ds_read_b128 a[16:19], v245 offset:4096\n"
"v_and_b32 v253, s82, v253\n"
"v_mfma_f32_16x16x32_bf16 v[168:171], v[228:231], a[20:23], v[168:171]\n"
"ds_read_b128 a[20:23], v246 offset:4096\n"
"v_and_b32 v233, s82, v233\n"
"v_mfma_f32_16x16x32_bf16 v[172:175], v[224:227], a[24:27], v[172:175]\n"
"ds_read_b128 a[24:27], v245 offset:6144\n"
"buffer_load_dwordx4 a[108:111], v240, s[36:39], s80 offen offset:3072 sc1\n"
"v_mfma_f32_16x16x32_bf16 v[172:175], v[228:231], a[28:31], v[172:175]\n"
"s_add_u32 s80, s80, 0x1000\n"
"ds_read_b128 a[28:31], v246 offset:6144\n"
"v_fmac_f32 v233, v253, v79\n"
"v_mfma_f32_16x16x32_bf16 v[176:179], v[224:227], a[32:35], v[176:179]\n"
"ds_read_b128 a[32:35], v245 offset:8192\n"
"v_cvt_pk_bf16_f32 v77, v252, v233\n"
"v_mfma_f32_16x16x32_bf16 v[176:179], v[228:231], a[36:39], v[176:179]\n"
"ds_read_b128 a[36:39], v246 offset:8192\n"
"v_accvgpr_read_b32 v253, a194\n"
"v_mfma_f32_16x16x32_bf16 v[180:183], v[224:227], a[40:43], v[180:183]\n"
"ds_read_b128 a[40:43], v245 offset:10240\n"
"v_lshlrev_b32 v251, 16, v253\n"
"v_mfma_f32_16x16x32_bf16 v[180:183], v[228:231], a[44:47], v[180:183]\n"
"ds_read_b128 a[44:47], v246 offset:10240\n"
"v_lshlrev_b32 v252, 16, v234\n"
"v_mfma_f32_16x16x32_bf16 v[184:187], v[224:227], a[48:51], v[184:187]\n"
"ds_read_b128 a[48:51], v245 offset:12288\n"
"v_fmac_f32 v252, v251, v108\n"
"v_mfma_f32_16x16x32_bf16 v[184:187], v[228:231], a[52:55], v[184:187]\n"
"ds_read_b128 a[52:55], v246 offset:12288\n"
"v_and_b32 v253, s82, v253\n"
"v_mfma_f32_16x16x32_bf16 v[188:191], v[224:227], a[56:59], v[188:191]\n"
"ds_read_b128 a[56:59], v245 offset:14336\n"
"buffer_load_dwordx4 v[224:227], v240, s[36:39], s80 offen offset:0 sc1\n"
"v_mfma_f32_16x16x32_bf16 v[188:191], v[228:231], a[60:63], v[188:191]\n"
"s_waitcnt vmcnt(14) lgkmcnt(11)\n"
"ds_read_b128 a[60:63], v246 offset:14336\n"
"v_and_b32 v234, s82, v234\n"
"v_mfma_f32_16x16x32_bf16 v[128:131], a[64:67], a[0:3], v[128:131]\n"
"v_fmac_f32 v234, v253, v109\n"
"v_cvt_pk_bf16_f32 v78, v252, v234\n"
"v_mfma_f32_16x16x32_bf16 v[128:131], a[68:71], a[4:7], v[128:131]\n"
"v_accvgpr_read_b32 v253, a195\n"
"v_lshlrev_b32 v251, 16, v253\n"
"v_mfma_f32_16x16x32_bf16 v[132:135], a[64:67], a[8:11], v[132:135]\n"
"v_lshlrev_b32 v252, 16, v235\n"
"v_fmac_f32 v252, v251, v110\n"
"v_mfma_f32_16x16x32_bf16 v[132:135], a[68:71], a[12:15], v[132:135]\n"
"s_waitcnt lgkmcnt(8)\n"
"v_and_b32 v253, s82, v253\n"
"v_and_b32 v235, s82, v235\n"
"v_mfma_f32_16x16x32_bf16 v[136:139], a[64:67], a[16:19], v[136:139]\n"
"v_fmac_f32 v235, v253, v111\n"
"v_cvt_pk_bf16_f32 v79, v252, v235\n"
"v_mfma_f32_16x16x32_bf16 v[136:139], a[68:71], a[20:23], v[136:139]\n"
"ds_read_b128 v[232:235], v249 offset:43008\n"
"v_accvgpr_read_b32 v253, a196\n"
"v_mfma_f32_16x16x32_bf16 v[140:143], a[64:67], a[24:27], v[140:143]\n"
"buffer_load_dwordx4 v[228:231], v240, s[36:39], s80 offen offset:1024 sc1\n"
"v_lshlrev_b32 v251, 16, v253\n"
"v_mfma_f32_16x16x32_bf16 v[140:143], a[68:71], a[28:31], v[140:143]\n"
"s_waitcnt lgkmcnt(5)\n"
"buffer_store_dwordx4 v[76:79], v242, s[48:51], s71 offen offset:256 sc1\n"
"v_mfma_f32_16x16x32_bf16 v[144:147], a[64:67], a[32:35], v[144:147]\n"
"v_lshlrev_b32 v252, 16, v236\n"
"v_fmac_f32 v252, v251, v80\n"
"v_mfma_f32_16x16x32_bf16 v[144:147], a[68:71], a[36:39], v[144:147]\n"
"v_and_b32 v253, s82, v253\n"
"v_and_b32 v236, s82, v236\n"
"v_mfma_f32_16x16x32_bf16 v[148:151], a[64:67], a[40:43], v[148:151]\n"
"v_fmac_f32 v236, v253, v81\n"
"v_cvt_pk_bf16_f32 v80, v252, v236\n"
"v_mfma_f32_16x16x32_bf16 v[148:151], a[68:71], a[44:47], v[148:151]\n"
"s_waitcnt lgkmcnt(1)\n"
"v_accvgpr_read_b32 v253, a197\n"
"v_lshlrev_b32 v251, 16, v253\n"
"v_mfma_f32_16x16x32_bf16 v[152:155], a[64:67], a[48:51], v[152:155]\n"
"v_lshlrev_b32 v252, 16, v237\n"
"v_fmac_f32 v252, v251, v82\n"
"v_mfma_f32_16x16x32_bf16 v[152:155], a[68:71], a[52:55], v[152:155]\n"
"v_and_b32 v253, s82, v253\n"
"v_and_b32 v237, s82, v237\n"
"v_mfma_f32_16x16x32_bf16 v[156:159], a[64:67], a[56:59], v[156:159]\n"
"buffer_load_dwordx4 a[64:67], v240, s[36:39], s80 offen offset:2048 sc1\n"
"v_fmac_f32 v237, v253, v83\n"
"v_mfma_f32_16x16x32_bf16 v[156:159], a[68:71], a[60:63], v[156:159]\n"
"s_waitcnt vmcnt(15)\n"
"v_cvt_pk_bf16_f32 v81, v252, v237\n"
"v_accvgpr_read_b32 v253, a198\n"
"v_mfma_f32_16x16x32_bf16 v[160:163], a[72:75], a[0:3], v[160:163]\n"
"ds_read_b128 a[0:3], v245 offset:16384\n"
"v_lshlrev_b32 v251, 16, v253\n"
"v_mfma_f32_16x16x32_bf16 v[160:163], a[76:79], a[4:7], v[160:163]\n"
"ds_read_b128 a[4:7], v246 offset:16384\n"
"v_lshlrev_b32 v252, 16, v238\n"
"v_mfma_f32_16x16x32_bf16 v[164:167], a[72:75], a[8:11], v[164:167]\n"
"ds_read_b128 a[8:11], v245 offset:18432\n"
"v_fmac_f32 v252, v251, v112\n"
"v_mfma_f32_16x16x32_bf16 v[164:167], a[76:79], a[12:15], v[164:167]\n"
"ds_read_b128 a[12:15], v246 offset:18432\n"
"v_and_b32 v253, s82, v253\n"
"v_mfma_f32_16x16x32_bf16 v[168:171], a[72:75], a[16:19], v[168:171]\n"
"ds_read_b128 a[16:19], v245 offset:20480\n"
"v_and_b32 v238, s82, v238\n"
"v_mfma_f32_16x16x32_bf16 v[168:171], a[76:79], a[20:23], v[168:171]\n"
"ds_read_b128 a[20:23], v246 offset:20480\n"
"v_fmac_f32 v238, v253, v113\n"
"v_mfma_f32_16x16x32_bf16 v[172:175], a[72:75], a[24:27], v[172:175]\n"
"ds_read_b128 a[24:27], v245 offset:22528\n"
"buffer_load_dwordx4 a[68:71], v240, s[36:39], s80 offen offset:3072 sc1\n"
"v_mfma_f32_16x16x32_bf16 v[172:175], a[76:79], a[28:31], v[172:175]\n"
"s_add_u32 s80, s80, 0x1000\n"
"ds_read_b128 a[28:31], v246 offset:22528\n"
"v_cvt_pk_bf16_f32 v82, v252, v238\n"
"v_mfma_f32_16x16x32_bf16 v[176:179], a[72:75], a[32:35], v[176:179]\n"
"ds_read_b128 a[32:35], v245 offset:24576\n"
"v_accvgpr_read_b32 v253, a199\n"
"v_mfma_f32_16x16x32_bf16 v[176:179], a[76:79], a[36:39], v[176:179]\n"
"ds_read_b128 a[36:39], v246 offset:24576\n"
"v_lshlrev_b32 v251, 16, v253\n"
"v_mfma_f32_16x16x32_bf16 v[180:183], a[72:75], a[40:43], v[180:183]\n"
"ds_read_b128 a[40:43], v245 offset:26624\n"
"v_lshlrev_b32 v252, 16, v239\n"
"v_mfma_f32_16x16x32_bf16 v[180:183], a[76:79], a[44:47], v[180:183]\n"
"ds_read_b128 a[44:47], v246 offset:26624\n"
"v_fmac_f32 v252, v251, v114\n"
"v_mfma_f32_16x16x32_bf16 v[184:187], a[72:75], a[48:51], v[184:187]\n"
"ds_read_b128 a[48:51], v245 offset:28672\n"
"v_and_b32 v253, s82, v253\n"
"v_mfma_f32_16x16x32_bf16 v[184:187], a[76:79], a[52:55], v[184:187]\n"
"ds_read_b128 a[52:55], v246 offset:28672\n"
"v_and_b32 v239, s82, v239\n"
"v_mfma_f32_16x16x32_bf16 v[188:191], a[72:75], a[56:59], v[188:191]\n"
"ds_read_b128 a[56:59], v245 offset:30720\n"
"buffer_load_dwordx4 a[72:75], v240, s[36:39], s80 offen offset:0 sc1\n"
"v_mfma_f32_16x16x32_bf16 v[188:191], a[76:79], a[60:63], v[188:191]\n"
"s_waitcnt vmcnt(14) lgkmcnt(11)\n"
"ds_read_b128 a[60:63], v246 offset:30720\n"
"v_fmac_f32 v239, v253, v115\n"
"v_mfma_f32_16x16x32_bf16 v[128:131], a[80:83], a[0:3], v[128:131]\n"
"v_cvt_pk_bf16_f32 v83, v252, v239\n"
"v_accvgpr_read_b32 v253, a200\n"
"v_mfma_f32_16x16x32_bf16 v[128:131], a[84:87], a[4:7], v[128:131]\n"
"buffer_store_dwordx4 v[80:83], v242, s[48:51], s72 offen offset:256 sc1\n"
"v_mfma_f32_16x16x32_bf16 v[132:135], a[80:83], a[8:11], v[132:135]\n"
"ds_read_b128 v[236:239], v249 offset:45056\n"
"v_lshlrev_b32 v251, 16, v253\n"
"v_mfma_f32_16x16x32_bf16 v[132:135], a[84:87], a[12:15], v[132:135]\n"
"s_waitcnt lgkmcnt(9)\n"
"v_lshlrev_b32 v252, 16, v232\n"
"v_fmac_f32 v252, v251, v84\n"
"v_mfma_f32_16x16x32_bf16 v[136:139], a[80:83], a[16:19], v[136:139]\n"
"v_and_b32 v253, s82, v253\n"
"v_and_b32 v232, s82, v232\n"
"v_mfma_f32_16x16x32_bf16 v[136:139], a[84:87], a[20:23], v[136:139]\n"
"v_fmac_f32 v232, v253, v85\n"
"v_cvt_pk_bf16_f32 v84, v252, v232\n"
"v_mfma_f32_16x16x32_bf16 v[140:143], a[80:83], a[24:27], v[140:143]\n"
"buffer_load_dwordx4 a[76:79], v240, s[36:39], s80 offen offset:1024 sc1\n"
"v_accvgpr_read_b32 v253, a201\n"
"v_mfma_f32_16x16x32_bf16 v[140:143], a[84:87], a[28:31], v[140:143]\n"
"s_waitcnt lgkmcnt(5)\n"
"v_lshlrev_b32 v251, 16, v253\n"
"v_lshlrev_b32 v252, 16, v233\n"
"v_mfma_f32_16x16x32_bf16 v[144:147], a[80:83], a[32:35], v[144:147]\n"
"v_fmac_f32 v252, v251, v86\n"
"v_and_b32 v253, s82, v253\n"
"v_mfma_f32_16x16x32_bf16 v[144:147], a[84:87], a[36:39], v[144:147]\n"
"v_and_b32 v233, s82, v233\n"
"v_fmac_f32 v233, v253, v87\n"
"v_mfma_f32_16x16x32_bf16 v[148:151], a[80:83], a[40:43], v[148:151]\n"
"v_cvt_pk_bf16_f32 v85, v252, v233\n"
"v_accvgpr_read_b32 v253, a202\n"
"v_mfma_f32_16x16x32_bf16 v[148:151], a[84:87], a[44:47], v[148:151]\n"
"s_waitcnt lgkmcnt(1)\n"
"v_lshlrev_b32 v251, 16, v253\n"
"v_lshlrev_b32 v252, 16, v234\n"
"v_mfma_f32_16x16x32_bf16 v[152:155], a[80:83], a[48:51], v[152:155]\n"
"v_fmac_f32 v252, v251, v116\n"
"v_and_b32 v253, s82, v253\n"
"v_mfma_f32_16x16x32_bf16 v[152:155], a[84:87], a[52:55], v[152:155]\n"
"v_and_b32 v234, s82, v234\n"
"v_fmac_f32 v234, v253, v117\n"
"v_mfma_f32_16x16x32_bf16 v[156:159], a[80:83], a[56:59], v[156:159]\n"
"buffer_load_dwordx4 a[80:83], v240, s[36:39], s80 offen offset:2048 sc1\n"
"v_cvt_pk_bf16_f32 v86, v252, v234\n"
"v_mfma_f32_16x16x32_bf16 v[156:159], a[84:87], a[60:63], v[156:159]\n"
"s_waitcnt vmcnt(14)\n"
"v_accvgpr_read_b32 v253, a203\n"
"v_lshlrev_b32 v251, 16, v253\n"
"v_mfma_f32_16x16x32_bf16 v[160:163], a[88:91], a[0:3], v[160:163]\n"
"ds_read_b128 a[0:3], v245 offset:32768\n"
"v_lshlrev_b32 v252, 16, v235\n"
"v_mfma_f32_16x16x32_bf16 v[160:163], a[92:95], a[4:7], v[160:163]\n"
"ds_read_b128 a[4:7], v246 offset:32768\n"
"v_fmac_f32 v252, v251, v118\n"
"v_mfma_f32_16x16x32_bf16 v[164:167], a[88:91], a[8:11], v[164:167]\n"
"ds_read_b128 a[8:11], v245 offset:34816\n"
"v_and_b32 v253, s82, v253\n"
"v_mfma_f32_16x16x32_bf16 v[164:167], a[92:95], a[12:15], v[164:167]\n"
"ds_read_b128 a[12:15], v246 offset:34816\n"
"v_and_b32 v235, s82, v235\n"
"v_mfma_f32_16x16x32_bf16 v[168:171], a[88:91], a[16:19], v[168:171]\n"
"ds_read_b128 a[16:19], v245 offset:36864\n"
"v_fmac_f32 v235, v253, v119\n"
"v_mfma_f32_16x16x32_bf16 v[168:171], a[92:95], a[20:23], v[168:171]\n"
"ds_read_b128 a[20:23], v246 offset:36864\n"
"v_cvt_pk_bf16_f32 v87, v252, v235\n"
"v_mfma_f32_16x16x32_bf16 v[172:175], a[88:91], a[24:27], v[172:175]\n"
"ds_read_b128 a[24:27], v245 offset:38912\n"
"buffer_load_dwordx4 a[84:87], v240, s[36:39], s80 offen offset:3072 sc1\n"
"v_mfma_f32_16x16x32_bf16 v[172:175], a[92:95], a[28:31], v[172:175]\n"
"s_add_u32 s80, s80, 0x1000\n"
"ds_read_b128 a[28:31], v246 offset:38912\n"
"v_mfma_f32_16x16x32_bf16 v[176:179], a[88:91], a[32:35], v[176:179]\n"
"s_waitcnt lgkmcnt(8)\n"
"ds_read_b128 a[32:35], v245 offset:40960\n"
"v_accvgpr_read_b32 v253, a204\n"
"v_mfma_f32_16x16x32_bf16 v[176:179], a[92:95], a[36:39], v[176:179]\n"
"ds_read_b128 a[36:39], v246 offset:40960\n"
"v_lshlrev_b32 v251, 16, v253\n"
"v_mfma_f32_16x16x32_bf16 v[180:183], a[88:91], a[40:43], v[180:183]\n"
"ds_read_b128 a[40:43], v245 offset:43008\n"
"v_lshlrev_b32 v252, 16, v236\n"
"v_mfma_f32_16x16x32_bf16 v[180:183], a[92:95], a[44:47], v[180:183]\n"
"ds_read_b128 a[44:47], v246 offset:43008\n"
"v_fmac_f32 v252, v251, v88\n"
"v_mfma_f32_16x16x32_bf16 v[184:187], a[88:91], a[48:51], v[184:187]\n"
"ds_read_b128 a[48:51], v245 offset:45056\n"
"v_and_b32 v253, s82, v253\n"
"v_mfma_f32_16x16x32_bf16 v[184:187], a[92:95], a[52:55], v[184:187]\n"
"ds_read_b128 a[52:55], v246 offset:45056\n"
"v_and_b32 v236, s82, v236\n"
"v_mfma_f32_16x16x32_bf16 v[188:191], a[88:91], a[56:59], v[188:191]\n"
"ds_read_b128 a[56:59], v245 offset:47104\n"
"buffer_load_dwordx4 a[88:91], v240, s[36:39], s80 offen offset:0 sc1\n"
"v_mfma_f32_16x16x32_bf16 v[188:191], a[92:95], a[60:63], v[188:191]\n"
"s_waitcnt vmcnt(14) lgkmcnt(11)\n"
"ds_read_b128 a[60:63], v246 offset:47104\n"
"v_fmac_f32 v236, v253, v89\n"
"v_mfma_f32_16x16x32_bf16 v[128:131], a[96:99], a[0:3], v[128:131]\n"
"buffer_store_dwordx4 v[84:87], v242, s[48:51], s73 offen offset:256 sc1\n"
"v_mfma_f32_16x16x32_bf16 v[128:131], a[100:103], a[4:7], v[128:131]\n"
"ds_read_b128 v[232:235], v250 offset:14336\n"
"v_cvt_pk_bf16_f32 v88, v252, v236\n"
"v_mfma_f32_16x16x32_bf16 v[132:135], a[96:99], a[8:11], v[132:135]\n"
"v_accvgpr_read_b32 v253, a205\n"
"v_lshlrev_b32 v251, 16, v253\n"
"v_mfma_f32_16x16x32_bf16 v[132:135], a[100:103], a[12:15], v[132:135]\n"
"s_waitcnt lgkmcnt(9)\n"
"v_lshlrev_b32 v252, 16, v237\n"
"v_fmac_f32 v252, v251, v90\n"
"v_mfma_f32_16x16x32_bf16 v[136:139], a[96:99], a[16:19], v[136:139]\n"
"v_and_b32 v253, s82, v253\n"
"v_and_b32 v237, s82, v237\n"
"v_mfma_f32_16x16x32_bf16 v[136:139], a[100:103], a[20:23], v[136:139]\n"
"v_fmac_f32 v237, v253, v91\n"
"v_cvt_pk_bf16_f32 v89, v252, v237\n"
"v_mfma_f32_16x16x32_bf16 v[140:143], a[96:99], a[24:27], v[140:143]\n"
"buffer_load_dwordx4 a[92:95], v240, s[36:39], s80 offen offset:1024 sc1\n"
"v_accvgpr_read_b32 v253, a206\n"
"v_mfma_f32_16x16x32_bf16 v[140:143], a[100:103], a[28:31], v[140:143]\n"
"s_waitcnt lgkmcnt(5)\n"
"v_lshlrev_b32 v251, 16, v253\n"
"v_lshlrev_b32 v252, 16, v238\n"
"v_mfma_f32_16x16x32_bf16 v[144:147], a[96:99], a[32:35], v[144:147]\n"
"v_fmac_f32 v252, v251, v120\n"
"v_and_b32 v253, s82, v253\n"
"v_mfma_f32_16x16x32_bf16 v[144:147], a[100:103], a[36:39], v[144:147]\n"
"v_and_b32 v238, s82, v238\n"
"v_fmac_f32 v238, v253, v121\n"
"v_mfma_f32_16x16x32_bf16 v[148:151], a[96:99], a[40:43], v[148:151]\n"
"v_cvt_pk_bf16_f32 v90, v252, v238\n"
"v_accvgpr_read_b32 v253, a207\n"
"v_mfma_f32_16x16x32_bf16 v[148:151], a[100:103], a[44:47], v[148:151]\n"
"s_waitcnt lgkmcnt(1)\n"
"v_lshlrev_b32 v251, 16, v253\n"
"v_lshlrev_b32 v252, 16, v239\n"
"v_mfma_f32_16x16x32_bf16 v[152:155], a[96:99], a[48:51], v[152:155]\n"
"v_fmac_f32 v252, v251, v122\n"
"v_and_b32 v253, s82, v253\n"
"v_mfma_f32_16x16x32_bf16 v[152:155], a[100:103], a[52:55], v[152:155]\n"
"v_and_b32 v239, s82, v239\n"
"v_fmac_f32 v239, v253, v123\n"
"v_mfma_f32_16x16x32_bf16 v[156:159], a[96:99], a[56:59], v[156:159]\n"
"buffer_load_dwordx4 a[96:99], v240, s[36:39], s80 offen offset:2048 sc1\n"
"v_cvt_pk_bf16_f32 v91, v252, v239\n"
"v_mfma_f32_16x16x32_bf16 v[156:159], a[100:103], a[60:63], v[156:159]\n"
"s_waitcnt vmcnt(14)\n"
"buffer_store_dwordx4 v[88:91], v242, s[48:51], s74 offen offset:256 sc1\n"
"v_mfma_f32_16x16x32_bf16 v[160:163], a[104:107], a[0:3], v[160:163]\n"
"ds_read_b128 a[0:3], v245 offset:49152\n"
"v_mfma_f32_16x16x32_bf16 v[160:163], a[108:111], a[4:7], v[160:163]\n"
"ds_read_b128 a[4:7], v246 offset:49152\n"
"v_mfma_f32_16x16x32_bf16 v[164:167], a[104:107], a[8:11], v[164:167]\n"
"ds_read_b128 a[8:11], v245 offset:51200\n"
"v_mfma_f32_16x16x32_bf16 v[164:167], a[108:111], a[12:15], v[164:167]\n"
"ds_read_b128 a[12:15], v246 offset:51200\n"
"v_mfma_f32_16x16x32_bf16 v[168:171], a[104:107], a[16:19], v[168:171]\n"
"ds_read_b128 a[16:19], v245 offset:53248\n"
"v_mfma_f32_16x16x32_bf16 v[168:171], a[108:111], a[20:23], v[168:171]\n"
"ds_read_b128 a[20:23], v246 offset:53248\n"
"v_mfma_f32_16x16x32_bf16 v[172:175], a[104:107], a[24:27], v[172:175]\n"
"ds_read_b128 a[24:27], v245 offset:55296\n"
"buffer_load_dwordx4 a[100:103], v240, s[36:39], s80 offen offset:3072 sc1\n"
"v_mfma_f32_16x16x32_bf16 v[172:175], a[108:111], a[28:31], v[172:175]\n"
"s_add_u32 s80, s80, 0x1000\n"
"ds_read_b128 a[28:31], v246 offset:55296\n"
"v_mfma_f32_16x16x32_bf16 v[176:179], a[104:107], a[32:35], v[176:179]\n"
"ds_read_b128 a[32:35], v245 offset:57344\n"
"v_mfma_f32_16x16x32_bf16 v[176:179], a[108:111], a[36:39], v[176:179]\n"
"ds_read_b128 a[36:39], v246 offset:57344\n"
"v_mfma_f32_16x16x32_bf16 v[180:183], a[104:107], a[40:43], v[180:183]\n"
"ds_read_b128 a[40:43], v245 offset:59392\n"
"v_mfma_f32_16x16x32_bf16 v[180:183], a[108:111], a[44:47], v[180:183]\n"
"ds_read_b128 a[44:47], v246 offset:59392\n"
"v_mfma_f32_16x16x32_bf16 v[184:187], a[104:107], a[48:51], v[184:187]\n"
"ds_read_b128 a[48:51], v245 offset:61440\n"
"v_mfma_f32_16x16x32_bf16 v[184:187], a[108:111], a[52:55], v[184:187]\n"
"ds_read_b128 a[52:55], v246 offset:61440\n"
"v_mfma_f32_16x16x32_bf16 v[188:191], a[104:107], a[56:59], v[188:191]\n"
"ds_read_b128 a[56:59], v245 offset:63488\n"
"buffer_load_dwordx4 a[104:107], v240, s[36:39], s80 offen offset:0 sc1\n"
"v_mfma_f32_16x16x32_bf16 v[188:191], a[108:111], a[60:63], v[188:191]\n"
"s_waitcnt vmcnt(15) lgkmcnt(11)\n"
"ds_read_b128 a[60:63], v246 offset:63488\n"
"v_mfma_f32_16x16x32_bf16 v[128:131], v[224:227], a[0:3], v[128:131]\n"
"v_mfma_f32_16x16x32_bf16 v[128:131], v[228:231], a[4:7], v[128:131]\n"
"v_mfma_f32_16x16x32_bf16 v[132:135], v[224:227], a[8:11], v[132:135]\n"
"v_mfma_f32_16x16x32_bf16 v[132:135], v[228:231], a[12:15], v[132:135]\n"
"s_waitcnt lgkmcnt(8)\n"
"v_mfma_f32_16x16x32_bf16 v[136:139], v[224:227], a[16:19], v[136:139]\n"
"v_mfma_f32_16x16x32_bf16 v[136:139], v[228:231], a[20:23], v[136:139]\n"
"v_mfma_f32_16x16x32_bf16 v[140:143], v[224:227], a[24:27], v[140:143]\n"
"buffer_load_dwordx4 a[108:111], v240, s[36:39], s80 offen offset:1024 sc1\n"
"v_mfma_f32_16x16x32_bf16 v[140:143], v[228:231], a[28:31], v[140:143]\n"
"s_waitcnt lgkmcnt(4)\n"
"v_mfma_f32_16x16x32_bf16 v[144:147], v[224:227], a[32:35], v[144:147]\n"
"v_mfma_f32_16x16x32_bf16 v[144:147], v[228:231], a[36:39], v[144:147]\n"
"v_mfma_f32_16x16x32_bf16 v[148:151], v[224:227], a[40:43], v[148:151]\n"
"v_mfma_f32_16x16x32_bf16 v[148:151], v[228:231], a[44:47], v[148:151]\n"
"s_waitcnt lgkmcnt(0)\n"
"v_mfma_f32_16x16x32_bf16 v[152:155], v[224:227], a[48:51], v[152:155]\n"
"v_mfma_f32_16x16x32_bf16 v[152:155], v[228:231], a[52:55], v[152:155]\n"
"v_mfma_f32_16x16x32_bf16 v[156:159], v[224:227], a[56:59], v[156:159]\n"
"buffer_load_dwordx4 v[224:227], v240, s[36:39], s80 offen offset:2048 sc1\n"
"v_mfma_f32_16x16x32_bf16 v[156:159], v[228:231], a[60:63], v[156:159]\n"
"s_waitcnt vmcnt(14)\n"
"v_mfma_f32_16x16x32_bf16 v[160:163], a[64:67], a[0:3], v[160:163]\n"
"v_mfma_f32_16x16x32_bf16 v[160:163], a[68:71], a[4:7], v[160:163]\n"
"v_mfma_f32_16x16x32_bf16 v[164:167], a[64:67], a[8:11], v[164:167]\n"
"v_mfma_f32_16x16x32_bf16 v[164:167], a[68:71], a[12:15], v[164:167]\n"
"v_mfma_f32_16x16x32_bf16 v[168:171], a[64:67], a[16:19], v[168:171]\n"
"v_mfma_f32_16x16x32_bf16 v[168:171], a[68:71], a[20:23], v[168:171]\n"
"v_mfma_f32_16x16x32_bf16 v[172:175], a[64:67], a[24:27], v[172:175]\n"
"buffer_load_dwordx4 v[228:231], v240, s[36:39], s80 offen offset:3072 sc1\n"
"v_mfma_f32_16x16x32_bf16 v[172:175], a[68:71], a[28:31], v[172:175]\n"
"s_add_u32 s80, s80, 0x1000\n"
"v_mfma_f32_16x16x32_bf16 v[176:179], a[64:67], a[32:35], v[176:179]\n"
"v_mfma_f32_16x16x32_bf16 v[176:179], a[68:71], a[36:39], v[176:179]\n"
"v_mfma_f32_16x16x32_bf16 v[180:183], a[64:67], a[40:43], v[180:183]\n"
"v_mfma_f32_16x16x32_bf16 v[180:183], a[68:71], a[44:47], v[180:183]\n"
"v_mfma_f32_16x16x32_bf16 v[184:187], a[64:67], a[48:51], v[184:187]\n"
"v_mfma_f32_16x16x32_bf16 v[184:187], a[68:71], a[52:55], v[184:187]\n"
"v_mfma_f32_16x16x32_bf16 v[188:191], a[64:67], a[56:59], v[188:191]\n"
"buffer_load_dwordx4 a[64:67], v240, s[36:39], s80 offen offset:0 sc1\n"
"v_mfma_f32_16x16x32_bf16 v[188:191], a[68:71], a[60:63], v[188:191]\n"
"s_waitcnt vmcnt(13)\n"
"v_mfma_f32_16x16x32_bf16 v[192:195], a[72:75], a[0:3], v[192:195]\n"
"v_mfma_f32_16x16x32_bf16 v[192:195], a[76:79], a[4:7], v[192:195]\n"
"v_mfma_f32_16x16x32_bf16 v[196:199], a[72:75], a[8:11], v[196:199]\n"
"v_mfma_f32_16x16x32_bf16 v[196:199], a[76:79], a[12:15], v[196:199]\n"
"v_mfma_f32_16x16x32_bf16 v[200:203], a[72:75], a[16:19], v[200:203]\n"
"ds_read_b128 v[236:239], v250 offset:0\n"
"v_accvgpr_read_b32 v253, a208\n"
"v_mfma_f32_16x16x32_bf16 v[200:203], a[76:79], a[20:23], v[200:203]\n"
"v_lshlrev_b32 v251, 16, v253\n"
"v_lshlrev_b32 v252, 16, v232\n"
"v_mfma_f32_16x16x32_bf16 v[204:207], a[72:75], a[24:27], v[204:207]\n"
"buffer_load_dwordx4 a[68:71], v240, s[36:39], s80 offen offset:1024 sc1\n"
"v_fmac_f32 v252, v251, v156\n"
"v_mfma_f32_16x16x32_bf16 v[204:207], a[76:79], a[28:31], v[204:207]\n"
"v_and_b32 v253, s82, v253\n"
"v_and_b32 v232, s82, v232\n"
"v_mfma_f32_16x16x32_bf16 v[208:211], a[72:75], a[32:35], v[208:211]\n"
"v_fmac_f32 v232, v253, v157\n"
"v_cvt_pk_bf16_f32 v156, v252, v232\n"
"v_mfma_f32_16x16x32_bf16 v[208:211], a[76:79], a[36:39], v[208:211]\n"
"v_accvgpr_read_b32 v253, a209\n"
"v_lshlrev_b32 v251, 16, v253\n"
"v_mfma_f32_16x16x32_bf16 v[212:215], a[72:75], a[40:43], v[212:215]\n"
"v_lshlrev_b32 v252, 16, v233\n"
"v_fmac_f32 v252, v251, v158\n"
"v_mfma_f32_16x16x32_bf16 v[212:215], a[76:79], a[44:47], v[212:215]\n"
"v_and_b32 v253, s82, v253\n"
"v_and_b32 v233, s82, v233\n"
"v_mfma_f32_16x16x32_bf16 v[216:219], a[72:75], a[48:51], v[216:219]\n"
"v_fmac_f32 v233, v253, v159\n"
"v_cvt_pk_bf16_f32 v157, v252, v233\n"
"v_mfma_f32_16x16x32_bf16 v[216:219], a[76:79], a[52:55], v[216:219]\n"
"v_accvgpr_read_b32 v253, a210\n"
"v_lshlrev_b32 v251, 16, v253\n"
"v_mfma_f32_16x16x32_bf16 v[220:223], a[72:75], a[56:59], v[220:223]\n"
"buffer_load_dwordx4 a[72:75], v240, s[36:39], s80 offen offset:2048 sc1\n"
"v_lshlrev_b32 v252, 16, v234\n"
"v_mfma_f32_16x16x32_bf16 v[220:223], a[76:79], a[60:63], v[220:223]\n"
"s_waitcnt vmcnt(13)\n"
"v_fmac_f32 v252, v251, v188\n"
"v_and_b32 v253, s82, v253\n"
"v_mfma_f32_16x16x32_bf16 a[112:115], a[80:83], a[0:3], a[112:115]\n"
".Lpad21_%=:\n"
"s_cbranch_execz .Lret2_%=\n"
"ds_read_b128 a[0:3], v243 offset:32768\n"
"v_and_b32 v234, s82, v234\n"
"v_mfma_f32_16x16x32_bf16 a[112:115], a[84:87], a[4:7], a[112:115]\n"
"ds_read_b128 a[4:7], v244 offset:32768\n"
"v_fmac_f32 v234, v253, v189\n"
"v_mfma_f32_16x16x32_bf16 a[116:119], a[80:83], a[8:11], a[116:119]\n"
"ds_read_b128 a[8:11], v243 offset:34816\n"
"v_cvt_pk_bf16_f32 v158, v252, v234\n"
"v_mfma_f32_16x16x32_bf16 a[116:119], a[84:87], a[12:15], a[116:119]\n"
"ds_read_b128 a[12:15], v244 offset:34816\n"
"v_accvgpr_read_b32 v253, a211\n"
"v_mfma_f32_16x16x32_bf16 a[120:123], a[80:83], a[16:19], a[120:123]\n"
"ds_read_b128 a[16:19], v243 offset:36864\n"
"v_lshlrev_b32 v251, 16, v253\n"
"v_mfma_f32_16x16x32_bf16 a[120:123], a[84:87], a[20:23], a[120:123]\n"
"ds_read_b128 a[20:23], v244 offset:36864\n"
"v_lshlrev_b32 v252, 16, v235\n"
"v_mfma_f32_16x16x32_bf16 a[124:127], a[80:83], a[24:27], a[124:127]\n"
"ds_read_b128 a[24:27], v243 offset:38912\n"
"buffer_load_dwordx4 a[76:79], v240, s[36:39], s80 offen offset:3072 sc1\n"
"v_mfma_f32_16x16x32_bf16 a[124:127], a[84:87], a[28:31], a[124:127]\n"
"s_add_u32 s80, s80, 0x1000\n"
"ds_read_b128 a[28:31], v244 offset:38912\n"
"v_fmac_f32 v252, v251, v190\n"
"v_mfma_f32_16x16x32_bf16 a[128:131], a[80:83], a[32:35], a[128:131]\n"
"ds_read_b128 a[32:35], v243 offset:40960\n"
"v_and_b32 v253, s82, v253\n"
"v_mfma_f32_16x16x32_bf16 a[128:131], a[84:87], a[36:39], a[128:131]\n"
"ds_read_b128 a[36:39], v244 offset:40960\n"
"v_and_b32 v235, s82, v235\n"
"v_mfma_f32_16x16x32_bf16 a[132:135], a[80:83], a[40:43], a[132:135]\n"
"ds_read_b128 a[40:43], v243 offset:43008\n"
"v_fmac_f32 v235, v253, v191\n"
"v_mfma_f32_16x16x32_bf16 a[132:135], a[84:87], a[44:47], a[132:135]\n"
"ds_read_b128 a[44:47], v244 offset:43008\n"
"v_cvt_pk_bf16_f32 v159, v252, v235\n"
"v_mfma_f32_16x16x32_bf16 a[136:139], a[80:83], a[48:51], a[136:139]\n"
"s_waitcnt lgkmcnt(12)\n"
"ds_read_b128 a[48:51], v243 offset:45056\n"
"v_accvgpr_read_b32 v253, a212\n"
"v_mfma_f32_16x16x32_bf16 a[136:139], a[84:87], a[52:55], a[136:139]\n"
"ds_read_b128 a[52:55], v244 offset:45056\n"
"v_lshlrev_b32 v251, 16, v253\n"
"v_mfma_f32_16x16x32_bf16 a[140:143], a[80:83], a[56:59], a[140:143]\n"
"ds_read_b128 a[56:59], v243 offset:47104\n"
"buffer_load_dwordx4 a[80:83], v240, s[36:39], s80 offen offset:0 sc1\n"
"v_mfma_f32_16x16x32_bf16 a[140:143], a[84:87], a[60:63], a[140:143]\n"
"s_waitcnt vmcnt(12) lgkmcnt(11)\n"
"ds_read_b128 a[60:63], v244 offset:47104\n"
"v_lshlrev_b32 v252, 16, v236\n"
"v_mfma_f32_16x16x32_bf16 v[192:195], a[88:91], a[0:3], v[192:195]\n"
"buffer_store_dwordx4 v[156:159], v242, s[48:51], s75 offen offset:512 sc1\n"
"v_mfma_f32_16x16x32_bf16 v[192:195], a[92:95], a[4:7], v[192:195]\n"
"ds_read_b128 v[232:235], v250 offset:2048\n"
"v_fmac_f32 v252, v251, v128\n"
"v_mfma_f32_16x16x32_bf16 v[196:199], a[88:91], a[8:11], v[196:199]\n"
"v_and_b32 v253, s82, v253\n"
"v_and_b32 v236, s82, v236\n"
"v_mfma_f32_16x16x32_bf16 v[196:199], a[92:95], a[12:15], v[196:199]\n"
"s_waitcnt lgkmcnt(9)\n"
"v_fmac_f32 v236, v253, v129\n"
"v_cvt_pk_bf16_f32 v128, v252, v236\n"
"v_mfma_f32_16x16x32_bf16 v[200:203], a[88:91], a[16:19], v[200:203]\n"
"v_accvgpr_read_b32 v253, a213\n"
"v_lshlrev_b32 v251, 16, v253\n"
"v_mfma_f32_16x16x32_bf16 v[200:203], a[92:95], a[20:23], v[200:203]\n"
"v_lshlrev_b32 v252, 16, v237\n"
"v_fmac_f32 v252, v251, v130\n"
"v_mfma_f32_16x16x32_bf16 v[204:207], a[88:91], a[24:27], v[204:207]\n"
"buffer_load_dwordx4 a[84:87], v240, s[36:39], s80 offen offset:1024 sc1\n"
"v_and_b32 v253, s82, v253\n"
"v_mfma_f32_16x16x32_bf16 v[204:207], a[92:95], a[28:31], v[204:207]\n"
"s_waitcnt lgkmcnt(5)\n"
"v_and_b32 v237, s82, v237\n"
"v_fmac_f32 v237, v253, v131\n"
"v_mfma_f32_16x16x32_bf16 v[208:211], a[88:91], a[32:35], v[208:211]\n"
"v_cvt_pk_bf16_f32 v129, v252, v237\n"
"v_accvgpr_read_b32 v253, a214\n"
"v_mfma_f32_16x16x32_bf16 v[208:211], a[92:95], a[36:39], v[208:211]\n"
"v_lshlrev_b32 v251, 16, v253\n"
"v_lshlrev_b32 v252, 16, v238\n"
"v_mfma_f32_16x16x32_bf16 v[212:215], a[88:91], a[40:43], v[212:215]\n"
"v_fmac_f32 v252, v251, v160\n"
"v_and_b32 v253, s82, v253\n"
"v_mfma_f32_16x16x32_bf16 v[212:215], a[92:95], a[44:47], v[212:215]\n"
"s_waitcnt lgkmcnt(1)\n"
"v_and_b32 v238, s82, v238\n"
"v_fmac_f32 v238, v253, v161\n"
"v_mfma_f32_16x16x32_bf16 v[216:219], a[88:91], a[48:51], v[216:219]\n"
"v_cvt_pk_bf16_f32 v130, v252, v238\n"
"v_accvgpr_read_b32 v253, a215\n"
"v_mfma_f32_16x16x32_bf16 v[216:219], a[92:95], a[52:55], v[216:219]\n"
"v_lshlrev_b32 v251, 16, v253\n"
"v_lshlrev_b32 v252, 16, v239\n"
"v_mfma_f32_16x16x32_bf16 v[220:223], a[88:91], a[56:59], v[220:223]\n"
"buffer_load_dwordx4 a[88:91], v240, s[36:39], s80 offen offset:2048 sc1\n"
"v_fmac_f32 v252, v251, v162\n"
"v_mfma_f32_16x16x32_bf16 v[220:223], a[92:95], a[60:63], v[220:223]\n"
"s_waitcnt vmcnt(12)\n"
"v_and_b32 v253, s82, v253\n"
"v_and_b32 v239, s82, v239\n"
"v_mfma_f32_16x16x32_bf16 a[112:115], a[96:99], a[0:3], a[112:115]\n"
"ds_read_b128 a[0:3], v243 offset:49152\n"
"v_fmac_f32 v239, v253, v163\n"
"v_mfma_f32_16x16x32_bf16 a[112:115], a[100:103], a[4:7], a[112:115]\n"
"ds_read_b128 a[4:7], v244 offset:49152\n"
"v_cvt_pk_bf16_f32 v131, v252, v239\n"
"v_mfma_f32_16x16x32_bf16 a[116:119], a[96:99], a[8:11], a[116:119]\n"
"s_waitcnt lgkmcnt(2)\n"
"ds_read_b128 a[8:11], v243 offset:51200\n"
"v_accvgpr_read_b32 v253, a216\n"
"v_mfma_f32_16x16x32_bf16 a[116:119], a[100:103], a[12:15], a[116:119]\n"
"ds_read_b128 a[12:15], v244 offset:51200\n"
"v_lshlrev_b32 v251, 16, v253\n"
"v_mfma_f32_16x16x32_bf16 a[120:123], a[96:99], a[16:19], a[120:123]\n"
"ds_read_b128 a[16:19], v243 offset:53248\n"
"v_lshlrev_b32 v252, 16, v232\n"
"v_mfma_f32_16x16x32_bf16 a[120:123], a[100:103], a[20:23], a[120:123]\n"
"ds_read_b128 a[20:23], v244 offset:53248\n"
"v_fmac_f32 v252, v251, v132\n"
"v_mfma_f32_16x16x32_bf16 a[124:127], a[96:99], a[24:27], a[124:127]\n"
"ds_read_b128 a[24:27], v243 offset:55296\n"
"buffer_load_dwordx4 a[92:95], v240, s[36:39], s80 offen offset:3072 sc1\n"
"v_mfma_f32_16x16x32_bf16 a[124:127], a[100:103], a[28:31], a[124:127]\n"
"s_add_u32 s80, s80, 0x1000\n"
"ds_read_b128 a[28:31], v244 offset:55296\n"
"v_and_b32 v253, s82, v253\n"
"v_mfma_f32_16x16x32_bf16 a[128:131], a[96:99], a[32:35], a[128:131]\n"
"ds_read_b128 a[32:35], v243 offset:57344\n"
"v_and_b32 v232, s82, v232\n"
"v_mfma_f32_16x16x32_bf16 a[128:131], a[100:103], a[36:39], a[128:131]\n"
"ds_read_b128 a[36:39], v244 offset:57344\n"
"v_fmac_f32 v232, v253, v133\n"
"v_mfma_f32_16x16x32_bf16 a[132:135], a[96:99], a[40:43], a[132:135]\n"
"ds_read_b128 a[40:43], v243 offset:59392\n"
"v_cvt_pk_bf16_f32 v132, v252, v232\n"
"v_mfma_f32_16x16x32_bf16 a[132:135], a[100:103], a[44:47], a[132:135]\n"
"ds_read_b128 a[44:47], v244 offset:59392\n"
"v_accvgpr_read_b32 v253, a217\n"
"v_mfma_f32_16x16x32_bf16 a[136:139], a[96:99], a[48:51], a[136:139]\n"
"ds_read_b128 a[48:51], v243 offset:61440\n"
"v_lshlrev_b32 v251, 16, v253\n"
"v_mfma_f32_16x16x32_bf16 a[136:139], a[100:103], a[52:55], a[136:139]\n"
"ds_read_b128 a[52:55], v244 offset:61440\n"
"v_lshlrev_b32 v252, 16, v233\n"
"v_mfma_f32_16x16x32_bf16 a[140:143], a[96:99], a[56:59], a[140:143]\n"
"ds_read_b128 a[56:59], v243 offset:63488\n"
"buffer_load_dwordx4 a[96:99], v240, s[36:39], s80 offen offset:0 sc1\n"
"v_mfma_f32_16x16x32_bf16 a[140:143], a[100:103], a[60:63], a[140:143]\n"
"s_waitcnt vmcnt(12) lgkmcnt(11)\n"
"ds_read_b128 a[60:63], v244 offset:63488\n"
"v_fmac_f32 v252, v251, v134\n"
"v_mfma_f32_16x16x32_bf16 v[192:195], a[104:107], a[0:3], v[192:195]\n"
"buffer_store_dwordx4 v[128:131], v242, s[48:51], s68 offen offset:512 sc1\n"
"v_mfma_f32_16x16x32_bf16 v[192:195], a[108:111], a[4:7], v[192:195]\n"
"ds_read_b128 v[236:239], v250 offset:4096\n"
"v_and_b32 v253, s82, v253\n"
"v_mfma_f32_16x16x32_bf16 v[196:199], a[104:107], a[8:11], v[196:199]\n"
"ds_read_b128 v[160:163], v254 offset:0\n"
"v_and_b32 v233, s82, v233\n"
"v_mfma_f32_16x16x32_bf16 v[196:199], a[108:111], a[12:15], v[196:199]\n"
"s_waitcnt lgkmcnt(10)\n"
"v_fmac_f32 v233, v253, v135\n"
"v_cvt_pk_bf16_f32 v133, v252, v233\n"
"v_mfma_f32_16x16x32_bf16 v[200:203], a[104:107], a[16:19], v[200:203]\n"
"v_accvgpr_read_b32 v253, a218\n"
"v_lshlrev_b32 v251, 16, v253\n"
"v_mfma_f32_16x16x32_bf16 v[200:203], a[108:111], a[20:23], v[200:203]\n"
"v_lshlrev_b32 v252, 16, v234\n"
"v_fmac_f32 v252, v251, v164\n"
"v_mfma_f32_16x16x32_bf16 v[204:207], a[104:107], a[24:27], v[204:207]\n"
"buffer_load_dwordx4 a[100:103], v240, s[36:39], s80 offen offset:1024 sc1\n"
"v_and_b32 v253, s82, v253\n"
"v_mfma_f32_16x16x32_bf16 v[204:207], a[108:111], a[28:31], v[204:207]\n"
"s_waitcnt lgkmcnt(6)\n"
"v_and_b32 v234, s82, v234\n"
"v_fmac_f32 v234, v253, v165\n"
"v_mfma_f32_16x16x32_bf16 v[208:211], a[104:107], a[32:35], v[208:211]\n"
"v_cvt_pk_bf16_f32 v134, v252, v234\n"
"v_accvgpr_read_b32 v253, a219\n"
"v_mfma_f32_16x16x32_bf16 v[208:211], a[108:111], a[36:39], v[208:211]\n"
"v_lshlrev_b32 v251, 16, v253\n"
"v_lshlrev_b32 v252, 16, v235\n"
"v_mfma_f32_16x16x32_bf16 v[212:215], a[104:107], a[40:43], v[212:215]\n"
"v_fmac_f32 v252, v251, v166\n"
"v_and_b32 v253, s82, v253\n"
"v_mfma_f32_16x16x32_bf16 v[212:215], a[108:111], a[44:47], v[212:215]\n"
"s_waitcnt lgkmcnt(2)\n"
"v_and_b32 v235, s82, v235\n"
"v_fmac_f32 v235, v253, v167\n"
"v_mfma_f32_16x16x32_bf16 v[216:219], a[104:107], a[48:51], v[216:219]\n"
"ds_read_b128 v[164:167], v254 offset:1024\n"
"v_cvt_pk_bf16_f32 v135, v252, v235\n"
"v_mfma_f32_16x16x32_bf16 v[216:219], a[108:111], a[52:55], v[216:219]\n"
"s_waitcnt lgkmcnt(1)\n"
"ds_read_b128 v[232:235], v250 offset:6144\n"
"v_lshlrev_b32 v251, 16, v160\n"
"v_mfma_f32_16x16x32_bf16 v[220:223], a[104:107], a[56:59], v[220:223]\n"
"buffer_load_dwordx4 a[104:107], v240, s[36:39], s80 offen offset:2048 sc1\n"
"v_lshlrev_b32 v252, 16, v236\n"
"v_mfma_f32_16x16x32_bf16 v[220:223], a[108:111], a[60:63], v[220:223]\n"
"s_waitcnt vmcnt(13)\n"
"buffer_store_dwordx4 v[132:135], v242, s[48:51], s69 offen offset:512 sc1\n"
"v_mfma_f32_16x16x32_bf16 a[112:115], v[224:227], a[0:3], a[112:115]\n"
"ds_read_b128 a[0:3], v245 offset:0\n"
"v_fmac_f32 v252, v251, v136\n"
"v_mfma_f32_16x16x32_bf16 a[112:115], v[228:231], a[4:7], a[112:115]\n"
"ds_read_b128 a[4:7], v246 offset:0\n"
"v_and_b32 v160, s82, v160\n"
"v_mfma_f32_16x16x32_bf16 a[116:119], v[224:227], a[8:11], a[116:119]\n"
"ds_read_b128 a[8:11], v245 offset:2048\n"
"v_and_b32 v236, s82, v236\n"
"v_mfma_f32_16x16x32_bf16 a[116:119], v[228:231], a[12:15], a[116:119]\n"
"ds_read_b128 a[12:15], v246 offset:2048\n"
"v_fmac_f32 v236, v160, v137\n"
"v_mfma_f32_16x16x32_bf16 a[120:123], v[224:227], a[16:19], a[120:123]\n"
"ds_read_b128 a[16:19], v245 offset:4096\n"
"v_cvt_pk_bf16_f32 v136, v252, v236\n"
"v_mfma_f32_16x16x32_bf16 a[120:123], v[228:231], a[20:23], a[120:123]\n"
"ds_read_b128 a[20:23], v246 offset:4096\n"
"v_lshlrev_b32 v251, 16, v161\n"
"v_mfma_f32_16x16x32_bf16 a[124:127], v[224:227], a[24:27], a[124:127]\n"
"ds_read_b128 a[24:27], v245 offset:6144\n"
"buffer_load_dwordx4 a[108:111], v240, s[36:39], s80 offen offset:3072 sc1\n"
"v_mfma_f32_16x16x32_bf16 a[124:127], v[228:231], a[28:31], a[124:127]\n"
"s_add_u32 s80, s80, 0x1000\n"
"ds_read_b128 a[28:31], v246 offset:6144\n"
"v_lshlrev_b32 v252, 16, v237\n"
"v_mfma_f32_16x16x32_bf16 a[128:131], v[224:227], a[32:35], a[128:131]\n"
"ds_read_b128 a[32:35], v245 offset:8192\n"
"v_fmac_f32 v252, v251, v138\n"
"v_mfma_f32_16x16x32_bf16 a[128:131], v[228:231], a[36:39], a[128:131]\n"
"ds_read_b128 a[36:39], v246 offset:8192\n"
"v_and_b32 v161, s82, v161\n"
"v_mfma_f32_16x16x32_bf16 a[132:135], v[224:227], a[40:43], a[132:135]\n"
"ds_read_b128 a[40:43], v245 offset:10240\n"
"v_and_b32 v237, s82, v237\n"
"v_mfma_f32_16x16x32_bf16 a[132:135], v[228:231], a[44:47], a[132:135]\n"
"ds_read_b128 a[44:47], v246 offset:10240\n"
"v_fmac_f32 v237, v161, v139\n"
"v_mfma_f32_16x16x32_bf16 a[136:139], v[224:227], a[48:51], a[136:139]\n"
"ds_read_b128 a[48:51], v245 offset:12288\n"
"v_cvt_pk_bf16_f32 v137, v252, v237\n"
"v_mfma_f32_16x16x32_bf16 a[136:139], v[228:231], a[52:55], a[136:139]\n"
"ds_read_b128 a[52:55], v246 offset:12288\n"
"v_lshlrev_b32 v251, 16, v162\n"
"v_mfma_f32_16x16x32_bf16 a[140:143], v[224:227], a[56:59], a[140:143]\n"
"ds_read_b128 a[56:59], v245 offset:14336\n"
"v_lshlrev_b32 v252, 16, v238\n"
"v_mfma_f32_16x16x32_bf16 a[140:143], v[228:231], a[60:63], a[140:143]\n"
"s_waitcnt vmcnt(13) lgkmcnt(11)\n"
"ds_read_b128 a[60:63], v246 offset:14336\n"
"v_fmac_f32 v252, v251, v168\n"
"v_mfma_f32_16x16x32_bf16 v[192:195], a[64:67], a[0:3], v[192:195]\n"
"v_and_b32 v162, s82, v162\n"
"v_and_b32 v238, s82, v238\n"
"v_mfma_f32_16x16x32_bf16 v[192:195], a[68:71], a[4:7], v[192:195]\n"
"v_fmac_f32 v238, v162, v169\n"
"v_cvt_pk_bf16_f32 v138, v252, v238\n"
"v_mfma_f32_16x16x32_bf16 v[196:199], a[64:67], a[8:11], v[196:199]\n"
"v_lshlrev_b32 v251, 16, v163\n"
"v_lshlrev_b32 v252, 16, v239\n"
"v_mfma_f32_16x16x32_bf16 v[196:199], a[68:71], a[12:15], v[196:199]\n"
"s_waitcnt lgkmcnt(8)\n"
"v_fmac_f32 v252, v251, v170\n"
"v_and_b32 v163, s82, v163\n"
"v_mfma_f32_16x16x32_bf16 v[200:203], a[64:67], a[16:19], v[200:203]\n"
"v_and_b32 v239, s82, v239\n"
"v_fmac_f32 v239, v163, v171\n"
"v_mfma_f32_16x16x32_bf16 v[200:203], a[68:71], a[20:23], v[200:203]\n"
"ds_read_b128 v[168:171], v254 offset:2048\n"
"v_cvt_pk_bf16_f32 v139, v252, v239\n"
"v_mfma_f32_16x16x32_bf16 v[204:207], a[64:67], a[24:27], v[204:207]\n"
"buffer_store_dwordx4 v[136:139], v242, s[48:51], s70 offen offset:512 sc1\n"
"v_mfma_f32_16x16x32_bf16 v[204:207], a[68:71], a[28:31], v[204:207]\n"
"s_waitcnt lgkmcnt(5)\n"
"ds_read_b128 v[236:239], v250 offset:8192\n"
"v_lshlrev_b32 v251, 16, v164\n"
"v_mfma_f32_16x16x32_bf16 v[208:211], a[64:67], a[32:35], v[208:211]\n"
"v_lshlrev_b32 v252, 16, v232\n"
"v_fmac_f32 v252, v251, v140\n"
"v_mfma_f32_16x16x32_bf16 v[208:211], a[68:71], a[36:39], v[208:211]\n"
"v_and_b32 v164, s82, v164\n"
"v_and_b32 v232, s82, v232\n"
"v_mfma_f32_16x16x32_bf16 v[212:215], a[64:67], a[40:43], v[212:215]\n"
"v_fmac_f32 v232, v164, v141\n"
"v_cvt_pk_bf16_f32 v140, v252, v232\n"
"v_mfma_f32_16x16x32_bf16 v[212:215], a[68:71], a[44:47], v[212:215]\n"
"s_waitcnt lgkmcnt(2)\n"
"v_lshlrev_b32 v251, 16, v165\n"
"v_lshlrev_b32 v252, 16, v233\n"
"v_mfma_f32_16x16x32_bf16 v[216:219], a[64:67], a[48:51], v[216:219]\n"
"v_fmac_f32 v252, v251, v142\n"
"v_and_b32 v165, s82, v165\n"
"v_mfma_f32_16x16x32_bf16 v[216:219], a[68:71], a[52:55], v[216:219]\n"
"v_and_b32 v233, s82, v233\n"
"v_fmac_f32 v233, v165, v143\n"
"v_mfma_f32_16x16x32_bf16 v[220:223], a[64:67], a[56:59], v[220:223]\n"
"v_cvt_pk_bf16_f32 v141, v252, v233\n"
"v_lshlrev_b32 v251, 16, v166\n"
"v_mfma_f32_16x16x32_bf16 v[220:223], a[68:71], a[60:63], v[220:223]\n"
"s_waitcnt vmcnt(12)\n"
"v_lshlrev_b32 v252, 16, v234\n"
"v_fmac_f32 v252, v251, v172\n"
"v_mfma_f32_16x16x32_bf16 a[112:115], a[72:75], a[0:3], a[112:115]\n"
"ds_read_b128 a[0:3], v245 offset:16384\n"
"v_and_b32 v166, s82, v166\n"
"v_mfma_f32_16x16x32_bf16 a[112:115], a[76:79], a[4:7], a[112:115]\n"
"ds_read_b128 a[4:7], v246 offset:16384\n"
"v_and_b32 v234, s82, v234\n"
"v_mfma_f32_16x16x32_bf16 a[116:119], a[72:75], a[8:11], a[116:119]\n"
"ds_read_b128 a[8:11], v245 offset:18432\n"
"v_fmac_f32 v234, v166, v173\n"
"v_mfma_f32_16x16x32_bf16 a[116:119], a[76:79], a[12:15], a[116:119]\n"
"ds_read_b128 a[12:15], v246 offset:18432\n"
"v_cvt_pk_bf16_f32 v142, v252, v234\n"
"v_mfma_f32_16x16x32_bf16 a[120:123], a[72:75], a[16:19], a[120:123]\n"
"ds_read_b128 a[16:19], v245 offset:20480\n"
"v_lshlrev_b32 v251, 16, v167\n"
"v_mfma_f32_16x16x32_bf16 a[120:123], a[76:79], a[20:23], a[120:123]\n"
"ds_read_b128 a[20:23], v246 offset:20480\n"
"v_lshlrev_b32 v252, 16, v235\n"
"v_mfma_f32_16x16x32_bf16 a[124:127], a[72:75], a[24:27], a[124:127]\n"
"ds_read_b128 a[24:27], v245 offset:22528\n"
"v_fmac_f32 v252, v251, v174\n"
"v_mfma_f32_16x16x32_bf16 a[124:127], a[76:79], a[28:31], a[124:127]\n"
"ds_read_b128 a[28:31], v246 offset:22528\n"
"v_and_b32 v167, s82, v167\n"
"v_mfma_f32_16x16x32_bf16 a[128:131], a[72:75], a[32:35], a[128:131]\n"
"ds_read_b128 a[32:35], v245 offset:24576\n"
"v_and_b32 v235, s82, v235\n"
"v_mfma_f32_16x16x32_bf16 a[128:131], a[76:79], a[36:39], a[128:131]\n"
"ds_read_b128 a[36:39], v246 offset:24576\n"
"v_fmac_f32 v235, v167, v175\n"
"v_mfma_f32_16x16x32_bf16 a[132:135], a[72:75], a[40:43], a[132:135]\n"
"ds_read_b128 a[40:43], v245 offset:26624\n"
"v_cvt_pk_bf16_f32 v143, v252, v235\n"
"v_mfma_f32_16x16x32_bf16 a[132:135], a[76:79], a[44:47], a[132:135]\n"
"s_waitcnt lgkmcnt(11)\n"
"ds_read_b128 a[44:47], v246 offset:26624\n"
"v_lshlrev_b32 v251, 16, v168\n"
"v_mfma_f32_16x16x32_bf16 a[136:139], a[72:75], a[48:51], a[136:139]\n"
"ds_read_b128 a[48:51], v245 offset:28672\n"
"v_lshlrev_b32 v252, 16, v236\n"
"v_mfma_f32_16x16x32_bf16 a[136:139], a[76:79], a[52:55], a[136:139]\n"
"ds_read_b128 a[52:55], v246 offset:28672\n"
"v_fmac_f32 v252, v251, v144\n"
"v_mfma_f32_16x16x32_bf16 a[140:143], a[72:75], a[56:59], a[140:143]\n"
"ds_read_b128 a[56:59], v245 offset:30720\n"
"v_and_b32 v168, s82, v168\n"
"v_mfma_f32_16x16x32_bf16 a[140:143], a[76:79], a[60:63], a[140:143]\n"
"s_waitcnt vmcnt(9) lgkmcnt(11)\n"
"ds_read_b128 a[60:63], v246 offset:30720\n"
"v_and_b32 v236, s82, v236\n"
"v_mfma_f32_16x16x32_bf16 v[192:195], a[80:83], a[0:3], v[192:195]\n"
"buffer_store_dwordx4 v[140:143], v242, s[48:51], s71 offen offset:512 sc1\n"
"v_mfma_f32_16x16x32_bf16 v[192:195], a[84:87], a[4:7], v[192:195]\n"
"ds_read_b128 v[232:235], v250 offset:10240\n"
"v_fmac_f32 v236, v168, v145\n"
"v_mfma_f32_16x16x32_bf16 v[196:199], a[80:83], a[8:11], v[196:199]\n"
"ds_read_b128 v[172:175], v254 offset:3072\n"
"v_cvt_pk_bf16_f32 v144, v252, v236\n"
"v_mfma_f32_16x16x32_bf16 v[196:199], a[84:87], a[12:15], v[196:199]\n"
"s_waitcnt lgkmcnt(10)\n"
"v_lshlrev_b32 v251, 16, v169\n"
"v_lshlrev_b32 v252, 16, v237\n"
"v_mfma_f32_16x16x32_bf16 v[200:203], a[80:83], a[16:19], v[200:203]\n"
"v_fmac_f32 v252, v251, v146\n"
"v_and_b32 v169, s82, v169\n"
"v_mfma_f32_16x16x32_bf16 v[200:203], a[84:87], a[20:23], v[200:203]\n"
"v_and_b32 v237, s82, v237\n"
"v_fmac_f32 v237, v169, v147\n"
"v_mfma_f32_16x16x32_bf16 v[204:207], a[80:83], a[24:27], v[204:207]\n"
"v_cvt_pk_bf16_f32 v145, v252, v237\n"
"v_lshlrev_b32 v251, 16, v170\n"
"v_mfma_f32_16x16x32_bf16 v[204:207], a[84:87], a[28:31], v[204:207]\n"
"s_waitcnt lgkmcnt(6)\n"
"v_lshlrev_b32 v252, 16, v238\n"
"v_fmac_f32 v252, v251, v176\n"
"v_mfma_f32_16x16x32_bf16 v[208:211], a[80:83], a[32:35], v[208:211]\n"
"v_and_b32 v170, s82, v170\n"
"v_and_b32 v238, s82, v238\n"
"v_mfma_f32_16x16x32_bf16 v[208:211], a[84:87], a[36:39], v[208:211]\n"
"v_fmac_f32 v238, v170, v177\n"
"v_cvt_pk_bf16_f32 v146, v252, v238\n"
"v_mfma_f32_16x16x32_bf16 v[212:215], a[80:83], a[40:43], v[212:215]\n"
"v_lshlrev_b32 v251, 16, v171\n"
"v_lshlrev_b32 v252, 16, v239\n"
"v_mfma_f32_16x16x32_bf16 v[212:215], a[84:87], a[44:47], v[212:215]\n"
"s_waitcnt lgkmcnt(2)\n"
"v_fmac_f32 v252, v251, v178\n"
"v_and_b32 v171, s82, v171\n"
"v_mfma_f32_16x16x32_bf16 v[216:219], a[80:83], a[48:51], v[216:219]\n"
"v_and_b32 v239, s82, v239\n"
"v_fmac_f32 v239, v171, v179\n"
"v_mfma_f32_16x16x32_bf16 v[216:219], a[84:87], a[52:55], v[216:219]\n"
"v_cvt_pk_bf16_f32 v147, v252, v239\n"
"v_mfma_f32_16x16x32_bf16 v[220:223], a[80:83], a[56:59], v[220:223]\n"
"buffer_store_dwordx4 v[144:147], v242, s[48:51], s72 offen offset:512 sc1\n"
"v_mfma_f32_16x16x32_bf16 v[220:223], a[84:87], a[60:63], v[220:223]\n"
"s_waitcnt vmcnt(9) lgkmcnt(0)\n"
"ds_read_b128 v[236:239], v250 offset:12288\n"
"v_lshlrev_b32 v251, 16, v172\n"
"v_mfma_f32_16x16x32_bf16 a[112:115], a[88:91], a[0:3], a[112:115]\n"
"ds_read_b128 a[0:3], v245 offset:32768\n"
"v_lshlrev_b32 v252, 16, v232\n"
"v_mfma_f32_16x16x32_bf16 a[112:115], a[92:95], a[4:7], a[112:115]\n"
"ds_read_b128 a[4:7], v246 offset:32768\n"
"v_fmac_f32 v252, v251, v148\n"
"v_mfma_f32_16x16x32_bf16 a[116:119], a[88:91], a[8:11], a[116:119]\n"
"ds_read_b128 a[8:11], v245 offset:34816\n"
"v_and_b32 v172, s82, v172\n"
"v_mfma_f32_16x16x32_bf16 a[116:119], a[92:95], a[12:15], a[116:119]\n"
"ds_read_b128 a[12:15], v246 offset:34816\n"
"v_and_b32 v232, s82, v232\n"
"v_mfma_f32_16x16x32_bf16 a[120:123], a[88:91], a[16:19], a[120:123]\n"
"ds_read_b128 a[16:19], v245 offset:36864\n"
"v_fmac_f32 v232, v172, v149\n"
"v_mfma_f32_16x16x32_bf16 a[120:123], a[92:95], a[20:23], a[120:123]\n"
"ds_read_b128 a[20:23], v246 offset:36864\n"
"v_cvt_pk_bf16_f32 v148, v252, v232\n"
"v_mfma_f32_16x16x32_bf16 a[124:127], a[88:91], a[24:27], a[124:127]\n"
"ds_read_b128 a[24:27], v245 offset:38912\n"
"v_lshlrev_b32 v251, 16, v173\n"
"v_mfma_f32_16x16x32_bf16 a[124:127], a[92:95], a[28:31], a[124:127]\n"
"ds_read_b128 a[28:31], v246 offset:38912\n"
"v_lshlrev_b32 v252, 16, v233\n"
"v_mfma_f32_16x16x32_bf16 a[128:131], a[88:91], a[32:35], a[128:131]\n"
"ds_read_b128 a[32:35], v245 offset:40960\n"
"v_fmac_f32 v252, v251, v150\n"
"v_mfma_f32_16x16x32_bf16 a[128:131], a[92:95], a[36:39], a[128:131]\n"
"ds_read_b128 a[36:39], v246 offset:40960\n"
"v_and_b32 v173, s82, v173\n"
"v_mfma_f32_16x16x32_bf16 a[132:135], a[88:91], a[40:43], a[132:135]\n"
"ds_read_b128 a[40:43], v245 offset:43008\n"
"v_and_b32 v233, s82, v233\n"
"v_mfma_f32_16x16x32_bf16 a[132:135], a[92:95], a[44:47], a[132:135]\n"
"ds_read_b128 a[44:47], v246 offset:43008\n"
"v_fmac_f32 v233, v173, v151\n"
"v_mfma_f32_16x16x32_bf16 a[136:139], a[88:91], a[48:51], a[136:139]\n"
"ds_read_b128 a[48:51], v245 offset:45056\n"
"v_cvt_pk_bf16_f32 v149, v252, v233\n"
"v_mfma_f32_16x16x32_bf16 a[136:139], a[92:95], a[52:55], a[136:139]\n"
"ds_read_b128 a[52:55], v246 offset:45056\n"
"v_lshlrev_b32 v251, 16, v174\n"
"v_mfma_f32_16x16x32_bf16 a[140:143], a[88:91], a[56:59], a[140:143]\n"
"ds_read_b128 a[56:59], v245 offset:47104\n"
"v_lshlrev_b32 v252, 16, v234\n"
"v_mfma_f32_16x16x32_bf16 a[140:143], a[92:95], a[60:63], a[140:143]\n"
"s_waitcnt vmcnt(6) lgkmcnt(11)\n"
"ds_read_b128 a[60:63], v246 offset:47104\n"
"v_fmac_f32 v252, v251, v180\n"
"v_mfma_f32_16x16x32_bf16 v[192:195], a[96:99], a[0:3], v[192:195]\n"
"v_and_b32 v174, s82, v174\n"
"v_and_b32 v234, s82, v234\n"
"v_mfma_f32_16x16x32_bf16 v[192:195], a[100:103], a[4:7], v[192:195]\n"
"v_fmac_f32 v234, v174, v181\n"
"v_cvt_pk_bf16_f32 v150, v252, v234\n"
"v_mfma_f32_16x16x32_bf16 v[196:199], a[96:99], a[8:11], v[196:199]\n"
"v_lshlrev_b32 v251, 16, v175\n"
"v_lshlrev_b32 v252, 16, v235\n"
"v_mfma_f32_16x16x32_bf16 v[196:199], a[100:103], a[12:15], v[196:199]\n"
"s_waitcnt lgkmcnt(8)\n"
"v_fmac_f32 v252, v251, v182\n"
"v_and_b32 v175, s82, v175\n"
"v_mfma_f32_16x16x32_bf16 v[200:203], a[96:99], a[16:19], v[200:203]\n"
"v_and_b32 v235, s82, v235\n"
"v_fmac_f32 v235, v175, v183\n"
"v_mfma_f32_16x16x32_bf16 v[200:203], a[100:103], a[20:23], v[200:203]\n"
"v_cvt_pk_bf16_f32 v151, v252, v235\n"
"v_accvgpr_read_b32 v253, a220\n"
"v_mfma_f32_16x16x32_bf16 v[204:207], a[96:99], a[24:27], v[204:207]\n"
"buffer_store_dwordx4 v[148:151], v242, s[48:51], s73 offen offset:512 sc1\n"
"v_mfma_f32_16x16x32_bf16 v[204:207], a[100:103], a[28:31], v[204:207]\n"
"s_waitcnt lgkmcnt(4)\n"
"ds_read_b128 v[232:235], v250 offset:47104\n"
"v_lshlrev_b32 v251, 16, v253\n"
"v_mfma_f32_16x16x32_bf16 v[208:211], a[96:99], a[32:35], v[208:211]\n"
"v_lshlrev_b32 v252, 16, v236\n"
"v_fmac_f32 v252, v251, v152\n"
"v_mfma_f32_16x16x32_bf16 v[208:211], a[100:103], a[36:39], v[208:211]\n"
"v_and_b32 v253, s82, v253\n"
"v_and_b32 v236, s82, v236\n"
"v_mfma_f32_16x16x32_bf16 v[212:215], a[96:99], a[40:43], v[212:215]\n"
"v_fmac_f32 v236, v253, v153\n"
"v_cvt_pk_bf16_f32 v152, v252, v236\n"
"v_mfma_f32_16x16x32_bf16 v[212:215], a[100:103], a[44:47], v[212:215]\n"
"s_waitcnt lgkmcnt(1)\n"
"v_accvgpr_read_b32 v253, a221\n"
"v_lshlrev_b32 v251, 16, v253\n"
"v_mfma_f32_16x16x32_bf16 v[216:219], a[96:99], a[48:51], v[216:219]\n"
"v_lshlrev_b32 v252, 16, v237\n"
"v_fmac_f32 v252, v251, v154\n"
"v_mfma_f32_16x16x32_bf16 v[216:219], a[100:103], a[52:55], v[216:219]\n"
"v_and_b32 v253, s82, v253\n"
"v_and_b32 v237, s82, v237\n"
"v_mfma_f32_16x16x32_bf16 v[220:223], a[96:99], a[56:59], v[220:223]\n"
"v_fmac_f32 v237, v253, v155\n"
"v_cvt_pk_bf16_f32 v153, v252, v237\n"
"v_mfma_f32_16x16x32_bf16 v[220:223], a[100:103], a[60:63], v[220:223]\n"
"s_waitcnt vmcnt(4)\n"
"v_accvgpr_read_b32 v253, a222\n"
"v_lshlrev_b32 v251, 16, v253\n"
"v_mfma_f32_16x16x32_bf16 a[112:115], a[104:107], a[0:3], a[112:115]\n"
"v_lshlrev_b32 v252, 16, v238\n"
"v_fmac_f32 v252, v251, v184\n"
"v_mfma_f32_16x16x32_bf16 a[112:115], a[108:111], a[4:7], a[112:115]\n"
"v_and_b32 v253, s82, v253\n"
"v_and_b32 v238, s82, v238\n"
"v_mfma_f32_16x16x32_bf16 a[116:119], a[104:107], a[8:11], a[116:119]\n"
"v_fmac_f32 v238, v253, v185\n"
"v_cvt_pk_bf16_f32 v154, v252, v238\n"
"v_mfma_f32_16x16x32_bf16 a[116:119], a[108:111], a[12:15], a[116:119]\n"
"v_accvgpr_read_b32 v253, a223\n"
"v_lshlrev_b32 v251, 16, v253\n"
"v_mfma_f32_16x16x32_bf16 a[120:123], a[104:107], a[16:19], a[120:123]\n"
"v_lshlrev_b32 v252, 16, v239\n"
"v_fmac_f32 v252, v251, v186\n"
"v_mfma_f32_16x16x32_bf16 a[120:123], a[108:111], a[20:23], a[120:123]\n"
"v_and_b32 v253, s82, v253\n"
"v_and_b32 v239, s82, v239\n"
"v_mfma_f32_16x16x32_bf16 a[124:127], a[104:107], a[24:27], a[124:127]\n"
"v_fmac_f32 v239, v253, v187\n"
"v_cvt_pk_bf16_f32 v155, v252, v239\n"
"v_mfma_f32_16x16x32_bf16 a[124:127], a[108:111], a[28:31], a[124:127]\n"
"buffer_store_dwordx4 v[152:155], v242, s[48:51], s74 offen offset:512 sc1\n"
"v_mfma_f32_16x16x32_bf16 a[128:131], a[104:107], a[32:35], a[128:131]\n"
"v_mfma_f32_16x16x32_bf16 a[128:131], a[108:111], a[36:39], a[128:131]\n"
"v_mfma_f32_16x16x32_bf16 a[132:135], a[104:107], a[40:43], a[132:135]\n"
"v_mfma_f32_16x16x32_bf16 a[132:135], a[108:111], a[44:47], a[132:135]\n"
"v_mfma_f32_16x16x32_bf16 a[136:139], a[104:107], a[48:51], a[136:139]\n"
"v_mfma_f32_16x16x32_bf16 a[136:139], a[108:111], a[52:55], a[136:139]\n"
"v_mfma_f32_16x16x32_bf16 a[140:143], a[104:107], a[56:59], a[140:143]\n"
"v_mfma_f32_16x16x32_bf16 a[140:143], a[108:111], a[60:63], a[140:143]\n"
"s_nop 7\n"
"s_nop 1\n"
"s_waitcnt lgkmcnt(0)\n"
"ds_read_b128 v[236:239], v250 offset:32768\n"
"v_accvgpr_read_b32 v253, a224\n"
"v_lshlrev_b32 v251, 16, v253\n"
"v_lshlrev_b32 v252, 16, v232\n"
"v_fmac_f32 v252, v251, v220\n"
"v_and_b32 v253, s82, v253\n"
"v_and_b32 v232, s82, v232\n"
"v_fmac_f32 v232, v253, v221\n"
"v_cvt_pk_bf16_f32 v220, v252, v232\n"
"v_accvgpr_read_b32 v253, a225\n"
"v_lshlrev_b32 v251, 16, v253\n"
"v_lshlrev_b32 v252, 16, v233\n"
"v_fmac_f32 v252, v251, v222\n"
"v_and_b32 v253, s82, v253\n"
"v_and_b32 v233, s82, v233\n"
"v_fmac_f32 v233, v253, v223\n"
"v_cvt_pk_bf16_f32 v221, v252, v233\n"
"v_accvgpr_read_b32 v253, a226\n"
"v_lshlrev_b32 v251, 16, v253\n"
"v_lshlrev_b32 v252, 16, v234\n"
"v_accvgpr_read_b32 v222, a140\n"
"v_fmac_f32 v252, v251, v222\n"
"v_and_b32 v253, s82, v253\n"
"v_and_b32 v234, s82, v234\n"
"v_accvgpr_read_b32 v251, a141\n"
"v_fmac_f32 v234, v253, v251\n"
"v_cvt_pk_bf16_f32 v222, v252, v234\n"
"v_accvgpr_read_b32 v253, a227\n"
"v_lshlrev_b32 v251, 16, v253\n"
"v_lshlrev_b32 v252, 16, v235\n"
"v_accvgpr_read_b32 v223, a142\n"
"v_fmac_f32 v252, v251, v223\n"
"v_and_b32 v253, s82, v253\n"
"v_and_b32 v235, s82, v235\n"
"v_accvgpr_read_b32 v251, a143\n"
"v_fmac_f32 v235, v253, v251\n"
"v_cvt_pk_bf16_f32 v223, v252, v235\n"
"ds_read_b128 v[232:235], v250 offset:34816\n"
"buffer_store_dwordx4 v[220:223], v242, s[48:51], s75 offen offset:768 sc1\n"
"s_waitcnt lgkmcnt(1)\n"
"v_accvgpr_read_b32 v253, a228\n"
"v_lshlrev_b32 v251, 16, v253\n"
"v_lshlrev_b32 v252, 16, v236\n"
"v_fmac_f32 v252, v251, v192\n"
"v_and_b32 v253, s82, v253\n"
"v_and_b32 v236, s82, v236\n"
"v_fmac_f32 v236, v253, v193\n"
"v_cvt_pk_bf16_f32 v192, v252, v236\n"
"v_accvgpr_read_b32 v253, a229\n"
"v_lshlrev_b32 v251, 16, v253\n"
"v_lshlrev_b32 v252, 16, v237\n"
"v_fmac_f32 v252, v251, v194\n"
"v_and_b32 v253, s82, v253\n"
"v_and_b32 v237, s82, v237\n"
"v_fmac_f32 v237, v253, v195\n"
"v_cvt_pk_bf16_f32 v193, v252, v237\n"
"v_accvgpr_read_b32 v253, a230\n"
"v_lshlrev_b32 v251, 16, v253\n"
"v_lshlrev_b32 v252, 16, v238\n"
"v_accvgpr_read_b32 v194, a112\n"
"v_fmac_f32 v252, v251, v194\n"
"v_and_b32 v253, s82, v253\n"
"v_and_b32 v238, s82, v238\n"
"v_accvgpr_read_b32 v251, a113\n"
"v_fmac_f32 v238, v253, v251\n"
"v_cvt_pk_bf16_f32 v194, v252, v238\n"
"v_accvgpr_read_b32 v253, a231\n"
"v_lshlrev_b32 v251, 16, v253\n"
"v_lshlrev_b32 v252, 16, v239\n"
"v_accvgpr_read_b32 v195, a114\n"
"v_fmac_f32 v252, v251, v195\n"
"v_and_b32 v253, s82, v253\n"
"v_and_b32 v239, s82, v239\n"
"v_accvgpr_read_b32 v251, a115\n"
"v_fmac_f32 v239, v253, v251\n"
"v_cvt_pk_bf16_f32 v195, v252, v239\n"
"buffer_store_dwordx4 v[192:195], v242, s[48:51], s68 offen offset:768 sc1\n"
"s_waitcnt lgkmcnt(0)\n"
"ds_read_b128 v[236:239], v250 offset:36864\n"
"v_accvgpr_read_b32 v253, a232\n"
"v_lshlrev_b32 v251, 16, v253\n"
"v_lshlrev_b32 v252, 16, v232\n"
"v_fmac_f32 v252, v251, v196\n"
"v_and_b32 v253, s82, v253\n"
"v_and_b32 v232, s82, v232\n"
"v_fmac_f32 v232, v253, v197\n"
"v_cvt_pk_bf16_f32 v196, v252, v232\n"
"v_accvgpr_read_b32 v253, a233\n"
"v_lshlrev_b32 v251, 16, v253\n"
"v_lshlrev_b32 v252, 16, v233\n"
"v_fmac_f32 v252, v251, v198\n"
"v_and_b32 v253, s82, v253\n"
"v_and_b32 v233, s82, v233\n"
"v_fmac_f32 v233, v253, v199\n"
"v_cvt_pk_bf16_f32 v197, v252, v233\n"
"v_accvgpr_read_b32 v253, a234\n"
"v_lshlrev_b32 v251, 16, v253\n"
"v_lshlrev_b32 v252, 16, v234\n"
"v_accvgpr_read_b32 v198, a116\n"
"v_fmac_f32 v252, v251, v198\n"
"v_and_b32 v253, s82, v253\n"
"v_and_b32 v234, s82, v234\n"
"v_accvgpr_read_b32 v251, a117\n"
"v_fmac_f32 v234, v253, v251\n"
"v_cvt_pk_bf16_f32 v198, v252, v234\n"
"v_accvgpr_read_b32 v253, a235\n"
"v_lshlrev_b32 v251, 16, v253\n"
"v_lshlrev_b32 v252, 16, v235\n"
"v_accvgpr_read_b32 v199, a118\n"
"v_fmac_f32 v252, v251, v199\n"
"v_and_b32 v253, s82, v253\n"
"v_and_b32 v235, s82, v235\n"
"v_accvgpr_read_b32 v251, a119\n"
"v_fmac_f32 v235, v253, v251\n"
".Lpad22_%=:\n"
"s_cbranch_execz .Lret2_%=\n"
"v_cvt_pk_bf16_f32 v199, v252, v235\n"
"ds_read_b128 v[232:235], v250 offset:38912\n"
"buffer_store_dwordx4 v[196:199], v242, s[48:51], s69 offen offset:768 sc1\n"
"s_waitcnt lgkmcnt(1)\n"
"v_accvgpr_read_b32 v253, a236\n"
"v_lshlrev_b32 v251, 16, v253\n"
"v_lshlrev_b32 v252, 16, v236\n"
"v_fmac_f32 v252, v251, v200\n"
"v_and_b32 v253, s82, v253\n"
"v_and_b32 v236, s82, v236\n"
"v_fmac_f32 v236, v253, v201\n"
"v_cvt_pk_bf16_f32 v200, v252, v236\n"
"v_accvgpr_read_b32 v253, a237\n"
"v_lshlrev_b32 v251, 16, v253\n"
"v_lshlrev_b32 v252, 16, v237\n"
"v_fmac_f32 v252, v251, v202\n"
"v_and_b32 v253, s82, v253\n"
"v_and_b32 v237, s82, v237\n"
"v_fmac_f32 v237, v253, v203\n"
"v_cvt_pk_bf16_f32 v201, v252, v237\n"
"v_accvgpr_read_b32 v253, a238\n"
"v_lshlrev_b32 v251, 16, v253\n"
"v_lshlrev_b32 v252, 16, v238\n"
"v_accvgpr_read_b32 v202, a120\n"
"v_fmac_f32 v252, v251, v202\n"
"v_and_b32 v253, s82, v253\n"
"v_and_b32 v238, s82, v238\n"
"v_accvgpr_read_b32 v251, a121\n"
"v_fmac_f32 v238, v253, v251\n"
"v_cvt_pk_bf16_f32 v202, v252, v238\n"
"v_accvgpr_read_b32 v253, a239\n"
"v_lshlrev_b32 v251, 16, v253\n"
"v_lshlrev_b32 v252, 16, v239\n"
"v_accvgpr_read_b32 v203, a122\n"
"v_fmac_f32 v252, v251, v203\n"
"v_and_b32 v253, s82, v253\n"
"v_and_b32 v239, s82, v239\n"
"v_accvgpr_read_b32 v251, a123\n"
"v_fmac_f32 v239, v253, v251\n"
"v_cvt_pk_bf16_f32 v203, v252, v239\n"
"buffer_store_dwordx4 v[200:203], v242, s[48:51], s70 offen offset:768 sc1\n"
"s_waitcnt lgkmcnt(0)\n"
"ds_read_b128 v[236:239], v250 offset:40960\n"
"v_accvgpr_read_b32 v253, a240\n"
"v_lshlrev_b32 v251, 16, v253\n"
"v_lshlrev_b32 v252, 16, v232\n"
"v_fmac_f32 v252, v251, v204\n"
"v_and_b32 v253, s82, v253\n"
"v_and_b32 v232, s82, v232\n"
"v_fmac_f32 v232, v253, v205\n"
"v_cvt_pk_bf16_f32 v204, v252, v232\n"
"v_accvgpr_read_b32 v253, a241\n"
"v_lshlrev_b32 v251, 16, v253\n"
"v_lshlrev_b32 v252, 16, v233\n"
"v_fmac_f32 v252, v251, v206\n"
"v_and_b32 v253, s82, v253\n"
"v_and_b32 v233, s82, v233\n"
"v_fmac_f32 v233, v253, v207\n"
"v_cvt_pk_bf16_f32 v205, v252, v233\n"
"v_accvgpr_read_b32 v253, a242\n"
"v_lshlrev_b32 v251, 16, v253\n"
"v_lshlrev_b32 v252, 16, v234\n"
"v_accvgpr_read_b32 v206, a124\n"
"v_fmac_f32 v252, v251, v206\n"
"v_and_b32 v253, s82, v253\n"
"v_and_b32 v234, s82, v234\n"
"v_accvgpr_read_b32 v251, a125\n"
"v_fmac_f32 v234, v253, v251\n"
"v_cvt_pk_bf16_f32 v206, v252, v234\n"
"v_accvgpr_read_b32 v253, a243\n"
"v_lshlrev_b32 v251, 16, v253\n"
"v_lshlrev_b32 v252, 16, v235\n"
"v_accvgpr_read_b32 v207, a126\n"
"v_fmac_f32 v252, v251, v207\n"
"v_and_b32 v253, s82, v253\n"
"v_and_b32 v235, s82, v235\n"
"v_accvgpr_read_b32 v251, a127\n"
"v_fmac_f32 v235, v253, v251\n"
"v_cvt_pk_bf16_f32 v207, v252, v235\n"
"ds_read_b128 v[232:235], v250 offset:43008\n"
"buffer_store_dwordx4 v[204:207], v242, s[48:51], s71 offen offset:768 sc1\n"
"s_waitcnt lgkmcnt(1)\n"
"v_accvgpr_read_b32 v253, a244\n"
"v_lshlrev_b32 v251, 16, v253\n"
"v_lshlrev_b32 v252, 16, v236\n"
"v_fmac_f32 v252, v251, v208\n"
"v_and_b32 v253, s82, v253\n"
"v_and_b32 v236, s82, v236\n"
"v_fmac_f32 v236, v253, v209\n"
"v_cvt_pk_bf16_f32 v208, v252, v236\n"
"v_accvgpr_read_b32 v253, a245\n"
"v_lshlrev_b32 v251, 16, v253\n"
"v_lshlrev_b32 v252, 16, v237\n"
"v_fmac_f32 v252, v251, v210\n"
"v_and_b32 v253, s82, v253\n"
"v_and_b32 v237, s82, v237\n"
"v_fmac_f32 v237, v253, v211\n"
"v_cvt_pk_bf16_f32 v209, v252, v237\n"
"v_accvgpr_read_b32 v253, a246\n"
"v_lshlrev_b32 v251, 16, v253\n"
"v_lshlrev_b32 v252, 16, v238\n"
"v_accvgpr_read_b32 v210, a128\n"
"v_fmac_f32 v252, v251, v210\n"
"v_and_b32 v253, s82, v253\n"
"v_and_b32 v238, s82, v238\n"
"v_accvgpr_read_b32 v251, a129\n"
"v_fmac_f32 v238, v253, v251\n"
"v_cvt_pk_bf16_f32 v210, v252, v238\n"
"v_accvgpr_read_b32 v253, a247\n"
"v_lshlrev_b32 v251, 16, v253\n"
"v_lshlrev_b32 v252, 16, v239\n"
"v_accvgpr_read_b32 v211, a130\n"
"v_fmac_f32 v252, v251, v211\n"
"v_and_b32 v253, s82, v253\n"
"v_and_b32 v239, s82, v239\n"
"v_accvgpr_read_b32 v251, a131\n"
"v_fmac_f32 v239, v253, v251\n"
"v_cvt_pk_bf16_f32 v211, v252, v239\n"
"buffer_store_dwordx4 v[208:211], v242, s[48:51], s72 offen offset:768 sc1\n"
"s_waitcnt lgkmcnt(0)\n"
"ds_read_b128 v[236:239], v250 offset:45056\n"
"v_accvgpr_read_b32 v253, a248\n"
"v_lshlrev_b32 v251, 16, v253\n"
"v_lshlrev_b32 v252, 16, v232\n"
"v_fmac_f32 v252, v251, v212\n"
"v_and_b32 v253, s82, v253\n"
"v_and_b32 v232, s82, v232\n"
"v_fmac_f32 v232, v253, v213\n"
"v_cvt_pk_bf16_f32 v212, v252, v232\n"
"v_accvgpr_read_b32 v253, a249\n"
"v_lshlrev_b32 v251, 16, v253\n"
"v_lshlrev_b32 v252, 16, v233\n"
"v_fmac_f32 v252, v251, v214\n"
"v_and_b32 v253, s82, v253\n"
"v_and_b32 v233, s82, v233\n"
"v_fmac_f32 v233, v253, v215\n"
"v_cvt_pk_bf16_f32 v213, v252, v233\n"
"v_accvgpr_read_b32 v253, a250\n"
"v_lshlrev_b32 v251, 16, v253\n"
"v_lshlrev_b32 v252, 16, v234\n"
"v_accvgpr_read_b32 v214, a132\n"
"v_fmac_f32 v252, v251, v214\n"
"v_and_b32 v253, s82, v253\n"
"v_and_b32 v234, s82, v234\n"
"v_accvgpr_read_b32 v251, a133\n"
"v_fmac_f32 v234, v253, v251\n"
"v_cvt_pk_bf16_f32 v214, v252, v234\n"
"v_accvgpr_read_b32 v253, a251\n"
"v_lshlrev_b32 v251, 16, v253\n"
"v_lshlrev_b32 v252, 16, v235\n"
"v_accvgpr_read_b32 v215, a134\n"
"v_fmac_f32 v252, v251, v215\n"
"v_and_b32 v253, s82, v253\n"
"v_and_b32 v235, s82, v235\n"
"v_accvgpr_read_b32 v251, a135\n"
"v_fmac_f32 v235, v253, v251\n"
"s_waitcnt lgkmcnt(0)\n"
"v_cvt_pk_bf16_f32 v215, v252, v235\n"
"v_accvgpr_read_b32 v253, a252\n"
"buffer_store_dwordx4 v[212:215], v242, s[48:51], s73 offen offset:768 sc1\n"
"v_lshlrev_b32 v251, 16, v253\n"
"v_lshlrev_b32 v252, 16, v236\n"
"v_fmac_f32 v252, v251, v216\n"
"v_and_b32 v253, s82, v253\n"
"v_and_b32 v236, s82, v236\n"
"v_fmac_f32 v236, v253, v217\n"
"v_cvt_pk_bf16_f32 v216, v252, v236\n"
"v_accvgpr_read_b32 v253, a253\n"
"v_lshlrev_b32 v251, 16, v253\n"
"v_lshlrev_b32 v252, 16, v237\n"
"v_fmac_f32 v252, v251, v218\n"
"v_and_b32 v253, s82, v253\n"
"v_and_b32 v237, s82, v237\n"
"v_fmac_f32 v237, v253, v219\n"
"v_cvt_pk_bf16_f32 v217, v252, v237\n"
"v_accvgpr_read_b32 v253, a254\n"
"v_lshlrev_b32 v251, 16, v253\n"
"v_lshlrev_b32 v252, 16, v238\n"
"v_accvgpr_read_b32 v218, a136\n"
"v_fmac_f32 v252, v251, v218\n"
"v_and_b32 v253, s82, v253\n"
"v_and_b32 v238, s82, v238\n"
"v_accvgpr_read_b32 v251, a137\n"
"v_fmac_f32 v238, v253, v251\n"
"v_cvt_pk_bf16_f32 v218, v252, v238\n"
"v_accvgpr_read_b32 v253, a255\n"
"v_lshlrev_b32 v251, 16, v253\n"
"v_lshlrev_b32 v252, 16, v239\n"
"v_accvgpr_read_b32 v219, a138\n"
"v_fmac_f32 v252, v251, v219\n"
"v_and_b32 v253, s82, v253\n"
"v_and_b32 v239, s82, v239\n"
"v_accvgpr_read_b32 v251, a139\n"
"v_fmac_f32 v239, v253, v251\n"
"v_cvt_pk_bf16_f32 v219, v252, v239\n"
"buffer_store_dwordx4 v[216:219], v242, s[48:51], s74 offen offset:768 sc1\n"
"s_endpgm\n"
".Lslow0_%=:\n"
"s_or_b32 s93, s93, 1\n"
"s_mov_b32 s88, 0x40000\n"
".Lspin0_%=:\n"
"s_sleep 2\n"
"buffer_load_dword v241, v240, s[64:67], 0 offen offset:0 sc1\n"
"s_waitcnt vmcnt(0)\n"
"v_cmp_ne_u32 vcc, s33, v241\n"
"s_nop 3\n"
"s_cmp_eq_u64 vcc, 0\n"
"s_cbranch_scc1 .Lback0_%=\n"
"s_sub_u32 s88, s88, 1\n"
"s_cmp_lg_u32 s88, 0\n"
"s_cbranch_scc1 .Lspin0_%=\n"
"s_branch .Lback0_%=\n"
".Lslow1_%=:\n"
"s_or_b32 s93, s93, 2\n"
"s_mov_b32 s88, 0x40000\n"
".Lspin1_%=:\n"
"s_sleep 2\n"
"buffer_load_dword v241, v240, s[64:67], 0 offen offset:1024 sc1\n"
"s_waitcnt vmcnt(0)\n"
"v_cmp_ne_u32 vcc, s33, v241\n"
"s_nop 3\n"
"s_cmp_eq_u64 vcc, 0\n"
"s_cbranch_scc1 .Lback1_%=\n"
"s_sub_u32 s88, s88, 1\n"
"s_cmp_lg_u32 s88, 0\n"
"s_cbranch_scc1 .Lspin1_%=\n"
"s_branch .Lback1_%=\n"
".Lslow2_%=:\n"
"s_or_b32 s93, s93, 4\n"
"s_mov_b32 s88, 0x40000\n"
".Lspin2_%=:\n"
"s_sleep 2\n"
"buffer_load_dword v241, v240, s[64:67], 0 offen offset:2048 sc1\n"
"s_waitcnt vmcnt(0)\n"
"v_cmp_ne_u32 vcc, s33, v241\n"
"s_nop 3\n"
"s_cmp_eq_u64 vcc, 0\n"
"s_cbranch_scc1 .Lback2_%=\n"
"s_sub_u32 s88, s88, 1\n"
"s_cmp_lg_u32 s88, 0\n"
"s_cbranch_scc1 .Lspin2_%=\n"
"s_branch .Lback2_%=\n"
".Lslow3_%=:\n"
"s_or_b32 s93, s93, 8\n"
"s_mov_b32 s88, 0x40000\n"
".Lspin3_%=:\n"
"s_sleep 2\n"
"buffer_load_dword v241, v240, s[64:67], 0 offen offset:3072 sc1\n"
"s_waitcnt vmcnt(0)\n"
"v_cmp_ne_u32 vcc, s33, v241\n"
"s_nop 3\n"
"s_cmp_eq_u64 vcc, 0\n"
"s_cbranch_scc1 .Lback3_%=\n"
"s_sub_u32 s88, s88, 1\n"
"s_cmp_lg_u32 s88, 0\n"
"s_cbranch_scc1 .Lspin3_%=\n"
"s_branch .Lback3_%=\n"
".Lslow6_%=:\n"
"s_or_b32 s93, s93, 64\n"
"s_mov_b32 s88, 0x40000\n"
".Lspin6_%=:\n"
"v_lshlrev_b32 v241, 4, v240\n"
"s_add_u32 s93, s93, 0x100\n"
"buffer_load_dword v241, v241, s[24:27], s84 offen sc1\n"
"s_waitcnt vmcnt(0)\n"
"v_cmp_ne_u32 vcc, s33, v241\n"
"s_nop 3\n"
"s_cmp_eq_u64 vcc, 0\n"
"s_cbranch_scc1 .Lback6_%=\n"
"s_sleep 1\n"
"s_sub_u32 s88, s88, 1\n"
"s_cmp_lg_u32 s88, 0\n"
"s_cbranch_scc1 .Lspin6_%=\n"
"s_branch .Lback6_%=\n"
".Lslow7_%=:\n"
"s_or_b32 s93, s93, 128\n"
"s_mov_b32 s88, 0x40000\n"
".Lspin7_%=:\n"
"v_lshlrev_b32 v241, 4, v240\n"
"s_add_u32 s93, s93, 0x100\n"
"buffer_load_dword v241, v241, s[24:27], s30 offen sc1\n"
"s_waitcnt vmcnt(0)\n"
"v_cmp_ne_u32 vcc, s33, v241\n"
"s_nop 3\n"
"s_cmp_eq_u64 vcc, 0\n"
"s_cbranch_scc1 .Lback7_%=\n"
"s_sleep 1\n"
"s_sub_u32 s88, s88, 1\n"
"s_cmp_lg_u32 s88, 0\n"
"s_cbranch_scc1 .Lspin7_%=\n"
"s_branch .Lback7_%=\n"
".p2align 3\n"
".Ltab_%=:\n"
".long 0x1003938, 0x100, 0x2033b3a, 0x203, 0x69686968, 0x6160, 0x6b6a6b6a, 0x6362, 0x19183d3c, 0x2120, 0x1a1b3f3e, 0x2223, 0x1d1c4140, 0x2524, 0x1e1f4342, 0x2627\n"
".long 0x1003938, 0x100, 0x2033b3a, 0x203, 0x69686968, 0x6160, 0x6b6a6b6a, 0x6362, 0x19183d3c, 0x2120, 0x1a1b3f3e, 0x2223, 0x1d1c4140, 0x2524, 0x1e1f4342, 0x2627\n"
".long 0x5044544, 0x504, 0x7064746, 0x706, 0x6d6c6d6c, 0x6564, 0x6f6e6f6e, 0x6766, 0x21204948, 0x2928, 0x23224b4a, 0x2b2a, 0x25244d4c, 0x2d2c, 0x27264f4e, 0x2f2e\n"
".long 0x5044544, 0x504, 0x7064746, 0x706, 0x6d6c6d6c, 0x6564, 0x6f6e6f6e, 0x6766, 0x21204948, 0x2928, 0x23224b4a, 0x2b2a, 0x25244d4c, 0x2d2c, 0x27264f4e, 0x2f2e\n"
".long 0x9085150, 0x908, 0xb0a5352, 0xb0a, 0x71707170, 0x6968, 0x73727372, 0x7776, 0x29285554, 0x3130, 0x2b2a5756, 0x3332, 0x2d2c5958, 0x3534, 0x2f2e5b5a, 0x3736\n"
".long 0x9085150, 0x908, 0xb0a5352, 0xb0a, 0x71707170, 0x6968, 0x73727372, 0x7776, 0x29285554, 0x3130, 0x2b2a5756, 0x3332, 0x2d2c5958, 0x3534, 0x2f2e5b5a, 0x3736\n"
".long 0xd0c5d5c, 0xd0c, 0xf0e5f5e, 0xf0e, 0x75747574, 0x6b6a, 0x77767776, 0x7978, 0x39386160, 0x3938, 0x3b3a6362, 0x3b3a, 0x3d3c6564, 0x3d3c, 0x3f3e6766, 0x3f3e\n"
".long 0xd0c5d5c, 0xd0c, 0xf0e5f5e, 0xf0e, 0x75747574, 0x6b6a, 0x77767776, 0x7978, 0x39386160, 0x3938, 0x3b3a6362, 0x3b3a, 0x3d3c6564, 0x3d3c, 0x3f3e6766, 0x3f3e\n"
".long 0x11102120, 0x1110, 0x13122322, 0x1312, 0x79787978, 0x6d6c, 0x7b7a7b7a, 0x7b7a, 0x41402524, 0x4140, 0x43422726, 0x4342, 0x45442928, 0x4544, 0x47462b2a, 0x4746\n"
".long 0x11102120, 0x1110, 0x13122322, 0x1312, 0x79787978, 0x6d6c, 0x7b7a7b7a, 0x7b7a, 0x41402524, 0x4140, 0x43422726, 0x4342, 0x45442928, 0x4544, 0x47462b2a, 0x4746\n"
".long 0x15142d2c, 0x1514, 0x17162f2e, 0x1716, 0x7d7c7d7c, 0x6f6e, 0x7f7e7f7e, 0x7d7c, 0x51503130, 0x4948, 0x53523332, 0x4b4a, 0x55543534, 0x4d4c, 0x57563736, 0x4f4e\n"
".long 0x15142d2c, 0x1514, 0x17162f2e, 0x1716, 0x7d7c7d7c, 0x6f6e, 0x7f7e7f7e, 0x7d7c, 0x51503130, 0x4948, 0x53523332, 0x4b4a, 0x55543534, 0x4d4c, 0x57563736, 0x4f4e\n"
".long 0x31300100, 0x1918, 0x33320302, 0x1b1a, 0x35340504, 0x7170, 0x37360706, 0x7f7e, 0x59580908, 0x5150, 0x5b5a0b0a, 0x5352, 0x5d5c0d0c, 0x5554, 0x5f5e0f0e, 0x5756\n"
".long 0x31300100, 0x1918, 0x33320302, 0x1b1a, 0x35340504, 0x7170, 0x37360706, 0x7f7e, 0x59580908, 0x5150, 0x5b5a0b0a, 0x5352, 0x5d5c0d0c, 0x5554, 0x5f5e0f0e, 0x5756\n"
".long 0x49481110, 0x1d1c, 0x4b4a1312, 0x1f1e, 0x4d4c1514, 0x7372, 0x4f4e1716, 0x7574, 0x61601918, 0x5958, 0x63621b1a, 0x5b5a, 0x65641d1c, 0x5d5c, 0x67661f1e, 0x5f5e\n"
".long 0x49481110, 0x1d1c, 0x4b4a1312, 0x1f1e, 0x4d4c1514, 0x7372, 0x4f4e1716, 0x7574, 0x61601918, 0x5958, 0x63621b1a, 0x5b5a, 0x65641d1c, 0x5d5c, 0x67661f1e, 0x5f5e\n"
".long 0x19183d3c, 0x2120, 0x1a1b3f3e, 0x2223, 0x1d1c4140, 0x2524, 0x1e1f4342, 0x2627, 0x1003938, 0x100, 0x2033b3a, 0x203, 0x69686968, 0x6160, 0x6b6a6b6a, 0x6362\n"
".long 0x19183d3c, 0x2120, 0x1a1b3f3e, 0x2223, 0x1d1c4140, 0x2524, 0x1e1f4342, 0x2627, 0x1003938, 0x100, 0x2033b3a, 0x203, 0x69686968, 0x6160, 0x6b6a6b6a, 0x6362\n"
".long 0x21204948, 0x2928, 0x23224b4a, 0x2b2a, 0x25244d4c, 0x2d2c, 0x27264f4e, 0x2f2e, 0x5044544, 0x504, 0x7064746, 0x706, 0x6d6c6d6c, 0x6564, 0x6f6e6f6e, 0x6766\n"
".long 0x21204948, 0x2928, 0x23224b4a, 0x2b2a, 0x25244d4c, 0x2d2c, 0x27264f4e, 0x2f2e, 0x5044544, 0x504, 0x7064746, 0x706, 0x6d6c6d6c, 0x6564, 0x6f6e6f6e, 0x6766\n"
".long 0x29285554, 0x3130, 0x2b2a5756, 0x3332, 0x2d2c5958, 0x3534, 0x2f2e5b5a, 0x3736, 0x9085150, 0x908, 0xb0a5352, 0xb0a, 0x71707170, 0x6968, 0x73727372, 0x7776\n"
".long 0x29285554, 0x3130, 0x2b2a5756, 0x3332, 0x2d2c5958, 0x3534, 0x2f2e5b5a, 0x3736, 0x9085150, 0x908, 0xb0a5352, 0xb0a, 0x71707170, 0x6968, 0x73727372, 0x7776\n"
".long 0x39386160, 0x3938, 0x3b3a6362, 0x3b3a, 0x3d3c6564, 0x3d3c, 0x3f3e6766, 0x3f3e, 0xd0c5d5c, 0xd0c, 0xf0e5f5e, 0xf0e, 0x75747574, 0x6b6a, 0x77767776, 0x7978\n"
".long 0x39386160, 0x3938, 0x3b3a6362, 0x3b3a, 0x3d3c6564, 0x3d3c, 0x3f3e6766, 0x3f3e, 0xd0c5d5c, 0xd0c, 0xf0e5f5e, 0xf0e, 0x75747574, 0x6b6a, 0x77767776, 0x7978\n"
".long 0x41402524, 0x4140, 0x43422726, 0x4342, 0x45442928, 0x4544, 0x47462b2a, 0x4746, 0x11102120, 0x1110, 0x13122322, 0x1312, 0x79787978, 0x6d6c, 0x7b7a7b7a, 0x7b7a\n"
".long 0x41402524, 0x4140, 0x43422726, 0x4342, 0x45442928, 0x4544, 0x47462b2a, 0x4746, 0x11102120, 0x1110, 0x13122322, 0x1312, 0x79787978, 0x6d6c, 0x7b7a7b7a, 0x7b7a\n"
".long 0x51503130, 0x4948, 0x53523332, 0x4b4a, 0x55543534, 0x4d4c, 0x57563736, 0x4f4e, 0x15142d2c, 0x1514, 0x17162f2e, 0x1716, 0x7d7c7d7c, 0x6f6e, 0x7f7e7f7e, 0x7d7c\n"
".long 0x51503130, 0x4948, 0x53523332, 0x4b4a, 0x55543534, 0x4d4c, 0x57563736, 0x4f4e, 0x15142d2c, 0x1514, 0x17162f2e, 0x1716, 0x7d7c7d7c, 0x6f6e, 0x7f7e7f7e, 0x7d7c\n"
".long 0x59580908, 0x5150, 0x5b5a0b0a, 0x5352, 0x5d5c0d0c, 0x5554, 0x5f5e0f0e, 0x5756, 0x31300100, 0x1918, 0x33320302, 0x1b1a, 0x35340504, 0x7170, 0x37360706, 0x7f7e\n"
".long 0x59580908, 0x5150, 0x5b5a0b0a, 0x5352, 0x5d5c0d0c, 0x5554, 0x5f5e0f0e, 0x5756, 0x31300100, 0x1918, 0x33320302, 0x1b1a, 0x35340504, 0x7170, 0x37360706, 0x7f7e\n"
".long 0x61601918, 0x5958, 0x63621b1a, 0x5b5a, 0x65641d1c, 0x5d5c, 0x67661f1e, 0x5f5e, 0x49481110, 0x1d1c, 0x4b4a1312, 0x1f1e, 0x4d4c1514, 0x7372, 0x4f4e1716, 0x7574\n"
".long 0x61601918, 0x5958, 0x63621b1a, 0x5b5a, 0x65641d1c, 0x5d5c, 0x67661f1e, 0x5f5e, 0x49481110, 0x1d1c, 0x4b4a1312, 0x1f1e, 0x4d4c1514, 0x7372, 0x4f4e1716, 0x7574\n"
".fill 4096, 4, 0xbf800000\n"
".Lend_%=:\n"
        :
        : "s"(x0), "s"(ws), "s"(out), "s"(blk), "s"(wave), "s"(W), "s"(bs)
        : "v0", "v1", "v2", "v3", "v4", "v5", "v6", "v7", "v8", "v9", "v10", "v11", "v12", "v13", "v14", "v15", "v16", "v17", "v18", "v19", "v20", "v21", "v22", "v23", "v24", "v25", "v26", "v27", "v28", "v29", "v30", "v31", "v32", "v33", "v34", "v35", "v36", "v37", "v38", "v39", "v40", "v41", "v42", "v43", "v44", "v45", "v46", "v47", "v48", "v49", "v50", "v51", "v52", "v53", "v54", "v55", "v56", "v57", "v58", "v59", "v60", "v61", "v62", "v63", "v64", "v65", "v66", "v67", "v68", "v69", "v70", "v71", "v72", "v73", "v74", "v75", "v76", "v77", "v78", "v79", "v80", "v81", "v82", "v83", "v84", "v85", "v86", "v87", "v88", "v89", "v90", "v91", "v92", "v93", "v94", "v95", "v96", "v97", "v98", "v99", "v100", "v101", "v102", "v103", "v104", "v105", "v106", "v107", "v108", "v109", "v110", "v111", "v112", "v113", "v114", "v115", "v116", "v117", "v118", "v119", "v120", "v121", "v122", "v123", "v124", "v125", "v126", "v127", "v128", "v129", "v130", "v131", "v132", "v133", "v134", "v135", "v136", "v137", "v138", "v139", "v140", "v141", "v142", "v143", "v144", "v145", "v146", "v147", "v148", "v149", "v150", "v151", "v152", "v153", "v154", "v155", "v156", "v157", "v158", "v159", "v160", "v161", "v162", "v163", "v164", "v165", "v166", "v167", "v168", "v169", "v170", "v171", "v172", "v173", "v174", "v175", "v176", "v177", "v178", "v179", "v180", "v181", "v182", "v183", "v184", "v185", "v186", "v187", "v188", "v189", "v190", "v191", "v192", "v193", "v194", "v195", "v196", "v197", "v198", "v199", "v200", "v201", "v202", "v203", "v204", "v205", "v206", "v207", "v208", "v209", "v210", "v211", "v212", "v213", "v214", "v215", "v216", "v217", "v218", "v219", "v220", "v221", "v222", "v223", "v224", "v225", "v226", "v227", "v228", "v229", "v230", "v231", "v232", "v233", "v234", "v235", "v236", "v237", "v238", "v239", "v240", "v241", "v242", "v243", "v244", "v245", "v246", "v247", "v248", "v249", "v250", "v251", "v252", "v253", "v254", "v255", "a0", "a1", "a2", "a3", "a4", "a5", "a6", "a7", "a8", "a9", "a10", "a11", "a12", "a13", "a14", "a15", "a16", "a17", "a18", "a19", "a20", "a21", "a22", "a23", "a24", "a25", "a26", "a27", "a28", "a29", "a30", "a31", "a32", "a33", "a34", "a35", "a36", "a37", "a38", "a39", "a40", "a41", "a42", "a43", "a44", "a45", "a46", "a47", "a48", "a49", "a50", "a51", "a52", "a53", "a54", "a55", "a56", "a57", "a58", "a59", "a60", "a61", "a62", "a63", "a64", "a65", "a66", "a67", "a68", "a69", "a70", "a71", "a72", "a73", "a74", "a75", "a76", "a77", "a78", "a79", "a80", "a81", "a82", "a83", "a84", "a85", "a86", "a87", "a88", "a89", "a90", "a91", "a92", "a93", "a94", "a95", "a96", "a97", "a98", "a99", "a100", "a101", "a102", "a103", "a104", "a105", "a106", "a107", "a108", "a109", "a110", "a111", "a112", "a113", "a114", "a115", "a116", "a117", "a118", "a119", "a120", "a121", "a122", "a123", "a124", "a125", "a126", "a127", "a128", "a129", "a130", "a131", "a132", "a133", "a134", "a135", "a136", "a137", "a138", "a139", "a140", "a141", "a142", "a143", "a144", "a145", "a146", "a147", "a148", "a149", "a150", "a151", "a152", "a153", "a154", "a155", "a156", "a157", "a158", "a159", "a160", "a161", "a162", "a163", "a164", "a165", "a166", "a167", "a168", "a169", "a170", "a171", "a172", "a173", "a174", "a175", "a176", "a177", "a178", "a179", "a180", "a181", "a182", "a183", "a184", "a185", "a186", "a187", "a188", "a189", "a190", "a191", "a192", "a193", "a194", "a195", "a196", "a197", "a198", "a199", "a200", "a201", "a202", "a203", "a204", "a205", "a206", "a207", "a208", "a209", "a210", "a211", "a212", "a213", "a214", "a215", "a216", "a217", "a218", "a219", "a220", "a221", "a222", "a223", "a224", "a225", "a226", "a227", "a228", "a229", "a230", "a231", "a232", "a233", "a234", "a235", "a236", "a237", "a238", "a239", "a240", "a241", "a242", "a243", "a244", "a245", "a246", "a247", "a248", "a249", "a250", "a251", "a252", "a253", "a254", "a255", "s14", "s15", "s16", "s17", "s18", "s19", "s20", "s21", "s22", "s23", "s24", "s25", "s26", "s27", "s28", "s29", "s30", "s31", "s33", "s34", "s35", "s36", "s37", "s38", "s39", "s40", "s41", "s42", "s43", "s44", "s45", "s46", "s47", "s48", "s49", "s50", "s51", "s52", "s53", "s54", "s55", "s56", "s57", "s58", "s59", "s60", "s61", "s62", "s63", "s64", "s65", "s66", "s67", "s68", "s69", "s70", "s71", "s72", "s73", "s74", "s75", "s76", "s77", "s78", "s79", "s80", "s81", "s82", "s83", "s84", "s85", "s86", "s87", "s88", "s89", "s90", "s91", "s92", "s93", "s94", "s95", "s96", "s97", "s98", "s99", "vcc", "scc", "memory");
}

extern "C" void kernel_launch(void* const* d_in, const int* in_sizes, int n_in, void* d_out, int out_size, void* d_ws,
                              size_t ws_size, hipStream_t stream) {
    (void)hipFuncSetAttribute((const void*)dcn_main, hipFuncAttributeMaxDynamicSharedMemorySize, 163840);
    dcn_main<<<256, 256, 163840, stream>>>((const u16*)d_in[0], (char*)d_ws, (u16*)d_out, (const u16*)d_in[1], (const u16*)d_in[2]);
}
```
